```python
import torch

def _softmax(x, axis=-1):
    return torch.softmax(x, dim=axis)
def _gelu(x):
    return torch.nn.functional.gelu(x, approximate="tanh")
def _silu(x):
    return torch.nn.functional.silu(x)
def _sigmoid(x):
    return torch.sigmoid(x)
def _layernorm(x, axis=-1, eps=1e-5):
    m = x.mean(dim=axis, keepdim=True)
    v = x.var(dim=axis, keepdim=True, unbiased=False)
    return (x - m) / torch.sqrt(v + eps)
def _scatter_add(tgt, idx, src, axis=0):
    return tgt.clone().index_add_(axis, idx, src)
def _segment_sum(src, seg, n):
    out = torch.zeros((n,) + src.shape[1:], dtype=src.dtype)
    return out.index_add_(0, seg, src)
def _pad(x, pw):
    flat = tuple(v for pair in reversed(pw) for v in pair)
    return torch.nn.functional.pad(x, flat)

kernel_name = 'parallel_sum__linear_kernel_hedgehog__dense_activation_gelu__prompt'

def setup_inputs():

    _w = (lambda a: (lambda t: t.to(torch.bfloat16) if t.is_floating_point() else t)(torch.as_tensor(a)))
    def _A_setup():
        B, S, D, H = 1, 2048, 512, 32
        Dh = D // H
        return {
            "x": _w((lambda *s: torch.randn(*s, dtype=torch.bfloat16) * 0.05)(B, S, D)),
            "wqkv": _w((lambda *s: torch.randn(*s, dtype=torch.bfloat16) * 0.2)(D, 3 * D)),
            "w_phi": _w((lambda *s: torch.randn(*s, dtype=torch.bfloat16) * 0.2)(H, Dh, Dh) * (Dh ** -0.5)),
            "wo": _w((lambda *s: torch.randn(*s, dtype=torch.bfloat16) * 0.05)(D, D)),
        }
    _A_in = _A_setup()

    def _B_setup():
        B, S, D, F = 1, 2048, 512, 1792
        return {
            "x": _w((lambda *s: torch.randn(*s, dtype=torch.bfloat16) * 0.05)(B, S, D)),
            "w1": _w((lambda *s: torch.randn(*s, dtype=torch.bfloat16) * 0.8)(D, F) * (D ** -0.5)),
            "b1": _w((lambda *s: torch.randn(*s, dtype=torch.bfloat16) * 12.8)(F) * 0.01),
            "w2": _w((lambda *s: torch.randn(*s, dtype=torch.bfloat16) * 0.4)(F, D) * (F ** -0.5)),
            "b2": _w((lambda *s: torch.randn(*s, dtype=torch.bfloat16) * 6.4)(D) * 0.01),
        }
    _B_in = _B_setup()

    return {
        "A_x": _A_in["x"],
        "A_wqkv": _A_in["wqkv"],
        "A_w_phi": _A_in["w_phi"],
        "A_wo": _A_in["wo"],
        "B_x": _B_in["x"],
        "B_w1": _B_in["w1"],
        "B_b1": _B_in["b1"],
        "B_w2": _B_in["w2"],
        "B_b2": _B_in["b2"],
    }

def reference(A_x, A_wqkv, A_w_phi, A_wo, B_x, B_w1, B_b1, B_w2, B_b2):
    def _A_ref(x, wqkv, w_phi, wo):
        B, S, D = x.shape
        H = 32; Dh = D // H
        qkv = (lambda x, *d: x.permute(*d))((x @ wqkv).reshape(B, S, 3, H, Dh), 2, 0, 3, 1, 4)
        q, k, v = qkv[0], qkv[1], qkv[2]
        pq = torch.einsum("bhsd,hdf->bhsf", q, w_phi)
        pk = torch.einsum("bhsd,hdf->bhsf", k, w_phi)
        phi_q = torch.concatenate([_softmax(pq, axis=-1), _softmax(-pq, axis=-1)], axis=-1)
        phi_k = torch.concatenate([_softmax(pk, axis=-1), _softmax(-pk, axis=-1)], axis=-1)
        kv = torch.einsum("bhsf,bhsd->bhfd", phi_k, v)
        z  = torch.einsum("bhsf->bhf", phi_k)
        num = torch.einsum("bhqf,bhfd->bhqd", phi_q, kv)
        den = torch.einsum("bhqf,bhf->bhq", phi_q, z)[..., None] + 1e-6
        o = num / den
        return (lambda x, *d: x.permute(*d))(o, 0, 2, 1, 3).reshape(B, S, D) @ wo

    def _B_ref(x, w1, b1, w2, b2):
        h = torch.einsum("bsd,df->bsf", x, w1) + b1
        h = _gelu(h)
        return torch.einsum("bsf,fd->bsd", h, w2) + b2

    x = A_x
    xn = _layernorm(x, axis=-1)
    return x + _A_ref(xn, A_wqkv, A_w_phi, A_wo) + _B_ref(xn, B_w1, B_b1, B_w2, B_b2)
```

```cpp
#include <hip/hip_runtime.h>
#include <hip/hip_bf16.h>
typedef unsigned short u16;
constexpr int S=2048, D=512, H=32, DH=16, F=1792;
__device__ __forceinline__ float b2f(u16 v){ return __uint_as_float(((unsigned)v)<<16); }
__device__ __forceinline__ u16 f2b(float f){ unsigned u=__float_as_uint(f); u+=0x7fffu+((u>>16)&1u); return (u16)(u>>16); }

__global__ void k_ln(const u16* x, float* xn){
  int row=blockIdx.x; int t=threadIdx.x;
  float v[8]; float s=0;
  for(int j=0;j<8;++j){ v[j]=b2f(x[row*D+t*8+j]); s+=v[j]; }
  for(int o=32;o>0;o>>=1) s+=__shfl_xor(s,o);
  float m=s/D; float q=0;
  for(int j=0;j<8;++j){ float d=v[j]-m; q+=d*d; }
  for(int o=32;o>0;o>>=1) q+=__shfl_xor(q,o);
  float r=1.0f/sqrtf(q/D+1e-5f);
  for(int j=0;j<8;++j) xn[row*D+t*8+j]=b2f(f2b((v[j]-m)*r));
}
__global__ void k_gemm(const float* A, const u16* B, float* C, int M, int N, int K, int lda, int ldc){
  int n=blockIdx.x*blockDim.x+threadIdx.x; int m=blockIdx.y;
  if(n>=N) return;
  float acc=0;
  for(int k=0;k<K;++k) acc+=A[m*lda+k]*b2f(B[k*N+n]);
  C[m*ldc+n]=acc;
}
__global__ void k_gelu(float* h, const u16* b1){
  int i=blockIdx.x*blockDim.x+threadIdx.x; if(i>=S*F) return;
  float x=h[i]+b2f(b1[i%F]);
  float u=0.7978845608028654f*(x+0.044715f*x*x*x);
  h[i]=b2f(f2b(0.5f*x*(1.0f+tanhf(u))));
}
__global__ void k_phi(const float* qkv, const u16* wphi, float* phi){
  int i=blockIdx.x*blockDim.x+threadIdx.x; if(i>=2*S*H) return;
  int h=i%H, s=(i/H)%S, w=i/(H*S);
  float q[16]; for(int d=0;d<16;++d) q[d]=b2f(f2b(qkv[s*1536+w*512+h*16+d]));
  float p[16]; float mx=-1e30f, mn=1e30f;
  for(int f=0;f<16;++f){ float a=0; for(int d=0;d<16;++d) a+=q[d]*b2f(wphi[(h*16+d)*16+f]); p[f]=a; mx=fmaxf(mx,a); mn=fminf(mn,a);}
  float s1=0,s2=0; float e1[16],e2[16];
  for(int f=0;f<16;++f){ e1[f]=__expf(p[f]-mx); e2[f]=__expf(mn-p[f]); s1+=e1[f]; s2+=e2[f]; }
  float* o=phi+((size_t)(w*S+s)*H+h)*32;
  for(int f=0;f<16;++f){ o[f]=e1[f]/s1; o[16+f]=e2[f]/s2; }
}
__global__ void k_kv(const float* phi, const float* qkv, float* kv){
  int i=blockIdx.x*blockDim.x+threadIdx.x; if(i>=H*32*17) return;
  int d=i%17, f=(i/17)%32, h=i/(17*32);
  const float* pk=phi+(size_t)S*H*32;
  float a=0;
  for(int s=0;s<S;++s){ float p=pk[((size_t)s*H+h)*32+f]; float v= d<16? qkv[s*1536+1024+h*16+d]:1.0f; a+=p*v; }
  kv[i]=a;
}
__global__ void k_o(const float* phi, const float* kv, float* o){
  int i=blockIdx.x*blockDim.x+threadIdx.x; if(i>=S*D) return;
  int d=i%16, h=(i/16)%H, s=i/D;
  const float* pq=phi+((size_t)s*H+h)*32;
  float num=0, den=0;
  for(int f=0;f<32;++f){ num+=pq[f]*kv[(h*32+f)*17+d]; den+=pq[f]*kv[(h*32+f)*17+16]; }
  o[i]=b2f(f2b(num/(den+1e-6f)));
}
__global__ void k_fin(const u16* x, const float* a, const float* b, const u16* b2, u16* out){
  int i=blockIdx.x*blockDim.x+threadIdx.x; if(i>=S*D) return;
  out[i]=f2b(b2f(x[i])+a[i]+b[i]+b2f(b2[i%D]));
}
extern "C" void kernel_launch(void* const* d_in, const int* in_sizes, int n_in,
                              void* d_out, int out_size, void* d_ws, size_t ws_size,
                              hipStream_t stream) {
  const u16* x=(const u16*)d_in[0]; const u16* wqkv=(const u16*)d_in[1]; const u16* wphi=(const u16*)d_in[2]; const u16* wo=(const u16*)d_in[3];
  const u16* w1=(const u16*)d_in[5]; const u16* b1=(const u16*)d_in[6]; const u16* w2=(const u16*)d_in[7]; const u16* b2=(const u16*)d_in[8];
  float* ws=(float*)d_ws;
  float* xn=ws; float* qkv=xn+S*D; float* h=qkv+S*1536; float* phi=h+S*F; float* kv=phi+(size_t)2*S*H*32; float* o=kv+H*32*17+64; float* a=o+S*D; float* b=a+S*D;
  k_ln<<<S,64,0,stream>>>(x,xn);
  k_gemm<<<dim3(1536/256,S),256,0,stream>>>(xn,wqkv,qkv,S,1536,D,D,1536);
  k_gemm<<<dim3(F/256,S),256,0,stream>>>(xn,w1,h,S,F,D,D,F);
  k_gelu<<<(S*F+255)/256,256,0,stream>>>(h,b1);
  k_phi<<<(2*S*H+255)/256,256,0,stream>>>(qkv,wphi,phi);
  k_kv<<<(H*32*17+63)/64,64,0,stream>>>(phi,qkv,kv);
  k_o<<<(S*D+255)/256,256,0,stream>>>(phi,kv,o);
  k_gemm<<<dim3(D/256,S),256,0,stream>>>(o,wo,a,S,D,D,D,D);
  k_gemm<<<dim3(D/256,S),256,0,stream>>>(h,w2,b,S,D,F,F,D);
  k_fin<<<(S*D+255)/256,256,0,stream>>>(x,a,b,b2,(u16*)d_out);
}
```

```cpp
#include <hip/hip_runtime.h>
#include <hip/hip_bf16.h>
typedef unsigned short u16;
constexpr int S=2048, D=512, H=32, DH=16, F=1792;
__device__ __forceinline__ float b2f(u16 v){ return __uint_as_float(((unsigned)v)<<16); }
__device__ __forceinline__ u16 f2b(float f){ unsigned u=__float_as_uint(f); u+=0x7fffu+((u>>16)&1u); return (u16)(u>>16); }

__global__ void k_ln(const u16* x, float* xn){
  int row=blockIdx.x; int t=threadIdx.x;
  float v[8]; float s=0;
  for(int j=0;j<8;++j){ v[j]=b2f(x[row*D+t*8+j]); s+=v[j]; }
  for(int o=32;o>0;o>>=1) s+=__shfl_xor(s,o);
  float m=s/D; float q=0;
  for(int j=0;j<8;++j){ float d=v[j]-m; q+=d*d; }
  for(int o=32;o>0;o>>=1) q+=__shfl_xor(q,o);
  float r=1.0f/sqrtf(q/D+1e-5f);
  for(int j=0;j<8;++j) xn[row*D+t*8+j]=b2f(f2b((v[j]-m)*r));
}
__global__ void k_gemm(const float* A, const u16* B, float* C, int M, int N, int K, int lda, int ldc){
  int n=blockIdx.x*blockDim.x+threadIdx.x; int m=blockIdx.y;
  if(n>=N) return;
  float acc=0;
  for(int k=0;k<K;++k) acc+=A[m*lda+k]*b2f(B[k*N+n]);
  C[m*ldc+n]=acc;
}
__global__ void k_gelu(float* h, const u16* b1){
  int i=blockIdx.x*blockDim.x+threadIdx.x; if(i>=S*F) return;
  float x=h[i]+b2f(b1[i%F]);
  float u=0.7978845608028654f*(x+0.044715f*x*x*x);
  h[i]=b2f(f2b(0.5f*x*(1.0f+tanhf(u))));
}
__global__ void k_phi(const float* qkv, const u16* wphi, float* phi){
  int i=blockIdx.x*blockDim.x+threadIdx.x; if(i>=2*S*H) return;
  int h=i%H, s=(i/H)%S, w=i/(H*S);
  float q[16]; for(int d=0;d<16;++d) q[d]=b2f(f2b(qkv[s*1536+w*512+h*16+d]));
  float p[16]; float mx=-1e30f, mn=1e30f;
  for(int f=0;f<16;++f){ float a=0; for(int d=0;d<16;++d) a+=q[d]*b2f(wphi[(h*16+d)*16+f]); p[f]=a; mx=fmaxf(mx,a); mn=fminf(mn,a);}
  float s1=0,s2=0; float e1[16],e2[16];
  for(int f=0;f<16;++f){ e1[f]=__expf(p[f]-mx); e2[f]=__expf(mn-p[f]); s1+=e1[f]; s2+=e2[f]; }
  float* o=phi+((size_t)(w*S+s)*H+h)*32;
  for(int f=0;f<16;++f){ o[f]=e1[f]/s1; o[16+f]=e2[f]/s2; }
}
__global__ void k_kv(const float* phi, const float* qkv, float* kv){
  int i=blockIdx.x*blockDim.x+threadIdx.x; if(i>=H*32*17) return;
  int d=i%17, f=(i/17)%32, h=i/(17*32);
  const float* pk=phi+(size_t)S*H*32;
  float a=0;
  for(int s=0;s<S;++s){ float p=pk[((size_t)s*H+h)*32+f]; float v= d<16? qkv[s*1536+1024+h*16+d]:1.0f; a+=p*v; }
  kv[i]=a;
}
__global__ void k_o(const float* phi, const float* kv, float* o){
  int i=blockIdx.x*blockDim.x+threadIdx.x; if(i>=S*D) return;
  int d=i%16, h=(i/16)%H, s=i/D;
  const float* pq=phi+((size_t)s*H+h)*32;
  float num=0, den=0;
  for(int f=0;f<32;++f){ num+=pq[f]*kv[(h*32+f)*17+d]; den+=pq[f]*kv[(h*32+f)*17+16]; }
  o[i]=b2f(f2b(num/(den+1e-6f)));
}
__global__ void k_fin(const u16* x, const float* a, const float* b, const u16* b2, u16* out){
  int i=blockIdx.x*blockDim.x+threadIdx.x; if(i>=S*D) return;
  out[i]=f2b(b2f(x[i])+a[i]+b[i]+b2f(b2[i%D]));
}

typedef unsigned v4u __attribute__((ext_vector_type(4)));
#define GAS __attribute__((address_space(1)))
__device__ __forceinline__ unsigned long long rt() { return __builtin_amdgcn_s_memrealtime(); }
__global__ __launch_bounds__(256,1) void k_probe(const char* base, unsigned* sink){
  int b=blockIdx.x; int wid=__builtin_amdgcn_readfirstlane(threadIdx.x>>6), lane=threadIdx.x&63;
  const char* hp=base+wid*32768+lane*16;
  unsigned long long t0=rt();
  v4u r[32]; v4u acc={0,0,0,0};
  #pragma unroll
  for(int i=0;i<32;++i) r[i]=*(const GAS v4u*)(hp+i*1024);
  __builtin_amdgcn_sched_barrier(0);
  #pragma unroll
  for(int i=0;i<32;++i) acc^=r[i];
  asm volatile("":"+v"(acc));
  __builtin_amdgcn_sched_barrier(0);
  unsigned long long t1=rt();
  if(acc.x==0x12345678 && acc.y==0x9abcdef) sink[0]=1;
  if(b==0 && wid==0){ unsigned long long d=(t1-t0)*300; while(rt()-t1<d) __builtin_amdgcn_s_sleep(10); }
}
extern "C" void kernel_launch(void* const* d_in, const int* in_sizes, int n_in,
                              void* d_out, int out_size, void* d_ws, size_t ws_size,
                              hipStream_t stream) {
  const u16* x=(const u16*)d_in[0]; const u16* wqkv=(const u16*)d_in[1]; const u16* wphi=(const u16*)d_in[2]; const u16* wo=(const u16*)d_in[3];
  const u16* w1=(const u16*)d_in[5]; const u16* b1=(const u16*)d_in[6]; const u16* w2=(const u16*)d_in[7]; const u16* b2=(const u16*)d_in[8];
  float* ws=(float*)d_ws;
  float* xn=ws; float* qkv=xn+S*D; float* h=qkv+S*1536; float* phi=h+S*F; float* kv=phi+(size_t)2*S*H*32; float* o=kv+H*32*17+64; float* a=o+S*D; float* b=a+S*D;
  k_probe<<<1,256,0,stream>>>((const char*)d_in[0],(unsigned*)((char*)d_ws+(200<<20)));
  k_ln<<<S,64,0,stream>>>(x,xn);
  k_gemm<<<dim3(1536/256,S),256,0,stream>>>(xn,wqkv,qkv,S,1536,D,D,1536);
  k_gemm<<<dim3(F/256,S),256,0,stream>>>(xn,w1,h,S,F,D,D,F);
  k_gelu<<<(S*F+255)/256,256,0,stream>>>(h,b1);
  k_phi<<<(2*S*H+255)/256,256,0,stream>>>(qkv,wphi,phi);
  k_kv<<<(H*32*17+63)/64,64,0,stream>>>(phi,qkv,kv);
  k_o<<<(S*D+255)/256,256,0,stream>>>(phi,kv,o);
  k_gemm<<<dim3(D/256,S),256,0,stream>>>(o,wo,a,S,D,D,D,D);
  k_gemm<<<dim3(D/256,S),256,0,stream>>>(h,w2,b,S,D,F,F,D);
  k_fin<<<(S*D+255)/256,256,0,stream>>>(x,a,b,b2,(u16*)d_out);
}
```

```cpp
#include <hip/hip_runtime.h>
#include <stdint.h>

namespace {

typedef unsigned short u16;
typedef unsigned long long u64;
typedef unsigned v4u __attribute__((ext_vector_type(4)));
typedef unsigned v2u __attribute__((ext_vector_type(2)));
typedef float f32x16 __attribute__((ext_vector_type(16)));
typedef float f32x4 __attribute__((ext_vector_type(4)));
typedef float f32x2 __attribute__((ext_vector_type(2)));
typedef __bf16 bf16x8 __attribute__((ext_vector_type(8)));
typedef __bf16 bf16x2 __attribute__((ext_vector_type(2)));
typedef short s16x4 __attribute__((ext_vector_type(4)));
#define LAS __attribute__((address_space(3)))
#define GAS __attribute__((address_space(1)))
#define DI __device__ __forceinline__
#define AI __attribute__((always_inline))

#define STAMPS 0
#define ROT1 1
#define ROT2 0

constexpr unsigned WS_EPOCH = 0;
constexpr unsigned FL_E = 4096;
constexpr unsigned FL_L = FL_E + 128 * 128;
constexpr unsigned FL_K = FL_L + 128 * 128;
constexpr unsigned FL_O = FL_K + 128 * 128;
constexpr unsigned WS_ST = 73728;
constexpr unsigned WS_KVP = 131072;
constexpr unsigned WS_ZP = WS_KVP + 524288;
constexpr unsigned WS_HB = 1048576;
constexpr unsigned HB_BYTES = 64u * 144u * 1024u;
constexpr unsigned WS_STAMP = 16u << 20;

constexpr unsigned SLOT = 32768;
constexpr unsigned L_MISC = 131072;
constexpr unsigned L_STATS = L_MISC;
constexpr unsigned L_CS0 = L_MISC + 1024;
constexpr unsigned L_CS1 = L_MISC + 1536;
constexpr unsigned L_B1 = L_MISC + 2048;
constexpr unsigned L_WPHI = L_MISC + 3072;
constexpr unsigned L_CNT = L_MISC + 5120;
constexpr unsigned L_TAG = L_MISC + 5248;
constexpr unsigned L_ZT = L_MISC + 5376;
constexpr unsigned L_AFR = L_MISC + 8192;
constexpr unsigned LDS_BYTES = 163840;
enum { C_CS1 = 0, C_LATE = 1, C_KV = 2, C_O = 3, C_AFR = 4, C_KVRDY = 5, C_LATERDY = 6, C_ORDY = 7, C_FREE = 8   };

constexpr unsigned kSpin = 1u << 16;

struct Params {
  const u16* x; const u16* wqkv; const u16* wphi; const u16* wo;
  const u16* w1; const u16* b1; const u16* w2; const u16* b2;
  u16* out; char* ws;
};

DI unsigned f2u(float f) { return __builtin_bit_cast(unsigned, f); }
DI float u2f(unsigned u) { return __builtin_bit_cast(float, u); }
DI float fmx(float a, float b) { return __builtin_fmaxf(a, b); }
DI f32x16 mfma(v4u a, v4u b, f32x16 c) {
  return __builtin_amdgcn_mfma_f32_32x32x16_bf16(__builtin_bit_cast(bf16x8, a), __builtin_bit_cast(bf16x8, b), c, 0, 0, 0);
}
DI f32x16 zero16() { f32x16 z; for (int r = 0; r < 16; ++r) z[r] = 0.f; return z; }
DI v4u lds128(unsigned a) { return *(const LAS v4u*)(size_t)a; }
DI f32x4 lds128f(unsigned a) { return *(const LAS f32x4*)(size_t)a; }
DI v2u tr64(unsigned a) { return __builtin_bit_cast(v2u, __builtin_amdgcn_ds_read_tr16_b64_v4i16((LAS s16x4*)(size_t)a)); }
DI v4u tr2(unsigned a0, unsigned a1) { v2u l = tr64(a0), h = tr64(a1); return v4u{l.x, l.y, h.x, h.y}; }
DI unsigned pk2(float a, float b) { return __builtin_bit_cast(unsigned, __builtin_convertvector(f32x2{a, b}, bf16x2)); }
DI float xhalf_add(float v) { auto r = __builtin_amdgcn_permlane32_swap(f2u(v), f2u(v), false, false); return u2f(r[0]) + u2f(r[1]); }
DI float xhalf_max(float v) { auto r = __builtin_amdgcn_permlane32_swap(f2u(v), f2u(v), false, false); return fmx(u2f(r[0]), u2f(r[1])); }
DI float shx(float v, int m, int lane) { return u2f((unsigned)__builtin_amdgcn_ds_bpermute((lane ^ m) << 2, (int)f2u(v))); }
DI f32x4 lanesum(v4u f, f32x4 c) {
  const s16x4 one = {0x3F80, 0x3F80, 0x3F80, 0x3F80};
  c = __builtin_amdgcn_mfma_f32_4x4x4bf16_1k(one, __builtin_bit_cast(s16x4, v2u{f.x, f.y}), c, 0, 0, 0);
  return __builtin_amdgcn_mfma_f32_4x4x4bf16_1k(one, __builtin_bit_cast(s16x4, v2u{f.z, f.w}), c, 0, 0, 0);
}
template <int CTRL> DI float dppadd(float v) { return v + u2f((unsigned)__builtin_amdgcn_update_dpp(0, (int)f2u(v), CTRL, 0xf, 0xf, true)); }
DI float wavesum(float v) {
  v = dppadd<0xB1>(v); v = dppadd<0x4E>(v); v = dppadd<0x141>(v); v = dppadd<0x140>(v);
  auto r = __builtin_amdgcn_permlane16_swap(f2u(v), f2u(v), false, false);
  return xhalf_add(u2f(r[0]) + u2f(r[1]));
}
DI void dma(unsigned m0v, unsigned voff, __amdgpu_buffer_rsrc_t rs, unsigned soff) {
  asm volatile("s_mov_b32 m0, %0\n\ts_nop 0\n\tbuffer_load_dwordx4 %1, %2, %3 offen lds" ::"s"(m0v), "v"(voff), "s"(rs), "s"(soff) : "memory");
}
DI void st128(v4u v, __amdgpu_buffer_rsrc_t rs, unsigned voff) { __builtin_amdgcn_raw_buffer_store_b128(v, rs, voff, 0, 16); }
#define BAR() do { __builtin_amdgcn_sched_barrier(0); asm volatile("s_waitcnt lgkmcnt(0)\n\ts_barrier" ::: "memory"); __builtin_amdgcn_sched_barrier(0); } while (0)
#define VMW(n) asm volatile("s_waitcnt vmcnt(" #n ")" ::: "memory")
constexpr unsigned ONES = 0x3F803F80u;

#define STAMP(i) do { } while (0)

DI void poll_flags(const char* ws, unsigned base, int n, u64 tag, int lane) {
  const GAS u64* f = (const GAS u64*)(ws + base + (unsigned)(lane < n ? lane : 0) * 128u);
#pragma unroll 1
  for (unsigned spin = 0; spin < kSpin; ++spin) {
    const u64 v = __hip_atomic_load(f, __ATOMIC_RELAXED, __HIP_MEMORY_SCOPE_AGENT);
    if (__builtin_amdgcn_ballot_w64(v != tag) == 0) break;
    __builtin_amdgcn_s_sleep(1);
  }
  __builtin_amdgcn_fence(__ATOMIC_ACQUIRE, "wavefront");
  asm volatile("" ::: "memory");
}
DI void lds_wait_ge(unsigned addr, unsigned want) {
#pragma unroll 1
  for (unsigned spin = 0; spin < (kSpin << 4); ++spin) {
    const unsigned v = *(volatile LAS unsigned*)(size_t)addr;
    if ((unsigned)__builtin_amdgcn_readfirstlane(v) >= want) break;
    __builtin_amdgcn_s_sleep(1);
  }
  asm volatile("" ::: "memory");
}
DI void lds_set(unsigned addr, unsigned v) { *(volatile LAS unsigned*)(size_t)addr = v; }
DI void lds_inc(unsigned addr, int lane) { if (lane == 0) __hip_atomic_fetch_add((LAS unsigned*)(size_t)addr, 1u, __ATOMIC_RELAXED, __HIP_MEMORY_SCOPE_WORKGROUP); }
DI void publish_last(unsigned cnt_addr, unsigned n, char* ws, unsigned flag_off, u64 tag, int lane) {
  VMW(0);
  if (lane == 0) {
    const unsigned old = __hip_atomic_fetch_add((LAS unsigned*)(size_t)cnt_addr, 1u, __ATOMIC_RELAXED, __HIP_MEMORY_SCOPE_WORKGROUP);
    if (old == n - 1) __hip_atomic_store((GAS u64*)(ws + flag_off), tag, __ATOMIC_RELAXED, __HIP_MEMORY_SCOPE_AGENT);
  }
}
DI u64 lds_tag() { const v2u t = *(const LAS v2u*)(size_t)L_TAG; return ((u64)(unsigned)__builtin_amdgcn_readfirstlane(t.y) << 32) | (unsigned)__builtin_amdgcn_readfirstlane(t.x); }

DI void fold8(const f32x16& acc, int r0, float rstd, float mr, unsigned cs, int hh, float* y) {
#pragma unroll
  for (int g = 0; g < 2; ++g) {
    const f32x4 t = lds128f(cs + (unsigned)((r0 >> 2) + g) * 32u + (unsigned)hh * 16u) * mr;
#pragma unroll
    for (int k = 0; k < 4; ++k) y[4 * g + k] = __builtin_fmaf(acc[r0 + 4 * g + k], rstd, t[k]);
  }
}
DI void ffn_tile(const f32x16& acc, float rstd, float mr, unsigned cs, unsigned bias, int hh, __amdgpu_buffer_rsrc_t rsHB, unsigned voff) {
  f32x2 y[8], d[8];
#pragma unroll
  for (int g = 0; g < 4; ++g) {
    const unsigned o = (unsigned)g * 32u + (unsigned)hh * 16u;
    const f32x4 t = lds128f(cs + o) * mr + lds128f(bias + o);
    y[2 * g] = f32x2{acc[4 * g], acc[4 * g + 1]} * rstd + f32x2{t[0], t[1]};
    y[2 * g + 1] = f32x2{acc[4 * g + 2], acc[4 * g + 3]} * rstd + f32x2{t[2], t[3]};
  }
#pragma unroll
  for (int k = 0; k < 8; ++k) d[k] = y[k] * ((y[k] * y[k]) * (-0.10294324f) + (-2.30220819f));
#pragma unroll
  for (int k = 0; k < 8; ++k) { d[k].x = __builtin_amdgcn_exp2f(d[k].x); d[k].y = __builtin_amdgcn_exp2f(d[k].y); }
#pragma unroll
  for (int k = 0; k < 8; ++k) d[k] += 1.0f;
#pragma unroll
  for (int k = 0; k < 8; ++k) { d[k].x = __builtin_amdgcn_rcpf(d[k].x); d[k].y = __builtin_amdgcn_rcpf(d[k].y); }
  v4u o0, o1;
#pragma unroll
  for (int k = 0; k < 4; ++k) {
    const f32x2 g0 = y[k] * d[k], g1 = y[4 + k] * d[4 + k];
    o0[k] = pk2(g0.x, g0.y); o1[k] = pk2(g1.x, g1.y);
  }
  st128(o0, rsHB, voff); st128(o1, rsHB, voff + 1024u);
}
DI void hedge(const f32x16& d, v4u& o0, v4u& o1) {
  constexpr float L = 1.4426950408889634f;
  float m0 = fmx(fmx(fmx(d[0], d[1]), fmx(d[2], d[3])), fmx(fmx(d[4], d[5]), fmx(d[6], d[7])));
  float m1 = fmx(fmx(fmx(d[8], d[9]), fmx(d[10], d[11])), fmx(fmx(d[12], d[13]), fmx(d[14], d[15])));
  m0 = xhalf_max(m0) * -L; m1 = xhalf_max(m1) * -L;
  float e[16];
#pragma unroll
  for (int i = 0; i < 8; ++i) { e[i] = __builtin_amdgcn_exp2f(__builtin_fmaf(d[i], L, m0)); e[8 + i] = __builtin_amdgcn_exp2f(__builtin_fmaf(d[8 + i], L, m1)); }
  float s0 = ((e[0] + e[1]) + (e[2] + e[3])) + ((e[4] + e[5]) + (e[6] + e[7]));
  float s1 = ((e[8] + e[9]) + (e[10] + e[11])) + ((e[12] + e[13]) + (e[14] + e[15]));
  s0 = __builtin_amdgcn_rcpf(xhalf_add(s0)); s1 = __builtin_amdgcn_rcpf(xhalf_add(s1));
#pragma unroll
  for (int k = 0; k < 4; ++k) { o0[k] = pk2(e[2 * k] * s0, e[2 * k + 1] * s0); o1[k] = pk2(e[8 + 2 * k] * s1, e[8 + 2 * k + 1] * s1); }
}
DI v4u wphi_frag(int hd, int lane) {
  const int hh = lane >> 5, q = (lane >> 2) & 3, pp = lane & 3;
  const unsigned a = L_WPHI + (unsigned)hd * 512u + (unsigned)(4 * hh + q) * 32u + (unsigned)pp * 8u;
  v4u f = tr2(a, a + 256u);
  const unsigned sg = (lane & 16) ? 0x80008000u : 0u;
  f.x ^= sg; f.y ^= sg; f.z ^= sg; f.w ^= sg;
  return f;
}
DI v4u selv(bool s, v4u a, v4u b) { const unsigned m = s ? 0xffffffffu : 0u; return v4u{(a.x & m) | (b.x & ~m), (a.y & m) | (b.y & ~m), (a.z & m) | (b.z & ~m), (a.w & m) | (b.w & ~m)}; }
DI v4u pack8(const float* y) { return v4u{pk2(y[0], y[1]), pk2(y[2], y[3]), pk2(y[4], y[5]), pk2(y[6], y[7])}; }

struct Ctx {
  int lane, tq, hh;
  int p, gi, G;
  int rot;
  int tt, ct;
  int rotE, rotL, rotO;
  bool ffn;
};

DI void role_a(const Params& P, const Ctx& c, int wid) {
  const int lane = c.lane, tq = c.tq, hh = c.hh, t = lane & 31;
  const int cb = (lane >> 4) & 1, q = (lane >> 2) & 3, pp = lane & 3, sw = (q >> 1) & 1;
  const int th = tq & 1, up = tq >> 1;
  unsigned xa[4];
#pragma unroll
  for (int ks = 0; ks < 4; ++ks) xa[ks] = (unsigned)(64 * th + t) * 128u + (unsigned)(((2 * ks + hh) ^ ((t >> 1) & 7)) * 16);
  const unsigned wl = 16384u + (unsigned)(8 * hh + q) * 128u + (unsigned)((4 * (up ^ sw) + 2 * cb + (pp >> 1)) * 16 + 8 * (pp & 1));
  f32x16 acc[2][2];
#pragma unroll
  for (int i = 0; i < 2; ++i) { acc[i][0] = zero16(); acc[i][1] = zero16(); }
  f32x4 cs = {0.f, 0.f, 0.f, 0.f};
  unsigned x0 = xa[0], x1 = xa[1], x2 = xa[2], x3 = xa[3], w0 = wl;
  v4u xf[4][2], wf[4][2];
  auto rd = [&](int ks, v4u* X, v4u* W) AI {
    const unsigned xb = ks == 0 ? x0 : (ks == 1 ? x1 : (ks == 2 ? x2 : x3));
    X[0] = lds128(xb); X[1] = lds128(xb + 4096u);
    const unsigned a = w0 + (unsigned)(ks * 2048);
    W[0] = tr2(a, a + 512u); W[1] = tr2(a + 8192u, a + 8704u);
  };
  BAR();
  rd(0, xf[0], wf[0]); rd(1, xf[1], wf[1]);
#pragma unroll 1
  for (int kc = 0; kc < 8; ++kc) {
#pragma unroll
    for (int ks = 0; ks < 4; ++ks) {
      if (ks == 2) {
        __builtin_amdgcn_sched_barrier(0);
        asm volatile("s_barrier" ::: "memory");
        __builtin_amdgcn_sched_barrier(0);
        const unsigned sb = (unsigned)((kc + 1) & 3) * SLOT;
        x0 = xa[0] + sb; x1 = xa[1] + sb; w0 = wl + sb;
        asm volatile("" : "+v"(x0), "+v"(x1), "+v"(w0));
      }
      if (ks == 0) {
        const unsigned sb = (unsigned)(kc & 3) * SLOT;
        x2 = xa[2] + sb; x3 = xa[3] + sb;
        asm volatile("" : "+v"(x2), "+v"(x3));
      }
      rd((ks + 2) & 3, xf[(ks + 2) & 3], wf[(ks + 2) & 3]);
#pragma unroll
      for (int i = 0; i < 2; ++i) { acc[i][0] = mfma(wf[ks][i], xf[ks][0], acc[i][0]); acc[i][1] = mfma(wf[ks][i], xf[ks][1], acc[i][1]); }
      cs = lanesum(selv(th != 0, wf[ks][1], wf[ks][0]), cs);
      __builtin_amdgcn_sched_group_barrier(0x008, 1, 0);
      __builtin_amdgcn_sched_group_barrier(0x100, 2, 0);
      __builtin_amdgcn_sched_group_barrier(0x008, 1, 0);
      __builtin_amdgcn_sched_group_barrier(0x100, 2, 0);
      __builtin_amdgcn_sched_group_barrier(0x008, 1, 0);
      __builtin_amdgcn_sched_group_barrier(0x100, 2, 0);
      __builtin_amdgcn_sched_group_barrier(0x002, 4, 0);
      __builtin_amdgcn_sched_group_barrier(0x008, 3, 0);
      __builtin_amdgcn_sched_barrier(0);
    }
    asm volatile("" : "+v"(acc[0][0]), "+v"(acc[0][1]), "+v"(acc[1][0]), "+v"(acc[1][1]), "+v"(cs));
  }
  STAMP(2);
  {
    const float s = xhalf_add(cs[0]);
    if (hh == 0) *(LAS float*)(size_t)(L_CS0 + (unsigned)(up + 2 * th) * 128u + (unsigned)t * 4u) = s;
  }
  BAR();
  const u64 tag = lds_tag();
  float rstd[2], mr[2];
#pragma unroll
  for (int b = 0; b < 2; ++b) {
    const f32x2 st = *(const LAS f32x2*)(size_t)(L_STATS + (unsigned)(64 * th + 32 * b + t) * 8u);
    rstd[b] = st.x; mr[b] = st.y;
  }
  const __amdgpu_buffer_rsrc_t rsHB = __builtin_amdgcn_make_buffer_rsrc(P.ws + WS_HB, 0, HB_BYTES, 0x00020000);
  if (c.ffn) {
#pragma unroll
    for (int b = 0; b < 2; ++b)
#pragma unroll
      for (int i = 0; i < 2; ++i) {
        const unsigned tile = (unsigned)(up + 2 * i), T32 = (unsigned)(c.p * 4 + 2 * th + b);
        ffn_tile(acc[i][b], rstd[b], mr[b], L_CS0 + tile * 128u, L_B1 + tile * 128u, hh, rsHB,
                 (unsigned)lane * 16u + (T32 * 144u + (unsigned)(8 * c.gi) + 2u * tile) * 1024u);
      }
    STAMP(8);
    VMW(0);
    BAR();
    if (wid == 0 && lane == 0) __hip_atomic_store((GAS u64*)(P.ws + FL_E + (unsigned)(c.p * 8 + c.gi) * 128u), tag, __ATOMIC_RELAXED, __HIP_MEMORY_SCOPE_AGENT);
  } else {
#pragma unroll
    for (int b = 0; b < 2; ++b)
#pragma unroll
      for (int hf = 0; hf < 2; ++hf) {
        const int hd = 2 * up + hf;
        float y[8];
        fold8(acc[0][b], 8 * hf, rstd[b], mr[b], L_CS0 + (unsigned)up * 128u, hh, y);
        v4u o0, o1;
        hedge(mfma(wphi_frag(hd, lane), pack8(y), zero16()), o0, o1);
        const unsigned img = (unsigned)hd * SLOT + 16384u;
        const unsigned row = (unsigned)(64 * th + 32 * b + t);
        const unsigned g = (row >> 1) & 7u;
        *(LAS v2u*)(size_t)(img + row * 64u + (((unsigned)(0 + hh)) ^ g) * 8u) = v2u{o0.x, o0.y};
        *(LAS v2u*)(size_t)(img + row * 64u + (((unsigned)(2 + hh)) ^ g) * 8u) = v2u{o0.z, o0.w};
        *(LAS v2u*)(size_t)(img + row * 64u + (((unsigned)(4 + hh)) ^ g) * 8u) = v2u{o1.x, o1.y};
        *(LAS v2u*)(size_t)(img + row * 64u + (((unsigned)(6 + hh)) ^ g) * 8u) = v2u{o1.z, o1.w};
        fold8(acc[1][b], 8 * hf, rstd[b], mr[b], L_CS0 + (unsigned)(2 + up) * 128u, hh, y);
        const unsigned gv = (row >> 2) & 3u;
        *(LAS v2u*)(size_t)(img + 8192u + row * 32u + (((unsigned)(0 + hh)) ^ gv) * 8u) = v2u{pk2(y[0], y[1]), pk2(y[2], y[3])};
        *(LAS v2u*)(size_t)(img + 8192u + row * 32u + (((unsigned)(2 + hh)) ^ gv) * 8u) = v2u{pk2(y[4], y[5]), pk2(y[6], y[7])};
      }
    STAMP(8);
    BAR();
    const unsigned img = (unsigned)tq * SLOT + 16384u;
    f32x16 kv = zero16();
    unsigned pa[2], va[2];
#pragma unroll
    for (int i2 = 0; i2 < 2; ++i2) {
      const unsigned r = (unsigned)(8 * hh + 4 * i2 + q);
      pa[i2] = img + r * 64u + (((unsigned)(4 * cb + pp)) ^ ((r >> 1) & 7u)) * 8u;
      va[i2] = img + 8192u + r * 32u + (((unsigned)pp) ^ ((r >> 2) & 3u)) * 8u;
    }
#pragma unroll
    for (int ks = 0; ks < 8; ++ks) {
      const v4u af = tr2(pa[0] + (unsigned)ks * 1024u, pa[1] + (unsigned)ks * 1024u);
      v4u bf = tr2(va[0] + (unsigned)ks * 512u, va[1] + (unsigned)ks * 512u);
      if (cb) bf = v4u{ONES, ONES, ONES, ONES};
      kv = mfma(af, bf, kv);
    }
    const unsigned hidx = (unsigned)(c.gi * 4 + tq) * 16u + (unsigned)c.p;
    const __amdgpu_buffer_rsrc_t rsKV = __builtin_amdgcn_make_buffer_rsrc(P.ws + WS_KVP, 0, 524288 + 65536, 0x00020000);
    if (cb == 0) {
#pragma unroll
      for (int s = 0; s < 2; ++s) {
        const v4u o = v4u{pk2(kv[8 * s], kv[8 * s + 1]), pk2(kv[8 * s + 2], kv[8 * s + 3]), pk2(kv[8 * s + 4], kv[8 * s + 5]), pk2(kv[8 * s + 6], kv[8 * s + 7])};
        st128(o, rsKV, (unsigned)(s * 512 + (hh * 16 + (lane & 15)) * 16) + hidx * 1024u);
      }
    } else if ((lane & 15) == 0) {
#pragma unroll
      for (int g4 = 0; g4 < 4; ++g4) {
        const v4u o = v4u{f2u(kv[4 * g4]), f2u(kv[4 * g4 + 1]), f2u(kv[4 * g4 + 2]), f2u(kv[4 * g4 + 3])};
        st128(o, rsKV, (unsigned)((8 * g4 + 4 * hh) * 4) + 524288u + hidx * 128u);
      }
    }
    publish_last(L_CNT + C_KV * 4, 4, P.ws, FL_K + (unsigned)(c.gi * 16 + c.p) * 128u, tag, lane);
  }
  STAMP(3);
  unsigned tabB, tabWlo, tabWhi;
  {
    const int jj = lane < 36 ? lane : 35;
    int sg;
    if (jj < 16) sg = 4 * ((jj + c.rotE) & 15) + tq;
    else if (jj < 28) sg = 64 + 4 * ((jj - 16 + c.rotL) % 12) + tq;
    else sg = 112 + 4 * ((jj - 28 + c.rotO) & 7) + tq;
    int row;
    if (sg < 64) { const int g = sg >> 3; row = 224 * g + 32 * (g & 1) + 16 * (sg & 7); }
    else if (sg < 112) {
      const int e = sg - 64, g = e / 6, i = e - 6 * g;
      row = i < 4 ? 224 * g + 32 * (g & 1) + 128 + 16 * i : ((g & 1) ? 224 * g : 224 * g + 192) + 16 * (i - 4);
    } else row = 16 * (sg - 112);
    const u64 a = (u64)(size_t)(sg < 112 ? P.w2 : P.wo) + (u64)row * 1024u + (u64)c.ct * 128u;
    tabB = (unsigned)sg * 1024u; tabWlo = (unsigned)a; tabWhi = (unsigned)(a >> 32);
  }
  const unsigned vw0 = (unsigned)(lane >> 3) * 1024u + (unsigned)(((lane & 7) ^ (((lane >> 4) & 1) << 2)) * 16), vw1 = vw0 + 8192u;
  const unsigned ring = (unsigned)tq * SLOT;
  auto wdma = [&](int j) AI {
    const unsigned lo = __builtin_amdgcn_readlane(tabWlo, j), hi = __builtin_amdgcn_readlane(tabWhi, j);
    const u64 b = ((u64)hi << 32) | lo;
    const unsigned m = ring + (unsigned)(j & 15) * 2048u;
    asm volatile("s_nop 4\n\ts_mov_b32 m0, %0\n\ts_nop 0\n\tglobal_load_lds_dwordx4 %1, %3\n\ts_add_u32 m0, m0, 0x400\n\ts_nop 0\n\tglobal_load_lds_dwordx4 %2, %3"
                 ::"s"(m), "v"(vw0), "v"(vw1), "s"(b) : "memory", "scc");
  };
  lds_wait_ge(L_CNT + (C_FREE + tq) * 4, 4);
  STAMP(9);
#pragma unroll 1
  for (int j = 0; j < 16; ++j) wdma(j);
  STAMP(4);
  const int pan = c.tt >> 1;
  poll_flags(P.ws, FL_E + (unsigned)pan * 8u * 128u, 8, tag, lane);
  STAMP(5);
  const unsigned vb0 = (unsigned)lane * 16u + (unsigned)(2 * c.tt) * 144u * 1024u, vb1 = vb0 + 144u * 1024u;
  constexpr int NB = 12;
  v4u br[NB][2];
#pragma unroll
  for (int u = 0; u < NB; ++u) {
    const unsigned so = __builtin_amdgcn_readlane(tabB, u);
    br[u][0] = __builtin_amdgcn_raw_buffer_load_b128(rsHB, vb0, so, 16);
    br[u][1] = __builtin_amdgcn_raw_buffer_load_b128(rsHB, vb1, so, 16);
  }
  unsigned w2l[2];
#pragma unroll
  for (int h = 0; h < 2; ++h) w2l[h] = ring + (unsigned)(4 * hh + q) * 128u + (unsigned)((4 * (h ^ sw) + 2 * cb + (pp >> 1)) * 16 + 8 * (pp & 1));
  f32x16 o[2][2];
#pragma unroll
  for (int a = 0; a < 2; ++a)
#pragma unroll
    for (int b = 0; b < 2; ++b) o[a][b] = zero16();
  v4u af[2];
  asm volatile("" ::"v"(br[0][0]), "v"(br[0][1]) : "memory");
  af[0] = tr2(w2l[0], w2l[0] + 1024u); af[1] = tr2(w2l[1], w2l[1] + 1024u);
#pragma unroll 1
  for (int it = 0; it < 36 / NB; ++it) {
    STAMP(10 + it);
#pragma unroll
    for (int u = 0; u < NB; ++u) {
      const int j = NB * it + u;
      v4u an[2];
      asm volatile("" ::"v"(br[(u + 1) % NB][0]), "v"(br[(u + 1) % NB][1]) : "memory");
      {
        unsigned p0 = w2l[0] + (unsigned)((j + 1) & 15) * 2048u, p1 = w2l[1] + (unsigned)((j + 1) & 15) * 2048u;
        asm volatile("" : "+v"(p0), "+v"(p1));
        an[0] = tr2(p0, p0 + 1024u); an[1] = tr2(p1, p1 + 1024u);
      }
      o[0][0] = mfma(af[0], br[u][0], o[0][0]); o[0][1] = mfma(af[0], br[u][1], o[0][1]);
      o[1][0] = mfma(af[1], br[u][0], o[1][0]); o[1][1] = mfma(af[1], br[u][1], o[1][1]);
      asm volatile("" ::"v"(af[0]), "v"(af[1]) : "memory");
      if (j < 20) wdma(j + 16);
      if (u == 4 && it == 0) lds_wait_ge(L_CNT + C_LATERDY * 4, 1);
      if (u == 4 && it == 1) lds_wait_ge(L_CNT + C_ORDY * 4, 1);
      {
        const int jn = j + NB;
        const unsigned so = jn < 36 ? (unsigned)__builtin_amdgcn_readlane(tabB, jn) : 0x7F000000u;
        br[u][0] = __builtin_amdgcn_raw_buffer_load_b128(rsHB, vb0, so, 16);
        br[u][1] = __builtin_amdgcn_raw_buffer_load_b128(rsHB, vb1, so, 16);
      }
      af[0] = an[0]; af[1] = an[1];
    }
  }
  STAMP(6);
#pragma unroll
  for (int a = 0; a < 2; ++a)
#pragma unroll
    for (int b = 0; b < 2; ++b)
#pragma unroll
      for (int g4 = 0; g4 < 4; ++g4)
        *(LAS f32x4*)(size_t)(ring + (unsigned)(32 * b + t) * 272u + (unsigned)(32 * a + 8 * g4 + 4 * hh) * 4u) =
            f32x4{o[a][b][4 * g4], o[a][b][4 * g4 + 1], o[a][b][4 * g4 + 2], o[a][b][4 * g4 + 3]};
  VMW(0);
  BAR();
  STAMP(7);
}

template <int NT> DI void pass1(const v4u (&xr)[32], f32x16 (&acc)[NT], f32x4& cs, const unsigned (&wb)[NT], int lane) {
#pragma unroll
  for (int qq = 0; qq < 4; ++qq) {
    unsigned a[NT];
#pragma unroll
    for (int i = 0; i < NT; ++i) { a[i] = wb[i] + (unsigned)qq * SLOT; asm volatile("" : "+v"(a[i])); }
    v4u f[3][NT];
    auto rd = [&](int k8, v4u* W) AI {
      const unsigned o = (unsigned)((k8 >> 2) * 8192 + (k8 & 3) * 2048);
#pragma unroll
      for (int i = 0; i < NT; ++i) W[i] = tr2(a[i] + o, a[i] + o + 512u);
    };
    rd(0, f[0]); rd(1, f[1]);
    __builtin_amdgcn_sched_barrier(0);
#pragma unroll
    for (int k8 = 0; k8 < 8; ++k8) {
      if (k8 < 6) rd(k8 + 2, f[(k8 + 2) % 3]);
      const v4u xf = xr[8 * qq + k8];
#pragma unroll
      for (int i = 0; i < NT; ++i) acc[i] = mfma(f[k8 % 3][i], xf, acc[i]);
      cs = lanesum(f[k8 % 3][0], cs);
      if (k8 < 6) {
#pragma unroll
        for (int i = 0; i < NT; ++i) {
          __builtin_amdgcn_sched_group_barrier(0x008, 1, 0);
          __builtin_amdgcn_sched_group_barrier(0x100, 2, 0);
        }
        __builtin_amdgcn_sched_group_barrier(0x008, 2, 0);
      }
      __builtin_amdgcn_sched_barrier(0);
    }
    asm volatile("" : "+v"(acc[0]), "+v"(acc[1]), "+v"(acc[NT - 1]), "+v"(cs) :: "memory");
    lds_inc(L_CNT + (C_FREE + qq) * 4, lane);
  }
}

template <bool ffn> DI u64 role_b_head(const Params& P, const Ctx& c, int wid) {
  const int lane = c.lane, tq = c.tq, hh = c.hh, t = lane & 31;
  const int g = c.gi;
  const unsigned tok = (unsigned)(c.p * 128 + 8 * c.G + 2 * tq);
  v4u r0, r1;
  {
    const u16* rp = P.x + (size_t)tok * 512 + lane * 8;
    asm volatile("global_load_dwordx4 %0, %2, off\n\tglobal_load_dwordx4 %1, %2, off offset:1024" : "=&v"(r0), "=&v"(r1) : "v"(rp) : "memory");
  }
  const __amdgpu_buffer_rsrc_t rsX = __builtin_amdgcn_make_buffer_rsrc((void*)P.x, 0, 2048 * 1024, 0x00020000);
  const __amdgpu_buffer_rsrc_t rsW = __builtin_amdgcn_make_buffer_rsrc((void*)(ffn ? P.w1 : P.wqkv), 0, ffn ? 512 * 3584 : 512 * 3072, 0x00020000);
  constexpr unsigned RS = ffn ? 3584u : 3072u;
  const unsigned cA = (unsigned)(224 * g + 32 * (g & 1));
  const unsigned u0 = ffn ? cA * 2u : (unsigned)(512 + 64 * g) * 2u, u1 = ffn ? cA * 2u + 128u : (unsigned)(1024 + 64 * g) * 2u;
  const unsigned ua = ffn ? (cA + 128u) * 2u : (unsigned)(64 * g) * 2u;
  const unsigned ub = (unsigned)((g & 1) ? 224 * g - 32 : 224 * g + 192) * 2u;
  const unsigned voffX = (unsigned)(lane >> 3) * 1024u + (unsigned)(((lane & 7) ^ ((4 * (tq & 1) + (lane >> 4)) & 7)) * 16);
  const unsigned voffW = (unsigned)(lane >> 3) * RS + (unsigned)(((lane & 7) ^ (((lane >> 4) & 1) << 2)) * 16);
  const unsigned soffX = (unsigned)(c.p * 128 + 8 * tq) * 1024u;
  const unsigned wrow = 32u * (unsigned)(tq & 1) * RS;
  auto fill = [&](int f) AI {
    const unsigned slot = (unsigned)(f & 3) * SLOT;
    if (f < 8) {
      const unsigned kcp = (unsigned)((f + c.rot) & 7);
      unsigned sx = soffX + kcp * 128u, sw = 64u * kcp * RS + wrow + ((tq >> 1) ? u1 : u0);
      unsigned mx = slot + (unsigned)tq * 1024u, mw = slot + 16384u + (unsigned)tq * 4096u;
#pragma unroll 1
      for (int m = 0; m < 4; ++m) {
        dma(mx, voffX, rsX, sx); dma(mw, voffW, rsW, sw);
        mx += 4096u; sx += 32768u; mw += 1024u; sw += 8u * RS;
      }
    } else {
      const unsigned kcp = (unsigned)((2 * (f - 8) + (tq >> 1) + c.rot) & 7);
      unsigned sw = 64u * kcp * RS + wrow, mw = slot + (unsigned)tq * 4096u;
#pragma unroll 1
      for (int m = 0; m < 4; ++m) {
        dma(mw, voffW, rsW, sw + ua);
        if (ffn) dma(mw + 16384u, voffW, rsW, sw + ub);
        mw += 1024u; sw += 8u * RS;
      }
    }
  };
  fill(0); fill(1);
  float st0;
  {
    asm volatile("s_waitcnt vmcnt(16)" : "+v"(r0), "+v"(r1) :: "memory");
    float v[2][8], mean[2], rs[2];
#pragma unroll
    for (int k = 0; k < 4; ++k) {
      v[0][2 * k] = u2f(r0[k] << 16); v[0][2 * k + 1] = u2f(r0[k] & 0xffff0000u);
      v[1][2 * k] = u2f(r1[k] << 16); v[1][2 * k + 1] = u2f(r1[k] & 0xffff0000u);
    }
#pragma unroll
    for (int i = 0; i < 2; ++i) mean[i] = wavesum(((v[i][0] + v[i][1]) + (v[i][2] + v[i][3])) + ((v[i][4] + v[i][5]) + (v[i][6] + v[i][7]))) * (1.0f / 512.0f);
#pragma unroll
    for (int i = 0; i < 2; ++i) {
      float q2 = 0.f;
#pragma unroll
      for (int k = 0; k < 8; ++k) { const float d = v[i][k] - mean[i]; q2 = __builtin_fmaf(d, d, q2); }
      rs[i] = __builtin_amdgcn_rsqf(wavesum(q2) * (1.0f / 512.0f) + 1e-5f);
    }
    st0 = (lane & 2) ? rs[1] : rs[0];
    st0 = (lane & 1) ? -((lane & 2) ? mean[1] : mean[0]) * st0 : st0;
    asm volatile("" : "+v"(st0));
  }
  unsigned xa[4];
#pragma unroll
  for (int ks = 0; ks < 4; ++ks) xa[ks] = (unsigned)(32 * tq + t) * 128u + (unsigned)(((2 * ks + hh) ^ ((t >> 1) & 7)) * 16);
  v4u xr[32];
  u64 tag = 0;
  v4u gr;
  const unsigned token = (unsigned)(c.p * 128 + 32 * tq + t);
#pragma unroll
  for (int kc = 0; kc < 8; ++kc) {
    if (kc < 7 || ffn) VMW(8); else VMW(4);
    BAR();
    if (kc == 0) {
      tag = lds_tag();
      if (lane < 4)
        __hip_atomic_store((GAS u64*)(P.ws + WS_ST + tok * 16u + (unsigned)lane * 8u), ((u64)(unsigned)tag << 32) | f2u(st0), __ATOMIC_RELAXED, __HIP_MEMORY_SCOPE_AGENT);
    }
    if (kc == 5) {
      const char* gp = P.ws + WS_ST + token * 16u;
      asm volatile("global_load_dwordx4 %0, %1, off sc1" : "=&v"(gr) : "v"(gp) : "memory");
    }
    fill(kc + 2);
    const unsigned sb = (unsigned)(kc & 3) * SLOT;
#pragma unroll
    for (int ks = 0; ks < 4; ++ks) xr[4 * kc + ks] = lds128(xa[ks] + sb);
    asm volatile("" : "+v"(xr[4 * kc]), "+v"(xr[4 * kc + 1]), "+v"(xr[4 * kc + 2]), "+v"(xr[4 * kc + 3]));
  }
  asm volatile("" : "+v"(gr));
  STAMP(2);
  BAR();
  fill(10); fill(11);
  if (__builtin_amdgcn_ballot_w64(gr.y != (unsigned)tag || gr.w != (unsigned)tag) != 0) {
    const __amdgpu_buffer_rsrc_t rs = __builtin_amdgcn_make_buffer_rsrc((void*)(P.ws + WS_ST), 0, 32768, 0x00020000);
#pragma unroll 1
    for (unsigned spin = 0; spin < kSpin; ++spin) {
      gr = __builtin_amdgcn_raw_buffer_load_b128(rs, token * 16u, 0, 16);
      if (__builtin_amdgcn_ballot_w64(gr.y != (unsigned)tag || gr.w != (unsigned)tag) == 0) break;
      __builtin_amdgcn_s_sleep(1);
    }
  }
  const float rstd = u2f(gr.x), mr = u2f(gr.z);
  if (hh == 0) *(LAS f32x2*)(size_t)(L_STATS + (unsigned)(32 * tq + t) * 8u) = f32x2{rstd, mr};
  BAR();
  VMW(0);
  BAR();
  const int cb = (lane >> 4) & 1, q = (lane >> 2) & 3, pp = lane & 3, sw = (q >> 1) & 1;
  unsigned wl[2];
#pragma unroll
  for (int h = 0; h < 2; ++h) wl[h] = (unsigned)(8 * hh + q) * 128u + (unsigned)((4 * (h ^ sw) + 2 * cb + (pp >> 1)) * 16 + 8 * (pp & 1));
  const __amdgpu_buffer_rsrc_t rsHB = __builtin_amdgcn_make_buffer_rsrc(P.ws + WS_HB, 0, HB_BYTES, 0x00020000);
  const unsigned T32 = (unsigned)(c.p * 4 + tq);
  f32x4 cs = {0.f, 0.f, 0.f, 0.f};
  auto cs_sync = [&](int tile, bool wr) AI {
    const float s = xhalf_add(cs[0]);
    if (hh == 0 && wr) *(LAS float*)(size_t)(L_CS1 + (unsigned)tile * 128u + (unsigned)t * 4u) = s;
    asm volatile("s_waitcnt lgkmcnt(0)" ::: "memory");
    lds_inc(L_CNT + C_CS1 * 4, lane);
    lds_wait_ge(L_CNT + C_CS1 * 4, 4);
  };
  if constexpr (ffn) {
    const unsigned w2a = ((g & 1) ? wl[1] : wl[0]) + 16384u;
    int ti[3];
    unsigned wb[3];
    f32x16 acc[3];
#pragma unroll
    for (int i = 0; i < 3; ++i) { ti[i] = (i + tq) % 3; wb[i] = ti[i] == 0 ? wl[0] : (ti[i] == 1 ? wl[1] : w2a); acc[i] = zero16(); }
    pass1<3>(xr, acc, cs, wb, lane);
    cs_sync(ti[0], tq < 3);
    STAMP(3);
#pragma unroll
    for (int i = 0; i < 3; ++i)
      ffn_tile(acc[i], rstd, mr, L_CS1 + (unsigned)ti[i] * 128u, L_B1 + (unsigned)(4 + ti[i]) * 128u, hh, rsHB,
               (unsigned)lane * 16u + (T32 * 144u + (unsigned)(64 + 6 * g + 2 * ti[i])) * 1024u);
    publish_last(L_CNT + C_LATE * 4, 4, P.ws, FL_L + (unsigned)(c.p * 8 + g) * 128u, tag, lane);
  } else {
    int ti[2];
    unsigned wb[2];
    f32x16 acc[2];
#pragma unroll
    for (int i = 0; i < 2; ++i) { ti[i] = (i + tq) & 1; wb[i] = ti[i] == 0 ? wl[0] : wl[1]; acc[i] = zero16(); }
    pass1<2>(xr, acc, cs, wb, lane);
    cs_sync(ti[0], tq < 2);
    STAMP(3);
    v4u pq[4][2];
#pragma unroll
    for (int hd = 0; hd < 4; ++hd) {
      float y[8];
      fold8(acc[hd >> 1], 8 * (hd & 1), rstd, mr, L_CS1 + (unsigned)ti[hd >> 1] * 128u, hh, y);
      hedge(mfma(wphi_frag(2 * ti[hd >> 1] + (hd & 1), lane), pack8(y), zero16()), pq[hd][0], pq[hd][1]);
    }
    STAMP(4);
    if (tq == 0) { poll_flags(P.ws, FL_K + (unsigned)g * 16u * 128u, 16, tag, lane); lds_set(L_CNT + C_KVRDY * 4, 1); }
    else lds_wait_ge(L_CNT + C_KVRDY * 4, 1);
    STAMP(5);
    {
      const __amdgpu_buffer_rsrc_t rsKV = __builtin_amdgcn_make_buffer_rsrc(P.ws + WS_KVP, 0, 524288 + 65536, 0x00020000);
      const unsigned hbase = (unsigned)(g * 4 + tq) * 16u;
      v4u pv[16];
#pragma unroll
      for (int i = 0; i < 16; ++i) pv[i] = __builtin_amdgcn_raw_buffer_load_b128(rsKV, (unsigned)lane * 16u, (hbase + (unsigned)i) * 1024u, 16);
      const v4u z0 = __builtin_amdgcn_raw_buffer_load_b128(rsKV, (unsigned)lane * 16u, 524288u + hbase * 128u, 16);
      const v4u z1 = __builtin_amdgcn_raw_buffer_load_b128(rsKV, (unsigned)lane * 16u, 524288u + hbase * 128u + 1024u, 16);
      float s[8];
#pragma unroll
      for (int k = 0; k < 8; ++k) s[k] = 0.f;
#pragma unroll
      for (int i = 0; i < 16; ++i)
#pragma unroll
        for (int k = 0; k < 4; ++k) { s[2 * k] += u2f(pv[i][k] << 16); s[2 * k + 1] += u2f(pv[i][k] & 0xffff0000u); }
      const unsigned afr = L_AFR + (unsigned)tq * 2048u + (unsigned)(lane >> 5) * 1024u;
      const unsigned hh2 = (unsigned)(lane >> 4) & 1u, c16 = (unsigned)lane & 15u;
      *(LAS v4u*)(size_t)(afr + (c16 + 32u * hh2) * 16u) = pack8(s);
      float zz[4];
#pragma unroll
      for (int k = 0; k < 4; ++k) {
        float v = u2f(z0[k]) + u2f(z1[k]);
        v += shx(v, 8, lane); v += shx(v, 16, lane); v += shx(v, 32, lane);
        zz[k] = v;
      }
      if (lane < 8) *(LAS f32x4*)(size_t)(L_ZT + (unsigned)tq * 128u + (unsigned)lane * 16u) = f32x4{zz[0], zz[1], zz[2], zz[3]};
      const unsigned f0 = 16u * (unsigned)(lane >> 5) + 4u * hh2;
      const f32x4 za = lds128f(L_ZT + (unsigned)tq * 128u + f0 * 4u), zb = lds128f(L_ZT + (unsigned)tq * 128u + (f0 + 8u) * 4u);
      *(LAS v4u*)(size_t)(afr + (16u + c16 + 32u * hh2) * 16u) = v4u{pk2(za[0], za[1]), pk2(za[2], za[3]), pk2(zb[0], zb[1]), pk2(zb[2], zb[3])};
      asm volatile("s_waitcnt lgkmcnt(0)" ::: "memory");
      lds_inc(L_CNT + C_AFR * 4, lane);
      lds_wait_ge(L_CNT + C_AFR * 4, 4);
    }
#pragma unroll
    for (int hd = 0; hd < 4; ++hd) {
      const int head = 2 * ti[hd >> 1] + (hd & 1);
      f32x16 d = mfma(lds128(L_AFR + (unsigned)head * 2048u + (unsigned)lane * 16u), pq[hd][0], zero16());
      d = mfma(lds128(L_AFR + (unsigned)head * 2048u + 1024u + (unsigned)lane * 16u), pq[hd][1], d);
      const float r = __builtin_amdgcn_rcpf(d[8] + 1e-6f);
      const v4u o = v4u{pk2(d[0] * r, d[1] * r), pk2(d[2] * r, d[3] * r), pk2(d[4] * r, d[5] * r), pk2(d[6] * r, d[7] * r)};
      st128(o, rsHB, (unsigned)lane * 16u + (T32 * 144u + (unsigned)(112 + 4 * g + head)) * 1024u);
    }
    publish_last(L_CNT + C_O * 4, 4, P.ws, FL_O + (unsigned)(c.p * 8 + g) * 128u, tag, lane);
  }
  STAMP(6);
  return tag;
}

DI void role_b(const Params& P, const Ctx& c, int wid) {
  const int lane = c.lane, tq = c.tq;
  const u64 tag = c.ffn ? role_b_head<true>(P, c, wid) : role_b_head<false>(P, c, wid);
  const int pan = c.tt >> 1;
  const int tb = tq * 64 + lane;
  v4u xres[2], bres;
#pragma unroll
  for (int m = 0; m < 2; ++m) {
    const int idx = tb + 256 * m;
    xres[m] = *(const GAS v4u*)(P.x + (size_t)(64 * c.tt + (idx >> 3)) * 512 + 64 * c.ct + 8 * (idx & 7));
  }
  bres = *(const GAS v4u*)(P.b2 + 64 * c.ct + 8 * (tb & 7));
  if (tq == 0) {
    poll_flags(P.ws, FL_L + (unsigned)pan * 8u * 128u, 8, tag, lane);
    lds_set(L_CNT + C_LATERDY * 4, 1);
    poll_flags(P.ws, FL_O + (unsigned)pan * 8u * 128u, 8, tag, lane);
    lds_set(L_CNT + C_ORDY * 4, 1);
  } else if (tq == 1 && __builtin_amdgcn_workgroup_id_x() == 0) {
    poll_flags(P.ws, FL_E, 64, tag, lane); poll_flags(P.ws, FL_E + 64 * 128, 64, tag, lane);
    poll_flags(P.ws, FL_K, 64, tag, lane); poll_flags(P.ws, FL_K + 64 * 128, 64, tag, lane);
    if (lane == 0) __hip_atomic_store((GAS u64*)(P.ws + WS_EPOCH), tag, __ATOMIC_RELAXED, __HIP_MEMORY_SCOPE_AGENT);
  }
  BAR();
#pragma unroll
  for (int m = 0; m < 2; ++m) {
    const int idx = tb + 256 * m;
    const unsigned a = (unsigned)(idx >> 3) * 272u + (unsigned)(idx & 7) * 32u;
    f32x4 lo = lds128f(a), hi = lds128f(a + 16u);
#pragma unroll
    for (unsigned w = 1; w < 4; ++w) { lo += lds128f(w * SLOT + a); hi += lds128f(w * SLOT + a + 16u); }
    v4u o;
#pragma unroll
    for (int k = 0; k < 4; ++k) {
      const float a0 = k < 2 ? lo[2 * k] : hi[2 * k - 4], a1 = k < 2 ? lo[2 * k + 1] : hi[2 * k - 3];
      const float x0 = u2f(xres[m][k] << 16), x1 = u2f(xres[m][k] & 0xffff0000u);
      const float b0 = u2f(bres[k] << 16), b1 = u2f(bres[k] & 0xffff0000u);
      o[k] = pk2(a0 + x0 + b0, a1 + x1 + b1);
    }
    *(GAS v4u*)(P.out + (size_t)(64 * c.tt + (idx >> 3)) * 512 + 64 * c.ct + 8 * (idx & 7)) = o;
  }
  STAMP(7);
}

__global__ __launch_bounds__(512, 2) void fused_kernel(Params P) {
  __shared__ __attribute__((aligned(1024))) char smem[LDS_BYTES];
  asm volatile("" ::"v"((unsigned)(size_t)(LAS char*)smem) : "memory");
  const int tid = __builtin_amdgcn_workitem_id_x(), lane = tid & 63;
  const int wid = __builtin_amdgcn_readfirstlane(tid >> 6);
  STAMP(0);
  Ctx c;
  c.lane = lane; c.tq = wid & 3; c.hh = lane >> 5;
  const int blk = __builtin_amdgcn_workgroup_id_x(), xcd = blk & 7, j = blk >> 3, ja = j & 7, jb = j >> 3;
  c.p = (xcd & 1) * 8 + ja;
  c.G = (xcd >> 1) * 4 + jb;
  c.ffn = !(c.G & 1);
  c.gi = c.G >> 1;
  c.rot = ROT1 == 0 ? 0 : (ROT1 == 1 ? (((ja + jb) % 3) * 3) & 7 : (ROT1 == 2 ? (ja + 2 * jb) & 7 : ((ja + jb) & 1) * 4));
  c.tt = (xcd >> 1) * 8 + ja; c.ct = (xcd & 1) * 4 + jb;
  c.rotE = ROT2 ? (2 * ja + 4 * jb) & 15 : 0; c.rotL = ROT2 ? (ja + 3 * jb) % 12 : 0; c.rotO = ROT2 ? (ja + 2 * jb) & 7 : 0;
  if (wid < 4) {
    const u64 e = __hip_atomic_load((const GAS u64*)(P.ws + WS_EPOCH), __ATOMIC_RELAXED, __HIP_MEMORY_SCOPE_AGENT);
    if (tid < 16) *(LAS unsigned*)(size_t)(L_CNT + (unsigned)tid * 4u) = 0u;
    if (c.ffn) {
      if (tid < 224) {
        const int t7 = tid >> 5, n = tid & 31, g = c.gi;
        const int cA = 224 * g + 32 * (g & 1);
        const int col = t7 < 4 ? cA + 32 * t7 : (t7 < 6 ? cA + 128 + 32 * (t7 - 4) : ((g & 1) ? 224 * g : 224 * g + 192));
        *(LAS float*)(size_t)(L_B1 + (unsigned)tid * 4u) = u2f((unsigned)P.b1[col + n] << 16);
      }
    } else if (tid < 128) {
      *(LAS v4u*)(size_t)(L_WPHI + (unsigned)tid * 16u) = *(const GAS v4u*)(P.wphi + (size_t)c.gi * 1024 + tid * 8);
    }
    const u64 tag = e + 1u;
    if (tid == 0) *(LAS v2u*)(size_t)L_TAG = v2u{(unsigned)tag, (unsigned)(tag >> 32)};
    STAMP(1);
    role_a(P, c, wid);
  } else {
    STAMP(1);
    role_b(P, c, wid);
  }
}

}

extern "C" void kernel_launch(void* const* d_in, const int* in_sizes, int n_in, void* d_out, int out_size, void* d_ws, size_t ws_size,
                              hipStream_t stream) {
  Params P;
  P.x = (const u16*)d_in[0]; P.wqkv = (const u16*)d_in[1]; P.wphi = (const u16*)d_in[2]; P.wo = (const u16*)d_in[3];
  P.w1 = (const u16*)d_in[5]; P.b1 = (const u16*)d_in[6]; P.w2 = (const u16*)d_in[7]; P.b2 = (const u16*)d_in[8];
  P.out = (u16*)d_out; P.ws = (char*)d_ws;
  fused_kernel<<<256, 512, 0, stream>>>(P);
}
```

```cpp
#include <hip/hip_runtime.h>
#include <stdint.h>

namespace {

typedef unsigned short u16;
typedef unsigned long long u64;
typedef unsigned v4u __attribute__((ext_vector_type(4)));
typedef unsigned v2u __attribute__((ext_vector_type(2)));
typedef float f32x16 __attribute__((ext_vector_type(16)));
typedef float f32x4 __attribute__((ext_vector_type(4)));
typedef float f32x2 __attribute__((ext_vector_type(2)));
typedef __bf16 bf16x8 __attribute__((ext_vector_type(8)));
typedef __bf16 bf16x2 __attribute__((ext_vector_type(2)));
typedef short s16x4 __attribute__((ext_vector_type(4)));
#define LAS __attribute__((address_space(3)))
#define GAS __attribute__((address_space(1)))
#if defined(__HIP_DEVICE_COMPILE__)
#define NOPK __attribute__((target("no-packed-fp32-ops")))
#else
#define NOPK
#endif
#define DI __device__ __forceinline__ NOPK
#define AI __attribute__((always_inline)) NOPK

#define STAMPS 0
#define ROT1 1
#define ROT2 0
#define STR2(x) #x
#define STR(x) STR2(x)
#define W1 8
#define W2 4
#define ORD 0
#define CODEPF 0
#define WARM2 1
#define CODE_KB 36

constexpr unsigned WS_EPOCH = 0;
constexpr unsigned FL_E = 4096;
constexpr unsigned FL_L = FL_E + 128 * 128;
constexpr unsigned FL_K = FL_L + 128 * 128;
constexpr unsigned FL_O = FL_K + 128 * 128;
constexpr unsigned WS_ST = 73728;
constexpr unsigned WS_KVP = 131072;
constexpr unsigned WS_ZP = WS_KVP + 524288;
constexpr unsigned WS_HB = 1048576;
constexpr unsigned HB_BYTES = 64u * 144u * 1024u;
constexpr unsigned WS_STAMP = 16u << 20;

constexpr unsigned SLOT = 32768;
constexpr unsigned L_MISC = 131072;
constexpr unsigned L_STATS = L_MISC;
constexpr unsigned L_CS0 = L_MISC + 1024;
constexpr unsigned L_CS1 = L_MISC + 1536;
constexpr unsigned L_B1 = L_MISC + 2048;
constexpr unsigned L_WPHI = L_MISC + 3072;
constexpr unsigned L_CNT = L_MISC + 5120;
constexpr unsigned L_TAG = L_MISC + 5248;
constexpr unsigned L_ZT = L_MISC + 5376;
constexpr unsigned L_AFR = L_MISC + 8192;
constexpr unsigned LDS_BYTES = 163840;
enum { C_CS1 = 0, C_LATE = 1, C_KV = 2, C_O = 3, C_AFR = 4, C_KVRDY = 5, C_LATERDY = 6, C_ORDY = 7, C_FREE = 8  , C_LAND = 12  , C_EARLY = 16, C_IMG = 17 };

constexpr unsigned kSpin = 1u << 16;

struct Params {
  const u16* x; const u16* wqkv; const u16* wphi; const u16* wo;
  const u16* w1; const u16* b1; const u16* w2; const u16* b2;
  u16* out; char* ws;
};

DI unsigned f2u(float f) { return __builtin_bit_cast(unsigned, f); }
DI float u2f(unsigned u) { return __builtin_bit_cast(float, u); }
DI float fmx(float a, float b) { return __builtin_fmaxf(a, b); }
DI f32x16 mfma(v4u a, v4u b, f32x16 c) {
  return __builtin_amdgcn_mfma_f32_32x32x16_bf16(__builtin_bit_cast(bf16x8, a), __builtin_bit_cast(bf16x8, b), c, 0, 0, 0);
}
DI f32x16 zero16() { f32x16 z; for (int r = 0; r < 16; ++r) z[r] = 0.f; return z; }
DI v4u lds128(unsigned a) { return *(const LAS v4u*)(size_t)a; }
DI f32x4 lds128f(unsigned a) { return *(const LAS f32x4*)(size_t)a; }
DI v2u tr64(unsigned a) { return __builtin_bit_cast(v2u, __builtin_amdgcn_ds_read_tr16_b64_v4i16((LAS s16x4*)(size_t)a)); }
DI v4u tr2(unsigned a0, unsigned a1) { v2u l = tr64(a0), h = tr64(a1); return v4u{l.x, l.y, h.x, h.y}; }
DI unsigned pk2(float a, float b) { return __builtin_bit_cast(unsigned, __builtin_convertvector(f32x2{a, b}, bf16x2)); }
DI float xhalf_add(float v) { auto r = __builtin_amdgcn_permlane32_swap(f2u(v), f2u(v), false, false); return u2f(r[0]) + u2f(r[1]); }
DI float xhalf_max(float v) { auto r = __builtin_amdgcn_permlane32_swap(f2u(v), f2u(v), false, false); return fmx(u2f(r[0]), u2f(r[1])); }
DI float shx(float v, int m, int lane) { return u2f((unsigned)__builtin_amdgcn_ds_bpermute((lane ^ m) << 2, (int)f2u(v))); }
DI f32x4 lanesum(v4u f, f32x4 c) {
  const s16x4 one = {0x3F80, 0x3F80, 0x3F80, 0x3F80};
  c = __builtin_amdgcn_mfma_f32_4x4x4bf16_1k(one, __builtin_bit_cast(s16x4, v2u{f.x, f.y}), c, 0, 0, 0);
  return __builtin_amdgcn_mfma_f32_4x4x4bf16_1k(one, __builtin_bit_cast(s16x4, v2u{f.z, f.w}), c, 0, 0, 0);
}
template <int CTRL> DI float dppadd(float v) { return v + u2f((unsigned)__builtin_amdgcn_update_dpp(0, (int)f2u(v), CTRL, 0xf, 0xf, true)); }
DI float wavesum(float v) {
  v = dppadd<0xB1>(v); v = dppadd<0x4E>(v); v = dppadd<0x141>(v); v = dppadd<0x140>(v);
  auto r = __builtin_amdgcn_permlane16_swap(f2u(v), f2u(v), false, false);
  return xhalf_add(u2f(r[0]) + u2f(r[1]));
}
DI void dma(unsigned m0v, unsigned voff, __amdgpu_buffer_rsrc_t rs, unsigned soff) {
  asm volatile("s_mov_b32 m0, %0\n\ts_nop 0\n\tbuffer_load_dwordx4 %1, %2, %3 offen lds" ::"s"(m0v), "v"(voff), "s"(rs), "s"(soff) : "memory");
}
DI void st128(v4u v, __amdgpu_buffer_rsrc_t rs, unsigned voff) { __builtin_amdgcn_raw_buffer_store_b128(v, rs, voff, 0, 16); }
#define BAR() do { __builtin_amdgcn_sched_barrier(0); asm volatile("s_waitcnt lgkmcnt(0)\n\ts_barrier" ::: "memory"); __builtin_amdgcn_sched_barrier(0); } while (0)
#define VMW(n) asm volatile("s_waitcnt vmcnt(" #n ")" ::: "memory")
constexpr unsigned ONES = 0x3F803F80u;

#define STAMP(i) do { } while (0)

DI void poll_flags(const char* ws, unsigned base, int n, u64 tag, int lane) {
  const GAS u64* f = (const GAS u64*)(ws + base + (unsigned)(lane < n ? lane : 0) * 128u);
#pragma unroll 1
  for (unsigned spin = 0; spin < kSpin; ++spin) {
    const u64 v = __hip_atomic_load(f, __ATOMIC_RELAXED, __HIP_MEMORY_SCOPE_AGENT);
    if (__builtin_amdgcn_ballot_w64(v != tag) == 0) break;
    __builtin_amdgcn_s_sleep(1);
  }
  __builtin_amdgcn_fence(__ATOMIC_ACQUIRE, "wavefront");
  asm volatile("" ::: "memory");
}
DI void lds_wait_ge(unsigned addr, unsigned want) {
#pragma unroll 1
  for (unsigned spin = 0; spin < (kSpin << 4); ++spin) {
    const unsigned v = *(volatile LAS unsigned*)(size_t)addr;
    if ((unsigned)__builtin_amdgcn_readfirstlane(v) >= want) break;
    __builtin_amdgcn_s_sleep(1);
  }
  asm volatile("" ::: "memory");
}
DI void lds_set(unsigned addr, unsigned v) { *(volatile LAS unsigned*)(size_t)addr = v; }
DI void lds_inc(unsigned addr, int lane) { if (lane == 0) __hip_atomic_fetch_add((LAS unsigned*)(size_t)addr, 1u, __ATOMIC_RELAXED, __HIP_MEMORY_SCOPE_WORKGROUP); }
DI void publish_last(unsigned cnt_addr, unsigned n, char* ws, unsigned flag_off, u64 tag, int lane) {
  VMW(0);
  if (lane == 0) {
    const unsigned old = __hip_atomic_fetch_add((LAS unsigned*)(size_t)cnt_addr, 1u, __ATOMIC_RELAXED, __HIP_MEMORY_SCOPE_WORKGROUP);
    if (old == n - 1) __hip_atomic_store((GAS u64*)(ws + flag_off), tag, __ATOMIC_RELAXED, __HIP_MEMORY_SCOPE_AGENT);
  }
}
DI u64 lds_tag() { const v2u t = *(const LAS v2u*)(size_t)L_TAG; return ((u64)(unsigned)__builtin_amdgcn_readfirstlane(t.y) << 32) | (unsigned)__builtin_amdgcn_readfirstlane(t.x); }

DI void fold8(const f32x16& acc, int r0, float rstd, float mr, unsigned cs, int hh, float* y) {
#pragma unroll
  for (int g = 0; g < 2; ++g) {
    const f32x4 t = lds128f(cs + (unsigned)((r0 >> 2) + g) * 32u + (unsigned)hh * 16u) * mr;
#pragma unroll
    for (int k = 0; k < 4; ++k) y[4 * g + k] = __builtin_fmaf(acc[r0 + 4 * g + k], rstd, t[k]);
  }
}
DI void load16(unsigned a, int hh, f32x4* v) {
#pragma unroll
  for (int g = 0; g < 4; ++g) v[g] = lds128f(a + (unsigned)g * 32u + (unsigned)hh * 16u);
}
DI void ffn_tile(const f32x16& acc, float rstd, float mr, const f32x4* cs, const f32x4* bs, __amdgpu_buffer_rsrc_t rsHB, unsigned voff) {
  f32x2 y[8], d[8];
#pragma unroll
  for (int g = 0; g < 4; ++g) {
    const f32x4 t = cs[g] * mr + bs[g];
    y[2 * g] = f32x2{acc[4 * g], acc[4 * g + 1]} * rstd + f32x2{t[0], t[1]};
    y[2 * g + 1] = f32x2{acc[4 * g + 2], acc[4 * g + 3]} * rstd + f32x2{t[2], t[3]};
  }
#pragma unroll
  for (int k = 0; k < 8; ++k) d[k] = y[k] * ((y[k] * y[k]) * (-0.10294324f) + (-2.30220819f));
#pragma unroll
  for (int k = 0; k < 8; ++k) { d[k].x = __builtin_amdgcn_exp2f(d[k].x); d[k].y = __builtin_amdgcn_exp2f(d[k].y); }
#pragma unroll
  for (int k = 0; k < 8; ++k) d[k] += 1.0f;
#pragma unroll
  for (int k = 0; k < 8; ++k) { d[k].x = __builtin_amdgcn_rcpf(d[k].x); d[k].y = __builtin_amdgcn_rcpf(d[k].y); }
  v4u o0, o1;
#pragma unroll
  for (int k = 0; k < 4; ++k) {
    const f32x2 g0 = y[k] * d[k], g1 = y[4 + k] * d[4 + k];
    o0[k] = pk2(g0.x, g0.y); o1[k] = pk2(g1.x, g1.y);
  }
  st128(o0, rsHB, voff); st128(o1, rsHB, voff + 1024u);
}
DI void hedge(const f32x16& d, v4u& o0, v4u& o1) {
  constexpr float L = 1.4426950408889634f;
  float m0 = fmx(fmx(fmx(d[0], d[1]), fmx(d[2], d[3])), fmx(fmx(d[4], d[5]), fmx(d[6], d[7])));
  float m1 = fmx(fmx(fmx(d[8], d[9]), fmx(d[10], d[11])), fmx(fmx(d[12], d[13]), fmx(d[14], d[15])));
  m0 = xhalf_max(m0) * -L; m1 = xhalf_max(m1) * -L;
  float e[16];
#pragma unroll
  for (int i = 0; i < 8; ++i) { e[i] = __builtin_amdgcn_exp2f(__builtin_fmaf(d[i], L, m0)); e[8 + i] = __builtin_amdgcn_exp2f(__builtin_fmaf(d[8 + i], L, m1)); }
  float s0 = ((e[0] + e[1]) + (e[2] + e[3])) + ((e[4] + e[5]) + (e[6] + e[7]));
  float s1 = ((e[8] + e[9]) + (e[10] + e[11])) + ((e[12] + e[13]) + (e[14] + e[15]));
  s0 = __builtin_amdgcn_rcpf(xhalf_add(s0)); s1 = __builtin_amdgcn_rcpf(xhalf_add(s1));
#pragma unroll
  for (int k = 0; k < 4; ++k) { o0[k] = pk2(e[2 * k] * s0, e[2 * k + 1] * s0); o1[k] = pk2(e[8 + 2 * k] * s1, e[8 + 2 * k + 1] * s1); }
}
DI v4u wphi_frag(int hd, int lane) {
  const int hh = lane >> 5, q = (lane >> 2) & 3, pp = lane & 3;
  const unsigned a = L_WPHI + (unsigned)hd * 512u + (unsigned)(4 * hh + q) * 32u + (unsigned)pp * 8u;
  v4u f = tr2(a, a + 256u);
  const unsigned sg = (lane & 16) ? 0x80008000u : 0u;
  f.x ^= sg; f.y ^= sg; f.z ^= sg; f.w ^= sg;
  return f;
}
DI v4u selv(bool s, v4u a, v4u b) { const unsigned m = s ? 0xffffffffu : 0u; return v4u{(a.x & m) | (b.x & ~m), (a.y & m) | (b.y & ~m), (a.z & m) | (b.z & ~m), (a.w & m) | (b.w & ~m)}; }
DI v4u pack8(const float* y) { return v4u{pk2(y[0], y[1]), pk2(y[2], y[3]), pk2(y[4], y[5]), pk2(y[6], y[7])}; }

struct Ctx {
  int lane, tq, hh;
  int p, gi, G;
  int rot;
  int tt, ct;
  int rotE, rotL, rotO;
  bool ffn;
};

DI void role_a(const Params& P, const Ctx& c, int wid) {
  const int lane = c.lane, tq = c.tq, hh = c.hh, t = lane & 31;
  const int cb = (lane >> 4) & 1, q = (lane >> 2) & 3, pp = lane & 3, sw = (q >> 1) & 1;
  const int th = tq & 1, up = tq >> 1;
  unsigned xa[4];
#pragma unroll
  for (int ks = 0; ks < 4; ++ks) xa[ks] = (unsigned)(64 * th + t) * 128u + (unsigned)(((2 * ks + hh) ^ ((t >> 1) & 7)) * 16);
  const unsigned wl = 16384u + (unsigned)(8 * hh + q) * 128u + (unsigned)((4 * (up ^ sw) + 2 * cb + (pp >> 1)) * 16 + 8 * (pp & 1));
  f32x16 acc[2][2];
#pragma unroll
  for (int i = 0; i < 2; ++i) { acc[i][0] = zero16(); acc[i][1] = zero16(); }
  f32x4 cs = {0.f, 0.f, 0.f, 0.f};
  unsigned x0 = xa[0], x1 = xa[1], x2 = xa[2], x3 = xa[3], w0 = wl;
  v4u xf[4][2], wf[4][2];
  auto rd = [&](int ks, v4u* X, v4u* W) AI {
    const unsigned xb = ks == 0 ? x0 : (ks == 1 ? x1 : (ks == 2 ? x2 : x3));
    X[0] = lds128(xb); X[1] = lds128(xb + 4096u);
    const unsigned a = w0 + (unsigned)(ks * 2048);
    W[0] = tr2(a, a + 512u); W[1] = tr2(a + 8192u, a + 8704u);
  };
  BAR();
  rd(0, xf[0], wf[0]); rd(1, xf[1], wf[1]);
#pragma unroll 1
  for (int kc = 0; kc < 8; ++kc) {
#pragma unroll
    for (int ks = 0; ks < 4; ++ks) {
      if (ks == 2) {
        __builtin_amdgcn_sched_barrier(0);
        asm volatile("s_barrier" ::: "memory");
        __builtin_amdgcn_sched_barrier(0);
        const unsigned sb = (unsigned)((kc + 1) & 3) * SLOT;
        x0 = xa[0] + sb; x1 = xa[1] + sb; w0 = wl + sb;
        asm volatile("" : "+v"(x0), "+v"(x1), "+v"(w0));
      }
      if (ks == 0) {
        const unsigned sb = (unsigned)(kc & 3) * SLOT;
        x2 = xa[2] + sb; x3 = xa[3] + sb;
        asm volatile("" : "+v"(x2), "+v"(x3));
      }
      rd((ks + 2) & 3, xf[(ks + 2) & 3], wf[(ks + 2) & 3]);
#pragma unroll
      for (int i = 0; i < 2; ++i) { acc[i][0] = mfma(wf[ks][i], xf[ks][0], acc[i][0]); acc[i][1] = mfma(wf[ks][i], xf[ks][1], acc[i][1]); }
      cs = lanesum(selv(th != 0, wf[ks][1], wf[ks][0]), cs);
      __builtin_amdgcn_sched_group_barrier(0x008, 1, 0);
      __builtin_amdgcn_sched_group_barrier(0x100, 2, 0);
      __builtin_amdgcn_sched_group_barrier(0x008, 1, 0);
      __builtin_amdgcn_sched_group_barrier(0x100, 2, 0);
      __builtin_amdgcn_sched_group_barrier(0x008, 1, 0);
      __builtin_amdgcn_sched_group_barrier(0x100, 2, 0);
      __builtin_amdgcn_sched_group_barrier(0x002, 4, 0);
      __builtin_amdgcn_sched_group_barrier(0x008, 3, 0);
      __builtin_amdgcn_sched_barrier(0);
    }
    asm volatile("" : "+v"(acc[0][0]), "+v"(acc[0][1]), "+v"(acc[1][0]), "+v"(acc[1][1]), "+v"(cs));
  }
  STAMP(2);
  {
    const float s = xhalf_add(cs[0]);
    if (hh == 0) *(LAS float*)(size_t)(L_CS0 + (unsigned)(up + 2 * th) * 128u + (unsigned)t * 4u) = s;
  }
  BAR();
  const u64 tag = lds_tag();
  float rstd[2], mr[2];
#pragma unroll
  for (int b = 0; b < 2; ++b) {
    const f32x2 st = *(const LAS f32x2*)(size_t)(L_STATS + (unsigned)(64 * th + 32 * b + t) * 8u);
    rstd[b] = st.x; mr[b] = st.y;
  }
  const __amdgpu_buffer_rsrc_t rsHB = __builtin_amdgcn_make_buffer_rsrc(P.ws + WS_HB, 0, HB_BYTES, 0x00020000);
  if (c.ffn) {
    f32x4 csv[2][4], bsv[2][4];
#pragma unroll
    for (int i = 0; i < 2; ++i) { load16(L_CS0 + (unsigned)(up + 2 * i) * 128u, hh, csv[i]); load16(L_B1 + (unsigned)(up + 2 * i) * 128u, hh, bsv[i]); }
#pragma unroll
    for (int i = 0; i < 2; ++i)
#pragma unroll
      for (int b = 0; b < 2; ++b) {
        const unsigned tile = (unsigned)(up + 2 * i), T32 = (unsigned)(c.p * 4 + 2 * th + b);
        ffn_tile(acc[i][b], rstd[b], mr[b], csv[i], bsv[i], rsHB, (unsigned)lane * 16u + (T32 * 144u + (unsigned)(8 * c.gi) + 2u * tile) * 1024u);
        if (2 * i + b < 3) BAR();
      }
    STAMP(8);
    publish_last(L_CNT + C_EARLY * 4, 4, P.ws, FL_E + (unsigned)(c.p * 8 + c.gi) * 128u, tag, lane);
    BAR();
  } else {
#pragma unroll
    for (int b = 0; b < 2; ++b)
#pragma unroll
      for (int hf = 0; hf < 2; ++hf) {
        const int hd = 2 * up + hf;
        float y[8];
        fold8(acc[0][b], 8 * hf, rstd[b], mr[b], L_CS0 + (unsigned)up * 128u, hh, y);
        v4u o0, o1;
        hedge(mfma(wphi_frag(hd, lane), pack8(y), zero16()), o0, o1);
        const unsigned img = (unsigned)hd * SLOT + 16384u;
        const unsigned row = (unsigned)(64 * th + 32 * b + t);
        const unsigned g = (row >> 1) & 7u;
        *(LAS v2u*)(size_t)(img + row * 64u + (((unsigned)(0 + hh)) ^ g) * 8u) = v2u{o0.x, o0.y};
        *(LAS v2u*)(size_t)(img + row * 64u + (((unsigned)(2 + hh)) ^ g) * 8u) = v2u{o0.z, o0.w};
        *(LAS v2u*)(size_t)(img + row * 64u + (((unsigned)(4 + hh)) ^ g) * 8u) = v2u{o1.x, o1.y};
        *(LAS v2u*)(size_t)(img + row * 64u + (((unsigned)(6 + hh)) ^ g) * 8u) = v2u{o1.z, o1.w};
        fold8(acc[1][b], 8 * hf, rstd[b], mr[b], L_CS0 + (unsigned)(2 + up) * 128u, hh, y);
        const unsigned gv = (row >> 2) & 3u;
        *(LAS v2u*)(size_t)(img + 8192u + row * 32u + (((unsigned)(0 + hh)) ^ gv) * 8u) = v2u{pk2(y[0], y[1]), pk2(y[2], y[3])};
        *(LAS v2u*)(size_t)(img + 8192u + row * 32u + (((unsigned)(2 + hh)) ^ gv) * 8u) = v2u{pk2(y[4], y[5]), pk2(y[6], y[7])};
        if (2 * b + hf < 3) BAR();
      }
    STAMP(8);
    BAR();
    const unsigned img = (unsigned)tq * SLOT + 16384u;
    f32x16 kv = zero16();
    unsigned pa[2], va[2];
#pragma unroll
    for (int i2 = 0; i2 < 2; ++i2) {
      const unsigned r = (unsigned)(8 * hh + 4 * i2 + q);
      pa[i2] = img + r * 64u + (((unsigned)(4 * cb + pp)) ^ ((r >> 1) & 7u)) * 8u;
      va[i2] = img + 8192u + r * 32u + (((unsigned)pp) ^ ((r >> 2) & 3u)) * 8u;
    }
#pragma unroll
    for (int ks = 0; ks < 8; ++ks) {
      const v4u af = tr2(pa[0] + (unsigned)ks * 1024u, pa[1] + (unsigned)ks * 1024u);
      v4u bf = tr2(va[0] + (unsigned)ks * 512u, va[1] + (unsigned)ks * 512u);
      if (cb) bf = v4u{ONES, ONES, ONES, ONES};
      kv = mfma(af, bf, kv);
    }
    const unsigned hidx = (unsigned)(c.gi * 4 + tq) * 16u + (unsigned)c.p;
    const __amdgpu_buffer_rsrc_t rsKV = __builtin_amdgcn_make_buffer_rsrc(P.ws + WS_KVP, 0, 524288 + 65536, 0x00020000);
    if (cb == 0) {
#pragma unroll
      for (int s = 0; s < 2; ++s) {
        const v4u o = v4u{pk2(kv[8 * s], kv[8 * s + 1]), pk2(kv[8 * s + 2], kv[8 * s + 3]), pk2(kv[8 * s + 4], kv[8 * s + 5]), pk2(kv[8 * s + 6], kv[8 * s + 7])};
        st128(o, rsKV, (unsigned)((16 * s + (lane & 15) + 32 * hh) * 16) + hidx * 1024u);
      }
    } else if ((lane & 15) == 0) {
#pragma unroll
      for (int g4 = 0; g4 < 4; ++g4) {
        const v4u o = v4u{f2u(kv[4 * g4]), f2u(kv[4 * g4 + 1]), f2u(kv[4 * g4 + 2]), f2u(kv[4 * g4 + 3])};
        st128(o, rsKV, (unsigned)((8 * g4 + 4 * hh) * 4) + 524288u + hidx * 128u);
      }
    }
    publish_last(L_CNT + C_KV * 4, 4, P.ws, FL_K + (unsigned)(c.gi * 16 + c.p) * 128u, tag, lane);
  }
  STAMP(3);
  unsigned tabB, tabWlo, tabWhi;
  {
    const int jj = lane < 36 ? lane : 35;
    constexpr int J2 = ORD ? 24 : 28;
    int sg;
    if (jj < 16) sg = 4 * ((jj + c.rotE) & 15) + tq;
    else if ((jj < J2) != (ORD != 0)) sg = 64 + 4 * ((jj - (ORD ? 24 : 16) + c.rotL) % 12) + tq;
    else sg = 112 + 4 * ((jj - (ORD ? 16 : 28) + c.rotO) & 7) + tq;
    int row;
    if (sg < 64) { const int g = sg >> 3; row = 224 * g + 32 * (g & 1) + 16 * (sg & 7); }
    else if (sg < 112) {
      const int e = sg - 64, g = e / 6, i = e - 6 * g;
      row = i < 4 ? 224 * g + 32 * (g & 1) + 128 + 16 * i : ((g & 1) ? 224 * g : 224 * g + 192) + 16 * (i - 4);
    } else row = 16 * (sg - 112);
    const u64 a = (u64)(size_t)(sg < 112 ? P.w2 : P.wo) + (u64)row * 1024u + (u64)c.ct * 128u;
    tabB = (unsigned)sg * 1024u; tabWlo = (unsigned)a; tabWhi = (unsigned)(a >> 32);
  }
  const unsigned vw0 = (unsigned)(lane >> 3) * 1024u + (unsigned)(((lane & 7) ^ (((lane >> 4) & 1) << 2)) * 16), vw1 = vw0 + 8192u;
  const unsigned ring = (unsigned)tq * SLOT;
  auto wdma = [&](int j) AI {
    const unsigned lo = __builtin_amdgcn_readlane(tabWlo, j), hi = __builtin_amdgcn_readlane(tabWhi, j);
    const u64 b = ((u64)hi << 32) | lo;
    const unsigned m = ring + (unsigned)(j & 15) * 2048u;
    asm volatile("s_nop 4\n\ts_mov_b32 m0, %0\n\ts_nop 0\n\tglobal_load_lds_dwordx4 %1, %3\n\ts_add_u32 m0, m0, 0x400\n\ts_nop 0\n\tglobal_load_lds_dwordx4 %2, %3"
                 ::"s"(m), "v"(vw0), "v"(vw1), "s"(b) : "memory", "scc");
  };
  const unsigned vb0 = (unsigned)lane * 16u + (unsigned)(2 * c.tt) * 144u * 1024u, vb1 = vb0 + 144u * 1024u;
  constexpr int NB = 4;
  v4u br[NB][2];
#pragma unroll
  for (int u = 0; u < NB; ++u) { br[u][0] = v4u{0u, 0u, 0u, 0u}; br[u][1] = br[u][0]; }
  auto bload = [&](unsigned so, v4u& d0, v4u& d1) AI {
    asm volatile("s_nop 4\n\tbuffer_load_dwordx4 %0, %2, %4, %5 offen sc1\n\tbuffer_load_dwordx4 %1, %3, %4, %5 offen sc1"
                 : "+v"(d0), "+v"(d1) : "v"(vb0), "v"(vb1), "s"(rsHB), "s"(so) : "memory");
  };
  STAMP(9);
#pragma unroll 1
  for (int j = 0; j < 14; ++j) wdma(j);
  STAMP(4);
  const int pan = c.tt >> 1;
  poll_flags(P.ws, FL_E + (unsigned)pan * 8u * 128u, 8, tag, lane);
  STAMP(5);
  bload(__builtin_amdgcn_readlane(tabB, 0), br[0][0], br[0][1]);
  bload(__builtin_amdgcn_readlane(tabB, 1), br[1][0], br[1][1]);
  wdma(14);
  bload(__builtin_amdgcn_readlane(tabB, 2), br[2][0], br[2][1]);
  wdma(15);
  bload(__builtin_amdgcn_readlane(tabB, 3), br[3][0], br[3][1]);
  unsigned w2l[2];
#pragma unroll
  for (int h = 0; h < 2; ++h) w2l[h] = ring + (unsigned)(4 * hh + q) * 128u + (unsigned)((4 * (h ^ sw) + 2 * cb + (pp >> 1)) * 16 + 8 * (pp & 1));
  f32x16 o[2][2];
#pragma unroll
  for (int a = 0; a < 2; ++a)
#pragma unroll
    for (int b = 0; b < 2; ++b) o[a][b] = zero16();
  v4u af[2];
  asm volatile("s_waitcnt vmcnt(10)" : "+v"(br[0][0]), "+v"(br[0][1]) :: "memory");
  af[0] = tr2(w2l[0], w2l[0] + 1024u); af[1] = tr2(w2l[1], w2l[1] + 1024u);
  auto step = [&](int j, int u, bool refill, bool first) AI {
    v4u an[2];
    if (first) asm volatile("s_waitcnt vmcnt(" STR(W1) ")" : "+v"(br[(u + 1) % NB][0]), "+v"(br[(u + 1) % NB][1]) :: "memory");
    else asm volatile("s_waitcnt vmcnt(" STR(W2) ")" : "+v"(br[(u + 1) % NB][0]), "+v"(br[(u + 1) % NB][1]) :: "memory");
    unsigned p0 = w2l[0] + (unsigned)((j + 1) & 15) * 2048u, p1 = w2l[1] + (unsigned)((j + 1) & 15) * 2048u;
    asm volatile("" : "+v"(p0), "+v"(p1));
    an[0] = tr2(p0, p0 + 1024u); an[1] = tr2(p1, p1 + 1024u);
    o[0][0] = mfma(af[0], br[u][0], o[0][0]); o[0][1] = mfma(af[0], br[u][1], o[0][1]);
    o[1][0] = mfma(af[1], br[u][0], o[1][0]); o[1][1] = mfma(af[1], br[u][1], o[1][1]);
    asm volatile("" : "+v"(o[0][0]), "+v"(o[0][1]), "+v"(o[1][0]), "+v"(o[1][1]) :: "memory");
    if (refill) wdma(j + 16);
    const int jn = j + NB;
    if (u == 0) {
      if (jn == 16) lds_wait_ge(L_CNT + (ORD ? C_ORDY : C_LATERDY) * 4, 1);
      if (jn == (ORD ? 24 : 28)) lds_wait_ge(L_CNT + (ORD ? C_LATERDY : C_ORDY) * 4, 1);
    }
    bload(jn < 36 ? (unsigned)__builtin_amdgcn_readlane(tabB, jn) : 0x7F000000u, br[u][0], br[u][1]);
    af[0] = an[0]; af[1] = an[1];
  };
#pragma unroll 1
  for (int it = 0; it < 5; ++it) {
    if (it == 3) STAMP(10);
#pragma unroll
    for (int u = 0; u < NB; ++u) step(NB * it + u, u, true, true);
  }
  STAMP(11);
#pragma unroll 1
  for (int it = 5; it < 9; ++it) {
#pragma unroll
    for (int u = 0; u < NB; ++u) step(NB * it + u, u, false, false);
  }
  asm volatile("s_waitcnt vmcnt(0)" : "+v"(br[0][0]), "+v"(br[0][1]), "+v"(br[1][0]), "+v"(br[1][1]), "+v"(br[2][0]), "+v"(br[2][1]), "+v"(br[3][0]), "+v"(br[3][1]) :: "memory");
  STAMP(6);
#pragma unroll
  for (int a = 0; a < 2; ++a)
#pragma unroll
    for (int b = 0; b < 2; ++b)
#pragma unroll
      for (int g4 = 0; g4 < 4; ++g4)
        *(LAS f32x4*)(size_t)(ring + (unsigned)(32 * b + t) * 272u + (unsigned)(32 * a + 8 * g4 + 4 * hh) * 4u) =
            f32x4{o[a][b][4 * g4], o[a][b][4 * g4 + 1], o[a][b][4 * g4 + 2], o[a][b][4 * g4 + 3]};
  VMW(0);
  BAR();
  STAMP(7);
}

template <int NT, int NF> DI void pass1(const Params& P, int wid, const v4u (&xr)[32], f32x16 (&acc)[NT], f32x4& cs, const unsigned (&wb)[NT], int lane) {
#pragma unroll
  for (int qq = 0; qq < 4; ++qq) {
    if (qq == 1) { if (NF == 8) VMW(16); else VMW(8); }
    if (qq == 2) { if (NF == 8) VMW(8); else VMW(4); }
    if (qq == 3) VMW(0);
    if (qq > 0) BAR();
    unsigned a[NT];
#pragma unroll
    for (int i = 0; i < NT; ++i) { a[i] = wb[i] + (unsigned)qq * SLOT; asm volatile("" : "+v"(a[i])); }
    v4u f[3][NT];
    auto rd = [&](int k8, v4u* W) AI {
      const unsigned o = (unsigned)((k8 >> 2) * 8192 + (k8 & 3) * 2048);
#pragma unroll
      for (int i = 0; i < NT; ++i) W[i] = tr2(a[i] + o, a[i] + o + 512u);
    };
    rd(0, f[0]); rd(1, f[1]);
    __builtin_amdgcn_sched_barrier(0);
#pragma unroll
    for (int k8 = 0; k8 < 8; ++k8) {
      if (k8 < 6) rd(k8 + 2, f[(k8 + 2) % 3]);
      const v4u xf = xr[8 * qq + k8];
#pragma unroll
      for (int i = 0; i < NT; ++i) acc[i] = mfma(f[k8 % 3][i], xf, acc[i]);
      cs = lanesum(f[k8 % 3][0], cs);
      if (k8 < 6) {
#pragma unroll
        for (int i = 0; i < NT; ++i) {
          __builtin_amdgcn_sched_group_barrier(0x008, 1, 0);
          __builtin_amdgcn_sched_group_barrier(0x100, 2, 0);
        }
        __builtin_amdgcn_sched_group_barrier(0x008, 2, 0);
      }
      __builtin_amdgcn_sched_barrier(0);
    }
    asm volatile("" : "+v"(acc[0]), "+v"(acc[1]), "+v"(acc[NT - 1]), "+v"(cs) :: "memory");
  }
}

template <bool ffn> DI u64 role_b_head(const Params& P, const Ctx& c, int wid) {
  const int lane = c.lane, tq = c.tq, hh = c.hh, t = lane & 31;
  const int g = c.gi;
  const unsigned tok = (unsigned)(c.p * 128 + 8 * c.G + 2 * tq);
  v4u r0, r1;
  {
    const u16* rp = P.x + (size_t)tok * 512 + lane * 8;
    asm volatile("global_load_dwordx4 %0, %2, off\n\tglobal_load_dwordx4 %1, %2, off offset:1024" : "=&v"(r0), "=&v"(r1) : "v"(rp) : "memory");
  }
  const __amdgpu_buffer_rsrc_t rsX = __builtin_amdgcn_make_buffer_rsrc((void*)P.x, 0, 2048 * 1024, 0x00020000);
  const __amdgpu_buffer_rsrc_t rsW = __builtin_amdgcn_make_buffer_rsrc((void*)(ffn ? P.w1 : P.wqkv), 0, ffn ? 512 * 3584 : 512 * 3072, 0x00020000);
  constexpr unsigned RS = ffn ? 3584u : 3072u;
  const unsigned cA = (unsigned)(224 * g + 32 * (g & 1));
  const unsigned u0 = ffn ? cA * 2u : (unsigned)(512 + 64 * g) * 2u, u1 = ffn ? cA * 2u + 128u : (unsigned)(1024 + 64 * g) * 2u;
  const unsigned ua = ffn ? (cA + 128u) * 2u : (unsigned)(64 * g) * 2u;
  const unsigned ub = (unsigned)((g & 1) ? 224 * g - 32 : 224 * g + 192) * 2u;
  const unsigned voffX = (unsigned)(lane >> 3) * 1024u + (unsigned)(((lane & 7) ^ ((4 * (tq & 1) + (lane >> 4)) & 7)) * 16);
  const unsigned voffW = (unsigned)(lane >> 3) * RS + (unsigned)(((lane & 7) ^ (((lane >> 4) & 1) << 2)) * 16);
  const unsigned soffX = (unsigned)(c.p * 128 + 8 * tq) * 1024u;
  const unsigned wrow = 32u * (unsigned)(tq & 1) * RS;
  auto fill = [&](int f) AI {
    const unsigned slot = (unsigned)(f & 3) * SLOT;
    if (f < 8) {
      const unsigned kcp = (unsigned)((f + c.rot) & 7);
      unsigned sx = soffX + kcp * 128u, sw = 64u * kcp * RS + wrow + ((tq >> 1) ? u1 : u0);
      unsigned mx = slot + (unsigned)tq * 1024u, mw = slot + 16384u + (unsigned)tq * 4096u;
#pragma unroll 1
      for (int m = 0; m < 4; ++m) {
        dma(mx, voffX, rsX, sx); dma(mw, voffW, rsW, sw);
        mx += 4096u; sx += 32768u; mw += 1024u; sw += 8u * RS;
      }
    } else {
      const unsigned kcp = (unsigned)((2 * (f - 8) + (tq >> 1) + c.rot) & 7);
      unsigned sw = 64u * kcp * RS + wrow, mw = slot + (unsigned)tq * 4096u;
#pragma unroll 1
      for (int m = 0; m < 4; ++m) {
        dma(mw, voffW, rsW, sw + ua);
        if (ffn) dma(mw + 16384u, voffW, rsW, sw + ub);
        mw += 1024u; sw += 8u * RS;
      }
    }
  };
  fill(0); fill(1);
  float st0;
  {
    asm volatile("s_waitcnt vmcnt(16)" : "+v"(r0), "+v"(r1) :: "memory");
    float v[2][8], mean[2], rs[2];
#pragma unroll
    for (int k = 0; k < 4; ++k) {
      v[0][2 * k] = u2f(r0[k] << 16); v[0][2 * k + 1] = u2f(r0[k] & 0xffff0000u);
      v[1][2 * k] = u2f(r1[k] << 16); v[1][2 * k + 1] = u2f(r1[k] & 0xffff0000u);
    }
#pragma unroll
    for (int i = 0; i < 2; ++i) mean[i] = wavesum(((v[i][0] + v[i][1]) + (v[i][2] + v[i][3])) + ((v[i][4] + v[i][5]) + (v[i][6] + v[i][7]))) * (1.0f / 512.0f);
#pragma unroll
    for (int i = 0; i < 2; ++i) {
      float q2 = 0.f;
#pragma unroll
      for (int k = 0; k < 8; ++k) { const float d = v[i][k] - mean[i]; q2 = __builtin_fmaf(d, d, q2); }
      rs[i] = __builtin_amdgcn_rsqf(wavesum(q2) * (1.0f / 512.0f) + 1e-5f);
    }
    st0 = (lane & 2) ? rs[1] : rs[0];
    st0 = (lane & 1) ? -((lane & 2) ? mean[1] : mean[0]) * st0 : st0;
    asm volatile("" : "+v"(st0));
  }
  unsigned xa[4];
#pragma unroll
  for (int ks = 0; ks < 4; ++ks) xa[ks] = (unsigned)(32 * tq + t) * 128u + (unsigned)(((2 * ks + hh) ^ ((t >> 1) & 7)) * 16);
  v4u xr[32];
  u64 tag = 0;
  v4u gr;
  const unsigned token = (unsigned)(c.p * 128 + 32 * tq + t);
#pragma unroll
  for (int kc = 0; kc < 8; ++kc) {
    if (kc < 7 || ffn) VMW(8); else VMW(4);
    BAR();
    if (kc == 0) {
      tag = lds_tag();
      if (lane < 4)
        __hip_atomic_store((GAS u64*)(P.ws + WS_ST + tok * 16u + (unsigned)lane * 8u), ((u64)(unsigned)tag << 32) | f2u(st0), __ATOMIC_RELAXED, __HIP_MEMORY_SCOPE_AGENT);
    }
    if (kc == 5) {
      const char* gp = P.ws + WS_ST + token * 16u;
      asm volatile("global_load_dwordx4 %0, %1, off sc1" : "=&v"(gr) : "v"(gp) : "memory");
    }
    fill(kc + 2);
    const unsigned sb = (unsigned)(kc & 3) * SLOT;
#pragma unroll
    for (int ks = 0; ks < 4; ++ks) xr[4 * kc + ks] = lds128(xa[ks] + sb);
    asm volatile("" : "+v"(xr[4 * kc]), "+v"(xr[4 * kc + 1]), "+v"(xr[4 * kc + 2]), "+v"(xr[4 * kc + 3]));
  }
  asm volatile("" : "+v"(gr));
  STAMP(2);
  BAR();
  fill(10); fill(11);
  if (__builtin_amdgcn_ballot_w64(gr.y != (unsigned)tag || gr.w != (unsigned)tag) != 0) {
    const __amdgpu_buffer_rsrc_t rs = __builtin_amdgcn_make_buffer_rsrc((void*)(P.ws + WS_ST), 0, 32768, 0x00020000);
#pragma unroll 1
    for (unsigned spin = 0; spin < kSpin; ++spin) {
      gr = __builtin_amdgcn_raw_buffer_load_b128(rs, token * 16u, 0, 16);
      if (__builtin_amdgcn_ballot_w64(gr.y != (unsigned)tag || gr.w != (unsigned)tag) == 0) break;
      __builtin_amdgcn_s_sleep(1);
    }
  }
  const float rstd = u2f(gr.x), mr = u2f(gr.z);
  if (hh == 0) *(LAS f32x2*)(size_t)(L_STATS + (unsigned)(32 * tq + t) * 8u) = f32x2{rstd, mr};
  if (ffn) VMW(24); else VMW(12);
  BAR();
  const int cb = (lane >> 4) & 1, q = (lane >> 2) & 3, pp = lane & 3, sw = (q >> 1) & 1;
  unsigned wl[2];
#pragma unroll
  for (int h = 0; h < 2; ++h) wl[h] = (unsigned)(8 * hh + q) * 128u + (unsigned)((4 * (h ^ sw) + 2 * cb + (pp >> 1)) * 16 + 8 * (pp & 1));
  const __amdgpu_buffer_rsrc_t rsHB = __builtin_amdgcn_make_buffer_rsrc(P.ws + WS_HB, 0, HB_BYTES, 0x00020000);
  const unsigned T32 = (unsigned)(c.p * 4 + tq);
  f32x4 cs = {0.f, 0.f, 0.f, 0.f};
  auto cs_sync = [&](int tile, bool wr) AI {
    const float s = xhalf_add(cs[0]);
    if (hh == 0 && wr) *(LAS float*)(size_t)(L_CS1 + (unsigned)tile * 128u + (unsigned)t * 4u) = s;
    BAR();
  };
  if constexpr (ffn) {
    const unsigned w2a = ((g & 1) ? wl[1] : wl[0]) + 16384u;
    int ti[3];
    unsigned wb[3];
    f32x16 acc[3];
#pragma unroll
    for (int i = 0; i < 3; ++i) { ti[i] = (i + tq) % 3; wb[i] = ti[i] == 0 ? wl[0] : (ti[i] == 1 ? wl[1] : w2a); acc[i] = zero16(); }
    pass1<3, 8>(P, wid, xr, acc, cs, wb, lane);
    cs_sync(ti[0], tq < 3);
    STAMP(3);
    f32x4 csv[3][4], bsv[3][4];
#pragma unroll
    for (int i = 0; i < 3; ++i) { load16(L_CS1 + (unsigned)ti[i] * 128u, hh, csv[i]); load16(L_B1 + (unsigned)(4 + ti[i]) * 128u, hh, bsv[i]); }
#pragma unroll
    for (int i = 0; i < 3; ++i)
      ffn_tile(acc[i], rstd, mr, csv[i], bsv[i], rsHB, (unsigned)lane * 16u + (T32 * 144u + (unsigned)(64 + 6 * g + 2 * ti[i])) * 1024u);
    publish_last(L_CNT + C_LATE * 4, 4, P.ws, FL_L + (unsigned)(c.p * 8 + g) * 128u, tag, lane);
  } else {
    int ti[2];
    unsigned wb[2];
    f32x16 acc[2];
#pragma unroll
    for (int i = 0; i < 2; ++i) { ti[i] = (i + tq) & 1; wb[i] = ti[i] == 0 ? wl[0] : wl[1]; acc[i] = zero16(); }
    pass1<2, 4>(P, wid, xr, acc, cs, wb, lane);
    cs_sync(ti[0], tq < 2);
    STAMP(3);
    v4u pq[4][2];
#pragma unroll
    for (int hd = 0; hd < 4; ++hd) {
      float y[8];
      fold8(acc[hd >> 1], 8 * (hd & 1), rstd, mr, L_CS1 + (unsigned)ti[hd >> 1] * 128u, hh, y);
      hedge(mfma(wphi_frag(2 * ti[hd >> 1] + (hd & 1), lane), pack8(y), zero16()), pq[hd][0], pq[hd][1]);
    }
    STAMP(4);
    if (tq == 0) { poll_flags(P.ws, FL_K + (unsigned)g * 16u * 128u, 16, tag, lane); lds_set(L_CNT + C_KVRDY * 4, 1); }
    else lds_wait_ge(L_CNT + C_KVRDY * 4, 1);
    STAMP(5);
    {
      const __amdgpu_buffer_rsrc_t rsKV = __builtin_amdgcn_make_buffer_rsrc(P.ws + WS_KVP, 0, 524288 + 65536, 0x00020000);
      const unsigned hbase = (unsigned)(g * 4 + tq) * 16u;
      v4u pv[16];
#pragma unroll
      for (int i = 0; i < 16; ++i) pv[i] = __builtin_amdgcn_raw_buffer_load_b128(rsKV, (unsigned)lane * 16u, (hbase + (unsigned)i) * 1024u, 16);
      const v4u z0 = __builtin_amdgcn_raw_buffer_load_b128(rsKV, (unsigned)lane * 16u, 524288u + hbase * 128u, 16);
      const v4u z1 = __builtin_amdgcn_raw_buffer_load_b128(rsKV, (unsigned)lane * 16u, 524288u + hbase * 128u + 1024u, 16);
      v4u idf;
      {
        const int m = lane & 31, h2 = lane >> 5;
        const int j = (m < 16 && ((m >> 2) & 1) == h2) ? (((m >> 3) << 2) | (m & 3)) : -1;
#pragma unroll
        for (int k = 0; k < 4; ++k) idf[k] = (j == 2 * k ? 0x3F80u : 0u) | (j == 2 * k + 1 ? 0x3F800000u : 0u);
      }
      f32x16 sum = zero16();
#pragma unroll
      for (int i = 0; i < 16; ++i) sum = mfma(idf, pv[i], sum);
      const float s[8] = {sum[0], sum[1], sum[2], sum[3], sum[4], sum[5], sum[6], sum[7]};
      const unsigned afr = L_AFR + (unsigned)tq * 2048u;
      *(LAS v4u*)(size_t)(afr + (unsigned)((lane >> 4) & 1) * 1024u + (unsigned)((lane & 15) + 32 * (lane >> 5)) * 16u) = pack8(s);
      const unsigned hh2 = (unsigned)(lane >> 4) & 1u, c16 = (unsigned)lane & 15u;
      float zz[4];
#pragma unroll
      for (int k = 0; k < 4; ++k) {
        float v = u2f(z0[k]) + u2f(z1[k]);
        v += shx(v, 8, lane); v += shx(v, 16, lane); v += shx(v, 32, lane);
        zz[k] = v;
      }
      if (lane < 8) *(LAS f32x4*)(size_t)(L_ZT + (unsigned)tq * 128u + (unsigned)lane * 16u) = f32x4{zz[0], zz[1], zz[2], zz[3]};
      const unsigned f0 = 16u * (unsigned)(lane >> 5) + 4u * hh2;
      const f32x4 za = lds128f(L_ZT + (unsigned)tq * 128u + f0 * 4u), zb = lds128f(L_ZT + (unsigned)tq * 128u + (f0 + 8u) * 4u);
      *(LAS v4u*)(size_t)(afr + (unsigned)(lane >> 5) * 1024u + (16u + c16 + 32u * hh2) * 16u) = v4u{pk2(za[0], za[1]), pk2(za[2], za[3]), pk2(zb[0], zb[1]), pk2(zb[2], zb[3])};
      asm volatile("s_waitcnt lgkmcnt(0)" ::: "memory");
      lds_inc(L_CNT + C_AFR * 4, lane);
      lds_wait_ge(L_CNT + C_AFR * 4, 4);
    }
#pragma unroll
    for (int hd = 0; hd < 4; ++hd) {
      const int head = 2 * ti[hd >> 1] + (hd & 1);
      f32x16 d = mfma(lds128(L_AFR + (unsigned)head * 2048u + (unsigned)lane * 16u), pq[hd][0], zero16());
      d = mfma(lds128(L_AFR + (unsigned)head * 2048u + 1024u + (unsigned)lane * 16u), pq[hd][1], d);
      const float r = __builtin_amdgcn_rcpf(d[8] + 1e-6f);
      const v4u o = v4u{pk2(d[0] * r, d[1] * r), pk2(d[2] * r, d[3] * r), pk2(d[4] * r, d[5] * r), pk2(d[6] * r, d[7] * r)};
      st128(o, rsHB, (unsigned)lane * 16u + (T32 * 144u + (unsigned)(112 + 4 * g + head)) * 1024u);
    }
    publish_last(L_CNT + C_O * 4, 4, P.ws, FL_O + (unsigned)(c.p * 8 + g) * 128u, tag, lane);
  }
  STAMP(6);
  return tag;
}

DI void role_b(const Params& P, const Ctx& c, int wid) {
  const int lane = c.lane, tq = c.tq;
  const u64 tag = c.ffn ? role_b_head<true>(P, c, wid) : role_b_head<false>(P, c, wid);
  const int pan = c.tt >> 1;
  const int tb = tq * 64 + lane;
  v4u xres[2], bres;
#pragma unroll
  for (int m = 0; m < 2; ++m) {
    const int idx = tb + 256 * m;
    xres[m] = *(const GAS v4u*)(P.x + (size_t)(64 * c.tt + (idx >> 3)) * 512 + 64 * c.ct + 8 * (idx & 7));
  }
  bres = *(const GAS v4u*)(P.b2 + 64 * c.ct + 8 * (tb & 7));
  if (tq == 0) {
    poll_flags(P.ws, (ORD ? FL_O : FL_L) + (unsigned)pan * 8u * 128u, 8, tag, lane);
    lds_set(L_CNT + (ORD ? C_ORDY : C_LATERDY) * 4, 1);
    poll_flags(P.ws, (ORD ? FL_L : FL_O) + (unsigned)pan * 8u * 128u, 8, tag, lane);
    lds_set(L_CNT + (ORD ? C_LATERDY : C_ORDY) * 4, 1);
  } else if (tq == 1 && __builtin_amdgcn_workgroup_id_x() == 0) {
    poll_flags(P.ws, FL_E, 64, tag, lane); poll_flags(P.ws, FL_E + 64 * 128, 64, tag, lane);
    poll_flags(P.ws, FL_K, 64, tag, lane); poll_flags(P.ws, FL_K + 64 * 128, 64, tag, lane);
    if (lane == 0) __hip_atomic_store((GAS u64*)(P.ws + WS_EPOCH), tag, __ATOMIC_RELAXED, __HIP_MEMORY_SCOPE_AGENT);
  }
  BAR();
#pragma unroll
  for (int m = 0; m < 2; ++m) {
    const int idx = tb + 256 * m;
    const unsigned a = (unsigned)(idx >> 3) * 272u + (unsigned)(idx & 7) * 32u;
    f32x4 lo = lds128f(a), hi = lds128f(a + 16u);
#pragma unroll
    for (unsigned w = 1; w < 4; ++w) { lo += lds128f(w * SLOT + a); hi += lds128f(w * SLOT + a + 16u); }
    v4u o;
#pragma unroll
    for (int k = 0; k < 4; ++k) {
      const float a0 = k < 2 ? lo[2 * k] : hi[2 * k - 4], a1 = k < 2 ? lo[2 * k + 1] : hi[2 * k - 3];
      const float x0 = u2f(xres[m][k] << 16), x1 = u2f(xres[m][k] & 0xffff0000u);
      const float b0 = u2f(bres[k] << 16), b1 = u2f(bres[k] & 0xffff0000u);
      o[k] = pk2(a0 + x0 + b0, a1 + x1 + b1);
    }
    *(GAS v4u*)(P.out + (size_t)(64 * c.tt + (idx >> 3)) * 512 + 64 * c.ct + 8 * (idx & 7)) = o;
  }
  STAMP(7);
}

__global__ __launch_bounds__(512, 2) NOPK void fused_kernel(Params P) {
  __shared__ __attribute__((aligned(1024))) char smem[LDS_BYTES];
  asm volatile("" ::"v"((unsigned)(size_t)(LAS char*)smem) : "memory");
  u64 pc;
  asm volatile("s_getpc_b64 %0" : "=s"(pc));
  const int tid = __builtin_amdgcn_workitem_id_x(), lane = tid & 63;
  const int wid = __builtin_amdgcn_readfirstlane(tid >> 6);
  STAMP(0);
  Ctx c;
  c.lane = lane; c.tq = wid & 3; c.hh = lane >> 5;
  const int blk = __builtin_amdgcn_workgroup_id_x(), xcd = blk & 7, j = blk >> 3, ja = j & 7, jb = j >> 3;
  c.p = (xcd & 1) * 8 + ja;
  c.G = (xcd >> 1) * 4 + jb;
  c.ffn = !(c.G & 1);
  c.gi = c.G >> 1;
  c.rot = ROT1 == 0 ? 0 : (ROT1 == 1 ? (((ja + jb) % 3) * 3) & 7 : (ROT1 == 2 ? (ja + 2 * jb) & 7 : ((ja + jb) & 1) * 4));
  c.tt = (xcd >> 1) * 8 + ja; c.ct = (xcd & 1) * 4 + jb;
  c.rotE = ROT2 ? (2 * ja + 4 * jb) & 15 : 0; c.rotL = ROT2 ? (ja + 3 * jb) % 12 : 0; c.rotO = ROT2 ? (ja + 2 * jb) & 7 : 0;
  if (wid < 4) {
    if (CODEPF) {
      const unsigned kb = (unsigned)(j + 32 * (wid & 1));
      if (wid < 2 && kb < CODE_KB) {
        const unsigned v = *(const GAS unsigned*)((pc & ~255ull) + kb * 1024u + (unsigned)lane * 16u);
        asm volatile("" ::"v"(v));
      }
    }
    const u64 e = __hip_atomic_load((const GAS u64*)(P.ws + WS_EPOCH), __ATOMIC_RELAXED, __HIP_MEMORY_SCOPE_AGENT);
    if (tid < 32) *(LAS unsigned*)(size_t)(L_CNT + (unsigned)tid * 4u) = 0u;
    if (c.ffn) {
      if (tid < 224) {
        const int t7 = tid >> 5, n = tid & 31, g = c.gi;
        const int cA = 224 * g + 32 * (g & 1);
        const int col = t7 < 4 ? cA + 32 * t7 : (t7 < 6 ? cA + 128 + 32 * (t7 - 4) : ((g & 1) ? 224 * g : 224 * g + 192));
        *(LAS float*)(size_t)(L_B1 + (unsigned)tid * 4u) = u2f((unsigned)P.b1[col + n] << 16);
      }
    } else if (tid < 128) {
      *(LAS v4u*)(size_t)(L_WPHI + (unsigned)tid * 16u) = *(const GAS v4u*)(P.wphi + (size_t)c.gi * 1024 + tid * 8);
    }
    const u64 tag = e + 1u;
    if (tid == 0) *(LAS v2u*)(size_t)L_TAG = v2u{(unsigned)tag, (unsigned)(tag >> 32)};
    STAMP(1);
    role_a(P, c, wid);
  } else {
    STAMP(1);
    role_b(P, c, wid);
  }
}

}

extern "C" void kernel_launch(void* const* d_in, const int* in_sizes, int n_in, void* d_out, int out_size, void* d_ws, size_t ws_size,
                              hipStream_t stream) {
  Params P;
  P.x = (const u16*)d_in[0]; P.wqkv = (const u16*)d_in[1]; P.wphi = (const u16*)d_in[2]; P.wo = (const u16*)d_in[3];
  P.w1 = (const u16*)d_in[5]; P.b1 = (const u16*)d_in[6]; P.w2 = (const u16*)d_in[7]; P.b2 = (const u16*)d_in[8];
  P.out = (u16*)d_out; P.ws = (char*)d_ws;
  fused_kernel<<<256, 512, 0, stream>>>(P);
}
```

```cpp
#include <hip/hip_runtime.h>
#include <stdint.h>

namespace {

typedef unsigned short u16;
typedef unsigned long long u64;
typedef unsigned v4u __attribute__((ext_vector_type(4)));
typedef unsigned v2u __attribute__((ext_vector_type(2)));
typedef float f32x16 __attribute__((ext_vector_type(16)));
typedef float f32x4 __attribute__((ext_vector_type(4)));
typedef float f32x2 __attribute__((ext_vector_type(2)));
typedef __bf16 bf16x8 __attribute__((ext_vector_type(8)));
typedef __bf16 bf16x2 __attribute__((ext_vector_type(2)));
typedef short s16x4 __attribute__((ext_vector_type(4)));
#define LAS __attribute__((address_space(3)))
#define GAS __attribute__((address_space(1)))
#if defined(__HIP_DEVICE_COMPILE__)
#define NOPK __attribute__((target("no-packed-fp32-ops")))
#else
#define NOPK
#endif
#define DI __device__ __forceinline__ NOPK
#define AI __attribute__((always_inline)) NOPK

#define STAMPS 0
#define ROT1 1
#define ROT2 0
#define STR2(x) #x
#define STR(x) STR2(x)
#define W1 8
#define W2 4
#define OUTAUX 2
#define ORD 0
#define CODEPF 1
#define WARM2 1
#define CODE_KB 36

constexpr unsigned WS_EPOCH = 0;
constexpr unsigned FL_E = 4096;
constexpr unsigned FL_L = FL_E + 128 * 128;
constexpr unsigned FL_K = FL_L + 128 * 128;
constexpr unsigned FL_O = FL_K + 128 * 128;
constexpr unsigned WS_ST = 73728;
constexpr unsigned WS_KVP = 131072;
constexpr unsigned WS_ZP = WS_KVP + 524288;
constexpr unsigned WS_HB = 1048576;
constexpr unsigned HB_BYTES = 64u * 144u * 1024u;
constexpr unsigned WS_STAMP = 16u << 20;

constexpr unsigned SLOT = 32768;
constexpr unsigned L_MISC = 131072;
constexpr unsigned L_STATS = L_MISC;
constexpr unsigned L_CS0 = L_MISC + 1024;
constexpr unsigned L_CS1 = L_MISC + 1536;
constexpr unsigned L_B1 = L_MISC + 2048;
constexpr unsigned L_WPHI = L_MISC + 3072;
constexpr unsigned L_CNT = L_MISC + 5120;
constexpr unsigned L_TAG = L_MISC + 5248;
constexpr unsigned L_ZT = L_MISC + 5376;
constexpr unsigned L_AFR = L_MISC + 8192;
constexpr unsigned LDS_BYTES = 163840;
enum { C_CS1 = 0, C_LATE = 1, C_KV = 2, C_O = 3, C_AFR = 4, C_KVRDY = 5, C_LATERDY = 6, C_ORDY = 7, C_FREE = 8  , C_LAND = 12  , C_EARLY = 16, C_IMG = 17 };

constexpr unsigned kSpin = 1u << 16;

struct Params {
  const u16* x; const u16* wqkv; const u16* wphi; const u16* wo;
  const u16* w1; const u16* b1; const u16* w2; const u16* b2;
  u16* out; char* ws;
};

DI unsigned f2u(float f) { return __builtin_bit_cast(unsigned, f); }
DI float u2f(unsigned u) { return __builtin_bit_cast(float, u); }
DI float fmx(float a, float b) { return __builtin_fmaxf(a, b); }
DI f32x16 mfma(v4u a, v4u b, f32x16 c) {
  return __builtin_amdgcn_mfma_f32_32x32x16_bf16(__builtin_bit_cast(bf16x8, a), __builtin_bit_cast(bf16x8, b), c, 0, 0, 0);
}
DI f32x16 zero16() { f32x16 z; for (int r = 0; r < 16; ++r) z[r] = 0.f; return z; }
DI v4u lds128(unsigned a) { return *(const LAS v4u*)(size_t)a; }
DI f32x4 lds128f(unsigned a) { return *(const LAS f32x4*)(size_t)a; }
DI v2u tr64(unsigned a) { return __builtin_bit_cast(v2u, __builtin_amdgcn_ds_read_tr16_b64_v4i16((LAS s16x4*)(size_t)a)); }
DI v4u tr2(unsigned a0, unsigned a1) { v2u l = tr64(a0), h = tr64(a1); return v4u{l.x, l.y, h.x, h.y}; }
DI unsigned pk2(float a, float b) { return __builtin_bit_cast(unsigned, __builtin_convertvector(f32x2{a, b}, bf16x2)); }
DI float xhalf_add(float v) { auto r = __builtin_amdgcn_permlane32_swap(f2u(v), f2u(v), false, false); return u2f(r[0]) + u2f(r[1]); }
DI float xhalf_max(float v) { auto r = __builtin_amdgcn_permlane32_swap(f2u(v), f2u(v), false, false); return fmx(u2f(r[0]), u2f(r[1])); }
DI float shx(float v, int m, int lane) { return u2f((unsigned)__builtin_amdgcn_ds_bpermute((lane ^ m) << 2, (int)f2u(v))); }
DI f32x4 lanesum(v4u f, f32x4 c) {
  const s16x4 one = {0x3F80, 0x3F80, 0x3F80, 0x3F80};
  c = __builtin_amdgcn_mfma_f32_4x4x4bf16_1k(one, __builtin_bit_cast(s16x4, v2u{f.x, f.y}), c, 0, 0, 0);
  return __builtin_amdgcn_mfma_f32_4x4x4bf16_1k(one, __builtin_bit_cast(s16x4, v2u{f.z, f.w}), c, 0, 0, 0);
}
template <int CTRL> DI float dppadd(float v) { return v + u2f((unsigned)__builtin_amdgcn_update_dpp(0, (int)f2u(v), CTRL, 0xf, 0xf, true)); }
DI float wavesum(float v) {
  v = dppadd<0xB1>(v); v = dppadd<0x4E>(v); v = dppadd<0x141>(v); v = dppadd<0x140>(v);
  auto r = __builtin_amdgcn_permlane16_swap(f2u(v), f2u(v), false, false);
  return xhalf_add(u2f(r[0]) + u2f(r[1]));
}
DI void dma(unsigned m0v, unsigned voff, __amdgpu_buffer_rsrc_t rs, unsigned soff) {
  asm volatile("s_mov_b32 m0, %0\n\ts_nop 0\n\tbuffer_load_dwordx4 %1, %2, %3 offen lds" ::"s"(m0v), "v"(voff), "s"(rs), "s"(soff) : "memory");
}
DI void st128(v4u v, __amdgpu_buffer_rsrc_t rs, unsigned voff) { __builtin_amdgcn_raw_buffer_store_b128(v, rs, voff, 0, 16); }
#define BAR() do { __builtin_amdgcn_sched_barrier(0); asm volatile("s_waitcnt lgkmcnt(0)\n\ts_barrier" ::: "memory"); __builtin_amdgcn_sched_barrier(0); } while (0)
#define VMW(n) asm volatile("s_waitcnt vmcnt(" #n ")" ::: "memory")
constexpr unsigned ONES = 0x3F803F80u;

#define STAMP(i) do { } while (0)

DI void poll_flags(const char* ws, unsigned base, int n, u64 tag, int lane) {
  const GAS u64* f = (const GAS u64*)(ws + base + (unsigned)(lane < n ? lane : 0) * 128u);
#pragma unroll 1
  for (unsigned spin = 0; spin < kSpin; ++spin) {
    const u64 v = __hip_atomic_load(f, __ATOMIC_RELAXED, __HIP_MEMORY_SCOPE_AGENT);
    if (__builtin_amdgcn_ballot_w64(v != tag) == 0) break;
    __builtin_amdgcn_s_sleep(1);
  }
  __builtin_amdgcn_fence(__ATOMIC_ACQUIRE, "wavefront");
  asm volatile("" ::: "memory");
}
DI void lds_wait_ge(unsigned addr, unsigned want) {
#pragma unroll 1
  for (unsigned spin = 0; spin < (kSpin << 4); ++spin) {
    const unsigned v = *(volatile LAS unsigned*)(size_t)addr;
    if ((unsigned)__builtin_amdgcn_readfirstlane(v) >= want) break;
    __builtin_amdgcn_s_sleep(1);
  }
  asm volatile("" ::: "memory");
}
DI void lds_set(unsigned addr, unsigned v) { *(volatile LAS unsigned*)(size_t)addr = v; }
DI void lds_inc(unsigned addr, int lane) { if (lane == 0) __hip_atomic_fetch_add((LAS unsigned*)(size_t)addr, 1u, __ATOMIC_RELAXED, __HIP_MEMORY_SCOPE_WORKGROUP); }
DI void publish_last(unsigned cnt_addr, unsigned n, char* ws, unsigned flag_off, u64 tag, int lane) {
  VMW(0);
  if (lane == 0) {
    const unsigned old = __hip_atomic_fetch_add((LAS unsigned*)(size_t)cnt_addr, 1u, __ATOMIC_RELAXED, __HIP_MEMORY_SCOPE_WORKGROUP);
    if (old == n - 1) __hip_atomic_store((GAS u64*)(ws + flag_off), tag, __ATOMIC_RELAXED, __HIP_MEMORY_SCOPE_AGENT);
  }
}
DI u64 lds_tag() { const v2u t = *(const LAS v2u*)(size_t)L_TAG; return ((u64)(unsigned)__builtin_amdgcn_readfirstlane(t.y) << 32) | (unsigned)__builtin_amdgcn_readfirstlane(t.x); }

DI void fold8(const f32x16& acc, int r0, float rstd, float mr, unsigned cs, int hh, float* y) {
#pragma unroll
  for (int g = 0; g < 2; ++g) {
    const f32x4 t = lds128f(cs + (unsigned)((r0 >> 2) + g) * 32u + (unsigned)hh * 16u) * mr;
#pragma unroll
    for (int k = 0; k < 4; ++k) y[4 * g + k] = __builtin_fmaf(acc[r0 + 4 * g + k], rstd, t[k]);
  }
}
DI void load16(unsigned a, int hh, f32x4* v) {
#pragma unroll
  for (int g = 0; g < 4; ++g) v[g] = lds128f(a + (unsigned)g * 32u + (unsigned)hh * 16u);
}
DI void ffn_tile(const f32x16& acc, float rstd, float mr, const f32x4* cs, const f32x4* bs, __amdgpu_buffer_rsrc_t rsHB, unsigned voff) {
  f32x2 y[8], d[8];
#pragma unroll
  for (int g = 0; g < 4; ++g) {
    const f32x4 t = cs[g] * mr + bs[g];
    y[2 * g] = f32x2{acc[4 * g], acc[4 * g + 1]} * rstd + f32x2{t[0], t[1]};
    y[2 * g + 1] = f32x2{acc[4 * g + 2], acc[4 * g + 3]} * rstd + f32x2{t[2], t[3]};
  }
#pragma unroll
  for (int k = 0; k < 8; ++k) d[k] = y[k] * ((y[k] * y[k]) * (-0.10294324f) + (-2.30220819f));
#pragma unroll
  for (int k = 0; k < 8; ++k) { d[k].x = __builtin_amdgcn_exp2f(d[k].x); d[k].y = __builtin_amdgcn_exp2f(d[k].y); }
#pragma unroll
  for (int k = 0; k < 8; ++k) d[k] += 1.0f;
#pragma unroll
  for (int k = 0; k < 8; ++k) { d[k].x = __builtin_amdgcn_rcpf(d[k].x); d[k].y = __builtin_amdgcn_rcpf(d[k].y); }
  v4u o0, o1;
#pragma unroll
  for (int k = 0; k < 4; ++k) {
    const f32x2 g0 = y[k] * d[k], g1 = y[4 + k] * d[4 + k];
    o0[k] = pk2(g0.x, g0.y); o1[k] = pk2(g1.x, g1.y);
  }
  st128(o0, rsHB, voff); st128(o1, rsHB, voff + 1024u);
}
DI void hedge(const f32x16& d, v4u& o0, v4u& o1) {
  constexpr float L = 1.4426950408889634f;
  float m0 = fmx(fmx(fmx(d[0], d[1]), fmx(d[2], d[3])), fmx(fmx(d[4], d[5]), fmx(d[6], d[7])));
  float m1 = fmx(fmx(fmx(d[8], d[9]), fmx(d[10], d[11])), fmx(fmx(d[12], d[13]), fmx(d[14], d[15])));
  m0 = xhalf_max(m0) * -L; m1 = xhalf_max(m1) * -L;
  float e[16];
#pragma unroll
  for (int i = 0; i < 8; ++i) { e[i] = __builtin_amdgcn_exp2f(__builtin_fmaf(d[i], L, m0)); e[8 + i] = __builtin_amdgcn_exp2f(__builtin_fmaf(d[8 + i], L, m1)); }
  float s0 = ((e[0] + e[1]) + (e[2] + e[3])) + ((e[4] + e[5]) + (e[6] + e[7]));
  float s1 = ((e[8] + e[9]) + (e[10] + e[11])) + ((e[12] + e[13]) + (e[14] + e[15]));
  s0 = __builtin_amdgcn_rcpf(xhalf_add(s0)); s1 = __builtin_amdgcn_rcpf(xhalf_add(s1));
#pragma unroll
  for (int k = 0; k < 4; ++k) { o0[k] = pk2(e[2 * k] * s0, e[2 * k + 1] * s0); o1[k] = pk2(e[8 + 2 * k] * s1, e[8 + 2 * k + 1] * s1); }
}
DI v4u wphi_frag(int hd, int lane) {
  const int hh = lane >> 5, q = (lane >> 2) & 3, pp = lane & 3;
  const unsigned a = L_WPHI + (unsigned)hd * 512u + (unsigned)(4 * hh + q) * 32u + (unsigned)pp * 8u;
  v4u f = tr2(a, a + 256u);
  const unsigned sg = (lane & 16) ? 0x80008000u : 0u;
  f.x ^= sg; f.y ^= sg; f.z ^= sg; f.w ^= sg;
  return f;
}
DI v4u selv(bool s, v4u a, v4u b) { const unsigned m = s ? 0xffffffffu : 0u; return v4u{(a.x & m) | (b.x & ~m), (a.y & m) | (b.y & ~m), (a.z & m) | (b.z & ~m), (a.w & m) | (b.w & ~m)}; }
DI v4u pack8(const float* y) { return v4u{pk2(y[0], y[1]), pk2(y[2], y[3]), pk2(y[4], y[5]), pk2(y[6], y[7])}; }

struct Ctx {
  int lane, tq, hh;
  int p, gi, G;
  int rot;
  int tt, ct;
  int rotE, rotL, rotO;
  bool ffn;
};

DI void role_a(const Params& P, const Ctx& c, int wid, u64 amp_a, u64 amp_b) {
  const int lane = c.lane, tq = c.tq, hh = c.hh, t = lane & 31;
  const int cb = (lane >> 4) & 1, q = (lane >> 2) & 3, pp = lane & 3, sw = (q >> 1) & 1;
  const int th = tq & 1, up = tq >> 1;
  unsigned xa[4];
#pragma unroll
  for (int ks = 0; ks < 4; ++ks) xa[ks] = (unsigned)(64 * th + t) * 128u + (unsigned)(((2 * ks + hh) ^ ((t >> 1) & 7)) * 16);
  const unsigned wl = 16384u + (unsigned)(8 * hh + q) * 128u + (unsigned)((4 * (up ^ sw) + 2 * cb + (pp >> 1)) * 16 + 8 * (pp & 1));
  f32x16 acc[2][2];
#pragma unroll
  for (int i = 0; i < 2; ++i) { acc[i][0] = zero16(); acc[i][1] = zero16(); }
  f32x4 cs = {0.f, 0.f, 0.f, 0.f};
  unsigned x0 = xa[0], x1 = xa[1], x2 = xa[2], x3 = xa[3], w0 = wl;
  v4u xf[4][2], wf[4][2];
  auto rd = [&](int ks, v4u* X, v4u* W) AI {
    const unsigned xb = ks == 0 ? x0 : (ks == 1 ? x1 : (ks == 2 ? x2 : x3));
    X[0] = lds128(xb); X[1] = lds128(xb + 4096u);
    const unsigned a = w0 + (unsigned)(ks * 2048);
    W[0] = tr2(a, a + 512u); W[1] = tr2(a + 8192u, a + 8704u);
  };
  BAR();
  rd(0, xf[0], wf[0]); rd(1, xf[1], wf[1]);
#pragma unroll 1
  for (int kc = 0; kc < 8; ++kc) {
#pragma unroll
    for (int ks = 0; ks < 4; ++ks) {
      if (ks == 2) {
        __builtin_amdgcn_sched_barrier(0);
        asm volatile("s_barrier" ::: "memory");
        __builtin_amdgcn_sched_barrier(0);
        const unsigned sb = (unsigned)((kc + 1) & 3) * SLOT;
        x0 = xa[0] + sb; x1 = xa[1] + sb; w0 = wl + sb;
        asm volatile("" : "+v"(x0), "+v"(x1), "+v"(w0));
      }
      if (ks == 0) {
        const unsigned sb = (unsigned)(kc & 3) * SLOT;
        x2 = xa[2] + sb; x3 = xa[3] + sb;
        asm volatile("" : "+v"(x2), "+v"(x3));
      }
      rd((ks + 2) & 3, xf[(ks + 2) & 3], wf[(ks + 2) & 3]);
#pragma unroll
      for (int i = 0; i < 2; ++i) { acc[i][0] = mfma(wf[ks][i], xf[ks][0], acc[i][0]); acc[i][1] = mfma(wf[ks][i], xf[ks][1], acc[i][1]); }
      cs = lanesum(selv(th != 0, wf[ks][1], wf[ks][0]), cs);
      __builtin_amdgcn_sched_group_barrier(0x008, 1, 0);
      __builtin_amdgcn_sched_group_barrier(0x100, 2, 0);
      __builtin_amdgcn_sched_group_barrier(0x008, 1, 0);
      __builtin_amdgcn_sched_group_barrier(0x100, 2, 0);
      __builtin_amdgcn_sched_group_barrier(0x008, 1, 0);
      __builtin_amdgcn_sched_group_barrier(0x100, 2, 0);
      __builtin_amdgcn_sched_group_barrier(0x002, 4, 0);
      __builtin_amdgcn_sched_group_barrier(0x008, 3, 0);
      __builtin_amdgcn_sched_barrier(0);
    }
    asm volatile("" : "+v"(acc[0][0]), "+v"(acc[0][1]), "+v"(acc[1][0]), "+v"(acc[1][1]), "+v"(cs));
  }
  STAMP(2);
  {
    const float s = xhalf_add(cs[0]);
    if (hh == 0) *(LAS float*)(size_t)(L_CS0 + (unsigned)(up + 2 * th) * 128u + (unsigned)t * 4u) = s;
  }
  BAR();
  const u64 tag = lds_tag();
  float rstd[2], mr[2];
#pragma unroll
  for (int b = 0; b < 2; ++b) {
    const f32x2 st = *(const LAS f32x2*)(size_t)(L_STATS + (unsigned)(64 * th + 32 * b + t) * 8u);
    rstd[b] = st.x; mr[b] = st.y;
  }
  const __amdgpu_buffer_rsrc_t rsHB = __builtin_amdgcn_make_buffer_rsrc(P.ws + WS_HB, 0, HB_BYTES, 0x00020000);
  if (c.ffn) {
    f32x4 csv[2][4], bsv[2][4];
#pragma unroll
    for (int i = 0; i < 2; ++i) { load16(L_CS0 + (unsigned)(up + 2 * i) * 128u, hh, csv[i]); load16(L_B1 + (unsigned)(up + 2 * i) * 128u, hh, bsv[i]); }
#pragma unroll
    for (int i = 0; i < 2; ++i)
#pragma unroll
      for (int b = 0; b < 2; ++b) {
        const unsigned tile = (unsigned)(up + 2 * i), T32 = (unsigned)(c.p * 4 + 2 * th + b);
        ffn_tile(acc[i][b], rstd[b], mr[b], csv[i], bsv[i], rsHB, (unsigned)lane * 16u + (T32 * 144u + (unsigned)(8 * c.gi) + 2u * tile) * 1024u);
        if (2 * i + b < 3) BAR();
      }
    STAMP(8);
    publish_last(L_CNT + C_EARLY * 4, 4, P.ws, FL_E + (unsigned)(c.p * 8 + c.gi) * 128u, tag, lane);
    BAR();
  } else {
#pragma unroll
    for (int b = 0; b < 2; ++b)
#pragma unroll
      for (int hf = 0; hf < 2; ++hf) {
        const int hd = 2 * up + hf;
        float y[8];
        fold8(acc[0][b], 8 * hf, rstd[b], mr[b], L_CS0 + (unsigned)up * 128u, hh, y);
        v4u o0, o1;
        hedge(mfma(wphi_frag(hd, lane), pack8(y), zero16()), o0, o1);
        const unsigned img = (unsigned)hd * SLOT + 16384u;
        const unsigned row = (unsigned)(64 * th + 32 * b + t);
        const unsigned g = (row >> 1) & 7u;
        *(LAS v2u*)(size_t)(img + row * 64u + (((unsigned)(0 + hh)) ^ g) * 8u) = v2u{o0.x, o0.y};
        *(LAS v2u*)(size_t)(img + row * 64u + (((unsigned)(2 + hh)) ^ g) * 8u) = v2u{o0.z, o0.w};
        *(LAS v2u*)(size_t)(img + row * 64u + (((unsigned)(4 + hh)) ^ g) * 8u) = v2u{o1.x, o1.y};
        *(LAS v2u*)(size_t)(img + row * 64u + (((unsigned)(6 + hh)) ^ g) * 8u) = v2u{o1.z, o1.w};
        fold8(acc[1][b], 8 * hf, rstd[b], mr[b], L_CS0 + (unsigned)(2 + up) * 128u, hh, y);
        const unsigned gv = (row >> 2) & 3u;
        *(LAS v2u*)(size_t)(img + 8192u + row * 32u + (((unsigned)(0 + hh)) ^ gv) * 8u) = v2u{pk2(y[0], y[1]), pk2(y[2], y[3])};
        *(LAS v2u*)(size_t)(img + 8192u + row * 32u + (((unsigned)(2 + hh)) ^ gv) * 8u) = v2u{pk2(y[4], y[5]), pk2(y[6], y[7])};
        if (2 * b + hf < 3) BAR();
      }
    STAMP(8);
    BAR();
    const unsigned img = (unsigned)tq * SLOT + 16384u;
    f32x16 kv = zero16();
    unsigned pa[2], va[2];
#pragma unroll
    for (int i2 = 0; i2 < 2; ++i2) {
      const unsigned r = (unsigned)(8 * hh + 4 * i2 + q);
      pa[i2] = img + r * 64u + (((unsigned)(4 * cb + pp)) ^ ((r >> 1) & 7u)) * 8u;
      va[i2] = img + 8192u + r * 32u + (((unsigned)pp) ^ ((r >> 2) & 3u)) * 8u;
    }
#pragma unroll
    for (int ks = 0; ks < 8; ++ks) {
      const v4u af = tr2(pa[0] + (unsigned)ks * 1024u, pa[1] + (unsigned)ks * 1024u);
      v4u bf = tr2(va[0] + (unsigned)ks * 512u, va[1] + (unsigned)ks * 512u);
      if (cb) bf = v4u{ONES, ONES, ONES, ONES};
      kv = mfma(af, bf, kv);
    }
    const unsigned hidx = (unsigned)(c.gi * 4 + tq) * 16u + (unsigned)c.p;
    const __amdgpu_buffer_rsrc_t rsKV = __builtin_amdgcn_make_buffer_rsrc(P.ws + WS_KVP, 0, 524288 + 65536, 0x00020000);
    if (cb == 0) {
#pragma unroll
      for (int s = 0; s < 2; ++s) {
        const v4u o = v4u{pk2(kv[8 * s], kv[8 * s + 1]), pk2(kv[8 * s + 2], kv[8 * s + 3]), pk2(kv[8 * s + 4], kv[8 * s + 5]), pk2(kv[8 * s + 6], kv[8 * s + 7])};
        st128(o, rsKV, (unsigned)((16 * s + (lane & 15) + 32 * hh) * 16) + hidx * 1024u);
      }
    } else if ((lane & 15) == 0) {
#pragma unroll
      for (int g4 = 0; g4 < 4; ++g4) {
        const v4u o = v4u{f2u(kv[4 * g4]), f2u(kv[4 * g4 + 1]), f2u(kv[4 * g4 + 2]), f2u(kv[4 * g4 + 3])};
        st128(o, rsKV, (unsigned)((8 * g4 + 4 * hh) * 4) + 524288u + hidx * 128u);
      }
    }
    publish_last(L_CNT + C_KV * 4, 4, P.ws, FL_K + (unsigned)(c.gi * 16 + c.p) * 128u, tag, lane);
  }
  STAMP(3);
  unsigned tabB, tabWlo, tabWhi;
  {
    const int jj = lane < 36 ? lane : 35;
    constexpr int J2 = ORD ? 24 : 28;
    int sg;
    if (jj < 16) sg = 4 * ((jj + c.rotE) & 15) + tq;
    else if ((jj < J2) != (ORD != 0)) sg = 64 + 4 * ((jj - (ORD ? 24 : 16) + c.rotL) % 12) + tq;
    else sg = 112 + 4 * ((jj - (ORD ? 16 : 28) + c.rotO) & 7) + tq;
    int row;
    if (sg < 64) { const int g = sg >> 3; row = 224 * g + 32 * (g & 1) + 16 * (sg & 7); }
    else if (sg < 112) {
      const int e = sg - 64, g = e / 6, i = e - 6 * g;
      row = i < 4 ? 224 * g + 32 * (g & 1) + 128 + 16 * i : ((g & 1) ? 224 * g : 224 * g + 192) + 16 * (i - 4);
    } else row = 16 * (sg - 112);
    const u64 a = (u64)(size_t)(sg < 112 ? P.w2 : P.wo) + (u64)row * 1024u + (u64)c.ct * 128u;
    tabB = (unsigned)sg * 1024u; tabWlo = (unsigned)a; tabWhi = (unsigned)(a >> 32);
  }
  const unsigned vw0 = (unsigned)(lane >> 3) * 1024u + (unsigned)(((lane & 7) ^ (((lane >> 4) & 1) << 2)) * 16), vw1 = vw0 + 8192u;
  const unsigned ring = (unsigned)tq * SLOT;
  auto wdma = [&](int j) AI {
    const unsigned lo = __builtin_amdgcn_readlane(tabWlo, j), hi = __builtin_amdgcn_readlane(tabWhi, j);
    const u64 b = ((u64)hi << 32) | lo;
    const unsigned m = ring + (unsigned)(j & 15) * 2048u;
    asm volatile("s_nop 4\n\ts_mov_b32 m0, %0\n\ts_nop 0\n\tglobal_load_lds_dwordx4 %1, %3\n\ts_add_u32 m0, m0, 0x400\n\ts_nop 0\n\tglobal_load_lds_dwordx4 %2, %3"
                 ::"s"(m), "v"(vw0), "v"(vw1), "s"(b) : "memory", "scc");
  };
  const unsigned vb0 = (unsigned)lane * 16u + (unsigned)(2 * c.tt) * 144u * 1024u, vb1 = vb0 + 144u * 1024u;
  constexpr int NB = 4;
  v4u br[NB][2];
#pragma unroll
  for (int u = 0; u < NB; ++u) { br[u][0] = v4u{0u, 0u, 0u, 0u}; br[u][1] = br[u][0]; }
  auto bload = [&](unsigned so, v4u& d0, v4u& d1) AI {
    asm volatile("s_nop 4\n\tbuffer_load_dwordx4 %0, %2, %4, %5 offen sc1\n\tbuffer_load_dwordx4 %1, %3, %4, %5 offen sc1"
                 : "+v"(d0), "+v"(d1) : "v"(vb0), "v"(vb1), "s"(rsHB), "s"(so) : "memory");
  };
  STAMP(9);
#pragma unroll 1
  for (int j = 0; j < 14; ++j) wdma(j);
  STAMP(4);
  const int pan = c.tt >> 1;
  poll_flags(P.ws, FL_E + (unsigned)pan * 8u * 128u, 8, tag, lane);
  STAMP(5);
  bload(__builtin_amdgcn_readlane(tabB, 0), br[0][0], br[0][1]);
  bload(__builtin_amdgcn_readlane(tabB, 1), br[1][0], br[1][1]);
  wdma(14);
  bload(__builtin_amdgcn_readlane(tabB, 2), br[2][0], br[2][1]);
  wdma(15);
  bload(__builtin_amdgcn_readlane(tabB, 3), br[3][0], br[3][1]);
  unsigned w2l[2];
#pragma unroll
  for (int h = 0; h < 2; ++h) w2l[h] = ring + (unsigned)(4 * hh + q) * 128u + (unsigned)((4 * (h ^ sw) + 2 * cb + (pp >> 1)) * 16 + 8 * (pp & 1));
  f32x16 o[2][2];
#pragma unroll
  for (int a = 0; a < 2; ++a)
#pragma unroll
    for (int b = 0; b < 2; ++b) o[a][b] = zero16();
  v4u af[2];
  asm volatile("s_waitcnt vmcnt(10)" : "+v"(br[0][0]), "+v"(br[0][1]) :: "memory");
  af[0] = tr2(w2l[0], w2l[0] + 1024u); af[1] = tr2(w2l[1], w2l[1] + 1024u);
  auto step = [&](int j, int u, bool refill, bool first) AI {
    v4u an[2];
    if (first) asm volatile("s_waitcnt vmcnt(" STR(W1) ")" : "+v"(br[(u + 1) % NB][0]), "+v"(br[(u + 1) % NB][1]) :: "memory");
    else asm volatile("s_waitcnt vmcnt(" STR(W2) ")" : "+v"(br[(u + 1) % NB][0]), "+v"(br[(u + 1) % NB][1]) :: "memory");
    unsigned p0 = w2l[0] + (unsigned)((j + 1) & 15) * 2048u, p1 = w2l[1] + (unsigned)((j + 1) & 15) * 2048u;
    asm volatile("" : "+v"(p0), "+v"(p1));
    an[0] = tr2(p0, p0 + 1024u); an[1] = tr2(p1, p1 + 1024u);
    o[0][0] = mfma(af[0], br[u][0], o[0][0]); o[0][1] = mfma(af[0], br[u][1], o[0][1]);
    o[1][0] = mfma(af[1], br[u][0], o[1][0]); o[1][1] = mfma(af[1], br[u][1], o[1][1]);
    asm volatile("" : "+v"(o[0][0]), "+v"(o[0][1]), "+v"(o[1][0]), "+v"(o[1][1]) :: "memory");
    if (refill) wdma(j + 16);
    const int jn = j + NB;
    if (u == 0) {
      if (jn == 16) lds_wait_ge(L_CNT + (ORD ? C_ORDY : C_LATERDY) * 4, 1);
      if (jn == (ORD ? 24 : 28)) lds_wait_ge(L_CNT + (ORD ? C_LATERDY : C_ORDY) * 4, 1);
    }
    bload(jn < 36 ? (unsigned)__builtin_amdgcn_readlane(tabB, jn) : 0x7F000000u, br[u][0], br[u][1]);
    af[0] = an[0]; af[1] = an[1];
  };
#pragma unroll 1
  for (int it = 0; it < 5; ++it) {
    if (it == 3) STAMP(10);
#pragma unroll
    for (int u = 0; u < NB; ++u) step(NB * it + u, u, true, true);
  }
  STAMP(11);
#pragma unroll 1
  for (int it = 5; it < 9; ++it) {
#pragma unroll
    for (int u = 0; u < NB; ++u) step(NB * it + u, u, false, false);
  }
  asm volatile("s_waitcnt vmcnt(0)" : "+v"(br[0][0]), "+v"(br[0][1]), "+v"(br[1][0]), "+v"(br[1][1]), "+v"(br[2][0]), "+v"(br[2][1]), "+v"(br[3][0]), "+v"(br[3][1]) :: "memory");
  STAMP(6);
#pragma unroll
  for (int a = 0; a < 2; ++a)
#pragma unroll
    for (int b = 0; b < 2; ++b)
#pragma unroll
      for (int g4 = 0; g4 < 4; ++g4)
        *(LAS f32x4*)(size_t)(ring + (unsigned)(32 * b + t) * 272u + (unsigned)(32 * a + 8 * g4 + 4 * hh) * 4u) =
            f32x4{o[a][b][4 * g4], o[a][b][4 * g4 + 1], o[a][b][4 * g4 + 2], o[a][b][4 * g4 + 3]};
  VMW(0);
  BAR();
  STAMP(7);
}

template <int NT, int NF> DI void pass1(const Params& P, int wid, u64& amp_a, u64& amp_b, const v4u (&xr)[32], f32x16 (&acc)[NT], f32x4& cs, const unsigned (&wb)[NT], int lane) {
#pragma unroll
  for (int qq = 0; qq < 4; ++qq) {
    if (qq == 1) { if (NF == 8) VMW(16); else VMW(8); }
    if (qq == 2) { if (NF == 8) VMW(8); else VMW(4); }
    if (qq == 3) VMW(0);
    if (qq > 0) BAR();
    unsigned a[NT];
#pragma unroll
    for (int i = 0; i < NT; ++i) { a[i] = wb[i] + (unsigned)qq * SLOT; asm volatile("" : "+v"(a[i])); }
    v4u f[3][NT];
    auto rd = [&](int k8, v4u* W) AI {
      const unsigned o = (unsigned)((k8 >> 2) * 8192 + (k8 & 3) * 2048);
#pragma unroll
      for (int i = 0; i < NT; ++i) W[i] = tr2(a[i] + o, a[i] + o + 512u);
    };
    rd(0, f[0]); rd(1, f[1]);
    __builtin_amdgcn_sched_barrier(0);
#pragma unroll
    for (int k8 = 0; k8 < 8; ++k8) {
      if (k8 < 6) rd(k8 + 2, f[(k8 + 2) % 3]);
      const v4u xf = xr[8 * qq + k8];
#pragma unroll
      for (int i = 0; i < NT; ++i) acc[i] = mfma(f[k8 % 3][i], xf, acc[i]);
      cs = lanesum(f[k8 % 3][0], cs);
      if (k8 < 6) {
#pragma unroll
        for (int i = 0; i < NT; ++i) {
          __builtin_amdgcn_sched_group_barrier(0x008, 1, 0);
          __builtin_amdgcn_sched_group_barrier(0x100, 2, 0);
        }
        __builtin_amdgcn_sched_group_barrier(0x008, 2, 0);
      }
      __builtin_amdgcn_sched_barrier(0);
    }
    asm volatile("" : "+v"(acc[0]), "+v"(acc[1]), "+v"(acc[NT - 1]), "+v"(cs) :: "memory");
  }
}

template <bool ffn> DI u64 role_b_head(const Params& P, const Ctx& c, int wid) {
  u64 amp_a = 0, amp_b = 0; (void)amp_a; (void)amp_b;
  const int lane = c.lane, tq = c.tq, hh = c.hh, t = lane & 31;
  const int g = c.gi;
  const unsigned tok = (unsigned)(c.p * 128 + 8 * c.G + 2 * tq);
  v4u r0, r1;
  {
    const u16* rp = P.x + (size_t)tok * 512 + lane * 8;
    asm volatile("global_load_dwordx4 %0, %2, off\n\tglobal_load_dwordx4 %1, %2, off offset:1024" : "=&v"(r0), "=&v"(r1) : "v"(rp) : "memory");
  }
  const __amdgpu_buffer_rsrc_t rsX = __builtin_amdgcn_make_buffer_rsrc((void*)P.x, 0, 2048 * 1024, 0x00020000);
  const __amdgpu_buffer_rsrc_t rsW = __builtin_amdgcn_make_buffer_rsrc((void*)(ffn ? P.w1 : P.wqkv), 0, ffn ? 512 * 3584 : 512 * 3072, 0x00020000);
  constexpr unsigned RS = ffn ? 3584u : 3072u;
  const unsigned cA = (unsigned)(224 * g + 32 * (g & 1));
  const unsigned u0 = ffn ? cA * 2u : (unsigned)(512 + 64 * g) * 2u, u1 = ffn ? cA * 2u + 128u : (unsigned)(1024 + 64 * g) * 2u;
  const unsigned ua = ffn ? (cA + 128u) * 2u : (unsigned)(64 * g) * 2u;
  const unsigned ub = (unsigned)((g & 1) ? 224 * g - 32 : 224 * g + 192) * 2u;
  const unsigned voffX = (unsigned)(lane >> 3) * 1024u + (unsigned)(((lane & 7) ^ ((4 * (tq & 1) + (lane >> 4)) & 7)) * 16);
  const unsigned voffW = (unsigned)(lane >> 3) * RS + (unsigned)(((lane & 7) ^ (((lane >> 4) & 1) << 2)) * 16);
  const unsigned soffX = (unsigned)(c.p * 128 + 8 * tq) * 1024u;
  const unsigned wrow = 32u * (unsigned)(tq & 1) * RS;
  auto fill = [&](int f) AI {
    const unsigned slot = (unsigned)(f & 3) * SLOT;
    if (f < 8) {
      const unsigned kcp = (unsigned)((f + c.rot) & 7);
      unsigned sx = soffX + kcp * 128u, sw = 64u * kcp * RS + wrow + ((tq >> 1) ? u1 : u0);
      unsigned mx = slot + (unsigned)tq * 1024u, mw = slot + 16384u + (unsigned)tq * 4096u;
#pragma unroll 1
      for (int m = 0; m < 4; ++m) {
        dma(mx, voffX, rsX, sx); dma(mw, voffW, rsW, sw);
        mx += 4096u; sx += 32768u; mw += 1024u; sw += 8u * RS;
      }
    } else {
      const unsigned kcp = (unsigned)((2 * (f - 8) + (tq >> 1) + c.rot) & 7);
      unsigned sw = 64u * kcp * RS + wrow, mw = slot + (unsigned)tq * 4096u;
#pragma unroll 1
      for (int m = 0; m < 4; ++m) {
        dma(mw, voffW, rsW, sw + ua);
        if (ffn) dma(mw + 16384u, voffW, rsW, sw + ub);
        mw += 1024u; sw += 8u * RS;
      }
    }
  };
  fill(0); fill(1);
  float st0;
  {
    asm volatile("s_waitcnt vmcnt(16)" : "+v"(r0), "+v"(r1) :: "memory");
    float v[2][8], mean[2], rs[2];
#pragma unroll
    for (int k = 0; k < 4; ++k) {
      v[0][2 * k] = u2f(r0[k] << 16); v[0][2 * k + 1] = u2f(r0[k] & 0xffff0000u);
      v[1][2 * k] = u2f(r1[k] << 16); v[1][2 * k + 1] = u2f(r1[k] & 0xffff0000u);
    }
#pragma unroll
    for (int i = 0; i < 2; ++i) mean[i] = wavesum(((v[i][0] + v[i][1]) + (v[i][2] + v[i][3])) + ((v[i][4] + v[i][5]) + (v[i][6] + v[i][7]))) * (1.0f / 512.0f);
#pragma unroll
    for (int i = 0; i < 2; ++i) {
      float q2 = 0.f;
#pragma unroll
      for (int k = 0; k < 8; ++k) { const float d = v[i][k] - mean[i]; q2 = __builtin_fmaf(d, d, q2); }
      rs[i] = __builtin_amdgcn_rsqf(wavesum(q2) * (1.0f / 512.0f) + 1e-5f);
    }
    st0 = (lane & 2) ? rs[1] : rs[0];
    st0 = (lane & 1) ? -((lane & 2) ? mean[1] : mean[0]) * st0 : st0;
    asm volatile("" : "+v"(st0));
  }
  unsigned xa[4];
#pragma unroll
  for (int ks = 0; ks < 4; ++ks) xa[ks] = (unsigned)(32 * tq + t) * 128u + (unsigned)(((2 * ks + hh) ^ ((t >> 1) & 7)) * 16);
  v4u xr[32];
  u64 tag = 0;
  v4u gr;
  const unsigned token = (unsigned)(c.p * 128 + 32 * tq + t);
#pragma unroll
  for (int kc = 0; kc < 8; ++kc) {
    if (kc < 7 || ffn) VMW(8); else VMW(4);
    BAR();
    if (kc == 0) {
      tag = lds_tag();
      if (lane < 4)
        __hip_atomic_store((GAS u64*)(P.ws + WS_ST + tok * 16u + (unsigned)lane * 8u), ((u64)(unsigned)tag << 32) | f2u(st0), __ATOMIC_RELAXED, __HIP_MEMORY_SCOPE_AGENT);
    }
    if (kc == 5) {
      const char* gp = P.ws + WS_ST + token * 16u;
      asm volatile("global_load_dwordx4 %0, %1, off sc1" : "=&v"(gr) : "v"(gp) : "memory");
    }
    fill(kc + 2);
    const unsigned sb = (unsigned)(kc & 3) * SLOT;
#pragma unroll
    for (int ks = 0; ks < 4; ++ks) xr[4 * kc + ks] = lds128(xa[ks] + sb);
    asm volatile("" : "+v"(xr[4 * kc]), "+v"(xr[4 * kc + 1]), "+v"(xr[4 * kc + 2]), "+v"(xr[4 * kc + 3]));
  }
  asm volatile("" : "+v"(gr));
  STAMP(2);
  BAR();
  fill(10); fill(11);
  if (__builtin_amdgcn_ballot_w64(gr.y != (unsigned)tag || gr.w != (unsigned)tag) != 0) {
    const __amdgpu_buffer_rsrc_t rs = __builtin_amdgcn_make_buffer_rsrc((void*)(P.ws + WS_ST), 0, 32768, 0x00020000);
#pragma unroll 1
    for (unsigned spin = 0; spin < kSpin; ++spin) {
      gr = __builtin_amdgcn_raw_buffer_load_b128(rs, token * 16u, 0, 16);
      if (__builtin_amdgcn_ballot_w64(gr.y != (unsigned)tag || gr.w != (unsigned)tag) == 0) break;
      __builtin_amdgcn_s_sleep(1);
    }
  }
  const float rstd = u2f(gr.x), mr = u2f(gr.z);
  if (hh == 0) *(LAS f32x2*)(size_t)(L_STATS + (unsigned)(32 * tq + t) * 8u) = f32x2{rstd, mr};
  if (ffn) VMW(24); else VMW(12);
  BAR();
  const int cb = (lane >> 4) & 1, q = (lane >> 2) & 3, pp = lane & 3, sw = (q >> 1) & 1;
  unsigned wl[2];
#pragma unroll
  for (int h = 0; h < 2; ++h) wl[h] = (unsigned)(8 * hh + q) * 128u + (unsigned)((4 * (h ^ sw) + 2 * cb + (pp >> 1)) * 16 + 8 * (pp & 1));
  const __amdgpu_buffer_rsrc_t rsHB = __builtin_amdgcn_make_buffer_rsrc(P.ws + WS_HB, 0, HB_BYTES, 0x00020000);
  const unsigned T32 = (unsigned)(c.p * 4 + tq);
  f32x4 cs = {0.f, 0.f, 0.f, 0.f};
  auto cs_sync = [&](int tile, bool wr) AI {
    const float s = xhalf_add(cs[0]);
    if (hh == 0 && wr) *(LAS float*)(size_t)(L_CS1 + (unsigned)tile * 128u + (unsigned)t * 4u) = s;
    BAR();
  };
  if constexpr (ffn) {
    const unsigned w2a = ((g & 1) ? wl[1] : wl[0]) + 16384u;
    int ti[3];
    unsigned wb[3];
    f32x16 acc[3];
#pragma unroll
    for (int i = 0; i < 3; ++i) { ti[i] = (i + tq) % 3; wb[i] = ti[i] == 0 ? wl[0] : (ti[i] == 1 ? wl[1] : w2a); acc[i] = zero16(); }
    pass1<3, 8>(P, wid, amp_a, amp_b, xr, acc, cs, wb, lane);
    cs_sync(ti[0], tq < 3);
    STAMP(3);
    f32x4 csv[3][4], bsv[3][4];
#pragma unroll
    for (int i = 0; i < 3; ++i) { load16(L_CS1 + (unsigned)ti[i] * 128u, hh, csv[i]); load16(L_B1 + (unsigned)(4 + ti[i]) * 128u, hh, bsv[i]); }
#pragma unroll
    for (int i = 0; i < 3; ++i)
      ffn_tile(acc[i], rstd, mr, csv[i], bsv[i], rsHB, (unsigned)lane * 16u + (T32 * 144u + (unsigned)(64 + 6 * g + 2 * ti[i])) * 1024u);
    publish_last(L_CNT + C_LATE * 4, 4, P.ws, FL_L + (unsigned)(c.p * 8 + g) * 128u, tag, lane);
  } else {
    int ti[2];
    unsigned wb[2];
    f32x16 acc[2];
#pragma unroll
    for (int i = 0; i < 2; ++i) { ti[i] = (i + tq) & 1; wb[i] = ti[i] == 0 ? wl[0] : wl[1]; acc[i] = zero16(); }
    pass1<2, 4>(P, wid, amp_a, amp_b, xr, acc, cs, wb, lane);
    cs_sync(ti[0], tq < 2);
    STAMP(3);
    v4u pq[4][2];
#pragma unroll
    for (int hd = 0; hd < 4; ++hd) {
      float y[8];
      fold8(acc[hd >> 1], 8 * (hd & 1), rstd, mr, L_CS1 + (unsigned)ti[hd >> 1] * 128u, hh, y);
      hedge(mfma(wphi_frag(2 * ti[hd >> 1] + (hd & 1), lane), pack8(y), zero16()), pq[hd][0], pq[hd][1]);
    }
    STAMP(4);
    if (tq == 0) { poll_flags(P.ws, FL_K + (unsigned)g * 16u * 128u, 16, tag, lane); lds_set(L_CNT + C_KVRDY * 4, 1); }
    else lds_wait_ge(L_CNT + C_KVRDY * 4, 1);
    STAMP(5);
    {
      const __amdgpu_buffer_rsrc_t rsKV = __builtin_amdgcn_make_buffer_rsrc(P.ws + WS_KVP, 0, 524288 + 65536, 0x00020000);
      const unsigned hbase = (unsigned)(g * 4 + tq) * 16u;
      v4u pv[16];
#pragma unroll
      for (int i = 0; i < 16; ++i) pv[i] = __builtin_amdgcn_raw_buffer_load_b128(rsKV, (unsigned)lane * 16u, (hbase + (unsigned)i) * 1024u, 16);
      const v4u z0 = __builtin_amdgcn_raw_buffer_load_b128(rsKV, (unsigned)lane * 16u, 524288u + hbase * 128u, 16);
      const v4u z1 = __builtin_amdgcn_raw_buffer_load_b128(rsKV, (unsigned)lane * 16u, 524288u + hbase * 128u + 1024u, 16);
      v4u idf;
      {
        const int m = lane & 31, h2 = lane >> 5;
        const int j = (m < 16 && ((m >> 2) & 1) == h2) ? (((m >> 3) << 2) | (m & 3)) : -1;
#pragma unroll
        for (int k = 0; k < 4; ++k) idf[k] = (j == 2 * k ? 0x3F80u : 0u) | (j == 2 * k + 1 ? 0x3F800000u : 0u);
      }
      f32x16 sum = zero16();
#pragma unroll
      for (int i = 0; i < 16; ++i) sum = mfma(idf, pv[i], sum);
      const float s[8] = {sum[0], sum[1], sum[2], sum[3], sum[4], sum[5], sum[6], sum[7]};
      const unsigned afr = L_AFR + (unsigned)tq * 2048u;
      *(LAS v4u*)(size_t)(afr + (unsigned)((lane >> 4) & 1) * 1024u + (unsigned)((lane & 15) + 32 * (lane >> 5)) * 16u) = pack8(s);
      const unsigned hh2 = (unsigned)(lane >> 4) & 1u, c16 = (unsigned)lane & 15u;
      float zz[4];
#pragma unroll
      for (int k = 0; k < 4; ++k) {
        float v = u2f(z0[k]) + u2f(z1[k]);
        v += shx(v, 8, lane); v += shx(v, 16, lane); v += shx(v, 32, lane);
        zz[k] = v;
      }
      if (lane < 8) *(LAS f32x4*)(size_t)(L_ZT + (unsigned)tq * 128u + (unsigned)lane * 16u) = f32x4{zz[0], zz[1], zz[2], zz[3]};
      const unsigned f0 = 16u * (unsigned)(lane >> 5) + 4u * hh2;
      const f32x4 za = lds128f(L_ZT + (unsigned)tq * 128u + f0 * 4u), zb = lds128f(L_ZT + (unsigned)tq * 128u + (f0 + 8u) * 4u);
      *(LAS v4u*)(size_t)(afr + (unsigned)(lane >> 5) * 1024u + (16u + c16 + 32u * hh2) * 16u) = v4u{pk2(za[0], za[1]), pk2(za[2], za[3]), pk2(zb[0], zb[1]), pk2(zb[2], zb[3])};
      asm volatile("s_waitcnt lgkmcnt(0)" ::: "memory");
      lds_inc(L_CNT + C_AFR * 4, lane);
      lds_wait_ge(L_CNT + C_AFR * 4, 4);
    }
#pragma unroll
    for (int hd = 0; hd < 4; ++hd) {
      const int head = 2 * ti[hd >> 1] + (hd & 1);
      f32x16 d = mfma(lds128(L_AFR + (unsigned)head * 2048u + (unsigned)lane * 16u), pq[hd][0], zero16());
      d = mfma(lds128(L_AFR + (unsigned)head * 2048u + 1024u + (unsigned)lane * 16u), pq[hd][1], d);
      const float r = __builtin_amdgcn_rcpf(d[8] + 1e-6f);
      const v4u o = v4u{pk2(d[0] * r, d[1] * r), pk2(d[2] * r, d[3] * r), pk2(d[4] * r, d[5] * r), pk2(d[6] * r, d[7] * r)};
      st128(o, rsHB, (unsigned)lane * 16u + (T32 * 144u + (unsigned)(112 + 4 * g + head)) * 1024u);
    }
    publish_last(L_CNT + C_O * 4, 4, P.ws, FL_O + (unsigned)(c.p * 8 + g) * 128u, tag, lane);
  }
  STAMP(6);
  return tag;
}

DI void role_b(const Params& P, const Ctx& c, int wid) {
  u64 amp_a = 0, amp_b = 0; (void)amp_a; (void)amp_b;
  const int lane = c.lane, tq = c.tq;
  const u64 tag = c.ffn ? role_b_head<true>(P, c, wid) : role_b_head<false>(P, c, wid);
  const int pan = c.tt >> 1;
  const int tb = tq * 64 + lane;
  v4u xres[2], bres;
#pragma unroll
  for (int m = 0; m < 2; ++m) {
    const int idx = tb + 256 * m;
    xres[m] = *(const GAS v4u*)(P.x + (size_t)(64 * c.tt + (idx >> 3)) * 512 + 64 * c.ct + 8 * (idx & 7));
  }
  bres = *(const GAS v4u*)(P.b2 + 64 * c.ct + 8 * (tb & 7));
  if (tq == 0) {
    poll_flags(P.ws, (ORD ? FL_O : FL_L) + (unsigned)pan * 8u * 128u, 8, tag, lane);
    lds_set(L_CNT + (ORD ? C_ORDY : C_LATERDY) * 4, 1);
    poll_flags(P.ws, (ORD ? FL_L : FL_O) + (unsigned)pan * 8u * 128u, 8, tag, lane);
    lds_set(L_CNT + (ORD ? C_LATERDY : C_ORDY) * 4, 1);
  } else if (tq == 1 && __builtin_amdgcn_workgroup_id_x() == 0) {
    poll_flags(P.ws, FL_E, 64, tag, lane); poll_flags(P.ws, FL_E + 64 * 128, 64, tag, lane);
    poll_flags(P.ws, FL_K, 64, tag, lane); poll_flags(P.ws, FL_K + 64 * 128, 64, tag, lane);
    if (lane == 0) __hip_atomic_store((GAS u64*)(P.ws + WS_EPOCH), tag, __ATOMIC_RELAXED, __HIP_MEMORY_SCOPE_AGENT);
  }
  BAR();
#pragma unroll
  for (int m = 0; m < 2; ++m) {
    const int idx = tb + 256 * m;
    const unsigned a = (unsigned)(idx >> 3) * 272u + (unsigned)(idx & 7) * 32u;
    f32x4 lo = lds128f(a), hi = lds128f(a + 16u);
#pragma unroll
    for (unsigned w = 1; w < 4; ++w) { lo += lds128f(w * SLOT + a); hi += lds128f(w * SLOT + a + 16u); }
    v4u o;
#pragma unroll
    for (int k = 0; k < 4; ++k) {
      const float a0 = k < 2 ? lo[2 * k] : hi[2 * k - 4], a1 = k < 2 ? lo[2 * k + 1] : hi[2 * k - 3];
      const float x0 = u2f(xres[m][k] << 16), x1 = u2f(xres[m][k] & 0xffff0000u);
      const float b0 = u2f(bres[k] << 16), b1 = u2f(bres[k] & 0xffff0000u);
      o[k] = pk2(a0 + x0 + b0, a1 + x1 + b1);
    }
    __builtin_amdgcn_raw_buffer_store_b128(o, __builtin_amdgcn_make_buffer_rsrc((void*)P.out, 0, 2048 * 1024, 0x00020000), (unsigned)(((64 * c.tt + (idx >> 3)) * 512 + 64 * c.ct + 8 * (idx & 7)) * 2), 0, OUTAUX);
  }
  STAMP(7);
}

__global__ __launch_bounds__(512, 2) NOPK void fused_kernel(Params P) {
  __shared__ __attribute__((aligned(1024))) char smem[LDS_BYTES];
  asm volatile("" ::"v"((unsigned)(size_t)(LAS char*)smem) : "memory");
  u64 pc;
  asm volatile("s_getpc_b64 %0" : "=s"(pc));
  const int tid = __builtin_amdgcn_workitem_id_x(), lane = tid & 63;
  const int wid = __builtin_amdgcn_readfirstlane(tid >> 6);
  u64 amp_a = 0, amp_b = 0;
  STAMP(0);
  Ctx c;
  c.lane = lane; c.tq = wid & 3; c.hh = lane >> 5;
  const int blk = __builtin_amdgcn_workgroup_id_x(), xcd = blk & 7, j = blk >> 3, ja = j & 7, jb = j >> 3;
  c.p = (xcd & 1) * 8 + ja;
  c.G = (xcd >> 1) * 4 + jb;
  c.ffn = !(c.G & 1);
  c.gi = c.G >> 1;
  c.rot = ROT1 == 0 ? 0 : (ROT1 == 1 ? (((ja + jb) % 3) * 3) & 7 : (ROT1 == 2 ? (ja + 2 * jb) & 7 : ((ja + jb) & 1) * 4));
  c.tt = (xcd >> 1) * 8 + ja; c.ct = (xcd & 1) * 4 + jb;
  c.rotE = ROT2 ? (2 * ja + 4 * jb) & 15 : 0; c.rotL = ROT2 ? (ja + 3 * jb) % 12 : 0; c.rotO = ROT2 ? (ja + 2 * jb) & 7 : 0;
  if (wid < 4) {
    if (CODEPF) {
      const unsigned kb = (unsigned)(j + 32 * (wid & 1));
      if (wid < 2 && kb < CODE_KB) {
        const unsigned v = *(const GAS unsigned*)((pc & ~255ull) + kb * 1024u + (unsigned)lane * 16u);
        asm volatile("" ::"v"(v));
      }
    }
    const u64 e = __hip_atomic_load((const GAS u64*)(P.ws + WS_EPOCH), __ATOMIC_RELAXED, __HIP_MEMORY_SCOPE_AGENT);
    if (tid < 32) *(LAS unsigned*)(size_t)(L_CNT + (unsigned)tid * 4u) = 0u;
    if (c.ffn) {
      if (tid < 224) {
        const int t7 = tid >> 5, n = tid & 31, g = c.gi;
        const int cA = 224 * g + 32 * (g & 1);
        const int col = t7 < 4 ? cA + 32 * t7 : (t7 < 6 ? cA + 128 + 32 * (t7 - 4) : ((g & 1) ? 224 * g : 224 * g + 192));
        *(LAS float*)(size_t)(L_B1 + (unsigned)tid * 4u) = u2f((unsigned)P.b1[col + n] << 16);
      }
    } else if (tid < 128) {
      *(LAS v4u*)(size_t)(L_WPHI + (unsigned)tid * 16u) = *(const GAS v4u*)(P.wphi + (size_t)c.gi * 1024 + tid * 8);
    }
    const u64 tag = e + 1u;
    if (tid == 0) *(LAS v2u*)(size_t)L_TAG = v2u{(unsigned)tag, (unsigned)(tag >> 32)};
    STAMP(1);
    role_a(P, c, wid, amp_a, amp_b);
  } else {
    STAMP(1);
    role_b(P, c, wid);
  }
}

}

extern "C" void kernel_launch(void* const* d_in, const int* in_sizes, int n_in, void* d_out, int out_size, void* d_ws, size_t ws_size,
                              hipStream_t stream) {
  Params P;
  P.x = (const u16*)d_in[0]; P.wqkv = (const u16*)d_in[1]; P.wphi = (const u16*)d_in[2]; P.wo = (const u16*)d_in[3];
  P.w1 = (const u16*)d_in[5]; P.b1 = (const u16*)d_in[6]; P.w2 = (const u16*)d_in[7]; P.b2 = (const u16*)d_in[8];
  P.out = (u16*)d_out; P.ws = (char*)d_ws;
  fused_kernel<<<256, 512, 0, stream>>>(P);
}
```

```cpp
#include <hip/hip_runtime.h>
#include <stdint.h>

namespace {

typedef unsigned short u16;
typedef unsigned long long u64;
typedef unsigned v4u __attribute__((ext_vector_type(4)));
typedef unsigned v2u __attribute__((ext_vector_type(2)));
typedef float f32x16 __attribute__((ext_vector_type(16)));
typedef float f32x4 __attribute__((ext_vector_type(4)));
typedef float f32x2 __attribute__((ext_vector_type(2)));
typedef __bf16 bf16x8 __attribute__((ext_vector_type(8)));
typedef __bf16 bf16x2 __attribute__((ext_vector_type(2)));
typedef short s16x4 __attribute__((ext_vector_type(4)));
#define LAS __attribute__((address_space(3)))
#define GAS __attribute__((address_space(1)))
#if defined(__HIP_DEVICE_COMPILE__)
#define NOPK __attribute__((target("no-packed-fp32-ops")))
#else
#define NOPK
#endif
#define DI __device__ __forceinline__ NOPK
#define AI __attribute__((always_inline)) NOPK

#define STAMPS 3
#define AMPI 99
#define AMPJ 0
#define AMPX 0
#define AMPB 0
#define ROT1 1
#define ROT2 0
#define STR2(x) #x
#define STR(x) STR2(x)
#define W1 8
#define W2 4
#define ORD 0
#define CODEPF 1
#define WARM2 1
#define CODE_KB 36

constexpr unsigned WS_EPOCH = 0;
constexpr unsigned FL_E = 4096;
constexpr unsigned FL_L = FL_E + 128 * 128;
constexpr unsigned FL_K = FL_L + 128 * 128;
constexpr unsigned FL_O = FL_K + 128 * 128;
constexpr unsigned WS_ST = 73728;
constexpr unsigned WS_KVP = 131072;
constexpr unsigned WS_ZP = WS_KVP + 524288;
constexpr unsigned WS_HB = 1048576;
constexpr unsigned HB_BYTES = 64u * 144u * 1024u;
constexpr unsigned WS_STAMP = 16u << 20;

constexpr unsigned SLOT = 32768;
constexpr unsigned L_MISC = 131072;
constexpr unsigned L_STATS = L_MISC;
constexpr unsigned L_CS0 = L_MISC + 1024;
constexpr unsigned L_CS1 = L_MISC + 1536;
constexpr unsigned L_B1 = L_MISC + 2048;
constexpr unsigned L_WPHI = L_MISC + 3072;
constexpr unsigned L_CNT = L_MISC + 5120;
constexpr unsigned L_TAG = L_MISC + 5248;
constexpr unsigned L_ZT = L_MISC + 5376;
constexpr unsigned L_AFR = L_MISC + 8192;
constexpr unsigned LDS_BYTES = 163840;
enum { C_CS1 = 0, C_LATE = 1, C_KV = 2, C_O = 3, C_AFR = 4, C_KVRDY = 5, C_LATERDY = 6, C_ORDY = 7, C_FREE = 8  , C_LAND = 12  , C_EARLY = 16, C_IMG = 17 };

constexpr unsigned kSpin = 1u << 16;

struct Params {
  const u16* x; const u16* wqkv; const u16* wphi; const u16* wo;
  const u16* w1; const u16* b1; const u16* w2; const u16* b2;
  u16* out; char* ws;
};

DI unsigned f2u(float f) { return __builtin_bit_cast(unsigned, f); }
DI float u2f(unsigned u) { return __builtin_bit_cast(float, u); }
DI float fmx(float a, float b) { return __builtin_fmaxf(a, b); }
DI f32x16 mfma(v4u a, v4u b, f32x16 c) {
  return __builtin_amdgcn_mfma_f32_32x32x16_bf16(__builtin_bit_cast(bf16x8, a), __builtin_bit_cast(bf16x8, b), c, 0, 0, 0);
}
DI f32x16 zero16() { f32x16 z; for (int r = 0; r < 16; ++r) z[r] = 0.f; return z; }
DI v4u lds128(unsigned a) { return *(const LAS v4u*)(size_t)a; }
DI f32x4 lds128f(unsigned a) { return *(const LAS f32x4*)(size_t)a; }
DI v2u tr64(unsigned a) { return __builtin_bit_cast(v2u, __builtin_amdgcn_ds_read_tr16_b64_v4i16((LAS s16x4*)(size_t)a)); }
DI v4u tr2(unsigned a0, unsigned a1) { v2u l = tr64(a0), h = tr64(a1); return v4u{l.x, l.y, h.x, h.y}; }
DI unsigned pk2(float a, float b) { return __builtin_bit_cast(unsigned, __builtin_convertvector(f32x2{a, b}, bf16x2)); }
DI float xhalf_add(float v) { auto r = __builtin_amdgcn_permlane32_swap(f2u(v), f2u(v), false, false); return u2f(r[0]) + u2f(r[1]); }
DI float xhalf_max(float v) { auto r = __builtin_amdgcn_permlane32_swap(f2u(v), f2u(v), false, false); return fmx(u2f(r[0]), u2f(r[1])); }
DI float shx(float v, int m, int lane) { return u2f((unsigned)__builtin_amdgcn_ds_bpermute((lane ^ m) << 2, (int)f2u(v))); }
DI f32x4 lanesum(v4u f, f32x4 c) {
  const s16x4 one = {0x3F80, 0x3F80, 0x3F80, 0x3F80};
  c = __builtin_amdgcn_mfma_f32_4x4x4bf16_1k(one, __builtin_bit_cast(s16x4, v2u{f.x, f.y}), c, 0, 0, 0);
  return __builtin_amdgcn_mfma_f32_4x4x4bf16_1k(one, __builtin_bit_cast(s16x4, v2u{f.z, f.w}), c, 0, 0, 0);
}
template <int CTRL> DI float dppadd(float v) { return v + u2f((unsigned)__builtin_amdgcn_update_dpp(0, (int)f2u(v), CTRL, 0xf, 0xf, true)); }
DI float wavesum(float v) {
  v = dppadd<0xB1>(v); v = dppadd<0x4E>(v); v = dppadd<0x141>(v); v = dppadd<0x140>(v);
  auto r = __builtin_amdgcn_permlane16_swap(f2u(v), f2u(v), false, false);
  return xhalf_add(u2f(r[0]) + u2f(r[1]));
}
DI void dma(unsigned m0v, unsigned voff, __amdgpu_buffer_rsrc_t rs, unsigned soff) {
  asm volatile("s_mov_b32 m0, %0\n\ts_nop 0\n\tbuffer_load_dwordx4 %1, %2, %3 offen lds" ::"s"(m0v), "v"(voff), "s"(rs), "s"(soff) : "memory");
}
DI void st128(v4u v, __amdgpu_buffer_rsrc_t rs, unsigned voff) { __builtin_amdgcn_raw_buffer_store_b128(v, rs, voff, 0, 16); }
#define BAR() do { __builtin_amdgcn_sched_barrier(0); asm volatile("s_waitcnt lgkmcnt(0)\n\ts_barrier" ::: "memory"); __builtin_amdgcn_sched_barrier(0); } while (0)
#define VMW(n) asm volatile("s_waitcnt vmcnt(" #n ")" ::: "memory")
constexpr unsigned ONES = 0x3F803F80u;

#define STAMP(i) do { if ((i) == AMPI) amp_a = __builtin_amdgcn_s_memrealtime(); if ((i) == AMPJ) amp_b = __builtin_amdgcn_s_memrealtime(); } while (0)

DI void poll_flags(const char* ws, unsigned base, int n, u64 tag, int lane) {
  const GAS u64* f = (const GAS u64*)(ws + base + (unsigned)(lane < n ? lane : 0) * 128u);
#pragma unroll 1
  for (unsigned spin = 0; spin < kSpin; ++spin) {
    const u64 v = __hip_atomic_load(f, __ATOMIC_RELAXED, __HIP_MEMORY_SCOPE_AGENT);
    if (__builtin_amdgcn_ballot_w64(v != tag) == 0) break;
    __builtin_amdgcn_s_sleep(1);
  }
  __builtin_amdgcn_fence(__ATOMIC_ACQUIRE, "wavefront");
  asm volatile("" ::: "memory");
}
DI void lds_wait_ge(unsigned addr, unsigned want) {
#pragma unroll 1
  for (unsigned spin = 0; spin < (kSpin << 4); ++spin) {
    const unsigned v = *(volatile LAS unsigned*)(size_t)addr;
    if ((unsigned)__builtin_amdgcn_readfirstlane(v) >= want) break;
    __builtin_amdgcn_s_sleep(1);
  }
  asm volatile("" ::: "memory");
}
DI void lds_set(unsigned addr, unsigned v) { *(volatile LAS unsigned*)(size_t)addr = v; }
DI void lds_inc(unsigned addr, int lane) { if (lane == 0) __hip_atomic_fetch_add((LAS unsigned*)(size_t)addr, 1u, __ATOMIC_RELAXED, __HIP_MEMORY_SCOPE_WORKGROUP); }
DI void publish_last(unsigned cnt_addr, unsigned n, char* ws, unsigned flag_off, u64 tag, int lane) {
  VMW(0);
  if (lane == 0) {
    const unsigned old = __hip_atomic_fetch_add((LAS unsigned*)(size_t)cnt_addr, 1u, __ATOMIC_RELAXED, __HIP_MEMORY_SCOPE_WORKGROUP);
    if (old == n - 1) __hip_atomic_store((GAS u64*)(ws + flag_off), tag, __ATOMIC_RELAXED, __HIP_MEMORY_SCOPE_AGENT);
  }
}
DI u64 lds_tag() { const v2u t = *(const LAS v2u*)(size_t)L_TAG; return ((u64)(unsigned)__builtin_amdgcn_readfirstlane(t.y) << 32) | (unsigned)__builtin_amdgcn_readfirstlane(t.x); }

DI void fold8(const f32x16& acc, int r0, float rstd, float mr, unsigned cs, int hh, float* y) {
#pragma unroll
  for (int g = 0; g < 2; ++g) {
    const f32x4 t = lds128f(cs + (unsigned)((r0 >> 2) + g) * 32u + (unsigned)hh * 16u) * mr;
#pragma unroll
    for (int k = 0; k < 4; ++k) y[4 * g + k] = __builtin_fmaf(acc[r0 + 4 * g + k], rstd, t[k]);
  }
}
DI void load16(unsigned a, int hh, f32x4* v) {
#pragma unroll
  for (int g = 0; g < 4; ++g) v[g] = lds128f(a + (unsigned)g * 32u + (unsigned)hh * 16u);
}
DI void ffn_tile(const f32x16& acc, float rstd, float mr, const f32x4* cs, const f32x4* bs, __amdgpu_buffer_rsrc_t rsHB, unsigned voff) {
  f32x2 y[8], d[8];
#pragma unroll
  for (int g = 0; g < 4; ++g) {
    const f32x4 t = cs[g] * mr + bs[g];
    y[2 * g] = f32x2{acc[4 * g], acc[4 * g + 1]} * rstd + f32x2{t[0], t[1]};
    y[2 * g + 1] = f32x2{acc[4 * g + 2], acc[4 * g + 3]} * rstd + f32x2{t[2], t[3]};
  }
#pragma unroll
  for (int k = 0; k < 8; ++k) d[k] = y[k] * ((y[k] * y[k]) * (-0.10294324f) + (-2.30220819f));
#pragma unroll
  for (int k = 0; k < 8; ++k) { d[k].x = __builtin_amdgcn_exp2f(d[k].x); d[k].y = __builtin_amdgcn_exp2f(d[k].y); }
#pragma unroll
  for (int k = 0; k < 8; ++k) d[k] += 1.0f;
#pragma unroll
  for (int k = 0; k < 8; ++k) { d[k].x = __builtin_amdgcn_rcpf(d[k].x); d[k].y = __builtin_amdgcn_rcpf(d[k].y); }
  v4u o0, o1;
#pragma unroll
  for (int k = 0; k < 4; ++k) {
    const f32x2 g0 = y[k] * d[k], g1 = y[4 + k] * d[4 + k];
    o0[k] = pk2(g0.x, g0.y); o1[k] = pk2(g1.x, g1.y);
  }
  st128(o0, rsHB, voff); st128(o1, rsHB, voff + 1024u);
}
DI void hedge(const f32x16& d, v4u& o0, v4u& o1) {
  constexpr float L = 1.4426950408889634f;
  float m0 = fmx(fmx(fmx(d[0], d[1]), fmx(d[2], d[3])), fmx(fmx(d[4], d[5]), fmx(d[6], d[7])));
  float m1 = fmx(fmx(fmx(d[8], d[9]), fmx(d[10], d[11])), fmx(fmx(d[12], d[13]), fmx(d[14], d[15])));
  m0 = xhalf_max(m0) * -L; m1 = xhalf_max(m1) * -L;
  float e[16];
#pragma unroll
  for (int i = 0; i < 8; ++i) { e[i] = __builtin_amdgcn_exp2f(__builtin_fmaf(d[i], L, m0)); e[8 + i] = __builtin_amdgcn_exp2f(__builtin_fmaf(d[8 + i], L, m1)); }
  float s0 = ((e[0] + e[1]) + (e[2] + e[3])) + ((e[4] + e[5]) + (e[6] + e[7]));
  float s1 = ((e[8] + e[9]) + (e[10] + e[11])) + ((e[12] + e[13]) + (e[14] + e[15]));
  s0 = __builtin_amdgcn_rcpf(xhalf_add(s0)); s1 = __builtin_amdgcn_rcpf(xhalf_add(s1));
#pragma unroll
  for (int k = 0; k < 4; ++k) { o0[k] = pk2(e[2 * k] * s0, e[2 * k + 1] * s0); o1[k] = pk2(e[8 + 2 * k] * s1, e[8 + 2 * k + 1] * s1); }
}
DI v4u wphi_frag(int hd, int lane) {
  const int hh = lane >> 5, q = (lane >> 2) & 3, pp = lane & 3;
  const unsigned a = L_WPHI + (unsigned)hd * 512u + (unsigned)(4 * hh + q) * 32u + (unsigned)pp * 8u;
  v4u f = tr2(a, a + 256u);
  const unsigned sg = (lane & 16) ? 0x80008000u : 0u;
  f.x ^= sg; f.y ^= sg; f.z ^= sg; f.w ^= sg;
  return f;
}
DI v4u selv(bool s, v4u a, v4u b) { const unsigned m = s ? 0xffffffffu : 0u; return v4u{(a.x & m) | (b.x & ~m), (a.y & m) | (b.y & ~m), (a.z & m) | (b.z & ~m), (a.w & m) | (b.w & ~m)}; }
DI v4u pack8(const float* y) { return v4u{pk2(y[0], y[1]), pk2(y[2], y[3]), pk2(y[4], y[5]), pk2(y[6], y[7])}; }

struct Ctx {
  int lane, tq, hh;
  int p, gi, G;
  int rot;
  int tt, ct;
  int rotE, rotL, rotO;
  bool ffn;
};

DI void role_a(const Params& P, const Ctx& c, int wid, u64 amp_a, u64 amp_b) {
  const int lane = c.lane, tq = c.tq, hh = c.hh, t = lane & 31;
  const int cb = (lane >> 4) & 1, q = (lane >> 2) & 3, pp = lane & 3, sw = (q >> 1) & 1;
  const int th = tq & 1, up = tq >> 1;
  unsigned xa[4];
#pragma unroll
  for (int ks = 0; ks < 4; ++ks) xa[ks] = (unsigned)(64 * th + t) * 128u + (unsigned)(((2 * ks + hh) ^ ((t >> 1) & 7)) * 16);
  const unsigned wl = 16384u + (unsigned)(8 * hh + q) * 128u + (unsigned)((4 * (up ^ sw) + 2 * cb + (pp >> 1)) * 16 + 8 * (pp & 1));
  f32x16 acc[2][2];
#pragma unroll
  for (int i = 0; i < 2; ++i) { acc[i][0] = zero16(); acc[i][1] = zero16(); }
  f32x4 cs = {0.f, 0.f, 0.f, 0.f};
  unsigned x0 = xa[0], x1 = xa[1], x2 = xa[2], x3 = xa[3], w0 = wl;
  v4u xf[4][2], wf[4][2];
  auto rd = [&](int ks, v4u* X, v4u* W) AI {
    const unsigned xb = ks == 0 ? x0 : (ks == 1 ? x1 : (ks == 2 ? x2 : x3));
    X[0] = lds128(xb); X[1] = lds128(xb + 4096u);
    const unsigned a = w0 + (unsigned)(ks * 2048);
    W[0] = tr2(a, a + 512u); W[1] = tr2(a + 8192u, a + 8704u);
  };
  BAR();
  rd(0, xf[0], wf[0]); rd(1, xf[1], wf[1]);
#pragma unroll 1
  for (int kc = 0; kc < 8; ++kc) {
#pragma unroll
    for (int ks = 0; ks < 4; ++ks) {
      if (ks == 2) {
        __builtin_amdgcn_sched_barrier(0);
        asm volatile("s_barrier" ::: "memory");
        __builtin_amdgcn_sched_barrier(0);
        const unsigned sb = (unsigned)((kc + 1) & 3) * SLOT;
        x0 = xa[0] + sb; x1 = xa[1] + sb; w0 = wl + sb;
        asm volatile("" : "+v"(x0), "+v"(x1), "+v"(w0));
      }
      if (ks == 0) {
        const unsigned sb = (unsigned)(kc & 3) * SLOT;
        x2 = xa[2] + sb; x3 = xa[3] + sb;
        asm volatile("" : "+v"(x2), "+v"(x3));
      }
      rd((ks + 2) & 3, xf[(ks + 2) & 3], wf[(ks + 2) & 3]);
#pragma unroll
      for (int i = 0; i < 2; ++i) { acc[i][0] = mfma(wf[ks][i], xf[ks][0], acc[i][0]); acc[i][1] = mfma(wf[ks][i], xf[ks][1], acc[i][1]); }
      cs = lanesum(selv(th != 0, wf[ks][1], wf[ks][0]), cs);
      __builtin_amdgcn_sched_group_barrier(0x008, 1, 0);
      __builtin_amdgcn_sched_group_barrier(0x100, 2, 0);
      __builtin_amdgcn_sched_group_barrier(0x008, 1, 0);
      __builtin_amdgcn_sched_group_barrier(0x100, 2, 0);
      __builtin_amdgcn_sched_group_barrier(0x008, 1, 0);
      __builtin_amdgcn_sched_group_barrier(0x100, 2, 0);
      __builtin_amdgcn_sched_group_barrier(0x002, 4, 0);
      __builtin_amdgcn_sched_group_barrier(0x008, 3, 0);
      __builtin_amdgcn_sched_barrier(0);
    }
    asm volatile("" : "+v"(acc[0][0]), "+v"(acc[0][1]), "+v"(acc[1][0]), "+v"(acc[1][1]), "+v"(cs));
  }
  STAMP(2);
  {
    const float s = xhalf_add(cs[0]);
    if (hh == 0) *(LAS float*)(size_t)(L_CS0 + (unsigned)(up + 2 * th) * 128u + (unsigned)t * 4u) = s;
  }
  BAR();
  const u64 tag = lds_tag();
  float rstd[2], mr[2];
#pragma unroll
  for (int b = 0; b < 2; ++b) {
    const f32x2 st = *(const LAS f32x2*)(size_t)(L_STATS + (unsigned)(64 * th + 32 * b + t) * 8u);
    rstd[b] = st.x; mr[b] = st.y;
  }
  const __amdgpu_buffer_rsrc_t rsHB = __builtin_amdgcn_make_buffer_rsrc(P.ws + WS_HB, 0, HB_BYTES, 0x00020000);
  if (c.ffn) {
    f32x4 csv[2][4], bsv[2][4];
#pragma unroll
    for (int i = 0; i < 2; ++i) { load16(L_CS0 + (unsigned)(up + 2 * i) * 128u, hh, csv[i]); load16(L_B1 + (unsigned)(up + 2 * i) * 128u, hh, bsv[i]); }
#pragma unroll
    for (int i = 0; i < 2; ++i)
#pragma unroll
      for (int b = 0; b < 2; ++b) {
        const unsigned tile = (unsigned)(up + 2 * i), T32 = (unsigned)(c.p * 4 + 2 * th + b);
        ffn_tile(acc[i][b], rstd[b], mr[b], csv[i], bsv[i], rsHB, (unsigned)lane * 16u + (T32 * 144u + (unsigned)(8 * c.gi) + 2u * tile) * 1024u);
        if (2 * i + b < 3) BAR();
      }
    STAMP(8);
    publish_last(L_CNT + C_EARLY * 4, 4, P.ws, FL_E + (unsigned)(c.p * 8 + c.gi) * 128u, tag, lane);
    BAR();
  } else {
#pragma unroll
    for (int b = 0; b < 2; ++b)
#pragma unroll
      for (int hf = 0; hf < 2; ++hf) {
        const int hd = 2 * up + hf;
        float y[8];
        fold8(acc[0][b], 8 * hf, rstd[b], mr[b], L_CS0 + (unsigned)up * 128u, hh, y);
        v4u o0, o1;
        hedge(mfma(wphi_frag(hd, lane), pack8(y), zero16()), o0, o1);
        const unsigned img = (unsigned)hd * SLOT + 16384u;
        const unsigned row = (unsigned)(64 * th + 32 * b + t);
        const unsigned g = (row >> 1) & 7u;
        *(LAS v2u*)(size_t)(img + row * 64u + (((unsigned)(0 + hh)) ^ g) * 8u) = v2u{o0.x, o0.y};
        *(LAS v2u*)(size_t)(img + row * 64u + (((unsigned)(2 + hh)) ^ g) * 8u) = v2u{o0.z, o0.w};
        *(LAS v2u*)(size_t)(img + row * 64u + (((unsigned)(4 + hh)) ^ g) * 8u) = v2u{o1.x, o1.y};
        *(LAS v2u*)(size_t)(img + row * 64u + (((unsigned)(6 + hh)) ^ g) * 8u) = v2u{o1.z, o1.w};
        fold8(acc[1][b], 8 * hf, rstd[b], mr[b], L_CS0 + (unsigned)(2 + up) * 128u, hh, y);
        const unsigned gv = (row >> 2) & 3u;
        *(LAS v2u*)(size_t)(img + 8192u + row * 32u + (((unsigned)(0 + hh)) ^ gv) * 8u) = v2u{pk2(y[0], y[1]), pk2(y[2], y[3])};
        *(LAS v2u*)(size_t)(img + 8192u + row * 32u + (((unsigned)(2 + hh)) ^ gv) * 8u) = v2u{pk2(y[4], y[5]), pk2(y[6], y[7])};
        if (2 * b + hf < 3) BAR();
      }
    STAMP(8);
    BAR();
    const unsigned img = (unsigned)tq * SLOT + 16384u;
    f32x16 kv = zero16();
    unsigned pa[2], va[2];
#pragma unroll
    for (int i2 = 0; i2 < 2; ++i2) {
      const unsigned r = (unsigned)(8 * hh + 4 * i2 + q);
      pa[i2] = img + r * 64u + (((unsigned)(4 * cb + pp)) ^ ((r >> 1) & 7u)) * 8u;
      va[i2] = img + 8192u + r * 32u + (((unsigned)pp) ^ ((r >> 2) & 3u)) * 8u;
    }
#pragma unroll
    for (int ks = 0; ks < 8; ++ks) {
      const v4u af = tr2(pa[0] + (unsigned)ks * 1024u, pa[1] + (unsigned)ks * 1024u);
      v4u bf = tr2(va[0] + (unsigned)ks * 512u, va[1] + (unsigned)ks * 512u);
      if (cb) bf = v4u{ONES, ONES, ONES, ONES};
      kv = mfma(af, bf, kv);
    }
    const unsigned hidx = (unsigned)(c.gi * 4 + tq) * 16u + (unsigned)c.p;
    const __amdgpu_buffer_rsrc_t rsKV = __builtin_amdgcn_make_buffer_rsrc(P.ws + WS_KVP, 0, 524288 + 65536, 0x00020000);
    if (cb == 0) {
#pragma unroll
      for (int s = 0; s < 2; ++s) {
        const v4u o = v4u{pk2(kv[8 * s], kv[8 * s + 1]), pk2(kv[8 * s + 2], kv[8 * s + 3]), pk2(kv[8 * s + 4], kv[8 * s + 5]), pk2(kv[8 * s + 6], kv[8 * s + 7])};
        st128(o, rsKV, (unsigned)((16 * s + (lane & 15) + 32 * hh) * 16) + hidx * 1024u);
      }
    } else if ((lane & 15) == 0) {
#pragma unroll
      for (int g4 = 0; g4 < 4; ++g4) {
        const v4u o = v4u{f2u(kv[4 * g4]), f2u(kv[4 * g4 + 1]), f2u(kv[4 * g4 + 2]), f2u(kv[4 * g4 + 3])};
        st128(o, rsKV, (unsigned)((8 * g4 + 4 * hh) * 4) + 524288u + hidx * 128u);
      }
    }
    publish_last(L_CNT + C_KV * 4, 4, P.ws, FL_K + (unsigned)(c.gi * 16 + c.p) * 128u, tag, lane);
  }
  STAMP(3);
  unsigned tabB, tabWlo, tabWhi;
  {
    const int jj = lane < 36 ? lane : 35;
    constexpr int J2 = ORD ? 24 : 28;
    int sg;
    if (jj < 16) sg = 4 * ((jj + c.rotE) & 15) + tq;
    else if ((jj < J2) != (ORD != 0)) sg = 64 + 4 * ((jj - (ORD ? 24 : 16) + c.rotL) % 12) + tq;
    else sg = 112 + 4 * ((jj - (ORD ? 16 : 28) + c.rotO) & 7) + tq;
    int row;
    if (sg < 64) { const int g = sg >> 3; row = 224 * g + 32 * (g & 1) + 16 * (sg & 7); }
    else if (sg < 112) {
      const int e = sg - 64, g = e / 6, i = e - 6 * g;
      row = i < 4 ? 224 * g + 32 * (g & 1) + 128 + 16 * i : ((g & 1) ? 224 * g : 224 * g + 192) + 16 * (i - 4);
    } else row = 16 * (sg - 112);
    const u64 a = (u64)(size_t)(sg < 112 ? P.w2 : P.wo) + (u64)row * 1024u + (u64)c.ct * 128u;
    tabB = (unsigned)sg * 1024u; tabWlo = (unsigned)a; tabWhi = (unsigned)(a >> 32);
  }
  const unsigned vw0 = (unsigned)(lane >> 3) * 1024u + (unsigned)(((lane & 7) ^ (((lane >> 4) & 1) << 2)) * 16), vw1 = vw0 + 8192u;
  const unsigned ring = (unsigned)tq * SLOT;
  auto wdma = [&](int j) AI {
    const unsigned lo = __builtin_amdgcn_readlane(tabWlo, j), hi = __builtin_amdgcn_readlane(tabWhi, j);
    const u64 b = ((u64)hi << 32) | lo;
    const unsigned m = ring + (unsigned)(j & 15) * 2048u;
    asm volatile("s_nop 4\n\ts_mov_b32 m0, %0\n\ts_nop 0\n\tglobal_load_lds_dwordx4 %1, %3\n\ts_add_u32 m0, m0, 0x400\n\ts_nop 0\n\tglobal_load_lds_dwordx4 %2, %3"
                 ::"s"(m), "v"(vw0), "v"(vw1), "s"(b) : "memory", "scc");
  };
  const unsigned vb0 = (unsigned)lane * 16u + (unsigned)(2 * c.tt) * 144u * 1024u, vb1 = vb0 + 144u * 1024u;
  constexpr int NB = 4;
  v4u br[NB][2];
#pragma unroll
  for (int u = 0; u < NB; ++u) { br[u][0] = v4u{0u, 0u, 0u, 0u}; br[u][1] = br[u][0]; }
  auto bload = [&](unsigned so, v4u& d0, v4u& d1) AI {
    asm volatile("s_nop 4\n\tbuffer_load_dwordx4 %0, %2, %4, %5 offen sc1\n\tbuffer_load_dwordx4 %1, %3, %4, %5 offen sc1"
                 : "+v"(d0), "+v"(d1) : "v"(vb0), "v"(vb1), "s"(rsHB), "s"(so) : "memory");
  };
  STAMP(9);
#pragma unroll 1
  for (int j = 0; j < 14; ++j) wdma(j);
  STAMP(4);
  const int pan = c.tt >> 1;
  poll_flags(P.ws, FL_E + (unsigned)pan * 8u * 128u, 8, tag, lane);
  STAMP(5);
  bload(__builtin_amdgcn_readlane(tabB, 0), br[0][0], br[0][1]);
  bload(__builtin_amdgcn_readlane(tabB, 1), br[1][0], br[1][1]);
  wdma(14);
  bload(__builtin_amdgcn_readlane(tabB, 2), br[2][0], br[2][1]);
  wdma(15);
  bload(__builtin_amdgcn_readlane(tabB, 3), br[3][0], br[3][1]);
  unsigned w2l[2];
#pragma unroll
  for (int h = 0; h < 2; ++h) w2l[h] = ring + (unsigned)(4 * hh + q) * 128u + (unsigned)((4 * (h ^ sw) + 2 * cb + (pp >> 1)) * 16 + 8 * (pp & 1));
  f32x16 o[2][2];
#pragma unroll
  for (int a = 0; a < 2; ++a)
#pragma unroll
    for (int b = 0; b < 2; ++b) o[a][b] = zero16();
  v4u af[2];
  asm volatile("s_waitcnt vmcnt(10)" : "+v"(br[0][0]), "+v"(br[0][1]) :: "memory");
  af[0] = tr2(w2l[0], w2l[0] + 1024u); af[1] = tr2(w2l[1], w2l[1] + 1024u);
  auto step = [&](int j, int u, bool refill, bool first) AI {
    v4u an[2];
    if (first) asm volatile("s_waitcnt vmcnt(" STR(W1) ")" : "+v"(br[(u + 1) % NB][0]), "+v"(br[(u + 1) % NB][1]) :: "memory");
    else asm volatile("s_waitcnt vmcnt(" STR(W2) ")" : "+v"(br[(u + 1) % NB][0]), "+v"(br[(u + 1) % NB][1]) :: "memory");
    unsigned p0 = w2l[0] + (unsigned)((j + 1) & 15) * 2048u, p1 = w2l[1] + (unsigned)((j + 1) & 15) * 2048u;
    asm volatile("" : "+v"(p0), "+v"(p1));
    an[0] = tr2(p0, p0 + 1024u); an[1] = tr2(p1, p1 + 1024u);
    o[0][0] = mfma(af[0], br[u][0], o[0][0]); o[0][1] = mfma(af[0], br[u][1], o[0][1]);
    o[1][0] = mfma(af[1], br[u][0], o[1][0]); o[1][1] = mfma(af[1], br[u][1], o[1][1]);
    asm volatile("" : "+v"(o[0][0]), "+v"(o[0][1]), "+v"(o[1][0]), "+v"(o[1][1]) :: "memory");
    if (refill) wdma(j + 16);
    const int jn = j + NB;
    if (u == 0) {
      if (jn == 16) lds_wait_ge(L_CNT + (ORD ? C_ORDY : C_LATERDY) * 4, 1);
      if (jn == (ORD ? 24 : 28)) lds_wait_ge(L_CNT + (ORD ? C_LATERDY : C_ORDY) * 4, 1);
    }
    bload(jn < 36 ? (unsigned)__builtin_amdgcn_readlane(tabB, jn) : 0x7F000000u, br[u][0], br[u][1]);
    af[0] = an[0]; af[1] = an[1];
  };
#pragma unroll 1
  for (int it = 0; it < 5; ++it) {
    if (it == 3) STAMP(10);
#pragma unroll
    for (int u = 0; u < NB; ++u) step(NB * it + u, u, true, true);
  }
  STAMP(11);
#pragma unroll 1
  for (int it = 5; it < 9; ++it) {
#pragma unroll
    for (int u = 0; u < NB; ++u) step(NB * it + u, u, false, false);
  }
  asm volatile("s_waitcnt vmcnt(0)" : "+v"(br[0][0]), "+v"(br[0][1]), "+v"(br[1][0]), "+v"(br[1][1]), "+v"(br[2][0]), "+v"(br[2][1]), "+v"(br[3][0]), "+v"(br[3][1]) :: "memory");
  STAMP(6);
#pragma unroll
  for (int a = 0; a < 2; ++a)
#pragma unroll
    for (int b = 0; b < 2; ++b)
#pragma unroll
      for (int g4 = 0; g4 < 4; ++g4)
        *(LAS f32x4*)(size_t)(ring + (unsigned)(32 * b + t) * 272u + (unsigned)(32 * a + 8 * g4 + 4 * hh) * 4u) =
            f32x4{o[a][b][4 * g4], o[a][b][4 * g4 + 1], o[a][b][4 * g4 + 2], o[a][b][4 * g4 + 3]};
  VMW(0);
  BAR();
  STAMP(7);
  if (wid == 0 && __builtin_amdgcn_workgroup_id_x() == AMPB) {
    const u64 d = (amp_b - amp_a) * AMPX, t1 = __builtin_amdgcn_s_memrealtime();
    while (__builtin_amdgcn_s_memrealtime() - t1 < d) __builtin_amdgcn_s_sleep(4);
  }
}

template <int NT, int NF> DI void pass1(const Params& P, int wid, u64& amp_a, u64& amp_b, const v4u (&xr)[32], f32x16 (&acc)[NT], f32x4& cs, const unsigned (&wb)[NT], int lane) {
#pragma unroll
  for (int qq = 0; qq < 4; ++qq) {
    if (qq == 1) { if (NF == 8) VMW(16); else VMW(8); }
    if (qq == 2) { if (NF == 8) VMW(8); else VMW(4); }
    if (qq == 3) VMW(0);
    if (qq > 0) BAR();
    unsigned a[NT];
#pragma unroll
    for (int i = 0; i < NT; ++i) { a[i] = wb[i] + (unsigned)qq * SLOT; asm volatile("" : "+v"(a[i])); }
    v4u f[3][NT];
    auto rd = [&](int k8, v4u* W) AI {
      const unsigned o = (unsigned)((k8 >> 2) * 8192 + (k8 & 3) * 2048);
#pragma unroll
      for (int i = 0; i < NT; ++i) W[i] = tr2(a[i] + o, a[i] + o + 512u);
    };
    rd(0, f[0]); rd(1, f[1]);
    __builtin_amdgcn_sched_barrier(0);
#pragma unroll
    for (int k8 = 0; k8 < 8; ++k8) {
      if (k8 < 6) rd(k8 + 2, f[(k8 + 2) % 3]);
      const v4u xf = xr[8 * qq + k8];
#pragma unroll
      for (int i = 0; i < NT; ++i) acc[i] = mfma(f[k8 % 3][i], xf, acc[i]);
      cs = lanesum(f[k8 % 3][0], cs);
      if (k8 < 6) {
#pragma unroll
        for (int i = 0; i < NT; ++i) {
          __builtin_amdgcn_sched_group_barrier(0x008, 1, 0);
          __builtin_amdgcn_sched_group_barrier(0x100, 2, 0);
        }
        __builtin_amdgcn_sched_group_barrier(0x008, 2, 0);
      }
      __builtin_amdgcn_sched_barrier(0);
    }
    asm volatile("" : "+v"(acc[0]), "+v"(acc[1]), "+v"(acc[NT - 1]), "+v"(cs) :: "memory");
  }
}

template <bool ffn> DI u64 role_b_head(const Params& P, const Ctx& c, int wid) {
  u64 amp_a = 0, amp_b = 0; (void)amp_a; (void)amp_b;
  const int lane = c.lane, tq = c.tq, hh = c.hh, t = lane & 31;
  const int g = c.gi;
  const unsigned tok = (unsigned)(c.p * 128 + 8 * c.G + 2 * tq);
  v4u r0, r1;
  {
    const u16* rp = P.x + (size_t)tok * 512 + lane * 8;
    asm volatile("global_load_dwordx4 %0, %2, off\n\tglobal_load_dwordx4 %1, %2, off offset:1024" : "=&v"(r0), "=&v"(r1) : "v"(rp) : "memory");
  }
  const __amdgpu_buffer_rsrc_t rsX = __builtin_amdgcn_make_buffer_rsrc((void*)P.x, 0, 2048 * 1024, 0x00020000);
  const __amdgpu_buffer_rsrc_t rsW = __builtin_amdgcn_make_buffer_rsrc((void*)(ffn ? P.w1 : P.wqkv), 0, ffn ? 512 * 3584 : 512 * 3072, 0x00020000);
  constexpr unsigned RS = ffn ? 3584u : 3072u;
  const unsigned cA = (unsigned)(224 * g + 32 * (g & 1));
  const unsigned u0 = ffn ? cA * 2u : (unsigned)(512 + 64 * g) * 2u, u1 = ffn ? cA * 2u + 128u : (unsigned)(1024 + 64 * g) * 2u;
  const unsigned ua = ffn ? (cA + 128u) * 2u : (unsigned)(64 * g) * 2u;
  const unsigned ub = (unsigned)((g & 1) ? 224 * g - 32 : 224 * g + 192) * 2u;
  const unsigned voffX = (unsigned)(lane >> 3) * 1024u + (unsigned)(((lane & 7) ^ ((4 * (tq & 1) + (lane >> 4)) & 7)) * 16);
  const unsigned voffW = (unsigned)(lane >> 3) * RS + (unsigned)(((lane & 7) ^ (((lane >> 4) & 1) << 2)) * 16);
  const unsigned soffX = (unsigned)(c.p * 128 + 8 * tq) * 1024u;
  const unsigned wrow = 32u * (unsigned)(tq & 1) * RS;
  auto fill = [&](int f) AI {
    const unsigned slot = (unsigned)(f & 3) * SLOT;
    if (f < 8) {
      const unsigned kcp = (unsigned)((f + c.rot) & 7);
      unsigned sx = soffX + kcp * 128u, sw = 64u * kcp * RS + wrow + ((tq >> 1) ? u1 : u0);
      unsigned mx = slot + (unsigned)tq * 1024u, mw = slot + 16384u + (unsigned)tq * 4096u;
#pragma unroll 1
      for (int m = 0; m < 4; ++m) {
        dma(mx, voffX, rsX, sx); dma(mw, voffW, rsW, sw);
        mx += 4096u; sx += 32768u; mw += 1024u; sw += 8u * RS;
      }
    } else {
      const unsigned kcp = (unsigned)((2 * (f - 8) + (tq >> 1) + c.rot) & 7);
      unsigned sw = 64u * kcp * RS + wrow, mw = slot + (unsigned)tq * 4096u;
#pragma unroll 1
      for (int m = 0; m < 4; ++m) {
        dma(mw, voffW, rsW, sw + ua);
        if (ffn) dma(mw + 16384u, voffW, rsW, sw + ub);
        mw += 1024u; sw += 8u * RS;
      }
    }
  };
  fill(0); fill(1);
  float st0;
  {
    asm volatile("s_waitcnt vmcnt(16)" : "+v"(r0), "+v"(r1) :: "memory");
    float v[2][8], mean[2], rs[2];
#pragma unroll
    for (int k = 0; k < 4; ++k) {
      v[0][2 * k] = u2f(r0[k] << 16); v[0][2 * k + 1] = u2f(r0[k] & 0xffff0000u);
      v[1][2 * k] = u2f(r1[k] << 16); v[1][2 * k + 1] = u2f(r1[k] & 0xffff0000u);
    }
#pragma unroll
    for (int i = 0; i < 2; ++i) mean[i] = wavesum(((v[i][0] + v[i][1]) + (v[i][2] + v[i][3])) + ((v[i][4] + v[i][5]) + (v[i][6] + v[i][7]))) * (1.0f / 512.0f);
#pragma unroll
    for (int i = 0; i < 2; ++i) {
      float q2 = 0.f;
#pragma unroll
      for (int k = 0; k < 8; ++k) { const float d = v[i][k] - mean[i]; q2 = __builtin_fmaf(d, d, q2); }
      rs[i] = __builtin_amdgcn_rsqf(wavesum(q2) * (1.0f / 512.0f) + 1e-5f);
    }
    st0 = (lane & 2) ? rs[1] : rs[0];
    st0 = (lane & 1) ? -((lane & 2) ? mean[1] : mean[0]) * st0 : st0;
    asm volatile("" : "+v"(st0));
  }
  unsigned xa[4];
#pragma unroll
  for (int ks = 0; ks < 4; ++ks) xa[ks] = (unsigned)(32 * tq + t) * 128u + (unsigned)(((2 * ks + hh) ^ ((t >> 1) & 7)) * 16);
  v4u xr[32];
  u64 tag = 0;
  v4u gr;
  const unsigned token = (unsigned)(c.p * 128 + 32 * tq + t);
#pragma unroll
  for (int kc = 0; kc < 8; ++kc) {
    if (kc < 7 || ffn) VMW(8); else VMW(4);
    BAR();
    if (kc == 0) {
      tag = lds_tag();
      if (lane < 4)
        __hip_atomic_store((GAS u64*)(P.ws + WS_ST + tok * 16u + (unsigned)lane * 8u), ((u64)(unsigned)tag << 32) | f2u(st0), __ATOMIC_RELAXED, __HIP_MEMORY_SCOPE_AGENT);
    }
    if (kc == 5) {
      const char* gp = P.ws + WS_ST + token * 16u;
      asm volatile("global_load_dwordx4 %0, %1, off sc1" : "=&v"(gr) : "v"(gp) : "memory");
    }
    fill(kc + 2);
    const unsigned sb = (unsigned)(kc & 3) * SLOT;
#pragma unroll
    for (int ks = 0; ks < 4; ++ks) xr[4 * kc + ks] = lds128(xa[ks] + sb);
    asm volatile("" : "+v"(xr[4 * kc]), "+v"(xr[4 * kc + 1]), "+v"(xr[4 * kc + 2]), "+v"(xr[4 * kc + 3]));
  }
  asm volatile("" : "+v"(gr));
  STAMP(2);
  BAR();
  fill(10); fill(11);
  if (__builtin_amdgcn_ballot_w64(gr.y != (unsigned)tag || gr.w != (unsigned)tag) != 0) {
    const __amdgpu_buffer_rsrc_t rs = __builtin_amdgcn_make_buffer_rsrc((void*)(P.ws + WS_ST), 0, 32768, 0x00020000);
#pragma unroll 1
    for (unsigned spin = 0; spin < kSpin; ++spin) {
      gr = __builtin_amdgcn_raw_buffer_load_b128(rs, token * 16u, 0, 16);
      if (__builtin_amdgcn_ballot_w64(gr.y != (unsigned)tag || gr.w != (unsigned)tag) == 0) break;
      __builtin_amdgcn_s_sleep(1);
    }
  }
  const float rstd = u2f(gr.x), mr = u2f(gr.z);
  if (hh == 0) *(LAS f32x2*)(size_t)(L_STATS + (unsigned)(32 * tq + t) * 8u) = f32x2{rstd, mr};
  if (ffn) VMW(24); else VMW(12);
  BAR();
  const int cb = (lane >> 4) & 1, q = (lane >> 2) & 3, pp = lane & 3, sw = (q >> 1) & 1;
  unsigned wl[2];
#pragma unroll
  for (int h = 0; h < 2; ++h) wl[h] = (unsigned)(8 * hh + q) * 128u + (unsigned)((4 * (h ^ sw) + 2 * cb + (pp >> 1)) * 16 + 8 * (pp & 1));
  const __amdgpu_buffer_rsrc_t rsHB = __builtin_amdgcn_make_buffer_rsrc(P.ws + WS_HB, 0, HB_BYTES, 0x00020000);
  const unsigned T32 = (unsigned)(c.p * 4 + tq);
  f32x4 cs = {0.f, 0.f, 0.f, 0.f};
  auto cs_sync = [&](int tile, bool wr) AI {
    const float s = xhalf_add(cs[0]);
    if (hh == 0 && wr) *(LAS float*)(size_t)(L_CS1 + (unsigned)tile * 128u + (unsigned)t * 4u) = s;
    BAR();
  };
  if constexpr (ffn) {
    const unsigned w2a = ((g & 1) ? wl[1] : wl[0]) + 16384u;
    int ti[3];
    unsigned wb[3];
    f32x16 acc[3];
#pragma unroll
    for (int i = 0; i < 3; ++i) { ti[i] = (i + tq) % 3; wb[i] = ti[i] == 0 ? wl[0] : (ti[i] == 1 ? wl[1] : w2a); acc[i] = zero16(); }
    pass1<3, 8>(P, wid, amp_a, amp_b, xr, acc, cs, wb, lane);
    cs_sync(ti[0], tq < 3);
    STAMP(3);
    f32x4 csv[3][4], bsv[3][4];
#pragma unroll
    for (int i = 0; i < 3; ++i) { load16(L_CS1 + (unsigned)ti[i] * 128u, hh, csv[i]); load16(L_B1 + (unsigned)(4 + ti[i]) * 128u, hh, bsv[i]); }
#pragma unroll
    for (int i = 0; i < 3; ++i)
      ffn_tile(acc[i], rstd, mr, csv[i], bsv[i], rsHB, (unsigned)lane * 16u + (T32 * 144u + (unsigned)(64 + 6 * g + 2 * ti[i])) * 1024u);
    publish_last(L_CNT + C_LATE * 4, 4, P.ws, FL_L + (unsigned)(c.p * 8 + g) * 128u, tag, lane);
  } else {
    int ti[2];
    unsigned wb[2];
    f32x16 acc[2];
#pragma unroll
    for (int i = 0; i < 2; ++i) { ti[i] = (i + tq) & 1; wb[i] = ti[i] == 0 ? wl[0] : wl[1]; acc[i] = zero16(); }
    pass1<2, 4>(P, wid, amp_a, amp_b, xr, acc, cs, wb, lane);
    cs_sync(ti[0], tq < 2);
    STAMP(3);
    v4u pq[4][2];
#pragma unroll
    for (int hd = 0; hd < 4; ++hd) {
      float y[8];
      fold8(acc[hd >> 1], 8 * (hd & 1), rstd, mr, L_CS1 + (unsigned)ti[hd >> 1] * 128u, hh, y);
      hedge(mfma(wphi_frag(2 * ti[hd >> 1] + (hd & 1), lane), pack8(y), zero16()), pq[hd][0], pq[hd][1]);
    }
    STAMP(4);
    if (tq == 0) { poll_flags(P.ws, FL_K + (unsigned)g * 16u * 128u, 16, tag, lane); lds_set(L_CNT + C_KVRDY * 4, 1); }
    else lds_wait_ge(L_CNT + C_KVRDY * 4, 1);
    STAMP(5);
    {
      const __amdgpu_buffer_rsrc_t rsKV = __builtin_amdgcn_make_buffer_rsrc(P.ws + WS_KVP, 0, 524288 + 65536, 0x00020000);
      const unsigned hbase = (unsigned)(g * 4 + tq) * 16u;
      v4u pv[16];
#pragma unroll
      for (int i = 0; i < 16; ++i) pv[i] = __builtin_amdgcn_raw_buffer_load_b128(rsKV, (unsigned)lane * 16u, (hbase + (unsigned)i) * 1024u, 16);
      const v4u z0 = __builtin_amdgcn_raw_buffer_load_b128(rsKV, (unsigned)lane * 16u, 524288u + hbase * 128u, 16);
      const v4u z1 = __builtin_amdgcn_raw_buffer_load_b128(rsKV, (unsigned)lane * 16u, 524288u + hbase * 128u + 1024u, 16);
      v4u idf;
      {
        const int m = lane & 31, h2 = lane >> 5;
        const int j = (m < 16 && ((m >> 2) & 1) == h2) ? (((m >> 3) << 2) | (m & 3)) : -1;
#pragma unroll
        for (int k = 0; k < 4; ++k) idf[k] = (j == 2 * k ? 0x3F80u : 0u) | (j == 2 * k + 1 ? 0x3F800000u : 0u);
      }
      f32x16 sum = zero16();
#pragma unroll
      for (int i = 0; i < 16; ++i) sum = mfma(idf, pv[i], sum);
      const float s[8] = {sum[0], sum[1], sum[2], sum[3], sum[4], sum[5], sum[6], sum[7]};
      const unsigned afr = L_AFR + (unsigned)tq * 2048u;
      *(LAS v4u*)(size_t)(afr + (unsigned)((lane >> 4) & 1) * 1024u + (unsigned)((lane & 15) + 32 * (lane >> 5)) * 16u) = pack8(s);
      const unsigned hh2 = (unsigned)(lane >> 4) & 1u, c16 = (unsigned)lane & 15u;
      float zz[4];
#pragma unroll
      for (int k = 0; k < 4; ++k) {
        float v = u2f(z0[k]) + u2f(z1[k]);
        v += shx(v, 8, lane); v += shx(v, 16, lane); v += shx(v, 32, lane);
        zz[k] = v;
      }
      if (lane < 8) *(LAS f32x4*)(size_t)(L_ZT + (unsigned)tq * 128u + (unsigned)lane * 16u) = f32x4{zz[0], zz[1], zz[2], zz[3]};
      const unsigned f0 = 16u * (unsigned)(lane >> 5) + 4u * hh2;
      const f32x4 za = lds128f(L_ZT + (unsigned)tq * 128u + f0 * 4u), zb = lds128f(L_ZT + (unsigned)tq * 128u + (f0 + 8u) * 4u);
      *(LAS v4u*)(size_t)(afr + (unsigned)(lane >> 5) * 1024u + (16u + c16 + 32u * hh2) * 16u) = v4u{pk2(za[0], za[1]), pk2(za[2], za[3]), pk2(zb[0], zb[1]), pk2(zb[2], zb[3])};
      asm volatile("s_waitcnt lgkmcnt(0)" ::: "memory");
      lds_inc(L_CNT + C_AFR * 4, lane);
      lds_wait_ge(L_CNT + C_AFR * 4, 4);
    }
#pragma unroll
    for (int hd = 0; hd < 4; ++hd) {
      const int head = 2 * ti[hd >> 1] + (hd & 1);
      f32x16 d = mfma(lds128(L_AFR + (unsigned)head * 2048u + (unsigned)lane * 16u), pq[hd][0], zero16());
      d = mfma(lds128(L_AFR + (unsigned)head * 2048u + 1024u + (unsigned)lane * 16u), pq[hd][1], d);
      const float r = __builtin_amdgcn_rcpf(d[8] + 1e-6f);
      const v4u o = v4u{pk2(d[0] * r, d[1] * r), pk2(d[2] * r, d[3] * r), pk2(d[4] * r, d[5] * r), pk2(d[6] * r, d[7] * r)};
      st128(o, rsHB, (unsigned)lane * 16u + (T32 * 144u + (unsigned)(112 + 4 * g + head)) * 1024u);
    }
    publish_last(L_CNT + C_O * 4, 4, P.ws, FL_O + (unsigned)(c.p * 8 + g) * 128u, tag, lane);
  }
  STAMP(6);
  return tag;
}

DI void role_b(const Params& P, const Ctx& c, int wid) {
  u64 amp_a = 0, amp_b = 0; (void)amp_a; (void)amp_b;
  const int lane = c.lane, tq = c.tq;
  const u64 tag = c.ffn ? role_b_head<true>(P, c, wid) : role_b_head<false>(P, c, wid);
  const int pan = c.tt >> 1;
  const int tb = tq * 64 + lane;
  v4u xres[2], bres;
#pragma unroll
  for (int m = 0; m < 2; ++m) {
    const int idx = tb + 256 * m;
    xres[m] = *(const GAS v4u*)(P.x + (size_t)(64 * c.tt + (idx >> 3)) * 512 + 64 * c.ct + 8 * (idx & 7));
  }
  bres = *(const GAS v4u*)(P.b2 + 64 * c.ct + 8 * (tb & 7));
  if (tq == 0) {
    poll_flags(P.ws, (ORD ? FL_O : FL_L) + (unsigned)pan * 8u * 128u, 8, tag, lane);
    lds_set(L_CNT + (ORD ? C_ORDY : C_LATERDY) * 4, 1);
    poll_flags(P.ws, (ORD ? FL_L : FL_O) + (unsigned)pan * 8u * 128u, 8, tag, lane);
    lds_set(L_CNT + (ORD ? C_LATERDY : C_ORDY) * 4, 1);
  } else if (tq == 1 && __builtin_amdgcn_workgroup_id_x() == 0) {
    poll_flags(P.ws, FL_E, 64, tag, lane); poll_flags(P.ws, FL_E + 64 * 128, 64, tag, lane);
    poll_flags(P.ws, FL_K, 64, tag, lane); poll_flags(P.ws, FL_K + 64 * 128, 64, tag, lane);
    if (lane == 0) __hip_atomic_store((GAS u64*)(P.ws + WS_EPOCH), tag, __ATOMIC_RELAXED, __HIP_MEMORY_SCOPE_AGENT);
  }
  BAR();
#pragma unroll
  for (int m = 0; m < 2; ++m) {
    const int idx = tb + 256 * m;
    const unsigned a = (unsigned)(idx >> 3) * 272u + (unsigned)(idx & 7) * 32u;
    f32x4 lo = lds128f(a), hi = lds128f(a + 16u);
#pragma unroll
    for (unsigned w = 1; w < 4; ++w) { lo += lds128f(w * SLOT + a); hi += lds128f(w * SLOT + a + 16u); }
    v4u o;
#pragma unroll
    for (int k = 0; k < 4; ++k) {
      const float a0 = k < 2 ? lo[2 * k] : hi[2 * k - 4], a1 = k < 2 ? lo[2 * k + 1] : hi[2 * k - 3];
      const float x0 = u2f(xres[m][k] << 16), x1 = u2f(xres[m][k] & 0xffff0000u);
      const float b0 = u2f(bres[k] << 16), b1 = u2f(bres[k] & 0xffff0000u);
      o[k] = pk2(a0 + x0 + b0, a1 + x1 + b1);
    }
    *(GAS v4u*)(P.out + (size_t)(64 * c.tt + (idx >> 3)) * 512 + 64 * c.ct + 8 * (idx & 7)) = o;
  }
  STAMP(7);
}

__global__ __launch_bounds__(512, 2) NOPK void fused_kernel(Params P) {
  __shared__ __attribute__((aligned(1024))) char smem[LDS_BYTES];
  asm volatile("" ::"v"((unsigned)(size_t)(LAS char*)smem) : "memory");
  u64 pc;
  asm volatile("s_getpc_b64 %0" : "=s"(pc));
  const int tid = __builtin_amdgcn_workitem_id_x(), lane = tid & 63;
  const int wid = __builtin_amdgcn_readfirstlane(tid >> 6);
  u64 amp_a = 0, amp_b = 0;
  STAMP(0);
  amp_a = *(const volatile GAS u64*)(P.ws + (20u << 20));
  amp_a = ((u64)(unsigned)__builtin_amdgcn_readfirstlane((unsigned)(amp_a >> 32)) << 32) | (unsigned)__builtin_amdgcn_readfirstlane((unsigned)amp_a);
  Ctx c;
  c.lane = lane; c.tq = wid & 3; c.hh = lane >> 5;
  const int blk = __builtin_amdgcn_workgroup_id_x(), xcd = blk & 7, j = blk >> 3, ja = j & 7, jb = j >> 3;
  c.p = (xcd & 1) * 8 + ja;
  c.G = (xcd >> 1) * 4 + jb;
  c.ffn = !(c.G & 1);
  c.gi = c.G >> 1;
  c.rot = ROT1 == 0 ? 0 : (ROT1 == 1 ? (((ja + jb) % 3) * 3) & 7 : (ROT1 == 2 ? (ja + 2 * jb) & 7 : ((ja + jb) & 1) * 4));
  c.tt = (xcd >> 1) * 8 + ja; c.ct = (xcd & 1) * 4 + jb;
  c.rotE = ROT2 ? (2 * ja + 4 * jb) & 15 : 0; c.rotL = ROT2 ? (ja + 3 * jb) % 12 : 0; c.rotO = ROT2 ? (ja + 2 * jb) & 7 : 0;
  if (wid < 4) {
    if (CODEPF) {
      const unsigned kb = (unsigned)(j + 32 * (wid & 1));
      if (wid < 2 && kb < CODE_KB) {
        const unsigned v = *(const GAS unsigned*)((pc & ~255ull) + kb * 1024u + (unsigned)lane * 16u);
        asm volatile("" ::"v"(v));
      }
    }
    const u64 e = __hip_atomic_load((const GAS u64*)(P.ws + WS_EPOCH), __ATOMIC_RELAXED, __HIP_MEMORY_SCOPE_AGENT);
    if (tid < 32) *(LAS unsigned*)(size_t)(L_CNT + (unsigned)tid * 4u) = 0u;
    if (c.ffn) {
      if (tid < 224) {
        const int t7 = tid >> 5, n = tid & 31, g = c.gi;
        const int cA = 224 * g + 32 * (g & 1);
        const int col = t7 < 4 ? cA + 32 * t7 : (t7 < 6 ? cA + 128 + 32 * (t7 - 4) : ((g & 1) ? 224 * g : 224 * g + 192));
        *(LAS float*)(size_t)(L_B1 + (unsigned)tid * 4u) = u2f((unsigned)P.b1[col + n] << 16);
      }
    } else if (tid < 128) {
      *(LAS v4u*)(size_t)(L_WPHI + (unsigned)tid * 16u) = *(const GAS v4u*)(P.wphi + (size_t)c.gi * 1024 + tid * 8);
    }
    const u64 tag = e + 1u;
    if (tid == 0) *(LAS v2u*)(size_t)L_TAG = v2u{(unsigned)tag, (unsigned)(tag >> 32)};
    STAMP(1);
    role_a(P, c, wid, amp_a, amp_b);
  } else {
    STAMP(1);
    role_b(P, c, wid);
  }
}

}

__global__ void k_t0(char* ws) { if (threadIdx.x == 0) *(unsigned long long*)(ws + (20u << 20)) = __builtin_amdgcn_s_memrealtime(); }
extern "C" void kernel_launch(void* const* d_in, const int* in_sizes, int n_in, void* d_out, int out_size, void* d_ws, size_t ws_size,
                              hipStream_t stream) {
  Params P;
  P.x = (const u16*)d_in[0]; P.wqkv = (const u16*)d_in[1]; P.wphi = (const u16*)d_in[2]; P.wo = (const u16*)d_in[3];
  P.w1 = (const u16*)d_in[5]; P.b1 = (const u16*)d_in[6]; P.w2 = (const u16*)d_in[7]; P.b2 = (const u16*)d_in[8];
  P.out = (u16*)d_out; P.ws = (char*)d_ws;
  k_t0<<<1, 64, 0, stream>>>((char*)d_ws);
  fused_kernel<<<256, 512, 0, stream>>>(P);
}
```

```cpp
#include <hip/hip_runtime.h>
#include <stdint.h>

namespace {

typedef unsigned short u16;
typedef unsigned long long u64;
typedef unsigned v4u __attribute__((ext_vector_type(4)));
typedef unsigned v2u __attribute__((ext_vector_type(2)));
typedef float f32x16 __attribute__((ext_vector_type(16)));
typedef float f32x4 __attribute__((ext_vector_type(4)));
typedef float f32x2 __attribute__((ext_vector_type(2)));
typedef __bf16 bf16x8 __attribute__((ext_vector_type(8)));
typedef __bf16 bf16x2 __attribute__((ext_vector_type(2)));
typedef short s16x4 __attribute__((ext_vector_type(4)));
#define LAS __attribute__((address_space(3)))
#define GAS __attribute__((address_space(1)))
#if defined(__HIP_DEVICE_COMPILE__)
#define NOPK __attribute__((target("no-packed-fp32-ops")))
#else
#define NOPK
#endif
#define DI __device__ __forceinline__ NOPK
#define AI __attribute__((always_inline)) NOPK

#define STAMPS 0
#define ROT1 1
#define ROT2 0
#define STR2(x) #x
#define STR(x) STR2(x)
#define W1 8
#define W2 4
#define ORD 0
#define CODEPF 1
#define WARM2 1
#define CODE_KB 36

constexpr unsigned WS_EPOCH = 0;
constexpr unsigned FL_E = 4096;
constexpr unsigned FL_L = FL_E + 128 * 128;
constexpr unsigned FL_K = FL_L + 128 * 128;
constexpr unsigned FL_O = FL_K + 128 * 128;
constexpr unsigned WS_ST = 73728;
constexpr unsigned WS_KVP = 131072;
constexpr unsigned WS_ZP = WS_KVP + 524288;
constexpr unsigned WS_HB = 1048576;
constexpr unsigned HB_BYTES = 64u * 144u * 1024u;
constexpr unsigned WS_STAMP = 16u << 20;

constexpr unsigned SLOT = 32768;
constexpr unsigned L_MISC = 131072;
constexpr unsigned L_STATS = L_MISC;
constexpr unsigned L_CS0 = L_MISC + 1024;
constexpr unsigned L_CS1 = L_MISC + 1536;
constexpr unsigned L_B1 = L_MISC + 2048;
constexpr unsigned L_WPHI = L_MISC + 3072;
constexpr unsigned L_CNT = L_MISC + 5120;
constexpr unsigned L_TAG = L_MISC + 5248;
constexpr unsigned L_ZT = L_MISC + 5376;
constexpr unsigned L_AFR = L_MISC + 8192;
constexpr unsigned LDS_BYTES = 163840;
enum { C_CS1 = 0, C_LATE = 1, C_KV = 2, C_O = 3, C_AFR = 4, C_KVRDY = 5, C_LATERDY = 6, C_ORDY = 7, C_FREE = 8  , C_LAND = 12  , C_EARLY = 16, C_IMG = 17 };

constexpr unsigned kSpin = 1u << 16;

struct Params {
  const u16* x; const u16* wqkv; const u16* wphi; const u16* wo;
  const u16* w1; const u16* b1; const u16* w2; const u16* b2;
  u16* out; char* ws;
};

DI unsigned f2u(float f) { return __builtin_bit_cast(unsigned, f); }
DI float u2f(unsigned u) { return __builtin_bit_cast(float, u); }
DI float fmx(float a, float b) { return __builtin_fmaxf(a, b); }
DI f32x16 mfma(v4u a, v4u b, f32x16 c) {
  return __builtin_amdgcn_mfma_f32_32x32x16_bf16(__builtin_bit_cast(bf16x8, a), __builtin_bit_cast(bf16x8, b), c, 0, 0, 0);
}
DI f32x16 zero16() { f32x16 z; for (int r = 0; r < 16; ++r) z[r] = 0.f; return z; }
DI v4u lds128(unsigned a) { return *(const LAS v4u*)(size_t)a; }
DI f32x4 lds128f(unsigned a) { return *(const LAS f32x4*)(size_t)a; }
DI v2u tr64(unsigned a) { return __builtin_bit_cast(v2u, __builtin_amdgcn_ds_read_tr16_b64_v4i16((LAS s16x4*)(size_t)a)); }
DI v4u tr2(unsigned a0, unsigned a1) { v2u l = tr64(a0), h = tr64(a1); return v4u{l.x, l.y, h.x, h.y}; }
DI unsigned pk2(float a, float b) { return __builtin_bit_cast(unsigned, __builtin_convertvector(f32x2{a, b}, bf16x2)); }
DI float xhalf_add(float v) { auto r = __builtin_amdgcn_permlane32_swap(f2u(v), f2u(v), false, false); return u2f(r[0]) + u2f(r[1]); }
DI float xhalf_max(float v) { auto r = __builtin_amdgcn_permlane32_swap(f2u(v), f2u(v), false, false); return fmx(u2f(r[0]), u2f(r[1])); }
DI float shx(float v, int m, int lane) { return u2f((unsigned)__builtin_amdgcn_ds_bpermute((lane ^ m) << 2, (int)f2u(v))); }
DI f32x4 lanesum(v4u f, f32x4 c) {
  const s16x4 one = {0x3F80, 0x3F80, 0x3F80, 0x3F80};
  c = __builtin_amdgcn_mfma_f32_4x4x4bf16_1k(one, __builtin_bit_cast(s16x4, v2u{f.x, f.y}), c, 0, 0, 0);
  return __builtin_amdgcn_mfma_f32_4x4x4bf16_1k(one, __builtin_bit_cast(s16x4, v2u{f.z, f.w}), c, 0, 0, 0);
}
template <int CTRL> DI float dppadd(float v) { return v + u2f((unsigned)__builtin_amdgcn_update_dpp(0, (int)f2u(v), CTRL, 0xf, 0xf, true)); }
DI float wavesum(float v) {
  v = dppadd<0xB1>(v); v = dppadd<0x4E>(v); v = dppadd<0x141>(v); v = dppadd<0x140>(v);
  auto r = __builtin_amdgcn_permlane16_swap(f2u(v), f2u(v), false, false);
  return xhalf_add(u2f(r[0]) + u2f(r[1]));
}
DI void dma(unsigned m0v, unsigned voff, __amdgpu_buffer_rsrc_t rs, unsigned soff) {
  asm volatile("s_mov_b32 m0, %0\n\ts_nop 0\n\tbuffer_load_dwordx4 %1, %2, %3 offen lds" ::"s"(m0v), "v"(voff), "s"(rs), "s"(soff) : "memory");
}
DI void st128(v4u v, __amdgpu_buffer_rsrc_t rs, unsigned voff) { __builtin_amdgcn_raw_buffer_store_b128(v, rs, voff, 0, 16); }
#define BAR() do { __builtin_amdgcn_sched_barrier(0); asm volatile("s_waitcnt lgkmcnt(0)\n\ts_barrier" ::: "memory"); __builtin_amdgcn_sched_barrier(0); } while (0)
#define VMW(n) asm volatile("s_waitcnt vmcnt(" #n ")" ::: "memory")
constexpr unsigned ONES = 0x3F803F80u;

#define STAMP(i) do { } while (0)

DI void poll_flags(const char* ws, unsigned base, int n, u64 tag, int lane) {
  const GAS u64* f = (const GAS u64*)(ws + base + (unsigned)(lane < n ? lane : 0) * 128u);
#pragma unroll 1
  for (unsigned spin = 0; spin < kSpin; ++spin) {
    const u64 v = __hip_atomic_load(f, __ATOMIC_RELAXED, __HIP_MEMORY_SCOPE_AGENT);
    if (__builtin_amdgcn_ballot_w64(v != tag) == 0) break;
    __builtin_amdgcn_s_sleep(1);
  }
  __builtin_amdgcn_fence(__ATOMIC_ACQUIRE, "wavefront");
  asm volatile("" ::: "memory");
}
DI void lds_wait_ge(unsigned addr, unsigned want) {
#pragma unroll 1
  for (unsigned spin = 0; spin < (kSpin << 4); ++spin) {
    const unsigned v = *(volatile LAS unsigned*)(size_t)addr;
    if ((unsigned)__builtin_amdgcn_readfirstlane(v) >= want) break;
    __builtin_amdgcn_s_sleep(1);
  }
  asm volatile("" ::: "memory");
}
DI void lds_set(unsigned addr, unsigned v) { *(volatile LAS unsigned*)(size_t)addr = v; }
DI void lds_inc(unsigned addr, int lane) { if (lane == 0) __hip_atomic_fetch_add((LAS unsigned*)(size_t)addr, 1u, __ATOMIC_RELAXED, __HIP_MEMORY_SCOPE_WORKGROUP); }
DI void publish_last(unsigned cnt_addr, unsigned n, char* ws, unsigned flag_off, u64 tag, int lane) {
  VMW(0);
  if (lane == 0) {
    const unsigned old = __hip_atomic_fetch_add((LAS unsigned*)(size_t)cnt_addr, 1u, __ATOMIC_RELAXED, __HIP_MEMORY_SCOPE_WORKGROUP);
    if (old == n - 1) __hip_atomic_store((GAS u64*)(ws + flag_off), tag, __ATOMIC_RELAXED, __HIP_MEMORY_SCOPE_AGENT);
  }
}
DI u64 lds_tag() { const v2u t = *(const LAS v2u*)(size_t)L_TAG; return ((u64)(unsigned)__builtin_amdgcn_readfirstlane(t.y) << 32) | (unsigned)__builtin_amdgcn_readfirstlane(t.x); }

DI void fold8(const f32x16& acc, int r0, float rstd, float mr, unsigned cs, int hh, float* y) {
#pragma unroll
  for (int g = 0; g < 2; ++g) {
    const f32x4 t = lds128f(cs + (unsigned)((r0 >> 2) + g) * 32u + (unsigned)hh * 16u) * mr;
#pragma unroll
    for (int k = 0; k < 4; ++k) y[4 * g + k] = __builtin_fmaf(acc[r0 + 4 * g + k], rstd, t[k]);
  }
}
DI void load16(unsigned a, int hh, f32x4* v) {
#pragma unroll
  for (int g = 0; g < 4; ++g) v[g] = lds128f(a + (unsigned)g * 32u + (unsigned)hh * 16u);
}
DI void ffn_tile(const f32x16& acc, float rstd, float mr, const f32x4* cs, const f32x4* bs, __amdgpu_buffer_rsrc_t rsHB, unsigned voff) {
  f32x2 y[8], d[8];
#pragma unroll
  for (int g = 0; g < 4; ++g) {
    const f32x4 t = cs[g] * mr + bs[g];
    y[2 * g] = f32x2{acc[4 * g], acc[4 * g + 1]} * rstd + f32x2{t[0], t[1]};
    y[2 * g + 1] = f32x2{acc[4 * g + 2], acc[4 * g + 3]} * rstd + f32x2{t[2], t[3]};
  }
#pragma unroll
  for (int k = 0; k < 8; ++k) d[k] = y[k] * ((y[k] * y[k]) * (-0.10294324f) + (-2.30220819f));
#pragma unroll
  for (int k = 0; k < 8; ++k) { d[k].x = __builtin_amdgcn_exp2f(d[k].x); d[k].y = __builtin_amdgcn_exp2f(d[k].y); }
#pragma unroll
  for (int k = 0; k < 8; ++k) d[k] += 1.0f;
#pragma unroll
  for (int k = 0; k < 8; ++k) { d[k].x = __builtin_amdgcn_rcpf(d[k].x); d[k].y = __builtin_amdgcn_rcpf(d[k].y); }
  v4u o0, o1;
#pragma unroll
  for (int k = 0; k < 4; ++k) {
    const f32x2 g0 = y[k] * d[k], g1 = y[4 + k] * d[4 + k];
    o0[k] = pk2(g0.x, g0.y); o1[k] = pk2(g1.x, g1.y);
  }
  st128(o0, rsHB, voff); st128(o1, rsHB, voff + 1024u);
}
DI void hedge(const f32x16& d, v4u& o0, v4u& o1) {
  constexpr float L = 1.4426950408889634f;
  float m0 = fmx(fmx(fmx(d[0], d[1]), fmx(d[2], d[3])), fmx(fmx(d[4], d[5]), fmx(d[6], d[7])));
  float m1 = fmx(fmx(fmx(d[8], d[9]), fmx(d[10], d[11])), fmx(fmx(d[12], d[13]), fmx(d[14], d[15])));
  m0 = xhalf_max(m0) * -L; m1 = xhalf_max(m1) * -L;
  float e[16];
#pragma unroll
  for (int i = 0; i < 8; ++i) { e[i] = __builtin_amdgcn_exp2f(__builtin_fmaf(d[i], L, m0)); e[8 + i] = __builtin_amdgcn_exp2f(__builtin_fmaf(d[8 + i], L, m1)); }
  float s0 = ((e[0] + e[1]) + (e[2] + e[3])) + ((e[4] + e[5]) + (e[6] + e[7]));
  float s1 = ((e[8] + e[9]) + (e[10] + e[11])) + ((e[12] + e[13]) + (e[14] + e[15]));
  s0 = __builtin_amdgcn_rcpf(xhalf_add(s0)); s1 = __builtin_amdgcn_rcpf(xhalf_add(s1));
#pragma unroll
  for (int k = 0; k < 4; ++k) { o0[k] = pk2(e[2 * k] * s0, e[2 * k + 1] * s0); o1[k] = pk2(e[8 + 2 * k] * s1, e[8 + 2 * k + 1] * s1); }
}
DI v4u wphi_frag(int hd, int lane) {
  const int hh = lane >> 5, q = (lane >> 2) & 3, pp = lane & 3;
  const unsigned a = L_WPHI + (unsigned)hd * 512u + (unsigned)(4 * hh + q) * 32u + (unsigned)pp * 8u;
  v4u f = tr2(a, a + 256u);
  const unsigned sg = (lane & 16) ? 0x80008000u : 0u;
  f.x ^= sg; f.y ^= sg; f.z ^= sg; f.w ^= sg;
  return f;
}
DI v4u selv(bool s, v4u a, v4u b) { const unsigned m = s ? 0xffffffffu : 0u; return v4u{(a.x & m) | (b.x & ~m), (a.y & m) | (b.y & ~m), (a.z & m) | (b.z & ~m), (a.w & m) | (b.w & ~m)}; }
DI v4u pack8(const float* y) { return v4u{pk2(y[0], y[1]), pk2(y[2], y[3]), pk2(y[4], y[5]), pk2(y[6], y[7])}; }

template <int NI> DI void warm_w2(const Params& P, int ct, int ja, int tq, int i0, int lane, v4u* d) {
#pragma unroll
  for (int i = 0; i < NI; ++i) {
    const int row = 288 * ja + 72 * tq + 8 * (i0 + i);
    const u64 a = row < 1792 ? (u64)(size_t)P.w2 + (u64)(unsigned)row * 1024u : (u64)(size_t)P.wo + (u64)(unsigned)(row - 1792) * 1024u;
    const u64 base = ((u64)(unsigned)__builtin_amdgcn_readfirstlane((unsigned)(a >> 32)) << 32) | (unsigned)__builtin_amdgcn_readfirstlane((unsigned)a);
    const unsigned voff = (unsigned)(lane >> 3) * 1024u + (unsigned)(lane & 7) * 16u + (unsigned)ct * 128u;
    asm volatile("s_nop 4\n\tglobal_load_dwordx4 %0, %1, %2" : "=&v"(d[i]) : "v"(voff), "s"(base) : "memory");
  }
}
struct Ctx {
  int lane, tq, hh;
  int p, gi, G;
  int rot;
  int tt, ct, ja;
  int rotE, rotL, rotO;
  bool ffn;
};

DI void role_a(const Params& P, const Ctx& c, int wid, u64 amp_a, u64 amp_b) {
  const int lane = c.lane, tq = c.tq, hh = c.hh, t = lane & 31;
  const int cb = (lane >> 4) & 1, q = (lane >> 2) & 3, pp = lane & 3, sw = (q >> 1) & 1;
  const int th = tq & 1, up = tq >> 1;
  unsigned xa[4];
#pragma unroll
  for (int ks = 0; ks < 4; ++ks) xa[ks] = (unsigned)(64 * th + t) * 128u + (unsigned)(((2 * ks + hh) ^ ((t >> 1) & 7)) * 16);
  const unsigned wl = 16384u + (unsigned)(8 * hh + q) * 128u + (unsigned)((4 * (up ^ sw) + 2 * cb + (pp >> 1)) * 16 + 8 * (pp & 1));
  f32x16 acc[2][2];
#pragma unroll
  for (int i = 0; i < 2; ++i) { acc[i][0] = zero16(); acc[i][1] = zero16(); }
  f32x4 cs = {0.f, 0.f, 0.f, 0.f};
  unsigned x0 = xa[0], x1 = xa[1], x2 = xa[2], x3 = xa[3], w0 = wl;
  v4u xf[4][2], wf[4][2];
  auto rd = [&](int ks, v4u* X, v4u* W) AI {
    const unsigned xb = ks == 0 ? x0 : (ks == 1 ? x1 : (ks == 2 ? x2 : x3));
    X[0] = lds128(xb); X[1] = lds128(xb + 4096u);
    const unsigned a = w0 + (unsigned)(ks * 2048);
    W[0] = tr2(a, a + 512u); W[1] = tr2(a + 8192u, a + 8704u);
  };
  BAR();
  rd(0, xf[0], wf[0]); rd(1, xf[1], wf[1]);
#pragma unroll 1
  for (int kc = 0; kc < 8; ++kc) {
#pragma unroll
    for (int ks = 0; ks < 4; ++ks) {
      if (ks == 2) {
        __builtin_amdgcn_sched_barrier(0);
        asm volatile("s_barrier" ::: "memory");
        __builtin_amdgcn_sched_barrier(0);
        const unsigned sb = (unsigned)((kc + 1) & 3) * SLOT;
        x0 = xa[0] + sb; x1 = xa[1] + sb; w0 = wl + sb;
        asm volatile("" : "+v"(x0), "+v"(x1), "+v"(w0));
      }
      if (ks == 0) {
        const unsigned sb = (unsigned)(kc & 3) * SLOT;
        x2 = xa[2] + sb; x3 = xa[3] + sb;
        asm volatile("" : "+v"(x2), "+v"(x3));
      }
      rd((ks + 2) & 3, xf[(ks + 2) & 3], wf[(ks + 2) & 3]);
#pragma unroll
      for (int i = 0; i < 2; ++i) { acc[i][0] = mfma(wf[ks][i], xf[ks][0], acc[i][0]); acc[i][1] = mfma(wf[ks][i], xf[ks][1], acc[i][1]); }
      cs = lanesum(selv(th != 0, wf[ks][1], wf[ks][0]), cs);
      __builtin_amdgcn_sched_group_barrier(0x008, 1, 0);
      __builtin_amdgcn_sched_group_barrier(0x100, 2, 0);
      __builtin_amdgcn_sched_group_barrier(0x008, 1, 0);
      __builtin_amdgcn_sched_group_barrier(0x100, 2, 0);
      __builtin_amdgcn_sched_group_barrier(0x008, 1, 0);
      __builtin_amdgcn_sched_group_barrier(0x100, 2, 0);
      __builtin_amdgcn_sched_group_barrier(0x002, 4, 0);
      __builtin_amdgcn_sched_group_barrier(0x008, 3, 0);
      __builtin_amdgcn_sched_barrier(0);
    }
    asm volatile("" : "+v"(acc[0][0]), "+v"(acc[0][1]), "+v"(acc[1][0]), "+v"(acc[1][1]), "+v"(cs));
  }
  STAMP(2);
  {
    const float s = xhalf_add(cs[0]);
    if (hh == 0) *(LAS float*)(size_t)(L_CS0 + (unsigned)(up + 2 * th) * 128u + (unsigned)t * 4u) = s;
  }
  BAR();
  const u64 tag = lds_tag();
  float rstd[2], mr[2];
#pragma unroll
  for (int b = 0; b < 2; ++b) {
    const f32x2 st = *(const LAS f32x2*)(size_t)(L_STATS + (unsigned)(64 * th + 32 * b + t) * 8u);
    rstd[b] = st.x; mr[b] = st.y;
  }
  const __amdgpu_buffer_rsrc_t rsHB = __builtin_amdgcn_make_buffer_rsrc(P.ws + WS_HB, 0, HB_BYTES, 0x00020000);
  v4u wm[9];
  if (c.ffn) {
    f32x4 csv[2][4], bsv[2][4];
#pragma unroll
    for (int i = 0; i < 2; ++i) { load16(L_CS0 + (unsigned)(up + 2 * i) * 128u, hh, csv[i]); load16(L_B1 + (unsigned)(up + 2 * i) * 128u, hh, bsv[i]); }
#pragma unroll
    for (int i = 0; i < 2; ++i)
#pragma unroll
      for (int b = 0; b < 2; ++b) {
        const unsigned tile = (unsigned)(up + 2 * i), T32 = (unsigned)(c.p * 4 + 2 * th + b);
        ffn_tile(acc[i][b], rstd[b], mr[b], csv[i], bsv[i], rsHB, (unsigned)lane * 16u + (T32 * 144u + (unsigned)(8 * c.gi) + 2u * tile) * 1024u);
        if (2 * i + b < 3) BAR();
        if (WARM2 && 2 * i + b < 3) warm_w2<3>(P, c.ct, c.ja, tq, 3 * (2 * i + b), lane, wm + 3 * (2 * i + b));
      }
    STAMP(8);
    publish_last(L_CNT + C_EARLY * 4, 4, P.ws, FL_E + (unsigned)(c.p * 8 + c.gi) * 128u, tag, lane);
    if (WARM2) asm volatile("" :: "v"(wm[0]), "v"(wm[1]), "v"(wm[2]), "v"(wm[3]), "v"(wm[4]), "v"(wm[5]), "v"(wm[6]), "v"(wm[7]), "v"(wm[8]));
    BAR();
  } else {
#pragma unroll
    for (int b = 0; b < 2; ++b)
#pragma unroll
      for (int hf = 0; hf < 2; ++hf) {
        const int hd = 2 * up + hf;
        float y[8];
        fold8(acc[0][b], 8 * hf, rstd[b], mr[b], L_CS0 + (unsigned)up * 128u, hh, y);
        v4u o0, o1;
        hedge(mfma(wphi_frag(hd, lane), pack8(y), zero16()), o0, o1);
        const unsigned img = (unsigned)hd * SLOT + 16384u;
        const unsigned row = (unsigned)(64 * th + 32 * b + t);
        const unsigned g = (row >> 1) & 7u;
        *(LAS v2u*)(size_t)(img + row * 64u + (((unsigned)(0 + hh)) ^ g) * 8u) = v2u{o0.x, o0.y};
        *(LAS v2u*)(size_t)(img + row * 64u + (((unsigned)(2 + hh)) ^ g) * 8u) = v2u{o0.z, o0.w};
        *(LAS v2u*)(size_t)(img + row * 64u + (((unsigned)(4 + hh)) ^ g) * 8u) = v2u{o1.x, o1.y};
        *(LAS v2u*)(size_t)(img + row * 64u + (((unsigned)(6 + hh)) ^ g) * 8u) = v2u{o1.z, o1.w};
        fold8(acc[1][b], 8 * hf, rstd[b], mr[b], L_CS0 + (unsigned)(2 + up) * 128u, hh, y);
        const unsigned gv = (row >> 2) & 3u;
        *(LAS v2u*)(size_t)(img + 8192u + row * 32u + (((unsigned)(0 + hh)) ^ gv) * 8u) = v2u{pk2(y[0], y[1]), pk2(y[2], y[3])};
        *(LAS v2u*)(size_t)(img + 8192u + row * 32u + (((unsigned)(2 + hh)) ^ gv) * 8u) = v2u{pk2(y[4], y[5]), pk2(y[6], y[7])};
        if (2 * b + hf < 3) BAR();
        if (WARM2 && 2 * b + hf < 3) warm_w2<3>(P, c.ct, c.ja, tq, 3 * (2 * b + hf), lane, wm + 3 * (2 * b + hf));
      }
    STAMP(8);
    BAR();
    const unsigned img = (unsigned)tq * SLOT + 16384u;
    f32x16 kv = zero16();
    unsigned pa[2], va[2];
#pragma unroll
    for (int i2 = 0; i2 < 2; ++i2) {
      const unsigned r = (unsigned)(8 * hh + 4 * i2 + q);
      pa[i2] = img + r * 64u + (((unsigned)(4 * cb + pp)) ^ ((r >> 1) & 7u)) * 8u;
      va[i2] = img + 8192u + r * 32u + (((unsigned)pp) ^ ((r >> 2) & 3u)) * 8u;
    }
#pragma unroll
    for (int ks = 0; ks < 8; ++ks) {
      const v4u af = tr2(pa[0] + (unsigned)ks * 1024u, pa[1] + (unsigned)ks * 1024u);
      v4u bf = tr2(va[0] + (unsigned)ks * 512u, va[1] + (unsigned)ks * 512u);
      if (cb) bf = v4u{ONES, ONES, ONES, ONES};
      kv = mfma(af, bf, kv);
    }
    const unsigned hidx = (unsigned)(c.gi * 4 + tq) * 16u + (unsigned)c.p;
    const __amdgpu_buffer_rsrc_t rsKV = __builtin_amdgcn_make_buffer_rsrc(P.ws + WS_KVP, 0, 524288 + 65536, 0x00020000);
    if (cb == 0) {
#pragma unroll
      for (int s = 0; s < 2; ++s) {
        const v4u o = v4u{pk2(kv[8 * s], kv[8 * s + 1]), pk2(kv[8 * s + 2], kv[8 * s + 3]), pk2(kv[8 * s + 4], kv[8 * s + 5]), pk2(kv[8 * s + 6], kv[8 * s + 7])};
        st128(o, rsKV, (unsigned)((16 * s + (lane & 15) + 32 * hh) * 16) + hidx * 1024u);
      }
    } else if ((lane & 15) == 0) {
#pragma unroll
      for (int g4 = 0; g4 < 4; ++g4) {
        const v4u o = v4u{f2u(kv[4 * g4]), f2u(kv[4 * g4 + 1]), f2u(kv[4 * g4 + 2]), f2u(kv[4 * g4 + 3])};
        st128(o, rsKV, (unsigned)((8 * g4 + 4 * hh) * 4) + 524288u + hidx * 128u);
      }
    }
    publish_last(L_CNT + C_KV * 4, 4, P.ws, FL_K + (unsigned)(c.gi * 16 + c.p) * 128u, tag, lane);
    if (WARM2) asm volatile("" :: "v"(wm[0]), "v"(wm[1]), "v"(wm[2]), "v"(wm[3]), "v"(wm[4]), "v"(wm[5]), "v"(wm[6]), "v"(wm[7]), "v"(wm[8]));
  }
  STAMP(3);
  unsigned tabB, tabWlo, tabWhi;
  {
    const int jj = lane < 36 ? lane : 35;
    constexpr int J2 = ORD ? 24 : 28;
    int sg;
    if (jj < 16) sg = 4 * ((jj + c.rotE) & 15) + tq;
    else if ((jj < J2) != (ORD != 0)) sg = 64 + 4 * ((jj - (ORD ? 24 : 16) + c.rotL) % 12) + tq;
    else sg = 112 + 4 * ((jj - (ORD ? 16 : 28) + c.rotO) & 7) + tq;
    int row;
    if (sg < 64) { const int g = sg >> 3; row = 224 * g + 32 * (g & 1) + 16 * (sg & 7); }
    else if (sg < 112) {
      const int e = sg - 64, g = e / 6, i = e - 6 * g;
      row = i < 4 ? 224 * g + 32 * (g & 1) + 128 + 16 * i : ((g & 1) ? 224 * g : 224 * g + 192) + 16 * (i - 4);
    } else row = 16 * (sg - 112);
    const u64 a = (u64)(size_t)(sg < 112 ? P.w2 : P.wo) + (u64)row * 1024u + (u64)c.ct * 128u;
    tabB = (unsigned)sg * 1024u; tabWlo = (unsigned)a; tabWhi = (unsigned)(a >> 32);
  }
  const unsigned vw0 = (unsigned)(lane >> 3) * 1024u + (unsigned)(((lane & 7) ^ (((lane >> 4) & 1) << 2)) * 16), vw1 = vw0 + 8192u;
  const unsigned ring = (unsigned)tq * SLOT;
  auto wdma = [&](int j) AI {
    const unsigned lo = __builtin_amdgcn_readlane(tabWlo, j), hi = __builtin_amdgcn_readlane(tabWhi, j);
    const u64 b = ((u64)hi << 32) | lo;
    const unsigned m = ring + (unsigned)(j & 15) * 2048u;
    asm volatile("s_nop 4\n\ts_mov_b32 m0, %0\n\ts_nop 0\n\tglobal_load_lds_dwordx4 %1, %3\n\ts_add_u32 m0, m0, 0x400\n\ts_nop 0\n\tglobal_load_lds_dwordx4 %2, %3"
                 ::"s"(m), "v"(vw0), "v"(vw1), "s"(b) : "memory", "scc");
  };
  const unsigned vb0 = (unsigned)lane * 16u + (unsigned)(2 * c.tt) * 144u * 1024u, vb1 = vb0 + 144u * 1024u;
  constexpr int NB = 4;
  v4u br[NB][2];
#pragma unroll
  for (int u = 0; u < NB; ++u) { br[u][0] = v4u{0u, 0u, 0u, 0u}; br[u][1] = br[u][0]; }
  auto bload = [&](unsigned so, v4u& d0, v4u& d1) AI {
    asm volatile("s_nop 4\n\tbuffer_load_dwordx4 %0, %2, %4, %5 offen sc1\n\tbuffer_load_dwordx4 %1, %3, %4, %5 offen sc1"
                 : "+v"(d0), "+v"(d1) : "v"(vb0), "v"(vb1), "s"(rsHB), "s"(so) : "memory");
  };
  STAMP(9);
#pragma unroll 1
  for (int j = 0; j < 14; ++j) wdma(j);
  STAMP(4);
  const int pan = c.tt >> 1;
  poll_flags(P.ws, FL_E + (unsigned)pan * 8u * 128u, 8, tag, lane);
  STAMP(5);
  bload(__builtin_amdgcn_readlane(tabB, 0), br[0][0], br[0][1]);
  bload(__builtin_amdgcn_readlane(tabB, 1), br[1][0], br[1][1]);
  wdma(14);
  bload(__builtin_amdgcn_readlane(tabB, 2), br[2][0], br[2][1]);
  wdma(15);
  bload(__builtin_amdgcn_readlane(tabB, 3), br[3][0], br[3][1]);
  unsigned w2l[2];
#pragma unroll
  for (int h = 0; h < 2; ++h) w2l[h] = ring + (unsigned)(4 * hh + q) * 128u + (unsigned)((4 * (h ^ sw) + 2 * cb + (pp >> 1)) * 16 + 8 * (pp & 1));
  f32x16 o[2][2];
#pragma unroll
  for (int a = 0; a < 2; ++a)
#pragma unroll
    for (int b = 0; b < 2; ++b) o[a][b] = zero16();
  v4u af[2];
  asm volatile("s_waitcnt vmcnt(10)" : "+v"(br[0][0]), "+v"(br[0][1]) :: "memory");
  af[0] = tr2(w2l[0], w2l[0] + 1024u); af[1] = tr2(w2l[1], w2l[1] + 1024u);
  auto step = [&](int j, int u, bool refill, bool first) AI {
    v4u an[2];
    if (first) asm volatile("s_waitcnt vmcnt(" STR(W1) ")" : "+v"(br[(u + 1) % NB][0]), "+v"(br[(u + 1) % NB][1]) :: "memory");
    else asm volatile("s_waitcnt vmcnt(" STR(W2) ")" : "+v"(br[(u + 1) % NB][0]), "+v"(br[(u + 1) % NB][1]) :: "memory");
    unsigned p0 = w2l[0] + (unsigned)((j + 1) & 15) * 2048u, p1 = w2l[1] + (unsigned)((j + 1) & 15) * 2048u;
    asm volatile("" : "+v"(p0), "+v"(p1));
    an[0] = tr2(p0, p0 + 1024u); an[1] = tr2(p1, p1 + 1024u);
    o[0][0] = mfma(af[0], br[u][0], o[0][0]); o[0][1] = mfma(af[0], br[u][1], o[0][1]);
    o[1][0] = mfma(af[1], br[u][0], o[1][0]); o[1][1] = mfma(af[1], br[u][1], o[1][1]);
    asm volatile("" : "+v"(o[0][0]), "+v"(o[0][1]), "+v"(o[1][0]), "+v"(o[1][1]) :: "memory");
    if (refill) wdma(j + 16);
    const int jn = j + NB;
    if (u == 0) {
      if (jn == 16) lds_wait_ge(L_CNT + (ORD ? C_ORDY : C_LATERDY) * 4, 1);
      if (jn == (ORD ? 24 : 28)) lds_wait_ge(L_CNT + (ORD ? C_LATERDY : C_ORDY) * 4, 1);
    }
    bload(jn < 36 ? (unsigned)__builtin_amdgcn_readlane(tabB, jn) : 0x7F000000u, br[u][0], br[u][1]);
    af[0] = an[0]; af[1] = an[1];
  };
#pragma unroll 1
  for (int it = 0; it < 5; ++it) {
    if (it == 3) STAMP(10);
#pragma unroll
    for (int u = 0; u < NB; ++u) step(NB * it + u, u, true, true);
  }
  STAMP(11);
#pragma unroll 1
  for (int it = 5; it < 9; ++it) {
#pragma unroll
    for (int u = 0; u < NB; ++u) step(NB * it + u, u, false, false);
  }
  asm volatile("s_waitcnt vmcnt(0)" : "+v"(br[0][0]), "+v"(br[0][1]), "+v"(br[1][0]), "+v"(br[1][1]), "+v"(br[2][0]), "+v"(br[2][1]), "+v"(br[3][0]), "+v"(br[3][1]) :: "memory");
  STAMP(6);
#pragma unroll
  for (int a = 0; a < 2; ++a)
#pragma unroll
    for (int b = 0; b < 2; ++b)
#pragma unroll
      for (int g4 = 0; g4 < 4; ++g4)
        *(LAS f32x4*)(size_t)(ring + (unsigned)(32 * b + t) * 272u + (unsigned)(32 * a + 8 * g4 + 4 * hh) * 4u) =
            f32x4{o[a][b][4 * g4], o[a][b][4 * g4 + 1], o[a][b][4 * g4 + 2], o[a][b][4 * g4 + 3]};
  VMW(0);
  BAR();
  STAMP(7);
}

template <int NT, int NF> DI void pass1(const Params& P, int wid, u64& amp_a, u64& amp_b, const v4u (&xr)[32], f32x16 (&acc)[NT], f32x4& cs, const unsigned (&wb)[NT], int lane) {
#pragma unroll
  for (int qq = 0; qq < 4; ++qq) {
    if (qq == 1) { if (NF == 8) VMW(16); else VMW(8); }
    if (qq == 2) { if (NF == 8) VMW(8); else VMW(4); }
    if (qq == 3) VMW(0);
    if (qq > 0) BAR();
    unsigned a[NT];
#pragma unroll
    for (int i = 0; i < NT; ++i) { a[i] = wb[i] + (unsigned)qq * SLOT; asm volatile("" : "+v"(a[i])); }
    v4u f[3][NT];
    auto rd = [&](int k8, v4u* W) AI {
      const unsigned o = (unsigned)((k8 >> 2) * 8192 + (k8 & 3) * 2048);
#pragma unroll
      for (int i = 0; i < NT; ++i) W[i] = tr2(a[i] + o, a[i] + o + 512u);
    };
    rd(0, f[0]); rd(1, f[1]);
    __builtin_amdgcn_sched_barrier(0);
#pragma unroll
    for (int k8 = 0; k8 < 8; ++k8) {
      if (k8 < 6) rd(k8 + 2, f[(k8 + 2) % 3]);
      const v4u xf = xr[8 * qq + k8];
#pragma unroll
      for (int i = 0; i < NT; ++i) acc[i] = mfma(f[k8 % 3][i], xf, acc[i]);
      cs = lanesum(f[k8 % 3][0], cs);
      if (k8 < 6) {
#pragma unroll
        for (int i = 0; i < NT; ++i) {
          __builtin_amdgcn_sched_group_barrier(0x008, 1, 0);
          __builtin_amdgcn_sched_group_barrier(0x100, 2, 0);
        }
        __builtin_amdgcn_sched_group_barrier(0x008, 2, 0);
      }
      __builtin_amdgcn_sched_barrier(0);
    }
    asm volatile("" : "+v"(acc[0]), "+v"(acc[1]), "+v"(acc[NT - 1]), "+v"(cs) :: "memory");
  }
}

template <bool ffn> DI u64 role_b_head(const Params& P, const Ctx& c, int wid) {
  u64 amp_a = 0, amp_b = 0; (void)amp_a; (void)amp_b;
  const int lane = c.lane, tq = c.tq, hh = c.hh, t = lane & 31;
  const int g = c.gi;
  const unsigned tok = (unsigned)(c.p * 128 + 8 * c.G + 2 * tq);
  v4u r0, r1;
  {
    const u16* rp = P.x + (size_t)tok * 512 + lane * 8;
    asm volatile("global_load_dwordx4 %0, %2, off\n\tglobal_load_dwordx4 %1, %2, off offset:1024" : "=&v"(r0), "=&v"(r1) : "v"(rp) : "memory");
  }
  const __amdgpu_buffer_rsrc_t rsX = __builtin_amdgcn_make_buffer_rsrc((void*)P.x, 0, 2048 * 1024, 0x00020000);
  const __amdgpu_buffer_rsrc_t rsW = __builtin_amdgcn_make_buffer_rsrc((void*)(ffn ? P.w1 : P.wqkv), 0, ffn ? 512 * 3584 : 512 * 3072, 0x00020000);
  constexpr unsigned RS = ffn ? 3584u : 3072u;
  const unsigned cA = (unsigned)(224 * g + 32 * (g & 1));
  const unsigned u0 = ffn ? cA * 2u : (unsigned)(512 + 64 * g) * 2u, u1 = ffn ? cA * 2u + 128u : (unsigned)(1024 + 64 * g) * 2u;
  const unsigned ua = ffn ? (cA + 128u) * 2u : (unsigned)(64 * g) * 2u;
  const unsigned ub = (unsigned)((g & 1) ? 224 * g - 32 : 224 * g + 192) * 2u;
  const unsigned voffX = (unsigned)(lane >> 3) * 1024u + (unsigned)(((lane & 7) ^ ((4 * (tq & 1) + (lane >> 4)) & 7)) * 16);
  const unsigned voffW = (unsigned)(lane >> 3) * RS + (unsigned)(((lane & 7) ^ (((lane >> 4) & 1) << 2)) * 16);
  const unsigned soffX = (unsigned)(c.p * 128 + 8 * tq) * 1024u;
  const unsigned wrow = 32u * (unsigned)(tq & 1) * RS;
  auto fill = [&](int f) AI {
    const unsigned slot = (unsigned)(f & 3) * SLOT;
    if (f < 8) {
      const unsigned kcp = (unsigned)((f + c.rot) & 7);
      unsigned sx = soffX + kcp * 128u, sw = 64u * kcp * RS + wrow + ((tq >> 1) ? u1 : u0);
      unsigned mx = slot + (unsigned)tq * 1024u, mw = slot + 16384u + (unsigned)tq * 4096u;
#pragma unroll 1
      for (int m = 0; m < 4; ++m) {
        dma(mx, voffX, rsX, sx); dma(mw, voffW, rsW, sw);
        mx += 4096u; sx += 32768u; mw += 1024u; sw += 8u * RS;
      }
    } else {
      const unsigned kcp = (unsigned)((2 * (f - 8) + (tq >> 1) + c.rot) & 7);
      unsigned sw = 64u * kcp * RS + wrow, mw = slot + (unsigned)tq * 4096u;
#pragma unroll 1
      for (int m = 0; m < 4; ++m) {
        dma(mw, voffW, rsW, sw + ua);
        if (ffn) dma(mw + 16384u, voffW, rsW, sw + ub);
        mw += 1024u; sw += 8u * RS;
      }
    }
  };
  fill(0); fill(1);
  float st0;
  {
    asm volatile("s_waitcnt vmcnt(16)" : "+v"(r0), "+v"(r1) :: "memory");
    float v[2][8], mean[2], rs[2];
#pragma unroll
    for (int k = 0; k < 4; ++k) {
      v[0][2 * k] = u2f(r0[k] << 16); v[0][2 * k + 1] = u2f(r0[k] & 0xffff0000u);
      v[1][2 * k] = u2f(r1[k] << 16); v[1][2 * k + 1] = u2f(r1[k] & 0xffff0000u);
    }
#pragma unroll
    for (int i = 0; i < 2; ++i) mean[i] = wavesum(((v[i][0] + v[i][1]) + (v[i][2] + v[i][3])) + ((v[i][4] + v[i][5]) + (v[i][6] + v[i][7]))) * (1.0f / 512.0f);
#pragma unroll
    for (int i = 0; i < 2; ++i) {
      float q2 = 0.f;
#pragma unroll
      for (int k = 0; k < 8; ++k) { const float d = v[i][k] - mean[i]; q2 = __builtin_fmaf(d, d, q2); }
      rs[i] = __builtin_amdgcn_rsqf(wavesum(q2) * (1.0f / 512.0f) + 1e-5f);
    }
    st0 = (lane & 2) ? rs[1] : rs[0];
    st0 = (lane & 1) ? -((lane & 2) ? mean[1] : mean[0]) * st0 : st0;
    asm volatile("" : "+v"(st0));
  }
  unsigned xa[4];
#pragma unroll
  for (int ks = 0; ks < 4; ++ks) xa[ks] = (unsigned)(32 * tq + t) * 128u + (unsigned)(((2 * ks + hh) ^ ((t >> 1) & 7)) * 16);
  v4u xr[32];
  u64 tag = 0;
  v4u gr;
  const unsigned token = (unsigned)(c.p * 128 + 32 * tq + t);
#pragma unroll
  for (int kc = 0; kc < 8; ++kc) {
    if (kc < 7 || ffn) VMW(8); else VMW(4);
    BAR();
    if (kc == 0) {
      tag = lds_tag();
      if (lane < 4)
        __hip_atomic_store((GAS u64*)(P.ws + WS_ST + tok * 16u + (unsigned)lane * 8u), ((u64)(unsigned)tag << 32) | f2u(st0), __ATOMIC_RELAXED, __HIP_MEMORY_SCOPE_AGENT);
    }
    if (kc == 5) {
      const char* gp = P.ws + WS_ST + token * 16u;
      asm volatile("global_load_dwordx4 %0, %1, off sc1" : "=&v"(gr) : "v"(gp) : "memory");
    }
    fill(kc + 2);
    const unsigned sb = (unsigned)(kc & 3) * SLOT;
#pragma unroll
    for (int ks = 0; ks < 4; ++ks) xr[4 * kc + ks] = lds128(xa[ks] + sb);
    asm volatile("" : "+v"(xr[4 * kc]), "+v"(xr[4 * kc + 1]), "+v"(xr[4 * kc + 2]), "+v"(xr[4 * kc + 3]));
  }
  asm volatile("" : "+v"(gr));
  STAMP(2);
  BAR();
  fill(10); fill(11);
  if (__builtin_amdgcn_ballot_w64(gr.y != (unsigned)tag || gr.w != (unsigned)tag) != 0) {
    const __amdgpu_buffer_rsrc_t rs = __builtin_amdgcn_make_buffer_rsrc((void*)(P.ws + WS_ST), 0, 32768, 0x00020000);
#pragma unroll 1
    for (unsigned spin = 0; spin < kSpin; ++spin) {
      gr = __builtin_amdgcn_raw_buffer_load_b128(rs, token * 16u, 0, 16);
      if (__builtin_amdgcn_ballot_w64(gr.y != (unsigned)tag || gr.w != (unsigned)tag) == 0) break;
      __builtin_amdgcn_s_sleep(1);
    }
  }
  const float rstd = u2f(gr.x), mr = u2f(gr.z);
  if (hh == 0) *(LAS f32x2*)(size_t)(L_STATS + (unsigned)(32 * tq + t) * 8u) = f32x2{rstd, mr};
  if (ffn) VMW(24); else VMW(12);
  BAR();
  const int cb = (lane >> 4) & 1, q = (lane >> 2) & 3, pp = lane & 3, sw = (q >> 1) & 1;
  unsigned wl[2];
#pragma unroll
  for (int h = 0; h < 2; ++h) wl[h] = (unsigned)(8 * hh + q) * 128u + (unsigned)((4 * (h ^ sw) + 2 * cb + (pp >> 1)) * 16 + 8 * (pp & 1));
  const __amdgpu_buffer_rsrc_t rsHB = __builtin_amdgcn_make_buffer_rsrc(P.ws + WS_HB, 0, HB_BYTES, 0x00020000);
  const unsigned T32 = (unsigned)(c.p * 4 + tq);
  f32x4 cs = {0.f, 0.f, 0.f, 0.f};
  auto cs_sync = [&](int tile, bool wr) AI {
    const float s = xhalf_add(cs[0]);
    if (hh == 0 && wr) *(LAS float*)(size_t)(L_CS1 + (unsigned)tile * 128u + (unsigned)t * 4u) = s;
    BAR();
  };
  if constexpr (ffn) {
    const unsigned w2a = ((g & 1) ? wl[1] : wl[0]) + 16384u;
    int ti[3];
    unsigned wb[3];
    f32x16 acc[3];
#pragma unroll
    for (int i = 0; i < 3; ++i) { ti[i] = (i + tq) % 3; wb[i] = ti[i] == 0 ? wl[0] : (ti[i] == 1 ? wl[1] : w2a); acc[i] = zero16(); }
    pass1<3, 8>(P, wid, amp_a, amp_b, xr, acc, cs, wb, lane);
    cs_sync(ti[0], tq < 3);
    STAMP(3);
    f32x4 csv[3][4], bsv[3][4];
#pragma unroll
    for (int i = 0; i < 3; ++i) { load16(L_CS1 + (unsigned)ti[i] * 128u, hh, csv[i]); load16(L_B1 + (unsigned)(4 + ti[i]) * 128u, hh, bsv[i]); }
#pragma unroll
    for (int i = 0; i < 3; ++i)
      ffn_tile(acc[i], rstd, mr, csv[i], bsv[i], rsHB, (unsigned)lane * 16u + (T32 * 144u + (unsigned)(64 + 6 * g + 2 * ti[i])) * 1024u);
    publish_last(L_CNT + C_LATE * 4, 4, P.ws, FL_L + (unsigned)(c.p * 8 + g) * 128u, tag, lane);
  } else {
    int ti[2];
    unsigned wb[2];
    f32x16 acc[2];
#pragma unroll
    for (int i = 0; i < 2; ++i) { ti[i] = (i + tq) & 1; wb[i] = ti[i] == 0 ? wl[0] : wl[1]; acc[i] = zero16(); }
    pass1<2, 4>(P, wid, amp_a, amp_b, xr, acc, cs, wb, lane);
    cs_sync(ti[0], tq < 2);
    STAMP(3);
    v4u pq[4][2];
#pragma unroll
    for (int hd = 0; hd < 4; ++hd) {
      float y[8];
      fold8(acc[hd >> 1], 8 * (hd & 1), rstd, mr, L_CS1 + (unsigned)ti[hd >> 1] * 128u, hh, y);
      hedge(mfma(wphi_frag(2 * ti[hd >> 1] + (hd & 1), lane), pack8(y), zero16()), pq[hd][0], pq[hd][1]);
    }
    STAMP(4);
    if (tq == 0) { poll_flags(P.ws, FL_K + (unsigned)g * 16u * 128u, 16, tag, lane); lds_set(L_CNT + C_KVRDY * 4, 1); }
    else lds_wait_ge(L_CNT + C_KVRDY * 4, 1);
    STAMP(5);
    {
      const __amdgpu_buffer_rsrc_t rsKV = __builtin_amdgcn_make_buffer_rsrc(P.ws + WS_KVP, 0, 524288 + 65536, 0x00020000);
      const unsigned hbase = (unsigned)(g * 4 + tq) * 16u;
      v4u pv[16];
#pragma unroll
      for (int i = 0; i < 16; ++i) pv[i] = __builtin_amdgcn_raw_buffer_load_b128(rsKV, (unsigned)lane * 16u, (hbase + (unsigned)i) * 1024u, 16);
      const v4u z0 = __builtin_amdgcn_raw_buffer_load_b128(rsKV, (unsigned)lane * 16u, 524288u + hbase * 128u, 16);
      const v4u z1 = __builtin_amdgcn_raw_buffer_load_b128(rsKV, (unsigned)lane * 16u, 524288u + hbase * 128u + 1024u, 16);
      v4u idf;
      {
        const int m = lane & 31, h2 = lane >> 5;
        const int j = (m < 16 && ((m >> 2) & 1) == h2) ? (((m >> 3) << 2) | (m & 3)) : -1;
#pragma unroll
        for (int k = 0; k < 4; ++k) idf[k] = (j == 2 * k ? 0x3F80u : 0u) | (j == 2 * k + 1 ? 0x3F800000u : 0u);
      }
      f32x16 sum = zero16();
#pragma unroll
      for (int i = 0; i < 16; ++i) sum = mfma(idf, pv[i], sum);
      const float s[8] = {sum[0], sum[1], sum[2], sum[3], sum[4], sum[5], sum[6], sum[7]};
      const unsigned afr = L_AFR + (unsigned)tq * 2048u;
      *(LAS v4u*)(size_t)(afr + (unsigned)((lane >> 4) & 1) * 1024u + (unsigned)((lane & 15) + 32 * (lane >> 5)) * 16u) = pack8(s);
      const unsigned hh2 = (unsigned)(lane >> 4) & 1u, c16 = (unsigned)lane & 15u;
      float zz[4];
#pragma unroll
      for (int k = 0; k < 4; ++k) {
        float v = u2f(z0[k]) + u2f(z1[k]);
        v += shx(v, 8, lane); v += shx(v, 16, lane); v += shx(v, 32, lane);
        zz[k] = v;
      }
      if (lane < 8) *(LAS f32x4*)(size_t)(L_ZT + (unsigned)tq * 128u + (unsigned)lane * 16u) = f32x4{zz[0], zz[1], zz[2], zz[3]};
      const unsigned f0 = 16u * (unsigned)(lane >> 5) + 4u * hh2;
      const f32x4 za = lds128f(L_ZT + (unsigned)tq * 128u + f0 * 4u), zb = lds128f(L_ZT + (unsigned)tq * 128u + (f0 + 8u) * 4u);
      *(LAS v4u*)(size_t)(afr + (unsigned)(lane >> 5) * 1024u + (16u + c16 + 32u * hh2) * 16u) = v4u{pk2(za[0], za[1]), pk2(za[2], za[3]), pk2(zb[0], zb[1]), pk2(zb[2], zb[3])};
      asm volatile("s_waitcnt lgkmcnt(0)" ::: "memory");
      lds_inc(L_CNT + C_AFR * 4, lane);
      lds_wait_ge(L_CNT + C_AFR * 4, 4);
    }
#pragma unroll
    for (int hd = 0; hd < 4; ++hd) {
      const int head = 2 * ti[hd >> 1] + (hd & 1);
      f32x16 d = mfma(lds128(L_AFR + (unsigned)head * 2048u + (unsigned)lane * 16u), pq[hd][0], zero16());
      d = mfma(lds128(L_AFR + (unsigned)head * 2048u + 1024u + (unsigned)lane * 16u), pq[hd][1], d);
      const float r = __builtin_amdgcn_rcpf(d[8] + 1e-6f);
      const v4u o = v4u{pk2(d[0] * r, d[1] * r), pk2(d[2] * r, d[3] * r), pk2(d[4] * r, d[5] * r), pk2(d[6] * r, d[7] * r)};
      st128(o, rsHB, (unsigned)lane * 16u + (T32 * 144u + (unsigned)(112 + 4 * g + head)) * 1024u);
    }
    publish_last(L_CNT + C_O * 4, 4, P.ws, FL_O + (unsigned)(c.p * 8 + g) * 128u, tag, lane);
  }
  STAMP(6);
  return tag;
}

DI void role_b(const Params& P, const Ctx& c, int wid) {
  u64 amp_a = 0, amp_b = 0; (void)amp_a; (void)amp_b;
  const int lane = c.lane, tq = c.tq;
  const u64 tag = c.ffn ? role_b_head<true>(P, c, wid) : role_b_head<false>(P, c, wid);
  const int pan = c.tt >> 1;
  const int tb = tq * 64 + lane;
  v4u xres[2], bres;
#pragma unroll
  for (int m = 0; m < 2; ++m) {
    const int idx = tb + 256 * m;
    xres[m] = *(const GAS v4u*)(P.x + (size_t)(64 * c.tt + (idx >> 3)) * 512 + 64 * c.ct + 8 * (idx & 7));
  }
  bres = *(const GAS v4u*)(P.b2 + 64 * c.ct + 8 * (tb & 7));
  if (tq == 0) {
    poll_flags(P.ws, (ORD ? FL_O : FL_L) + (unsigned)pan * 8u * 128u, 8, tag, lane);
    lds_set(L_CNT + (ORD ? C_ORDY : C_LATERDY) * 4, 1);
    poll_flags(P.ws, (ORD ? FL_L : FL_O) + (unsigned)pan * 8u * 128u, 8, tag, lane);
    lds_set(L_CNT + (ORD ? C_LATERDY : C_ORDY) * 4, 1);
  } else if (tq == 1 && __builtin_amdgcn_workgroup_id_x() == 0) {
    poll_flags(P.ws, FL_E, 64, tag, lane); poll_flags(P.ws, FL_E + 64 * 128, 64, tag, lane);
    poll_flags(P.ws, FL_K, 64, tag, lane); poll_flags(P.ws, FL_K + 64 * 128, 64, tag, lane);
    if (lane == 0) __hip_atomic_store((GAS u64*)(P.ws + WS_EPOCH), tag, __ATOMIC_RELAXED, __HIP_MEMORY_SCOPE_AGENT);
  }
  BAR();
#pragma unroll
  for (int m = 0; m < 2; ++m) {
    const int idx = tb + 256 * m;
    const unsigned a = (unsigned)(idx >> 3) * 272u + (unsigned)(idx & 7) * 32u;
    f32x4 lo = lds128f(a), hi = lds128f(a + 16u);
#pragma unroll
    for (unsigned w = 1; w < 4; ++w) { lo += lds128f(w * SLOT + a); hi += lds128f(w * SLOT + a + 16u); }
    v4u o;
#pragma unroll
    for (int k = 0; k < 4; ++k) {
      const float a0 = k < 2 ? lo[2 * k] : hi[2 * k - 4], a1 = k < 2 ? lo[2 * k + 1] : hi[2 * k - 3];
      const float x0 = u2f(xres[m][k] << 16), x1 = u2f(xres[m][k] & 0xffff0000u);
      const float b0 = u2f(bres[k] << 16), b1 = u2f(bres[k] & 0xffff0000u);
      o[k] = pk2(a0 + x0 + b0, a1 + x1 + b1);
    }
    *(GAS v4u*)(P.out + (size_t)(64 * c.tt + (idx >> 3)) * 512 + 64 * c.ct + 8 * (idx & 7)) = o;
  }
  STAMP(7);
}

__global__ __launch_bounds__(512, 2) NOPK void fused_kernel(Params P) {
  __shared__ __attribute__((aligned(1024))) char smem[LDS_BYTES];
  asm volatile("" ::"v"((unsigned)(size_t)(LAS char*)smem) : "memory");
  u64 pc;
  asm volatile("s_getpc_b64 %0" : "=s"(pc));
  const int tid = __builtin_amdgcn_workitem_id_x(), lane = tid & 63;
  const int wid = __builtin_amdgcn_readfirstlane(tid >> 6);
  u64 amp_a = 0, amp_b = 0;
  STAMP(0);
  Ctx c;
  c.lane = lane; c.tq = wid & 3; c.hh = lane >> 5;
  const int blk = __builtin_amdgcn_workgroup_id_x(), xcd = blk & 7, j = blk >> 3, ja = j & 7, jb = j >> 3;
  c.p = (xcd & 1) * 8 + ja;
  c.G = (xcd >> 1) * 4 + jb;
  c.ffn = !(c.G & 1);
  c.gi = c.G >> 1;
  c.rot = ROT1 == 0 ? 0 : (ROT1 == 1 ? (((ja + jb) % 3) * 3) & 7 : (ROT1 == 2 ? (ja + 2 * jb) & 7 : ((ja + jb) & 1) * 4));
  c.tt = (xcd >> 1) * 8 + ja; c.ct = (xcd & 1) * 4 + jb; c.ja = ja;
  c.rotE = ROT2 ? (2 * ja + 4 * jb) & 15 : 0; c.rotL = ROT2 ? (ja + 3 * jb) % 12 : 0; c.rotO = ROT2 ? (ja + 2 * jb) & 7 : 0;
  if (wid < 4) {
    if (CODEPF) {
      const unsigned kb = (unsigned)(j + 32 * (wid & 1));
      if (wid < 2 && kb < CODE_KB) {
        const unsigned v = *(const GAS unsigned*)((pc & ~255ull) + kb * 1024u + (unsigned)lane * 16u);
        asm volatile("" ::"v"(v));
      }
    }
    const u64 e = __hip_atomic_load((const GAS u64*)(P.ws + WS_EPOCH), __ATOMIC_RELAXED, __HIP_MEMORY_SCOPE_AGENT);
    if (tid < 32) *(LAS unsigned*)(size_t)(L_CNT + (unsigned)tid * 4u) = 0u;
    if (c.ffn) {
      if (tid < 224) {
        const int t7 = tid >> 5, n = tid & 31, g = c.gi;
        const int cA = 224 * g + 32 * (g & 1);
        const int col = t7 < 4 ? cA + 32 * t7 : (t7 < 6 ? cA + 128 + 32 * (t7 - 4) : ((g & 1) ? 224 * g : 224 * g + 192));
        *(LAS float*)(size_t)(L_B1 + (unsigned)tid * 4u) = u2f((unsigned)P.b1[col + n] << 16);
      }
    } else if (tid < 128) {
      *(LAS v4u*)(size_t)(L_WPHI + (unsigned)tid * 16u) = *(const GAS v4u*)(P.wphi + (size_t)c.gi * 1024 + tid * 8);
    }
    const u64 tag = e + 1u;
    if (tid == 0) *(LAS v2u*)(size_t)L_TAG = v2u{(unsigned)tag, (unsigned)(tag >> 32)};
    STAMP(1);
    role_a(P, c, wid, amp_a, amp_b);
  } else {
    STAMP(1);
    role_b(P, c, wid);
  }
}

}

extern "C" void kernel_launch(void* const* d_in, const int* in_sizes, int n_in, void* d_out, int out_size, void* d_ws, size_t ws_size,
                              hipStream_t stream) {
  Params P;
  P.x = (const u16*)d_in[0]; P.wqkv = (const u16*)d_in[1]; P.wphi = (const u16*)d_in[2]; P.wo = (const u16*)d_in[3];
  P.w1 = (const u16*)d_in[5]; P.b1 = (const u16*)d_in[6]; P.w2 = (const u16*)d_in[7]; P.b2 = (const u16*)d_in[8];
  P.out = (u16*)d_out; P.ws = (char*)d_ws;
  fused_kernel<<<256, 512, 0, stream>>>(P);
}
```

```cpp
#include <hip/hip_runtime.h>
#include <stdint.h>

namespace {

typedef unsigned short u16;
typedef unsigned long long u64;
typedef unsigned v4u __attribute__((ext_vector_type(4)));
typedef unsigned v2u __attribute__((ext_vector_type(2)));
typedef float f32x16 __attribute__((ext_vector_type(16)));
typedef float f32x4 __attribute__((ext_vector_type(4)));
typedef float f32x2 __attribute__((ext_vector_type(2)));
typedef __bf16 bf16x8 __attribute__((ext_vector_type(8)));
typedef __bf16 bf16x2 __attribute__((ext_vector_type(2)));
typedef short s16x4 __attribute__((ext_vector_type(4)));
#define LAS __attribute__((address_space(3)))
#define GAS __attribute__((address_space(1)))
#if defined(__HIP_DEVICE_COMPILE__)
#define NOPK __attribute__((target("no-packed-fp32-ops")))
#else
#define NOPK
#endif
#define DI __device__ __forceinline__ NOPK
#define AI __attribute__((always_inline)) NOPK

#define STAMPS 3
#define AMPI 98
#define AMPJ 97
#define AMPX 0
#define AMPB 0
#define ROT1 1
#define ROT2 0
#define STR2(x) #x
#define STR(x) STR2(x)
#define W1 8
#define W2 4
#define ORD 0
#define CODEPF 1
#define WARM2 1
#define CODE_KB 36

constexpr unsigned WS_EPOCH = 0;
constexpr unsigned FL_E = 4096;
constexpr unsigned FL_L = FL_E + 128 * 128;
constexpr unsigned FL_K = FL_L + 128 * 128;
constexpr unsigned FL_O = FL_K + 128 * 128;
constexpr unsigned WS_ST = 73728;
constexpr unsigned WS_KVP = 131072;
constexpr unsigned WS_ZP = WS_KVP + 524288;
constexpr unsigned WS_HB = 1048576;
constexpr unsigned HB_BYTES = 64u * 144u * 1024u;
constexpr unsigned WS_STAMP = 16u << 20;

constexpr unsigned SLOT = 32768;
constexpr unsigned L_MISC = 131072;
constexpr unsigned L_STATS = L_MISC;
constexpr unsigned L_CS0 = L_MISC + 1024;
constexpr unsigned L_CS1 = L_MISC + 1536;
constexpr unsigned L_B1 = L_MISC + 2048;
constexpr unsigned L_WPHI = L_MISC + 3072;
constexpr unsigned L_CNT = L_MISC + 5120;
constexpr unsigned L_TAG = L_MISC + 5248;
constexpr unsigned L_ZT = L_MISC + 5376;
constexpr unsigned L_AFR = L_MISC + 8192;
constexpr unsigned LDS_BYTES = 163840;
enum { C_CS1 = 0, C_LATE = 1, C_KV = 2, C_O = 3, C_AFR = 4, C_KVRDY = 5, C_LATERDY = 6, C_ORDY = 7, C_FREE = 8  , C_LAND = 12  , C_EARLY = 16, C_IMG = 17 };

constexpr unsigned kSpin = 1u << 16;

struct Params {
  const u16* x; const u16* wqkv; const u16* wphi; const u16* wo;
  const u16* w1; const u16* b1; const u16* w2; const u16* b2;
  u16* out; char* ws;
};

DI unsigned f2u(float f) { return __builtin_bit_cast(unsigned, f); }
DI float u2f(unsigned u) { return __builtin_bit_cast(float, u); }
DI float fmx(float a, float b) { return __builtin_fmaxf(a, b); }
DI f32x16 mfma(v4u a, v4u b, f32x16 c) {
  return __builtin_amdgcn_mfma_f32_32x32x16_bf16(__builtin_bit_cast(bf16x8, a), __builtin_bit_cast(bf16x8, b), c, 0, 0, 0);
}
DI f32x16 zero16() { f32x16 z; for (int r = 0; r < 16; ++r) z[r] = 0.f; return z; }
DI v4u lds128(unsigned a) { return *(const LAS v4u*)(size_t)a; }
DI f32x4 lds128f(unsigned a) { return *(const LAS f32x4*)(size_t)a; }
DI v2u tr64(unsigned a) { return __builtin_bit_cast(v2u, __builtin_amdgcn_ds_read_tr16_b64_v4i16((LAS s16x4*)(size_t)a)); }
DI v4u tr2(unsigned a0, unsigned a1) { v2u l = tr64(a0), h = tr64(a1); return v4u{l.x, l.y, h.x, h.y}; }
DI unsigned pk2(float a, float b) { return __builtin_bit_cast(unsigned, __builtin_convertvector(f32x2{a, b}, bf16x2)); }
DI float xhalf_add(float v) { auto r = __builtin_amdgcn_permlane32_swap(f2u(v), f2u(v), false, false); return u2f(r[0]) + u2f(r[1]); }
DI float xhalf_max(float v) { auto r = __builtin_amdgcn_permlane32_swap(f2u(v), f2u(v), false, false); return fmx(u2f(r[0]), u2f(r[1])); }
DI float shx(float v, int m, int lane) { return u2f((unsigned)__builtin_amdgcn_ds_bpermute((lane ^ m) << 2, (int)f2u(v))); }
DI f32x4 lanesum(v4u f, f32x4 c) {
  const s16x4 one = {0x3F80, 0x3F80, 0x3F80, 0x3F80};
  c = __builtin_amdgcn_mfma_f32_4x4x4bf16_1k(one, __builtin_bit_cast(s16x4, v2u{f.x, f.y}), c, 0, 0, 0);
  return __builtin_amdgcn_mfma_f32_4x4x4bf16_1k(one, __builtin_bit_cast(s16x4, v2u{f.z, f.w}), c, 0, 0, 0);
}
template <int CTRL> DI float dppadd(float v) { return v + u2f((unsigned)__builtin_amdgcn_update_dpp(0, (int)f2u(v), CTRL, 0xf, 0xf, true)); }
DI float wavesum(float v) {
  v = dppadd<0xB1>(v); v = dppadd<0x4E>(v); v = dppadd<0x141>(v); v = dppadd<0x140>(v);
  auto r = __builtin_amdgcn_permlane16_swap(f2u(v), f2u(v), false, false);
  return xhalf_add(u2f(r[0]) + u2f(r[1]));
}
DI void dma(unsigned m0v, unsigned voff, __amdgpu_buffer_rsrc_t rs, unsigned soff) {
  asm volatile("s_mov_b32 m0, %0\n\ts_nop 0\n\tbuffer_load_dwordx4 %1, %2, %3 offen lds" ::"s"(m0v), "v"(voff), "s"(rs), "s"(soff) : "memory");
}
DI void st128(v4u v, __amdgpu_buffer_rsrc_t rs, unsigned voff) { __builtin_amdgcn_raw_buffer_store_b128(v, rs, voff, 0, 16); }
#define BAR() do { __builtin_amdgcn_sched_barrier(0); asm volatile("s_waitcnt lgkmcnt(0)\n\ts_barrier" ::: "memory"); __builtin_amdgcn_sched_barrier(0); } while (0)
#define VMW(n) asm volatile("s_waitcnt vmcnt(" #n ")" ::: "memory")
constexpr unsigned ONES = 0x3F803F80u;

#define STAMP(i) do { if ((i) == AMPI) amp_a = __builtin_amdgcn_s_memrealtime(); if ((i) == AMPJ) amp_b = __builtin_amdgcn_s_memrealtime(); } while (0)

DI void poll_flags(const char* ws, unsigned base, int n, u64 tag, int lane) {
  const GAS u64* f = (const GAS u64*)(ws + base + (unsigned)(lane < n ? lane : 0) * 128u);
#pragma unroll 1
  for (unsigned spin = 0; spin < kSpin; ++spin) {
    const u64 v = __hip_atomic_load(f, __ATOMIC_RELAXED, __HIP_MEMORY_SCOPE_AGENT);
    if (__builtin_amdgcn_ballot_w64(v != tag) == 0) break;
    __builtin_amdgcn_s_sleep(1);
  }
  __builtin_amdgcn_fence(__ATOMIC_ACQUIRE, "wavefront");
  asm volatile("" ::: "memory");
}
DI void lds_wait_ge(unsigned addr, unsigned want) {
#pragma unroll 1
  for (unsigned spin = 0; spin < (kSpin << 4); ++spin) {
    const unsigned v = *(volatile LAS unsigned*)(size_t)addr;
    if ((unsigned)__builtin_amdgcn_readfirstlane(v) >= want) break;
    __builtin_amdgcn_s_sleep(1);
  }
  asm volatile("" ::: "memory");
}
DI void lds_set(unsigned addr, unsigned v) { *(volatile LAS unsigned*)(size_t)addr = v; }
DI void lds_inc(unsigned addr, int lane) { if (lane == 0) __hip_atomic_fetch_add((LAS unsigned*)(size_t)addr, 1u, __ATOMIC_RELAXED, __HIP_MEMORY_SCOPE_WORKGROUP); }
DI void publish_last(unsigned cnt_addr, unsigned n, char* ws, unsigned flag_off, u64 tag, int lane) {
  VMW(0);
  if (lane == 0) {
    const unsigned old = __hip_atomic_fetch_add((LAS unsigned*)(size_t)cnt_addr, 1u, __ATOMIC_RELAXED, __HIP_MEMORY_SCOPE_WORKGROUP);
    if (old == n - 1) __hip_atomic_store((GAS u64*)(ws + flag_off), tag, __ATOMIC_RELAXED, __HIP_MEMORY_SCOPE_AGENT);
  }
}
DI u64 lds_tag() { const v2u t = *(const LAS v2u*)(size_t)L_TAG; return ((u64)(unsigned)__builtin_amdgcn_readfirstlane(t.y) << 32) | (unsigned)__builtin_amdgcn_readfirstlane(t.x); }

DI void fold8(const f32x16& acc, int r0, float rstd, float mr, unsigned cs, int hh, float* y) {
#pragma unroll
  for (int g = 0; g < 2; ++g) {
    const f32x4 t = lds128f(cs + (unsigned)((r0 >> 2) + g) * 32u + (unsigned)hh * 16u) * mr;
#pragma unroll
    for (int k = 0; k < 4; ++k) y[4 * g + k] = __builtin_fmaf(acc[r0 + 4 * g + k], rstd, t[k]);
  }
}
DI void load16(unsigned a, int hh, f32x4* v) {
#pragma unroll
  for (int g = 0; g < 4; ++g) v[g] = lds128f(a + (unsigned)g * 32u + (unsigned)hh * 16u);
}
DI void ffn_tile(const f32x16& acc, float rstd, float mr, const f32x4* cs, const f32x4* bs, __amdgpu_buffer_rsrc_t rsHB, unsigned voff) {
  f32x2 y[8], d[8];
#pragma unroll
  for (int g = 0; g < 4; ++g) {
    const f32x4 t = cs[g] * mr + bs[g];
    y[2 * g] = f32x2{acc[4 * g], acc[4 * g + 1]} * rstd + f32x2{t[0], t[1]};
    y[2 * g + 1] = f32x2{acc[4 * g + 2], acc[4 * g + 3]} * rstd + f32x2{t[2], t[3]};
  }
#pragma unroll
  for (int k = 0; k < 8; ++k) d[k] = y[k] * ((y[k] * y[k]) * (-0.10294324f) + (-2.30220819f));
#pragma unroll
  for (int k = 0; k < 8; ++k) { d[k].x = __builtin_amdgcn_exp2f(d[k].x); d[k].y = __builtin_amdgcn_exp2f(d[k].y); }
#pragma unroll
  for (int k = 0; k < 8; ++k) d[k] += 1.0f;
#pragma unroll
  for (int k = 0; k < 8; ++k) { d[k].x = __builtin_amdgcn_rcpf(d[k].x); d[k].y = __builtin_amdgcn_rcpf(d[k].y); }
  v4u o0, o1;
#pragma unroll
  for (int k = 0; k < 4; ++k) {
    const f32x2 g0 = y[k] * d[k], g1 = y[4 + k] * d[4 + k];
    o0[k] = pk2(g0.x, g0.y); o1[k] = pk2(g1.x, g1.y);
  }
  st128(o0, rsHB, voff); st128(o1, rsHB, voff + 1024u);
}
DI void hedge(const f32x16& d, v4u& o0, v4u& o1) {
  constexpr float L = 1.4426950408889634f;
  float m0 = fmx(fmx(fmx(d[0], d[1]), fmx(d[2], d[3])), fmx(fmx(d[4], d[5]), fmx(d[6], d[7])));
  float m1 = fmx(fmx(fmx(d[8], d[9]), fmx(d[10], d[11])), fmx(fmx(d[12], d[13]), fmx(d[14], d[15])));
  m0 = xhalf_max(m0) * -L; m1 = xhalf_max(m1) * -L;
  float e[16];
#pragma unroll
  for (int i = 0; i < 8; ++i) { e[i] = __builtin_amdgcn_exp2f(__builtin_fmaf(d[i], L, m0)); e[8 + i] = __builtin_amdgcn_exp2f(__builtin_fmaf(d[8 + i], L, m1)); }
  float s0 = ((e[0] + e[1]) + (e[2] + e[3])) + ((e[4] + e[5]) + (e[6] + e[7]));
  float s1 = ((e[8] + e[9]) + (e[10] + e[11])) + ((e[12] + e[13]) + (e[14] + e[15]));
  s0 = __builtin_amdgcn_rcpf(xhalf_add(s0)); s1 = __builtin_amdgcn_rcpf(xhalf_add(s1));
#pragma unroll
  for (int k = 0; k < 4; ++k) { o0[k] = pk2(e[2 * k] * s0, e[2 * k + 1] * s0); o1[k] = pk2(e[8 + 2 * k] * s1, e[8 + 2 * k + 1] * s1); }
}
DI v4u wphi_frag(int hd, int lane) {
  const int hh = lane >> 5, q = (lane >> 2) & 3, pp = lane & 3;
  const unsigned a = L_WPHI + (unsigned)hd * 512u + (unsigned)(4 * hh + q) * 32u + (unsigned)pp * 8u;
  v4u f = tr2(a, a + 256u);
  const unsigned sg = (lane & 16) ? 0x80008000u : 0u;
  f.x ^= sg; f.y ^= sg; f.z ^= sg; f.w ^= sg;
  return f;
}
DI v4u selv(bool s, v4u a, v4u b) { const unsigned m = s ? 0xffffffffu : 0u; return v4u{(a.x & m) | (b.x & ~m), (a.y & m) | (b.y & ~m), (a.z & m) | (b.z & ~m), (a.w & m) | (b.w & ~m)}; }
DI v4u pack8(const float* y) { return v4u{pk2(y[0], y[1]), pk2(y[2], y[3]), pk2(y[4], y[5]), pk2(y[6], y[7])}; }

template <int NI> DI void warm_w2(const Params& P, int ct, int ja, int tq, int i0, int lane, v4u* d) {
#pragma unroll
  for (int i = 0; i < NI; ++i) {
    const int row = 288 * ja + 72 * tq + 8 * (i0 + i);
    const u64 a = row < 1792 ? (u64)(size_t)P.w2 + (u64)(unsigned)row * 1024u : (u64)(size_t)P.wo + (u64)(unsigned)(row - 1792) * 1024u;
    const u64 base = ((u64)(unsigned)__builtin_amdgcn_readfirstlane((unsigned)(a >> 32)) << 32) | (unsigned)__builtin_amdgcn_readfirstlane((unsigned)a);
    const unsigned voff = (unsigned)(lane >> 3) * 1024u + (unsigned)(lane & 7) * 16u + (unsigned)ct * 128u;
    asm volatile("s_nop 4\n\tglobal_load_dwordx4 %0, %1, %2" : "=&v"(d[i]) : "v"(voff), "s"(base) : "memory");
  }
}
struct Ctx {
  int lane, tq, hh;
  int p, gi, G;
  int rot;
  int tt, ct, ja;
  int rotE, rotL, rotO;
  bool ffn;
};

DI void role_a(const Params& P, const Ctx& c, int wid, u64 amp_a, u64 amp_b) {
  const int lane = c.lane, tq = c.tq, hh = c.hh, t = lane & 31;
  const int cb = (lane >> 4) & 1, q = (lane >> 2) & 3, pp = lane & 3, sw = (q >> 1) & 1;
  const int th = tq & 1, up = tq >> 1;
  unsigned xa[4];
#pragma unroll
  for (int ks = 0; ks < 4; ++ks) xa[ks] = (unsigned)(64 * th + t) * 128u + (unsigned)(((2 * ks + hh) ^ ((t >> 1) & 7)) * 16);
  const unsigned wl = 16384u + (unsigned)(8 * hh + q) * 128u + (unsigned)((4 * (up ^ sw) + 2 * cb + (pp >> 1)) * 16 + 8 * (pp & 1));
  f32x16 acc[2][2];
#pragma unroll
  for (int i = 0; i < 2; ++i) { acc[i][0] = zero16(); acc[i][1] = zero16(); }
  f32x4 cs = {0.f, 0.f, 0.f, 0.f};
  unsigned x0 = xa[0], x1 = xa[1], x2 = xa[2], x3 = xa[3], w0 = wl;
  v4u xf[4][2], wf[4][2];
  auto rd = [&](int ks, v4u* X, v4u* W) AI {
    const unsigned xb = ks == 0 ? x0 : (ks == 1 ? x1 : (ks == 2 ? x2 : x3));
    X[0] = lds128(xb); X[1] = lds128(xb + 4096u);
    const unsigned a = w0 + (unsigned)(ks * 2048);
    W[0] = tr2(a, a + 512u); W[1] = tr2(a + 8192u, a + 8704u);
  };
  BAR();
  rd(0, xf[0], wf[0]); rd(1, xf[1], wf[1]);
#pragma unroll 1
  for (int kc = 0; kc < 8; ++kc) {
#pragma unroll
    for (int ks = 0; ks < 4; ++ks) {
      if (ks == 2) {
        __builtin_amdgcn_sched_barrier(0);
        asm volatile("s_barrier" ::: "memory");
        __builtin_amdgcn_sched_barrier(0);
        const unsigned sb = (unsigned)((kc + 1) & 3) * SLOT;
        x0 = xa[0] + sb; x1 = xa[1] + sb; w0 = wl + sb;
        asm volatile("" : "+v"(x0), "+v"(x1), "+v"(w0));
      }
      if (ks == 0) {
        const unsigned sb = (unsigned)(kc & 3) * SLOT;
        x2 = xa[2] + sb; x3 = xa[3] + sb;
        asm volatile("" : "+v"(x2), "+v"(x3));
      }
      rd((ks + 2) & 3, xf[(ks + 2) & 3], wf[(ks + 2) & 3]);
#pragma unroll
      for (int i = 0; i < 2; ++i) { acc[i][0] = mfma(wf[ks][i], xf[ks][0], acc[i][0]); acc[i][1] = mfma(wf[ks][i], xf[ks][1], acc[i][1]); }
      cs = lanesum(selv(th != 0, wf[ks][1], wf[ks][0]), cs);
      __builtin_amdgcn_sched_group_barrier(0x008, 1, 0);
      __builtin_amdgcn_sched_group_barrier(0x100, 2, 0);
      __builtin_amdgcn_sched_group_barrier(0x008, 1, 0);
      __builtin_amdgcn_sched_group_barrier(0x100, 2, 0);
      __builtin_amdgcn_sched_group_barrier(0x008, 1, 0);
      __builtin_amdgcn_sched_group_barrier(0x100, 2, 0);
      __builtin_amdgcn_sched_group_barrier(0x002, 4, 0);
      __builtin_amdgcn_sched_group_barrier(0x008, 3, 0);
      __builtin_amdgcn_sched_barrier(0);
    }
    asm volatile("" : "+v"(acc[0][0]), "+v"(acc[0][1]), "+v"(acc[1][0]), "+v"(acc[1][1]), "+v"(cs));
  }
  STAMP(2);
  {
    const float s = xhalf_add(cs[0]);
    if (hh == 0) *(LAS float*)(size_t)(L_CS0 + (unsigned)(up + 2 * th) * 128u + (unsigned)t * 4u) = s;
  }
  BAR();
  const u64 tag = lds_tag();
  float rstd[2], mr[2];
#pragma unroll
  for (int b = 0; b < 2; ++b) {
    const f32x2 st = *(const LAS f32x2*)(size_t)(L_STATS + (unsigned)(64 * th + 32 * b + t) * 8u);
    rstd[b] = st.x; mr[b] = st.y;
  }
  const __amdgpu_buffer_rsrc_t rsHB = __builtin_amdgcn_make_buffer_rsrc(P.ws + WS_HB, 0, HB_BYTES, 0x00020000);
  v4u wm[9];
  if (c.ffn) {
    f32x4 csv[2][4], bsv[2][4];
#pragma unroll
    for (int i = 0; i < 2; ++i) { load16(L_CS0 + (unsigned)(up + 2 * i) * 128u, hh, csv[i]); load16(L_B1 + (unsigned)(up + 2 * i) * 128u, hh, bsv[i]); }
#pragma unroll
    for (int i = 0; i < 2; ++i)
#pragma unroll
      for (int b = 0; b < 2; ++b) {
        const unsigned tile = (unsigned)(up + 2 * i), T32 = (unsigned)(c.p * 4 + 2 * th + b);
        ffn_tile(acc[i][b], rstd[b], mr[b], csv[i], bsv[i], rsHB, (unsigned)lane * 16u + (T32 * 144u + (unsigned)(8 * c.gi) + 2u * tile) * 1024u);
        if (2 * i + b < 3) BAR();
        if (WARM2 && 2 * i + b < 3) warm_w2<3>(P, c.ct, c.ja, tq, 3 * (2 * i + b), lane, wm + 3 * (2 * i + b));
      }
    STAMP(8);
    publish_last(L_CNT + C_EARLY * 4, 4, P.ws, FL_E + (unsigned)(c.p * 8 + c.gi) * 128u, tag, lane);
    if (WARM2) asm volatile("" :: "v"(wm[0]), "v"(wm[1]), "v"(wm[2]), "v"(wm[3]), "v"(wm[4]), "v"(wm[5]), "v"(wm[6]), "v"(wm[7]), "v"(wm[8]));
    BAR();
  } else {
#pragma unroll
    for (int b = 0; b < 2; ++b)
#pragma unroll
      for (int hf = 0; hf < 2; ++hf) {
        const int hd = 2 * up + hf;
        float y[8];
        fold8(acc[0][b], 8 * hf, rstd[b], mr[b], L_CS0 + (unsigned)up * 128u, hh, y);
        v4u o0, o1;
        hedge(mfma(wphi_frag(hd, lane), pack8(y), zero16()), o0, o1);
        const unsigned img = (unsigned)hd * SLOT + 16384u;
        const unsigned row = (unsigned)(64 * th + 32 * b + t);
        const unsigned g = (row >> 1) & 7u;
        *(LAS v2u*)(size_t)(img + row * 64u + (((unsigned)(0 + hh)) ^ g) * 8u) = v2u{o0.x, o0.y};
        *(LAS v2u*)(size_t)(img + row * 64u + (((unsigned)(2 + hh)) ^ g) * 8u) = v2u{o0.z, o0.w};
        *(LAS v2u*)(size_t)(img + row * 64u + (((unsigned)(4 + hh)) ^ g) * 8u) = v2u{o1.x, o1.y};
        *(LAS v2u*)(size_t)(img + row * 64u + (((unsigned)(6 + hh)) ^ g) * 8u) = v2u{o1.z, o1.w};
        fold8(acc[1][b], 8 * hf, rstd[b], mr[b], L_CS0 + (unsigned)(2 + up) * 128u, hh, y);
        const unsigned gv = (row >> 2) & 3u;
        *(LAS v2u*)(size_t)(img + 8192u + row * 32u + (((unsigned)(0 + hh)) ^ gv) * 8u) = v2u{pk2(y[0], y[1]), pk2(y[2], y[3])};
        *(LAS v2u*)(size_t)(img + 8192u + row * 32u + (((unsigned)(2 + hh)) ^ gv) * 8u) = v2u{pk2(y[4], y[5]), pk2(y[6], y[7])};
        if (2 * b + hf < 3) BAR();
        if (WARM2 && 2 * b + hf < 3) warm_w2<3>(P, c.ct, c.ja, tq, 3 * (2 * b + hf), lane, wm + 3 * (2 * b + hf));
      }
    STAMP(8);
    BAR();
    const unsigned img = (unsigned)tq * SLOT + 16384u;
    f32x16 kv = zero16();
    unsigned pa[2], va[2];
#pragma unroll
    for (int i2 = 0; i2 < 2; ++i2) {
      const unsigned r = (unsigned)(8 * hh + 4 * i2 + q);
      pa[i2] = img + r * 64u + (((unsigned)(4 * cb + pp)) ^ ((r >> 1) & 7u)) * 8u;
      va[i2] = img + 8192u + r * 32u + (((unsigned)pp) ^ ((r >> 2) & 3u)) * 8u;
    }
#pragma unroll
    for (int ks = 0; ks < 8; ++ks) {
      const v4u af = tr2(pa[0] + (unsigned)ks * 1024u, pa[1] + (unsigned)ks * 1024u);
      v4u bf = tr2(va[0] + (unsigned)ks * 512u, va[1] + (unsigned)ks * 512u);
      if (cb) bf = v4u{ONES, ONES, ONES, ONES};
      kv = mfma(af, bf, kv);
    }
    const unsigned hidx = (unsigned)(c.gi * 4 + tq) * 16u + (unsigned)c.p;
    const __amdgpu_buffer_rsrc_t rsKV = __builtin_amdgcn_make_buffer_rsrc(P.ws + WS_KVP, 0, 524288 + 65536, 0x00020000);
    if (cb == 0) {
#pragma unroll
      for (int s = 0; s < 2; ++s) {
        const v4u o = v4u{pk2(kv[8 * s], kv[8 * s + 1]), pk2(kv[8 * s + 2], kv[8 * s + 3]), pk2(kv[8 * s + 4], kv[8 * s + 5]), pk2(kv[8 * s + 6], kv[8 * s + 7])};
        st128(o, rsKV, (unsigned)((16 * s + (lane & 15) + 32 * hh) * 16) + hidx * 1024u);
      }
    } else if ((lane & 15) == 0) {
#pragma unroll
      for (int g4 = 0; g4 < 4; ++g4) {
        const v4u o = v4u{f2u(kv[4 * g4]), f2u(kv[4 * g4 + 1]), f2u(kv[4 * g4 + 2]), f2u(kv[4 * g4 + 3])};
        st128(o, rsKV, (unsigned)((8 * g4 + 4 * hh) * 4) + 524288u + hidx * 128u);
      }
    }
    publish_last(L_CNT + C_KV * 4, 4, P.ws, FL_K + (unsigned)(c.gi * 16 + c.p) * 128u, tag, lane);
    if (WARM2) asm volatile("" :: "v"(wm[0]), "v"(wm[1]), "v"(wm[2]), "v"(wm[3]), "v"(wm[4]), "v"(wm[5]), "v"(wm[6]), "v"(wm[7]), "v"(wm[8]));
  }
  STAMP(3);
  unsigned tabB, tabWlo, tabWhi;
  {
    const int jj = lane < 36 ? lane : 35;
    constexpr int J2 = ORD ? 24 : 28;
    int sg;
    if (jj < 16) sg = 4 * ((jj + c.rotE) & 15) + tq;
    else if ((jj < J2) != (ORD != 0)) sg = 64 + 4 * ((jj - (ORD ? 24 : 16) + c.rotL) % 12) + tq;
    else sg = 112 + 4 * ((jj - (ORD ? 16 : 28) + c.rotO) & 7) + tq;
    int row;
    if (sg < 64) { const int g = sg >> 3; row = 224 * g + 32 * (g & 1) + 16 * (sg & 7); }
    else if (sg < 112) {
      const int e = sg - 64, g = e / 6, i = e - 6 * g;
      row = i < 4 ? 224 * g + 32 * (g & 1) + 128 + 16 * i : ((g & 1) ? 224 * g : 224 * g + 192) + 16 * (i - 4);
    } else row = 16 * (sg - 112);
    const u64 a = (u64)(size_t)(sg < 112 ? P.w2 : P.wo) + (u64)row * 1024u + (u64)c.ct * 128u;
    tabB = (unsigned)sg * 1024u; tabWlo = (unsigned)a; tabWhi = (unsigned)(a >> 32);
  }
  const unsigned vw0 = (unsigned)(lane >> 3) * 1024u + (unsigned)(((lane & 7) ^ (((lane >> 4) & 1) << 2)) * 16), vw1 = vw0 + 8192u;
  const unsigned ring = (unsigned)tq * SLOT;
  auto wdma = [&](int j) AI {
    const unsigned lo = __builtin_amdgcn_readlane(tabWlo, j), hi = __builtin_amdgcn_readlane(tabWhi, j);
    const u64 b = ((u64)hi << 32) | lo;
    const unsigned m = ring + (unsigned)(j & 15) * 2048u;
    asm volatile("s_nop 4\n\ts_mov_b32 m0, %0\n\ts_nop 0\n\tglobal_load_lds_dwordx4 %1, %3\n\ts_add_u32 m0, m0, 0x400\n\ts_nop 0\n\tglobal_load_lds_dwordx4 %2, %3"
                 ::"s"(m), "v"(vw0), "v"(vw1), "s"(b) : "memory", "scc");
  };
  const unsigned vb0 = (unsigned)lane * 16u + (unsigned)(2 * c.tt) * 144u * 1024u, vb1 = vb0 + 144u * 1024u;
  constexpr int NB = 4;
  v4u br[NB][2];
#pragma unroll
  for (int u = 0; u < NB; ++u) { br[u][0] = v4u{0u, 0u, 0u, 0u}; br[u][1] = br[u][0]; }
  auto bload = [&](unsigned so, v4u& d0, v4u& d1) AI {
    asm volatile("s_nop 4\n\tbuffer_load_dwordx4 %0, %2, %4, %5 offen sc1\n\tbuffer_load_dwordx4 %1, %3, %4, %5 offen sc1"
                 : "+v"(d0), "+v"(d1) : "v"(vb0), "v"(vb1), "s"(rsHB), "s"(so) : "memory");
  };
  STAMP(9);
#pragma unroll 1
  for (int j = 0; j < 14; ++j) wdma(j);
  STAMP(4);
  const int pan = c.tt >> 1;
  poll_flags(P.ws, FL_E + (unsigned)pan * 8u * 128u, 8, tag, lane);
  STAMP(5);
  bload(__builtin_amdgcn_readlane(tabB, 0), br[0][0], br[0][1]);
  bload(__builtin_amdgcn_readlane(tabB, 1), br[1][0], br[1][1]);
  wdma(14);
  bload(__builtin_amdgcn_readlane(tabB, 2), br[2][0], br[2][1]);
  wdma(15);
  bload(__builtin_amdgcn_readlane(tabB, 3), br[3][0], br[3][1]);
  unsigned w2l[2];
#pragma unroll
  for (int h = 0; h < 2; ++h) w2l[h] = ring + (unsigned)(4 * hh + q) * 128u + (unsigned)((4 * (h ^ sw) + 2 * cb + (pp >> 1)) * 16 + 8 * (pp & 1));
  f32x16 o[2][2];
#pragma unroll
  for (int a = 0; a < 2; ++a)
#pragma unroll
    for (int b = 0; b < 2; ++b) o[a][b] = zero16();
  v4u af[2];
  asm volatile("s_waitcnt vmcnt(10)" : "+v"(br[0][0]), "+v"(br[0][1]) :: "memory");
  af[0] = tr2(w2l[0], w2l[0] + 1024u); af[1] = tr2(w2l[1], w2l[1] + 1024u);
  auto step = [&](int j, int u, bool refill, bool first) AI {
    v4u an[2];
    if (first) asm volatile("s_waitcnt vmcnt(" STR(W1) ")" : "+v"(br[(u + 1) % NB][0]), "+v"(br[(u + 1) % NB][1]) :: "memory");
    else asm volatile("s_waitcnt vmcnt(" STR(W2) ")" : "+v"(br[(u + 1) % NB][0]), "+v"(br[(u + 1) % NB][1]) :: "memory");
    unsigned p0 = w2l[0] + (unsigned)((j + 1) & 15) * 2048u, p1 = w2l[1] + (unsigned)((j + 1) & 15) * 2048u;
    asm volatile("" : "+v"(p0), "+v"(p1));
    an[0] = tr2(p0, p0 + 1024u); an[1] = tr2(p1, p1 + 1024u);
    o[0][0] = mfma(af[0], br[u][0], o[0][0]); o[0][1] = mfma(af[0], br[u][1], o[0][1]);
    o[1][0] = mfma(af[1], br[u][0], o[1][0]); o[1][1] = mfma(af[1], br[u][1], o[1][1]);
    asm volatile("" : "+v"(o[0][0]), "+v"(o[0][1]), "+v"(o[1][0]), "+v"(o[1][1]) :: "memory");
    if (refill) wdma(j + 16);
    const int jn = j + NB;
    if (u == 0) {
      if (jn == 16) lds_wait_ge(L_CNT + (ORD ? C_ORDY : C_LATERDY) * 4, 1);
      if (jn == (ORD ? 24 : 28)) lds_wait_ge(L_CNT + (ORD ? C_LATERDY : C_ORDY) * 4, 1);
    }
    bload(jn < 36 ? (unsigned)__builtin_amdgcn_readlane(tabB, jn) : 0x7F000000u, br[u][0], br[u][1]);
    af[0] = an[0]; af[1] = an[1];
  };
#pragma unroll 1
  for (int it = 0; it < 5; ++it) {
    if (it == 3) STAMP(10);
#pragma unroll
    for (int u = 0; u < NB; ++u) step(NB * it + u, u, true, true);
  }
  STAMP(11);
#pragma unroll 1
  for (int it = 5; it < 9; ++it) {
#pragma unroll
    for (int u = 0; u < NB; ++u) step(NB * it + u, u, false, false);
  }
  asm volatile("s_waitcnt vmcnt(0)" : "+v"(br[0][0]), "+v"(br[0][1]), "+v"(br[1][0]), "+v"(br[1][1]), "+v"(br[2][0]), "+v"(br[2][1]), "+v"(br[3][0]), "+v"(br[3][1]) :: "memory");
  STAMP(6);
#pragma unroll
  for (int a = 0; a < 2; ++a)
#pragma unroll
    for (int b = 0; b < 2; ++b)
#pragma unroll
      for (int g4 = 0; g4 < 4; ++g4)
        *(LAS f32x4*)(size_t)(ring + (unsigned)(32 * b + t) * 272u + (unsigned)(32 * a + 8 * g4 + 4 * hh) * 4u) =
            f32x4{o[a][b][4 * g4], o[a][b][4 * g4 + 1], o[a][b][4 * g4 + 2], o[a][b][4 * g4 + 3]};
  VMW(0);
  BAR();
  STAMP(7);
  if (wid == 0 && __builtin_amdgcn_workgroup_id_x() == AMPB) {
    const u64 d = (amp_b - amp_a) * AMPX, t1 = __builtin_amdgcn_s_memrealtime();
    while (__builtin_amdgcn_s_memrealtime() - t1 < d) __builtin_amdgcn_s_sleep(4);
  }
}

template <int NT, int NF> DI void pass1(const Params& P, int wid, u64& amp_a, u64& amp_b, const v4u (&xr)[32], f32x16 (&acc)[NT], f32x4& cs, const unsigned (&wb)[NT], int lane) {
#pragma unroll
  for (int qq = 0; qq < 4; ++qq) {
    if (qq == 1) { if (NF == 8) VMW(16); else VMW(8); }
    if (qq == 2) { if (NF == 8) VMW(8); else VMW(4); }
    if (qq == 3) VMW(0);
    if (qq > 0) BAR();
    unsigned a[NT];
#pragma unroll
    for (int i = 0; i < NT; ++i) { a[i] = wb[i] + (unsigned)qq * SLOT; asm volatile("" : "+v"(a[i])); }
    v4u f[3][NT];
    auto rd = [&](int k8, v4u* W) AI {
      const unsigned o = (unsigned)((k8 >> 2) * 8192 + (k8 & 3) * 2048);
#pragma unroll
      for (int i = 0; i < NT; ++i) W[i] = tr2(a[i] + o, a[i] + o + 512u);
    };
    rd(0, f[0]); rd(1, f[1]);
    __builtin_amdgcn_sched_barrier(0);
#pragma unroll
    for (int k8 = 0; k8 < 8; ++k8) {
      if (k8 < 6) rd(k8 + 2, f[(k8 + 2) % 3]);
      const v4u xf = xr[8 * qq + k8];
#pragma unroll
      for (int i = 0; i < NT; ++i) acc[i] = mfma(f[k8 % 3][i], xf, acc[i]);
      cs = lanesum(f[k8 % 3][0], cs);
      if (k8 < 6) {
#pragma unroll
        for (int i = 0; i < NT; ++i) {
          __builtin_amdgcn_sched_group_barrier(0x008, 1, 0);
          __builtin_amdgcn_sched_group_barrier(0x100, 2, 0);
        }
        __builtin_amdgcn_sched_group_barrier(0x008, 2, 0);
      }
      __builtin_amdgcn_sched_barrier(0);
    }
    asm volatile("" : "+v"(acc[0]), "+v"(acc[1]), "+v"(acc[NT - 1]), "+v"(cs) :: "memory");
  }
}

template <bool ffn> DI u64 role_b_head(const Params& P, const Ctx& c, int wid) {
  u64 amp_a = 0, amp_b = 0; (void)amp_a; (void)amp_b;
  const int lane = c.lane, tq = c.tq, hh = c.hh, t = lane & 31;
  const int g = c.gi;
  const unsigned tok = (unsigned)(c.p * 128 + 8 * c.G + 2 * tq);
  v4u r0, r1;
  {
    const u16* rp = P.x + (size_t)tok * 512 + lane * 8;
    asm volatile("global_load_dwordx4 %0, %2, off\n\tglobal_load_dwordx4 %1, %2, off offset:1024" : "=&v"(r0), "=&v"(r1) : "v"(rp) : "memory");
  }
  const __amdgpu_buffer_rsrc_t rsX = __builtin_amdgcn_make_buffer_rsrc((void*)P.x, 0, 2048 * 1024, 0x00020000);
  const __amdgpu_buffer_rsrc_t rsW = __builtin_amdgcn_make_buffer_rsrc((void*)(ffn ? P.w1 : P.wqkv), 0, ffn ? 512 * 3584 : 512 * 3072, 0x00020000);
  constexpr unsigned RS = ffn ? 3584u : 3072u;
  const unsigned cA = (unsigned)(224 * g + 32 * (g & 1));
  const unsigned u0 = ffn ? cA * 2u : (unsigned)(512 + 64 * g) * 2u, u1 = ffn ? cA * 2u + 128u : (unsigned)(1024 + 64 * g) * 2u;
  const unsigned ua = ffn ? (cA + 128u) * 2u : (unsigned)(64 * g) * 2u;
  const unsigned ub = (unsigned)((g & 1) ? 224 * g - 32 : 224 * g + 192) * 2u;
  const unsigned voffX = (unsigned)(lane >> 3) * 1024u + (unsigned)(((lane & 7) ^ ((4 * (tq & 1) + (lane >> 4)) & 7)) * 16);
  const unsigned voffW = (unsigned)(lane >> 3) * RS + (unsigned)(((lane & 7) ^ (((lane >> 4) & 1) << 2)) * 16);
  const unsigned soffX = (unsigned)(c.p * 128 + 8 * tq) * 1024u;
  const unsigned wrow = 32u * (unsigned)(tq & 1) * RS;
  auto fill = [&](int f) AI {
    const unsigned slot = (unsigned)(f & 3) * SLOT;
    if (f < 8) {
      const unsigned kcp = (unsigned)((f + c.rot) & 7);
      unsigned sx = soffX + kcp * 128u, sw = 64u * kcp * RS + wrow + ((tq >> 1) ? u1 : u0);
      unsigned mx = slot + (unsigned)tq * 1024u, mw = slot + 16384u + (unsigned)tq * 4096u;
#pragma unroll 1
      for (int m = 0; m < 4; ++m) {
        dma(mx, voffX, rsX, sx); dma(mw, voffW, rsW, sw);
        mx += 4096u; sx += 32768u; mw += 1024u; sw += 8u * RS;
      }
    } else {
      const unsigned kcp = (unsigned)((2 * (f - 8) + (tq >> 1) + c.rot) & 7);
      unsigned sw = 64u * kcp * RS + wrow, mw = slot + (unsigned)tq * 4096u;
#pragma unroll 1
      for (int m = 0; m < 4; ++m) {
        dma(mw, voffW, rsW, sw + ua);
        if (ffn) dma(mw + 16384u, voffW, rsW, sw + ub);
        mw += 1024u; sw += 8u * RS;
      }
    }
  };
  fill(0); fill(1);
  float st0;
  {
    asm volatile("s_waitcnt vmcnt(16)" : "+v"(r0), "+v"(r1) :: "memory");
    float v[2][8], mean[2], rs[2];
#pragma unroll
    for (int k = 0; k < 4; ++k) {
      v[0][2 * k] = u2f(r0[k] << 16); v[0][2 * k + 1] = u2f(r0[k] & 0xffff0000u);
      v[1][2 * k] = u2f(r1[k] << 16); v[1][2 * k + 1] = u2f(r1[k] & 0xffff0000u);
    }
#pragma unroll
    for (int i = 0; i < 2; ++i) mean[i] = wavesum(((v[i][0] + v[i][1]) + (v[i][2] + v[i][3])) + ((v[i][4] + v[i][5]) + (v[i][6] + v[i][7]))) * (1.0f / 512.0f);
#pragma unroll
    for (int i = 0; i < 2; ++i) {
      float q2 = 0.f;
#pragma unroll
      for (int k = 0; k < 8; ++k) { const float d = v[i][k] - mean[i]; q2 = __builtin_fmaf(d, d, q2); }
      rs[i] = __builtin_amdgcn_rsqf(wavesum(q2) * (1.0f / 512.0f) + 1e-5f);
    }
    st0 = (lane & 2) ? rs[1] : rs[0];
    st0 = (lane & 1) ? -((lane & 2) ? mean[1] : mean[0]) * st0 : st0;
    asm volatile("" : "+v"(st0));
  }
  unsigned xa[4];
#pragma unroll
  for (int ks = 0; ks < 4; ++ks) xa[ks] = (unsigned)(32 * tq + t) * 128u + (unsigned)(((2 * ks + hh) ^ ((t >> 1) & 7)) * 16);
  v4u xr[32];
  u64 tag = 0;
  v4u gr;
  const unsigned token = (unsigned)(c.p * 128 + 32 * tq + t);
#pragma unroll
  for (int kc = 0; kc < 8; ++kc) {
    if (kc < 7 || ffn) VMW(8); else VMW(4);
    BAR();
    if (kc == 0) {
      tag = lds_tag();
      if (lane < 4)
        __hip_atomic_store((GAS u64*)(P.ws + WS_ST + tok * 16u + (unsigned)lane * 8u), ((u64)(unsigned)tag << 32) | f2u(st0), __ATOMIC_RELAXED, __HIP_MEMORY_SCOPE_AGENT);
    }
    if (kc == 5) {
      const char* gp = P.ws + WS_ST + token * 16u;
      asm volatile("global_load_dwordx4 %0, %1, off sc1" : "=&v"(gr) : "v"(gp) : "memory");
    }
    fill(kc + 2);
    const unsigned sb = (unsigned)(kc & 3) * SLOT;
#pragma unroll
    for (int ks = 0; ks < 4; ++ks) xr[4 * kc + ks] = lds128(xa[ks] + sb);
    asm volatile("" : "+v"(xr[4 * kc]), "+v"(xr[4 * kc + 1]), "+v"(xr[4 * kc + 2]), "+v"(xr[4 * kc + 3]));
  }
  asm volatile("" : "+v"(gr));
  STAMP(2);
  BAR();
  fill(10); fill(11);
  if (__builtin_amdgcn_ballot_w64(gr.y != (unsigned)tag || gr.w != (unsigned)tag) != 0) {
    const __amdgpu_buffer_rsrc_t rs = __builtin_amdgcn_make_buffer_rsrc((void*)(P.ws + WS_ST), 0, 32768, 0x00020000);
#pragma unroll 1
    for (unsigned spin = 0; spin < kSpin; ++spin) {
      gr = __builtin_amdgcn_raw_buffer_load_b128(rs, token * 16u, 0, 16);
      if (__builtin_amdgcn_ballot_w64(gr.y != (unsigned)tag || gr.w != (unsigned)tag) == 0) break;
      __builtin_amdgcn_s_sleep(1);
    }
  }
  const float rstd = u2f(gr.x), mr = u2f(gr.z);
  if (hh == 0) *(LAS f32x2*)(size_t)(L_STATS + (unsigned)(32 * tq + t) * 8u) = f32x2{rstd, mr};
  if (ffn) VMW(24); else VMW(12);
  BAR();
  const int cb = (lane >> 4) & 1, q = (lane >> 2) & 3, pp = lane & 3, sw = (q >> 1) & 1;
  unsigned wl[2];
#pragma unroll
  for (int h = 0; h < 2; ++h) wl[h] = (unsigned)(8 * hh + q) * 128u + (unsigned)((4 * (h ^ sw) + 2 * cb + (pp >> 1)) * 16 + 8 * (pp & 1));
  const __amdgpu_buffer_rsrc_t rsHB = __builtin_amdgcn_make_buffer_rsrc(P.ws + WS_HB, 0, HB_BYTES, 0x00020000);
  const unsigned T32 = (unsigned)(c.p * 4 + tq);
  f32x4 cs = {0.f, 0.f, 0.f, 0.f};
  auto cs_sync = [&](int tile, bool wr) AI {
    const float s = xhalf_add(cs[0]);
    if (hh == 0 && wr) *(LAS float*)(size_t)(L_CS1 + (unsigned)tile * 128u + (unsigned)t * 4u) = s;
    BAR();
  };
  if constexpr (ffn) {
    const unsigned w2a = ((g & 1) ? wl[1] : wl[0]) + 16384u;
    int ti[3];
    unsigned wb[3];
    f32x16 acc[3];
#pragma unroll
    for (int i = 0; i < 3; ++i) { ti[i] = (i + tq) % 3; wb[i] = ti[i] == 0 ? wl[0] : (ti[i] == 1 ? wl[1] : w2a); acc[i] = zero16(); }
    pass1<3, 8>(P, wid, amp_a, amp_b, xr, acc, cs, wb, lane);
    cs_sync(ti[0], tq < 3);
    STAMP(3);
    f32x4 csv[3][4], bsv[3][4];
#pragma unroll
    for (int i = 0; i < 3; ++i) { load16(L_CS1 + (unsigned)ti[i] * 128u, hh, csv[i]); load16(L_B1 + (unsigned)(4 + ti[i]) * 128u, hh, bsv[i]); }
#pragma unroll
    for (int i = 0; i < 3; ++i)
      ffn_tile(acc[i], rstd, mr, csv[i], bsv[i], rsHB, (unsigned)lane * 16u + (T32 * 144u + (unsigned)(64 + 6 * g + 2 * ti[i])) * 1024u);
    publish_last(L_CNT + C_LATE * 4, 4, P.ws, FL_L + (unsigned)(c.p * 8 + g) * 128u, tag, lane);
  } else {
    int ti[2];
    unsigned wb[2];
    f32x16 acc[2];
#pragma unroll
    for (int i = 0; i < 2; ++i) { ti[i] = (i + tq) & 1; wb[i] = ti[i] == 0 ? wl[0] : wl[1]; acc[i] = zero16(); }
    pass1<2, 4>(P, wid, amp_a, amp_b, xr, acc, cs, wb, lane);
    cs_sync(ti[0], tq < 2);
    STAMP(3);
    v4u pq[4][2];
#pragma unroll
    for (int hd = 0; hd < 4; ++hd) {
      float y[8];
      fold8(acc[hd >> 1], 8 * (hd & 1), rstd, mr, L_CS1 + (unsigned)ti[hd >> 1] * 128u, hh, y);
      hedge(mfma(wphi_frag(2 * ti[hd >> 1] + (hd & 1), lane), pack8(y), zero16()), pq[hd][0], pq[hd][1]);
    }
    STAMP(4);
    if (tq == 0) { poll_flags(P.ws, FL_K + (unsigned)g * 16u * 128u, 16, tag, lane); lds_set(L_CNT + C_KVRDY * 4, 1); }
    else lds_wait_ge(L_CNT + C_KVRDY * 4, 1);
    STAMP(5);
    {
      const __amdgpu_buffer_rsrc_t rsKV = __builtin_amdgcn_make_buffer_rsrc(P.ws + WS_KVP, 0, 524288 + 65536, 0x00020000);
      const unsigned hbase = (unsigned)(g * 4 + tq) * 16u;
      v4u pv[16];
#pragma unroll
      for (int i = 0; i < 16; ++i) pv[i] = __builtin_amdgcn_raw_buffer_load_b128(rsKV, (unsigned)lane * 16u, (hbase + (unsigned)i) * 1024u, 16);
      const v4u z0 = __builtin_amdgcn_raw_buffer_load_b128(rsKV, (unsigned)lane * 16u, 524288u + hbase * 128u, 16);
      const v4u z1 = __builtin_amdgcn_raw_buffer_load_b128(rsKV, (unsigned)lane * 16u, 524288u + hbase * 128u + 1024u, 16);
      v4u idf;
      {
        const int m = lane & 31, h2 = lane >> 5;
        const int j = (m < 16 && ((m >> 2) & 1) == h2) ? (((m >> 3) << 2) | (m & 3)) : -1;
#pragma unroll
        for (int k = 0; k < 4; ++k) idf[k] = (j == 2 * k ? 0x3F80u : 0u) | (j == 2 * k + 1 ? 0x3F800000u : 0u);
      }
      f32x16 sum = zero16();
#pragma unroll
      for (int i = 0; i < 16; ++i) sum = mfma(idf, pv[i], sum);
      const float s[8] = {sum[0], sum[1], sum[2], sum[3], sum[4], sum[5], sum[6], sum[7]};
      const unsigned afr = L_AFR + (unsigned)tq * 2048u;
      *(LAS v4u*)(size_t)(afr + (unsigned)((lane >> 4) & 1) * 1024u + (unsigned)((lane & 15) + 32 * (lane >> 5)) * 16u) = pack8(s);
      const unsigned hh2 = (unsigned)(lane >> 4) & 1u, c16 = (unsigned)lane & 15u;
      float zz[4];
#pragma unroll
      for (int k = 0; k < 4; ++k) {
        float v = u2f(z0[k]) + u2f(z1[k]);
        v += shx(v, 8, lane); v += shx(v, 16, lane); v += shx(v, 32, lane);
        zz[k] = v;
      }
      if (lane < 8) *(LAS f32x4*)(size_t)(L_ZT + (unsigned)tq * 128u + (unsigned)lane * 16u) = f32x4{zz[0], zz[1], zz[2], zz[3]};
      const unsigned f0 = 16u * (unsigned)(lane >> 5) + 4u * hh2;
      const f32x4 za = lds128f(L_ZT + (unsigned)tq * 128u + f0 * 4u), zb = lds128f(L_ZT + (unsigned)tq * 128u + (f0 + 8u) * 4u);
      *(LAS v4u*)(size_t)(afr + (unsigned)(lane >> 5) * 1024u + (16u + c16 + 32u * hh2) * 16u) = v4u{pk2(za[0], za[1]), pk2(za[2], za[3]), pk2(zb[0], zb[1]), pk2(zb[2], zb[3])};
      asm volatile("s_waitcnt lgkmcnt(0)" ::: "memory");
      lds_inc(L_CNT + C_AFR * 4, lane);
      lds_wait_ge(L_CNT + C_AFR * 4, 4);
    }
#pragma unroll
    for (int hd = 0; hd < 4; ++hd) {
      const int head = 2 * ti[hd >> 1] + (hd & 1);
      f32x16 d = mfma(lds128(L_AFR + (unsigned)head * 2048u + (unsigned)lane * 16u), pq[hd][0], zero16());
      d = mfma(lds128(L_AFR + (unsigned)head * 2048u + 1024u + (unsigned)lane * 16u), pq[hd][1], d);
      const float r = __builtin_amdgcn_rcpf(d[8] + 1e-6f);
      const v4u o = v4u{pk2(d[0] * r, d[1] * r), pk2(d[2] * r, d[3] * r), pk2(d[4] * r, d[5] * r), pk2(d[6] * r, d[7] * r)};
      st128(o, rsHB, (unsigned)lane * 16u + (T32 * 144u + (unsigned)(112 + 4 * g + head)) * 1024u);
    }
    publish_last(L_CNT + C_O * 4, 4, P.ws, FL_O + (unsigned)(c.p * 8 + g) * 128u, tag, lane);
  }
  STAMP(6);
  return tag;
}

DI void role_b(const Params& P, const Ctx& c, int wid, u64 amp_a0) {
  u64 amp_a = amp_a0, amp_b = 0; (void)amp_a; (void)amp_b;
  const int lane = c.lane, tq = c.tq;
  const u64 tag = c.ffn ? role_b_head<true>(P, c, wid) : role_b_head<false>(P, c, wid);
  const int pan = c.tt >> 1;
  const int tb = tq * 64 + lane;
  v4u xres[2], bres;
#pragma unroll
  for (int m = 0; m < 2; ++m) {
    const int idx = tb + 256 * m;
    xres[m] = *(const GAS v4u*)(P.x + (size_t)(64 * c.tt + (idx >> 3)) * 512 + 64 * c.ct + 8 * (idx & 7));
  }
  bres = *(const GAS v4u*)(P.b2 + 64 * c.ct + 8 * (tb & 7));
  if (tq == 0) {
    poll_flags(P.ws, (ORD ? FL_O : FL_L) + (unsigned)pan * 8u * 128u, 8, tag, lane);
    lds_set(L_CNT + (ORD ? C_ORDY : C_LATERDY) * 4, 1);
    poll_flags(P.ws, (ORD ? FL_L : FL_O) + (unsigned)pan * 8u * 128u, 8, tag, lane);
    lds_set(L_CNT + (ORD ? C_LATERDY : C_ORDY) * 4, 1);
  } else if (tq == 1 && __builtin_amdgcn_workgroup_id_x() == 0) {
    poll_flags(P.ws, FL_E, 64, tag, lane); poll_flags(P.ws, FL_E + 64 * 128, 64, tag, lane);
    poll_flags(P.ws, FL_K, 64, tag, lane); poll_flags(P.ws, FL_K + 64 * 128, 64, tag, lane);
    if (lane == 0) __hip_atomic_store((GAS u64*)(P.ws + WS_EPOCH), tag, __ATOMIC_RELAXED, __HIP_MEMORY_SCOPE_AGENT);
  }
  BAR();
#pragma unroll
  for (int m = 0; m < 2; ++m) {
    const int idx = tb + 256 * m;
    const unsigned a = (unsigned)(idx >> 3) * 272u + (unsigned)(idx & 7) * 32u;
    f32x4 lo = lds128f(a), hi = lds128f(a + 16u);
#pragma unroll
    for (unsigned w = 1; w < 4; ++w) { lo += lds128f(w * SLOT + a); hi += lds128f(w * SLOT + a + 16u); }
    v4u o;
#pragma unroll
    for (int k = 0; k < 4; ++k) {
      const float a0 = k < 2 ? lo[2 * k] : hi[2 * k - 4], a1 = k < 2 ? lo[2 * k + 1] : hi[2 * k - 3];
      const float x0 = u2f(xres[m][k] << 16), x1 = u2f(xres[m][k] & 0xffff0000u);
      const float b0 = u2f(bres[k] << 16), b1 = u2f(bres[k] & 0xffff0000u);
      o[k] = pk2(a0 + x0 + b0, a1 + x1 + b1);
    }
    *(GAS v4u*)(P.out + (size_t)(64 * c.tt + (idx >> 3)) * 512 + 64 * c.ct + 8 * (idx & 7)) = o;
  }
  STAMP(7);
}

__global__ __launch_bounds__(512, 2) NOPK void fused_kernel(Params P_unused) {
  u64 t_first;
  asm volatile("s_memrealtime %0\n\ts_waitcnt lgkmcnt(0)" : "=s"(t_first) :: "memory");
  Params P;
  {
    const volatile __attribute__((address_space(4))) u64* ka = (const volatile __attribute__((address_space(4))) u64*)__builtin_amdgcn_kernarg_segment_ptr();
    P.x = (const u16*)ka[0]; P.wqkv = (const u16*)ka[1]; P.wphi = (const u16*)ka[2]; P.wo = (const u16*)ka[3];
    P.w1 = (const u16*)ka[4]; P.b1 = (const u16*)ka[5]; P.w2 = (const u16*)ka[6]; P.b2 = (const u16*)ka[7];
    P.out = (u16*)ka[8]; P.ws = (char*)ka[9];
  }
  u64 t_second;
  asm volatile("s_waitcnt lgkmcnt(0)\n\ts_memrealtime %0\n\ts_waitcnt lgkmcnt(0)" : "=s"(t_second) : "s"(P.ws), "s"(P.x) : "memory");
  __shared__ __attribute__((aligned(1024))) char smem[LDS_BYTES];
  asm volatile("" ::"v"((unsigned)(size_t)(LAS char*)smem) : "memory");
  u64 pc;
  asm volatile("s_getpc_b64 %0" : "=s"(pc));
  const int tid = __builtin_amdgcn_workitem_id_x(), lane = tid & 63;
  const int wid = __builtin_amdgcn_readfirstlane(tid >> 6);
  u64 amp_a = t_first, amp_b = t_second;
  Ctx c;
  c.lane = lane; c.tq = wid & 3; c.hh = lane >> 5;
  const int blk = __builtin_amdgcn_workgroup_id_x(), xcd = blk & 7, j = blk >> 3, ja = j & 7, jb = j >> 3;
  c.p = (xcd & 1) * 8 + ja;
  c.G = (xcd >> 1) * 4 + jb;
  c.ffn = !(c.G & 1);
  c.gi = c.G >> 1;
  c.rot = ROT1 == 0 ? 0 : (ROT1 == 1 ? (((ja + jb) % 3) * 3) & 7 : (ROT1 == 2 ? (ja + 2 * jb) & 7 : ((ja + jb) & 1) * 4));
  c.tt = (xcd >> 1) * 8 + ja; c.ct = (xcd & 1) * 4 + jb; c.ja = ja;
  c.rotE = ROT2 ? (2 * ja + 4 * jb) & 15 : 0; c.rotL = ROT2 ? (ja + 3 * jb) % 12 : 0; c.rotO = ROT2 ? (ja + 2 * jb) & 7 : 0;
  if (wid < 4) {
    if (CODEPF) {
      const unsigned kb = (unsigned)(j + 32 * (wid & 1));
      if (wid < 2 && kb < CODE_KB) {
        const unsigned v = *(const GAS unsigned*)((pc & ~255ull) + kb * 1024u + (unsigned)lane * 16u);
        asm volatile("" ::"v"(v));
      }
    }
    const u64 e = __hip_atomic_load((const GAS u64*)(P.ws + WS_EPOCH), __ATOMIC_RELAXED, __HIP_MEMORY_SCOPE_AGENT);
    if (tid < 32) *(LAS unsigned*)(size_t)(L_CNT + (unsigned)tid * 4u) = 0u;
    if (c.ffn) {
      if (tid < 224) {
        const int t7 = tid >> 5, n = tid & 31, g = c.gi;
        const int cA = 224 * g + 32 * (g & 1);
        const int col = t7 < 4 ? cA + 32 * t7 : (t7 < 6 ? cA + 128 + 32 * (t7 - 4) : ((g & 1) ? 224 * g : 224 * g + 192));
        *(LAS float*)(size_t)(L_B1 + (unsigned)tid * 4u) = u2f((unsigned)P.b1[col + n] << 16);
      }
    } else if (tid < 128) {
      *(LAS v4u*)(size_t)(L_WPHI + (unsigned)tid * 16u) = *(const GAS v4u*)(P.wphi + (size_t)c.gi * 1024 + tid * 8);
    }
    const u64 tag = e + 1u;
    if (tid == 0) *(LAS v2u*)(size_t)L_TAG = v2u{(unsigned)tag, (unsigned)(tag >> 32)};
    STAMP(1);
    role_a(P, c, wid, amp_a, amp_b);
  } else {
    STAMP(1);
    role_b(P, c, wid, amp_a);
  }
}

}

extern "C" void kernel_launch(void* const* d_in, const int* in_sizes, int n_in, void* d_out, int out_size, void* d_ws, size_t ws_size,
                              hipStream_t stream) {
  Params P;
  P.x = (const u16*)d_in[0]; P.wqkv = (const u16*)d_in[1]; P.wphi = (const u16*)d_in[2]; P.wo = (const u16*)d_in[3];
  P.w1 = (const u16*)d_in[5]; P.b1 = (const u16*)d_in[6]; P.w2 = (const u16*)d_in[7]; P.b2 = (const u16*)d_in[8];
  P.out = (u16*)d_out; P.ws = (char*)d_ws;
  fused_kernel<<<256, 512, 0, stream>>>(P);
}
```

```cpp
#include <hip/hip_runtime.h>
#include <stdint.h>
#include <type_traits>

namespace {

typedef unsigned short u16;
typedef unsigned long long u64;
typedef unsigned v4u __attribute__((ext_vector_type(4)));
typedef unsigned v2u __attribute__((ext_vector_type(2)));
typedef float f32x16 __attribute__((ext_vector_type(16)));
typedef float f32x4 __attribute__((ext_vector_type(4)));
typedef float f32x2 __attribute__((ext_vector_type(2)));
typedef __bf16 bf16x8 __attribute__((ext_vector_type(8)));
typedef __bf16 bf16x2 __attribute__((ext_vector_type(2)));
typedef short s16x4 __attribute__((ext_vector_type(4)));
#define LAS __attribute__((address_space(3)))
#define GAS __attribute__((address_space(1)))
#if defined(__HIP_DEVICE_COMPILE__)
#define NOPK __attribute__((target("no-packed-fp32-ops")))
#else
#define NOPK
#endif
#define DI __device__ __forceinline__ NOPK
#define AI __attribute__((always_inline)) NOPK

#define STAMPS 0
#define ROT1 1
#define ROT2 0
#define STR2(x) #x
#define STR(x) STR2(x)
#define W1 8
#define W2 4
#define PRIO 1
#define SLP 1
#define KVFIRST 0
#define PRIOL 0
#define ORD 0
#define CODEPF 1
#define WARM2 1
#define CODE_KB 36

constexpr unsigned WS_EPOCH = 0;
constexpr unsigned FL_E = 4096;
constexpr unsigned FL_L = FL_E + 128 * 128;
constexpr unsigned FL_K = FL_L + 128 * 128;
constexpr unsigned FL_O = FL_K + 128 * 128;
constexpr unsigned WS_ST = 73728;
constexpr unsigned WS_KVP = 131072;
constexpr unsigned WS_ZP = WS_KVP + 524288;
constexpr unsigned WS_HB = 1048576;
constexpr unsigned HB_BYTES = 64u * 144u * 1024u;
constexpr unsigned WS_STAMP = 16u << 20;

constexpr unsigned SLOT = 32768;
constexpr unsigned L_MISC = 131072;
constexpr unsigned L_STATS = L_MISC;
constexpr unsigned L_CS0 = L_MISC + 1024;
constexpr unsigned L_CS1 = L_MISC + 1536;
constexpr unsigned L_B1 = L_MISC + 2048;
constexpr unsigned L_WPHI = L_MISC + 3072;
constexpr unsigned L_CNT = L_MISC + 5120;
constexpr unsigned L_TAG = L_MISC + 5248;
constexpr unsigned L_ZT = L_MISC + 5376;
constexpr unsigned L_AFR = L_MISC + 8192;
constexpr unsigned LDS_BYTES = 163840;
enum { C_CS1 = 0, C_LATE = 1, C_KV = 2, C_O = 3, C_AFR = 4, C_KVRDY = 5, C_LATERDY = 6, C_ORDY = 7, C_FREE = 8  , C_LAND = 12  , C_EARLY = 16, C_IMG = 17, C_KVIN = 18, C_IMGDONE = 19 };

constexpr unsigned kSpin = 1u << 16;

struct Params {
  const u16* x; const u16* wqkv; const u16* wphi; const u16* wo;
  const u16* w1; const u16* b1; const u16* w2; const u16* b2;
  u16* out; char* ws;
};

DI unsigned f2u(float f) { return __builtin_bit_cast(unsigned, f); }
DI float u2f(unsigned u) { return __builtin_bit_cast(float, u); }
DI float fmx(float a, float b) { return __builtin_fmaxf(a, b); }
DI f32x16 mfma(v4u a, v4u b, f32x16 c) {
  return __builtin_amdgcn_mfma_f32_32x32x16_bf16(__builtin_bit_cast(bf16x8, a), __builtin_bit_cast(bf16x8, b), c, 0, 0, 0);
}
DI f32x16 zero16() { f32x16 z; for (int r = 0; r < 16; ++r) z[r] = 0.f; return z; }
DI v4u lds128(unsigned a) { return *(const LAS v4u*)(size_t)a; }
DI f32x4 lds128f(unsigned a) { return *(const LAS f32x4*)(size_t)a; }
DI v2u tr64(unsigned a) { return __builtin_bit_cast(v2u, __builtin_amdgcn_ds_read_tr16_b64_v4i16((LAS s16x4*)(size_t)a)); }
DI v4u tr2(unsigned a0, unsigned a1) { v2u l = tr64(a0), h = tr64(a1); return v4u{l.x, l.y, h.x, h.y}; }
DI unsigned pk2(float a, float b) { return __builtin_bit_cast(unsigned, __builtin_convertvector(f32x2{a, b}, bf16x2)); }
DI float xhalf_add(float v) { auto r = __builtin_amdgcn_permlane32_swap(f2u(v), f2u(v), false, false); return u2f(r[0]) + u2f(r[1]); }
DI float xhalf_max(float v) { auto r = __builtin_amdgcn_permlane32_swap(f2u(v), f2u(v), false, false); return fmx(u2f(r[0]), u2f(r[1])); }
DI float shx(float v, int m, int lane) { return u2f((unsigned)__builtin_amdgcn_ds_bpermute((lane ^ m) << 2, (int)f2u(v))); }
DI f32x4 lanesum(v4u f, f32x4 c) {
  const s16x4 one = {0x3F80, 0x3F80, 0x3F80, 0x3F80};
  c = __builtin_amdgcn_mfma_f32_4x4x4bf16_1k(one, __builtin_bit_cast(s16x4, v2u{f.x, f.y}), c, 0, 0, 0);
  return __builtin_amdgcn_mfma_f32_4x4x4bf16_1k(one, __builtin_bit_cast(s16x4, v2u{f.z, f.w}), c, 0, 0, 0);
}
template <int CTRL> DI float dppadd(float v) { return v + u2f((unsigned)__builtin_amdgcn_update_dpp(0, (int)f2u(v), CTRL, 0xf, 0xf, true)); }
DI float wavesum(float v) {
  v = dppadd<0xB1>(v); v = dppadd<0x4E>(v); v = dppadd<0x141>(v); v = dppadd<0x140>(v);
  auto r = __builtin_amdgcn_permlane16_swap(f2u(v), f2u(v), false, false);
  return xhalf_add(u2f(r[0]) + u2f(r[1]));
}
DI void dma(unsigned m0v, unsigned voff, __amdgpu_buffer_rsrc_t rs, unsigned soff) {
  asm volatile("s_mov_b32 m0, %0\n\ts_nop 0\n\tbuffer_load_dwordx4 %1, %2, %3 offen lds" ::"s"(m0v), "v"(voff), "s"(rs), "s"(soff) : "memory");
}
DI void st128(v4u v, __amdgpu_buffer_rsrc_t rs, unsigned voff) { __builtin_amdgcn_raw_buffer_store_b128(v, rs, voff, 0, 16); }
#define BAR() do { __builtin_amdgcn_sched_barrier(0); asm volatile("s_waitcnt lgkmcnt(0)\n\ts_barrier" ::: "memory"); __builtin_amdgcn_sched_barrier(0); } while (0)
#define VMW(n) asm volatile("s_waitcnt vmcnt(" #n ")" ::: "memory")
constexpr unsigned ONES = 0x3F803F80u;

#define STAMP(i) do { } while (0)

DI void poll_flags(const char* ws, unsigned base, int n, u64 tag, int lane) {
  const GAS u64* f = (const GAS u64*)(ws + base + (unsigned)(lane < n ? lane : 0) * 128u);
#pragma unroll 1
  for (unsigned spin = 0; spin < kSpin; ++spin) {
    const u64 v = __hip_atomic_load(f, __ATOMIC_RELAXED, __HIP_MEMORY_SCOPE_AGENT);
    if (__builtin_amdgcn_ballot_w64(v != tag) == 0) break;
    __builtin_amdgcn_s_sleep(1);
  }
  __builtin_amdgcn_fence(__ATOMIC_ACQUIRE, "wavefront");
  asm volatile("" ::: "memory");
}
DI void lds_wait_ge(unsigned addr, unsigned want) {
#pragma unroll 1
  for (unsigned spin = 0; spin < (kSpin << 4); ++spin) {
    const unsigned v = *(volatile LAS unsigned*)(size_t)addr;
    if ((unsigned)__builtin_amdgcn_readfirstlane(v) >= want) break;
    if (SLP) __builtin_amdgcn_s_sleep(SLP);
  }
  asm volatile("" ::: "memory");
}
DI void lds_set(unsigned addr, unsigned v) { *(volatile LAS unsigned*)(size_t)addr = v; }
DI void lds_inc(unsigned addr, int lane) { if (lane == 0) __hip_atomic_fetch_add((LAS unsigned*)(size_t)addr, 1u, __ATOMIC_RELAXED, __HIP_MEMORY_SCOPE_WORKGROUP); }
DI void publish_last(unsigned cnt_addr, unsigned n, char* ws, unsigned flag_off, u64 tag, int lane) {
  VMW(0);
  if (lane == 0) {
    const unsigned old = __hip_atomic_fetch_add((LAS unsigned*)(size_t)cnt_addr, 1u, __ATOMIC_RELAXED, __HIP_MEMORY_SCOPE_WORKGROUP);
    if (old == n - 1) __hip_atomic_store((GAS u64*)(ws + flag_off), tag, __ATOMIC_RELAXED, __HIP_MEMORY_SCOPE_AGENT);
  }
}
DI u64 lds_tag() { const v2u t = *(const LAS v2u*)(size_t)L_TAG; return ((u64)(unsigned)__builtin_amdgcn_readfirstlane(t.y) << 32) | (unsigned)__builtin_amdgcn_readfirstlane(t.x); }

DI void fold8(const f32x16& acc, int r0, float rstd, float mr, unsigned cs, int hh, float* y) {
#pragma unroll
  for (int g = 0; g < 2; ++g) {
    const f32x4 t = lds128f(cs + (unsigned)((r0 >> 2) + g) * 32u + (unsigned)hh * 16u) * mr;
#pragma unroll
    for (int k = 0; k < 4; ++k) y[4 * g + k] = __builtin_fmaf(acc[r0 + 4 * g + k], rstd, t[k]);
  }
}
DI void load16(unsigned a, int hh, f32x4* v) {
#pragma unroll
  for (int g = 0; g < 4; ++g) v[g] = lds128f(a + (unsigned)g * 32u + (unsigned)hh * 16u);
}
DI void ffn_tile(const f32x16& acc, float rstd, float mr, const f32x4* cs, const f32x4* bs, __amdgpu_buffer_rsrc_t rsHB, unsigned voff) {
  f32x2 y[8], d[8];
#pragma unroll
  for (int g = 0; g < 4; ++g) {
    const f32x4 t = cs[g] * mr + bs[g];
    y[2 * g] = f32x2{acc[4 * g], acc[4 * g + 1]} * rstd + f32x2{t[0], t[1]};
    y[2 * g + 1] = f32x2{acc[4 * g + 2], acc[4 * g + 3]} * rstd + f32x2{t[2], t[3]};
  }
#pragma unroll
  for (int k = 0; k < 8; ++k) d[k] = y[k] * ((y[k] * y[k]) * (-0.10294324f) + (-2.30220819f));
#pragma unroll
  for (int k = 0; k < 8; ++k) { d[k].x = __builtin_amdgcn_exp2f(d[k].x); d[k].y = __builtin_amdgcn_exp2f(d[k].y); }
#pragma unroll
  for (int k = 0; k < 8; ++k) d[k] += 1.0f;
#pragma unroll
  for (int k = 0; k < 8; ++k) { d[k].x = __builtin_amdgcn_rcpf(d[k].x); d[k].y = __builtin_amdgcn_rcpf(d[k].y); }
  v4u o0, o1;
#pragma unroll
  for (int k = 0; k < 4; ++k) {
    const f32x2 g0 = y[k] * d[k], g1 = y[4 + k] * d[4 + k];
    o0[k] = pk2(g0.x, g0.y); o1[k] = pk2(g1.x, g1.y);
  }
  st128(o0, rsHB, voff); st128(o1, rsHB, voff + 1024u);
}
DI void hedge(const f32x16& d, v4u& o0, v4u& o1) {
  constexpr float L = 1.4426950408889634f;
  float m0 = fmx(fmx(fmx(d[0], d[1]), fmx(d[2], d[3])), fmx(fmx(d[4], d[5]), fmx(d[6], d[7])));
  float m1 = fmx(fmx(fmx(d[8], d[9]), fmx(d[10], d[11])), fmx(fmx(d[12], d[13]), fmx(d[14], d[15])));
  m0 = xhalf_max(m0) * -L; m1 = xhalf_max(m1) * -L;
  float e[16];
#pragma unroll
  for (int i = 0; i < 8; ++i) { e[i] = __builtin_amdgcn_exp2f(__builtin_fmaf(d[i], L, m0)); e[8 + i] = __builtin_amdgcn_exp2f(__builtin_fmaf(d[8 + i], L, m1)); }
  float s0 = ((e[0] + e[1]) + (e[2] + e[3])) + ((e[4] + e[5]) + (e[6] + e[7]));
  float s1 = ((e[8] + e[9]) + (e[10] + e[11])) + ((e[12] + e[13]) + (e[14] + e[15]));
  s0 = __builtin_amdgcn_rcpf(xhalf_add(s0)); s1 = __builtin_amdgcn_rcpf(xhalf_add(s1));
#pragma unroll
  for (int k = 0; k < 4; ++k) { o0[k] = pk2(e[2 * k] * s0, e[2 * k + 1] * s0); o1[k] = pk2(e[8 + 2 * k] * s1, e[8 + 2 * k + 1] * s1); }
}
DI v4u wphi_frag(int hd, int lane) {
  const int hh = lane >> 5, q = (lane >> 2) & 3, pp = lane & 3;
  const unsigned a = L_WPHI + (unsigned)hd * 512u + (unsigned)(4 * hh + q) * 32u + (unsigned)pp * 8u;
  v4u f = tr2(a, a + 256u);
  const unsigned sg = (lane & 16) ? 0x80008000u : 0u;
  f.x ^= sg; f.y ^= sg; f.z ^= sg; f.w ^= sg;
  return f;
}
DI v4u selv(bool s, v4u a, v4u b) { const unsigned m = s ? 0xffffffffu : 0u; return v4u{(a.x & m) | (b.x & ~m), (a.y & m) | (b.y & ~m), (a.z & m) | (b.z & ~m), (a.w & m) | (b.w & ~m)}; }
DI v4u pack8(const float* y) { return v4u{pk2(y[0], y[1]), pk2(y[2], y[3]), pk2(y[4], y[5]), pk2(y[6], y[7])}; }

template <int NI> DI void warm_w2(const Params& P, int ct, int ja, int tq, int i0, int lane, v4u* d) {
#pragma unroll
  for (int i = 0; i < NI; ++i) {
    const int row = 288 * ja + 72 * tq + 8 * (i0 + i);
    const u64 a = row < 1792 ? (u64)(size_t)P.w2 + (u64)(unsigned)row * 1024u : (u64)(size_t)P.wo + (u64)(unsigned)(row - 1792) * 1024u;
    const u64 base = ((u64)(unsigned)__builtin_amdgcn_readfirstlane((unsigned)(a >> 32)) << 32) | (unsigned)__builtin_amdgcn_readfirstlane((unsigned)a);
    const unsigned voff = (unsigned)(lane >> 3) * 1024u + (unsigned)(lane & 7) * 16u + (unsigned)ct * 128u;
    asm volatile("s_nop 4\n\tglobal_load_dwordx4 %0, %1, %2" : "=&v"(d[i]) : "v"(voff), "s"(base) : "memory");
  }
}
struct Ctx {
  int lane, tq, hh;
  int p, gi, G;
  int rot;
  int tt, ct, ja, jb;
  int rotE, rotL, rotO;
  bool ffn;
};

DI void role_a(const Params& P, const Ctx& c, int wid, u64 amp_a, u64 amp_b) {
  const int lane = c.lane, tq = c.tq, hh = c.hh, t = lane & 31;
  const int cb = (lane >> 4) & 1, q = (lane >> 2) & 3, pp = lane & 3, sw = (q >> 1) & 1;
  const int th = tq & 1, up = tq >> 1;
  unsigned xa[4];
#pragma unroll
  for (int ks = 0; ks < 4; ++ks) xa[ks] = (unsigned)(64 * th + t) * 128u + (unsigned)(((2 * ks + hh) ^ ((t >> 1) & 7)) * 16);
  const unsigned wl = 16384u + (unsigned)(8 * hh + q) * 128u + (unsigned)((4 * (up ^ sw) + 2 * cb + (pp >> 1)) * 16 + 8 * (pp & 1));
  f32x16 acc[2][2];
#pragma unroll
  for (int i = 0; i < 2; ++i) { acc[i][0] = zero16(); acc[i][1] = zero16(); }
  f32x4 cs = {0.f, 0.f, 0.f, 0.f};
  unsigned x0 = xa[0], x1 = xa[1], x2 = xa[2], x3 = xa[3], w0 = wl;
  v4u xf[4][2], wf[4][2];
  auto rd = [&](int ks, v4u* X, v4u* W) AI {
    const unsigned xb = ks == 0 ? x0 : (ks == 1 ? x1 : (ks == 2 ? x2 : x3));
    X[0] = lds128(xb); X[1] = lds128(xb + 4096u);
    const unsigned a = w0 + (unsigned)(ks * 2048);
    W[0] = tr2(a, a + 512u); W[1] = tr2(a + 8192u, a + 8704u);
  };
  f32x4 cs2 = {0.f, 0.f, 0.f, 0.f};
  BAR();
  rd(0, xf[0], wf[0]); rd(1, xf[1], wf[1]);
  auto pass0 = [&](auto TH) AI {
    constexpr int CT = decltype(TH)::value;
    const s16x4 one = {0x3F80, 0x3F80, 0x3F80, 0x3F80};
#pragma unroll 1
    for (int kc = 0; kc < 8; ++kc) {
#pragma unroll
      for (int ks = 0; ks < 4; ++ks) {
        if (ks == 2) {
          __builtin_amdgcn_sched_barrier(0);
          asm volatile("s_barrier" ::: "memory");
          __builtin_amdgcn_sched_barrier(0);
          const unsigned sb = (unsigned)((kc + 1) & 3) * SLOT;
          x0 = xa[0] + sb; x1 = xa[1] + sb; w0 = wl + sb;
          asm volatile("" : "+v"(x0), "+v"(x1), "+v"(w0));
        }
        if (ks == 0) {
          const unsigned sb = (unsigned)(kc & 3) * SLOT;
          x2 = xa[2] + sb; x3 = xa[3] + sb;
          asm volatile("" : "+v"(x2), "+v"(x3));
        }
        rd((ks + 2) & 3, xf[(ks + 2) & 3], wf[(ks + 2) & 3]);
        acc[0][0] = mfma(wf[ks][0], xf[ks][0], acc[0][0]); acc[0][1] = mfma(wf[ks][0], xf[ks][1], acc[0][1]);
        acc[1][0] = mfma(wf[ks][1], xf[ks][0], acc[1][0]); acc[1][1] = mfma(wf[ks][1], xf[ks][1], acc[1][1]);
        cs = __builtin_amdgcn_mfma_f32_4x4x4bf16_1k(one, __builtin_bit_cast(s16x4, v2u{wf[ks][CT].x, wf[ks][CT].y}), cs, 0, 0, 0);
        cs2 = __builtin_amdgcn_mfma_f32_4x4x4bf16_1k(one, __builtin_bit_cast(s16x4, v2u{wf[ks][CT].z, wf[ks][CT].w}), cs2, 0, 0, 0);
        __builtin_amdgcn_sched_group_barrier(0x008, 1, 0);
        __builtin_amdgcn_sched_group_barrier(0x100, 2, 0);
        __builtin_amdgcn_sched_group_barrier(0x008, 1, 0);
        __builtin_amdgcn_sched_group_barrier(0x100, 2, 0);
        __builtin_amdgcn_sched_group_barrier(0x008, 1, 0);
        __builtin_amdgcn_sched_group_barrier(0x100, 2, 0);
        __builtin_amdgcn_sched_group_barrier(0x008, 3, 0);
        __builtin_amdgcn_sched_barrier(0);
      }
      asm volatile("" : "+v"(acc[0][0]), "+v"(acc[0][1]), "+v"(acc[1][0]), "+v"(acc[1][1]), "+v"(cs), "+v"(cs2));
    }
  };
  if (th) pass0(std::integral_constant<int, 1>{}); else pass0(std::integral_constant<int, 0>{});
  cs[0] += cs2[0];
  STAMP(2);
  {
    const float s = xhalf_add(cs[0]);
    if (hh == 0) *(LAS float*)(size_t)(L_CS0 + (unsigned)(up + 2 * th) * 128u + (unsigned)t * 4u) = s;
  }
  BAR();
  const u64 tag = lds_tag();
  float rstd[2], mr[2];
#pragma unroll
  for (int b = 0; b < 2; ++b) {
    const f32x2 st = *(const LAS f32x2*)(size_t)(L_STATS + (unsigned)(64 * th + 32 * b + t) * 8u);
    rstd[b] = st.x; mr[b] = st.y;
  }
  const __amdgpu_buffer_rsrc_t rsHB = __builtin_amdgcn_make_buffer_rsrc(P.ws + WS_HB, 0, HB_BYTES, 0x00020000);
  v4u wm[9];
  if (c.ffn) {
    f32x4 csv[2][4], bsv[2][4];
#pragma unroll
    for (int i = 0; i < 2; ++i) { load16(L_CS0 + (unsigned)(up + 2 * i) * 128u, hh, csv[i]); load16(L_B1 + (unsigned)(up + 2 * i) * 128u, hh, bsv[i]); }
#pragma unroll
    for (int i = 0; i < 2; ++i)
#pragma unroll
      for (int b = 0; b < 2; ++b) {
        const unsigned tile = (unsigned)(up + 2 * i), T32 = (unsigned)(c.p * 4 + 2 * th + b);
        ffn_tile(acc[i][b], rstd[b], mr[b], csv[i], bsv[i], rsHB, (unsigned)lane * 16u + (T32 * 144u + (unsigned)(8 * c.gi) + 2u * tile) * 1024u);
        if (WARM2 && 2 * i + b < 3) warm_w2<3>(P, c.ct, c.ja, tq, 3 * (2 * i + b), lane, wm + 3 * (2 * i + b));
      }
    STAMP(8);
    publish_last(L_CNT + C_EARLY * 4, 4, P.ws, FL_E + (unsigned)(c.p * 8 + c.gi) * 128u, tag, lane);
    if (WARM2) asm volatile("" :: "v"(wm[0]), "v"(wm[1]), "v"(wm[2]), "v"(wm[3]), "v"(wm[4]), "v"(wm[5]), "v"(wm[6]), "v"(wm[7]), "v"(wm[8]));
  } else {
#pragma unroll
    for (int b = 0; b < 2; ++b)
#pragma unroll
      for (int hf = 0; hf < 2; ++hf) {
        const int hd = 2 * up + hf;
        float y[8];
        fold8(acc[0][b], 8 * hf, rstd[b], mr[b], L_CS0 + (unsigned)up * 128u, hh, y);
        v4u o0, o1;
        hedge(mfma(wphi_frag(hd, lane), pack8(y), zero16()), o0, o1);
        const unsigned img = (unsigned)hd * SLOT + 16384u;
        const unsigned row = (unsigned)(64 * th + 32 * b + t);
        const unsigned g = (row >> 1) & 7u;
        *(LAS v2u*)(size_t)(img + row * 64u + (((unsigned)(0 + hh)) ^ g) * 8u) = v2u{o0.x, o0.y};
        *(LAS v2u*)(size_t)(img + row * 64u + (((unsigned)(2 + hh)) ^ g) * 8u) = v2u{o0.z, o0.w};
        *(LAS v2u*)(size_t)(img + row * 64u + (((unsigned)(4 + hh)) ^ g) * 8u) = v2u{o1.x, o1.y};
        *(LAS v2u*)(size_t)(img + row * 64u + (((unsigned)(6 + hh)) ^ g) * 8u) = v2u{o1.z, o1.w};
        fold8(acc[1][b], 8 * hf, rstd[b], mr[b], L_CS0 + (unsigned)(2 + up) * 128u, hh, y);
        const unsigned gv = (row >> 2) & 3u;
        *(LAS v2u*)(size_t)(img + 8192u + row * 32u + (((unsigned)(0 + hh)) ^ gv) * 8u) = v2u{pk2(y[0], y[1]), pk2(y[2], y[3])};
        *(LAS v2u*)(size_t)(img + 8192u + row * 32u + (((unsigned)(2 + hh)) ^ gv) * 8u) = v2u{pk2(y[4], y[5]), pk2(y[6], y[7])};
        if (WARM2 && 2 * b + hf < 3) warm_w2<3>(P, c.ct, c.ja, tq, 3 * (2 * b + hf), lane, wm + 3 * (2 * b + hf));
      }
    STAMP(8);
    asm volatile("s_waitcnt lgkmcnt(0)" ::: "memory");
    lds_inc(L_CNT + C_IMG * 4, lane);
    lds_wait_ge(L_CNT + C_IMG * 4, 4);
    const unsigned img = (unsigned)tq * SLOT + 16384u;
    f32x16 kv = zero16();
    unsigned pa[2], va[2];
#pragma unroll
    for (int i2 = 0; i2 < 2; ++i2) {
      const unsigned r = (unsigned)(8 * hh + 4 * i2 + q);
      pa[i2] = img + r * 64u + (((unsigned)(4 * cb + pp)) ^ ((r >> 1) & 7u)) * 8u;
      va[i2] = img + 8192u + r * 32u + (((unsigned)pp) ^ ((r >> 2) & 3u)) * 8u;
    }
#pragma unroll
    for (int ks = 0; ks < 8; ++ks) {
      const v4u af = tr2(pa[0] + (unsigned)ks * 1024u, pa[1] + (unsigned)ks * 1024u);
      v4u bf = tr2(va[0] + (unsigned)ks * 512u, va[1] + (unsigned)ks * 512u);
      if (cb) bf = v4u{ONES, ONES, ONES, ONES};
      kv = mfma(af, bf, kv);
    }
    asm volatile("" : "+v"(kv) :: "memory");
    lds_inc(L_CNT + C_IMGDONE * 4, lane);
    const unsigned hidx = (unsigned)(c.gi * 4 + tq) * 16u + (unsigned)c.p;
    const __amdgpu_buffer_rsrc_t rsKV = __builtin_amdgcn_make_buffer_rsrc(P.ws + WS_KVP, 0, 524288 + 65536, 0x00020000);
    if (cb == 0) {
#pragma unroll
      for (int s = 0; s < 2; ++s) {
        const v4u o = v4u{pk2(kv[8 * s], kv[8 * s + 1]), pk2(kv[8 * s + 2], kv[8 * s + 3]), pk2(kv[8 * s + 4], kv[8 * s + 5]), pk2(kv[8 * s + 6], kv[8 * s + 7])};
        st128(o, rsKV, (unsigned)((16 * s + (lane & 15) + 32 * hh) * 16) + hidx * 1024u);
      }
    } else if ((lane & 15) == 0) {
#pragma unroll
      for (int g4 = 0; g4 < 4; ++g4) {
        const v4u o = v4u{f2u(kv[4 * g4]), f2u(kv[4 * g4 + 1]), f2u(kv[4 * g4 + 2]), f2u(kv[4 * g4 + 3])};
        st128(o, rsKV, (unsigned)((8 * g4 + 4 * hh) * 4) + 524288u + hidx * 128u);
      }
    }
    publish_last(L_CNT + C_KV * 4, 4, P.ws, FL_K + (unsigned)(c.gi * 16 + c.p) * 128u, tag, lane);
    if (WARM2) asm volatile("" :: "v"(wm[0]), "v"(wm[1]), "v"(wm[2]), "v"(wm[3]), "v"(wm[4]), "v"(wm[5]), "v"(wm[6]), "v"(wm[7]), "v"(wm[8]));
  }
  STAMP(3);
  unsigned tabB, tabWlo, tabWhi;
  {
    const int jj = lane < 36 ? lane : 35;
    constexpr int J2 = ORD ? 24 : 28;
    int sg;
    auto perm = [&](int i, int n) AI {
      if (!ROT2) return i;
      const int b = c.jb;
      if (i == 0) return b;
      const int m = (i - 1) >> 2, r = (i - 1) & 3;
      if (m < n - 1) return r == 0 ? 4 * (m + 1) + b : 4 * m + ((b + r) & 3);
      return 4 * m + ((b + 1 + r) & 3);
    };
    if (jj < 16) sg = 4 * perm(jj, 4) + tq;
    else if ((jj < J2) != (ORD != 0)) sg = 64 + 4 * perm(jj - (ORD ? 24 : 16), 3) + tq;
    else sg = 112 + 4 * perm(jj - (ORD ? 16 : 28), 2) + tq;
    int row;
    if (sg < 64) { const int g = sg >> 3; row = 224 * g + 32 * (g & 1) + 16 * (sg & 7); }
    else if (sg < 112) {
      const int e = sg - 64, g = e / 6, i = e - 6 * g;
      row = i < 4 ? 224 * g + 32 * (g & 1) + 128 + 16 * i : ((g & 1) ? 224 * g : 224 * g + 192) + 16 * (i - 4);
    } else row = 16 * (sg - 112);
    const u64 a = (u64)(size_t)(sg < 112 ? P.w2 : P.wo) + (u64)row * 1024u + (u64)c.ct * 128u;
    tabB = (unsigned)sg * 1024u; tabWlo = (unsigned)a; tabWhi = (unsigned)(a >> 32);
  }
  const unsigned vw0 = (unsigned)(lane >> 3) * 1024u + (unsigned)(((lane & 7) ^ (((lane >> 4) & 1) << 2)) * 16), vw1 = vw0 + 8192u;
  const unsigned ring = (unsigned)tq * 8192u;
  auto pofs = [&](int r) AI { return (unsigned)((r >> 2) & 3) * SLOT + (unsigned)(r & 3) * 2048u; };
  auto wdma = [&](int j) AI {
    const unsigned lo = __builtin_amdgcn_readlane(tabWlo, j), hi = __builtin_amdgcn_readlane(tabWhi, j);
    const u64 b = ((u64)hi << 32) | lo;
    const unsigned m = ring + pofs(j);
    asm volatile("s_nop 4\n\ts_mov_b32 m0, %0\n\ts_nop 0\n\tglobal_load_lds_dwordx4 %1, %3\n\ts_add_u32 m0, m0, 0x400\n\ts_nop 0\n\tglobal_load_lds_dwordx4 %2, %3"
                 ::"s"(m), "v"(vw0), "v"(vw1), "s"(b) : "memory", "scc");
  };
  const unsigned vb0 = (unsigned)lane * 16u + (unsigned)(2 * c.tt) * 144u * 1024u, vb1 = vb0 + 144u * 1024u;
  constexpr int NB = 4;
  v4u br[NB][2];
#pragma unroll
  for (int u = 0; u < NB; ++u) { br[u][0] = v4u{0u, 0u, 0u, 0u}; br[u][1] = br[u][0]; }
  auto bload = [&](unsigned so, v4u& d0, v4u& d1) AI {
    asm volatile("s_nop 4\n\tbuffer_load_dwordx4 %0, %2, %4, %5 offen sc1\n\tbuffer_load_dwordx4 %1, %3, %4, %5 offen sc1"
                 : "+v"(d0), "+v"(d1) : "v"(vb0), "v"(vb1), "s"(rsHB), "s"(so) : "memory");
  };
  if (!c.ffn && tq >= 2) lds_wait_ge(L_CNT + C_IMGDONE * 4, 4);
#pragma unroll 1
  for (int s = 0; s < 3; ++s) {
    lds_wait_ge(L_CNT + (C_FREE + s) * 4, 4);
#pragma unroll 1
    for (int j = 4 * s; j < (s < 2 ? 4 * s + 4 : 10); ++j) wdma(j);
  }
  STAMP(4);
  const int pan = c.tt >> 1;
  poll_flags(P.ws, FL_E + (unsigned)pan * 8u * 128u, 8, tag, lane);
  if (KVFIRST && !c.ffn) lds_wait_ge(L_CNT + C_KVIN * 4, 4);
  STAMP(5);
  bload(__builtin_amdgcn_readlane(tabB, 0), br[0][0], br[0][1]);
  bload(__builtin_amdgcn_readlane(tabB, 1), br[1][0], br[1][1]);
  wdma(10);
  bload(__builtin_amdgcn_readlane(tabB, 2), br[2][0], br[2][1]);
  wdma(11);
  bload(__builtin_amdgcn_readlane(tabB, 3), br[3][0], br[3][1]);
  unsigned w2l[2];
#pragma unroll
  for (int h = 0; h < 2; ++h) w2l[h] = ring + (unsigned)(4 * hh + q) * 128u + (unsigned)((4 * (h ^ sw) + 2 * cb + (pp >> 1)) * 16 + 8 * (pp & 1));
  f32x16 o[2][2];
#pragma unroll
  for (int a = 0; a < 2; ++a)
#pragma unroll
    for (int b = 0; b < 2; ++b) o[a][b] = zero16();
  v4u af[2];
  asm volatile("s_waitcnt vmcnt(10)" : "+v"(br[0][0]), "+v"(br[0][1]) :: "memory");
  af[0] = tr2(w2l[0], w2l[0] + 1024u); af[1] = tr2(w2l[1], w2l[1] + 1024u);
  auto step = [&](int j, int u, bool refill, bool first) AI {
    v4u an[2];
    if (first) asm volatile("s_waitcnt vmcnt(" STR(W1) ")" : "+v"(br[(u + 1) % NB][0]), "+v"(br[(u + 1) % NB][1]) :: "memory");
    else asm volatile("s_waitcnt vmcnt(" STR(W2) ")" : "+v"(br[(u + 1) % NB][0]), "+v"(br[(u + 1) % NB][1]) :: "memory");
    unsigned p0 = w2l[0] + pofs(j + 1), p1 = w2l[1] + pofs(j + 1);
    asm volatile("" : "+v"(p0), "+v"(p1));
    an[0] = tr2(p0, p0 + 1024u); an[1] = tr2(p1, p1 + 1024u);
    o[0][0] = mfma(af[0], br[u][0], o[0][0]); o[0][1] = mfma(af[0], br[u][1], o[0][1]);
    o[1][0] = mfma(af[1], br[u][0], o[1][0]); o[1][1] = mfma(af[1], br[u][1], o[1][1]);
    asm volatile("" : "+v"(o[0][0]), "+v"(o[0][1]), "+v"(o[1][0]), "+v"(o[1][1]) :: "memory");
    if (refill) wdma(j + 12);
    const int jn = j + NB;
    if (u == 0) {
      if (jn == 16) lds_wait_ge(L_CNT + (ORD ? C_ORDY : C_LATERDY) * 4, 1);
      if (jn == (ORD ? 24 : 28)) lds_wait_ge(L_CNT + (ORD ? C_LATERDY : C_ORDY) * 4, 1);
    }
    bload(jn < 36 ? (unsigned)__builtin_amdgcn_readlane(tabB, jn) : 0x7F000000u, br[u][0], br[u][1]);
    af[0] = an[0]; af[1] = an[1];
  };
  lds_wait_ge(L_CNT + (C_FREE + 3) * 4, 4);
#pragma unroll 1
  for (int it = 0; it < 6; ++it) {
    if (it == 3) STAMP(10);
#pragma unroll
    for (int u = 0; u < NB; ++u) step(NB * it + u, u, true, true);
  }
  STAMP(11);
#pragma unroll 1
  for (int it = 6; it < 9; ++it) {
#pragma unroll
    for (int u = 0; u < NB; ++u) step(NB * it + u, u, false, false);
  }
  asm volatile("s_waitcnt vmcnt(0)" : "+v"(br[0][0]), "+v"(br[0][1]), "+v"(br[1][0]), "+v"(br[1][1]), "+v"(br[2][0]), "+v"(br[2][1]), "+v"(br[3][0]), "+v"(br[3][1]) :: "memory");
  STAMP(6);
#pragma unroll
  for (int a = 0; a < 2; ++a)
#pragma unroll
    for (int b = 0; b < 2; ++b)
#pragma unroll
      for (int g4 = 0; g4 < 4; ++g4)
        *(LAS f32x4*)(size_t)(ring + (unsigned)b * SLOT + (unsigned)t * 256u + (unsigned)((8 * a + 2 * g4 + hh) ^ (t & 15)) * 16u) =
            f32x4{o[a][b][4 * g4], o[a][b][4 * g4 + 1], o[a][b][4 * g4 + 2], o[a][b][4 * g4 + 3]};
  VMW(0);
  BAR();
  STAMP(7);
}

template <int NT, int NF, class Fill> DI void pass1(const Params& P, int wid, u64& amp_a, u64& amp_b, Fill& fill, const v4u (&xr)[32], f32x16 (&acc)[NT], f32x4& cs, const unsigned (&wb)[NT], int lane) {
  const s16x4 one = {0x3F80, 0x3F80, 0x3F80, 0x3F80};
  f32x4 csa = {0.f, 0.f, 0.f, 0.f}, csb = csa;
#pragma unroll
  for (int qq = 0; qq < 4; ++qq) {
    if (qq > 0) lds_wait_ge(L_CNT + (C_LAND + qq) * 4, 4);
    if (qq < 2) fill(10 + qq);
    unsigned a[NT];
#pragma unroll
    for (int i = 0; i < NT; ++i) { a[i] = wb[i] + (unsigned)qq * SLOT; asm volatile("" : "+v"(a[i])); }
    v4u f[3][NT];
    auto rd = [&](int k8, v4u* W) AI {
      const unsigned o = (unsigned)((k8 >> 2) * 8192 + (k8 & 3) * 2048);
#pragma unroll
      for (int i = 0; i < NT; ++i) W[i] = tr2(a[i] + o, a[i] + o + 512u);
    };
    rd(0, f[0]); rd(1, f[1]);
    __builtin_amdgcn_sched_barrier(0);
#pragma unroll
    for (int k8 = 0; k8 < 8; ++k8) {
      if (k8 == 4 && qq < 3) {
        if (qq < 2) { if (NF == 8) VMW(8); else VMW(4); } else VMW(0);
        lds_inc(L_CNT + (C_LAND + qq + 1) * 4, lane);
      }
      if (k8 < 6) rd(k8 + 2, f[(k8 + 2) % 3]);
      const v4u xf = xr[8 * qq + k8];
#pragma unroll
      for (int i = 0; i < NT; ++i) acc[i] = mfma(f[k8 % 3][i], xf, acc[i]);
      csa = __builtin_amdgcn_mfma_f32_4x4x4bf16_1k(one, __builtin_bit_cast(s16x4, v2u{f[k8 % 3][0].x, f[k8 % 3][0].y}), csa, 0, 0, 0);
      csb = __builtin_amdgcn_mfma_f32_4x4x4bf16_1k(one, __builtin_bit_cast(s16x4, v2u{f[k8 % 3][0].z, f[k8 % 3][0].w}), csb, 0, 0, 0);
      if (k8 < 6) {
#pragma unroll
        for (int i = 0; i < NT; ++i) {
          __builtin_amdgcn_sched_group_barrier(0x008, 1, 0);
          __builtin_amdgcn_sched_group_barrier(0x100, 2, 0);
        }
        __builtin_amdgcn_sched_group_barrier(0x008, 2, 0);
      }
      __builtin_amdgcn_sched_barrier(0);
    }
    asm volatile("" : "+v"(acc[0]), "+v"(acc[1]), "+v"(acc[NT - 1]), "+v"(csa), "+v"(csb) :: "memory");
    lds_inc(L_CNT + (C_FREE + qq) * 4, lane);
  }
  cs[0] = csa[0] + csb[0];
}

template <bool ffn> DI u64 role_b_head(const Params& P, const Ctx& c, int wid) {
  u64 amp_a = 0, amp_b = 0; (void)amp_a; (void)amp_b;
  const int lane = c.lane, tq = c.tq, hh = c.hh, t = lane & 31;
  const int g = c.gi;
  const unsigned tok = (unsigned)(c.p * 128 + 8 * c.G + 2 * tq);
  v4u r0, r1;
  {
    const u16* rp = P.x + (size_t)tok * 512 + lane * 8;
    asm volatile("global_load_dwordx4 %0, %2, off\n\tglobal_load_dwordx4 %1, %2, off offset:1024" : "=&v"(r0), "=&v"(r1) : "v"(rp) : "memory");
  }
  const __amdgpu_buffer_rsrc_t rsX = __builtin_amdgcn_make_buffer_rsrc((void*)P.x, 0, 2048 * 1024, 0x00020000);
  const __amdgpu_buffer_rsrc_t rsW = __builtin_amdgcn_make_buffer_rsrc((void*)(ffn ? P.w1 : P.wqkv), 0, ffn ? 512 * 3584 : 512 * 3072, 0x00020000);
  constexpr unsigned RS = ffn ? 3584u : 3072u;
  const unsigned cA = (unsigned)(224 * g + 32 * (g & 1));
  const unsigned u0 = ffn ? cA * 2u : (unsigned)(512 + 64 * g) * 2u, u1 = ffn ? cA * 2u + 128u : (unsigned)(1024 + 64 * g) * 2u;
  const unsigned ua = ffn ? (cA + 128u) * 2u : (unsigned)(64 * g) * 2u;
  const unsigned ub = (unsigned)((g & 1) ? 224 * g - 32 : 224 * g + 192) * 2u;
  const unsigned voffX = (unsigned)(lane >> 3) * 1024u + (unsigned)(((lane & 7) ^ ((4 * (tq & 1) + (lane >> 4)) & 7)) * 16);
  const unsigned voffW = (unsigned)(lane >> 3) * RS + (unsigned)(((lane & 7) ^ (((lane >> 4) & 1) << 2)) * 16);
  const unsigned soffX = (unsigned)(c.p * 128 + 8 * tq) * 1024u;
  const unsigned wrow = 32u * (unsigned)(tq & 1) * RS;
  auto fill = [&](int f) AI {
    const unsigned slot = (unsigned)(f & 3) * SLOT;
    if (f < 8) {
      const unsigned kcp = (unsigned)((f + c.rot) & 7);
      unsigned sx = soffX + kcp * 128u, sw = 64u * kcp * RS + wrow + ((tq >> 1) ? u1 : u0);
      unsigned mx = slot + (unsigned)tq * 1024u, mw = slot + 16384u + (unsigned)tq * 4096u;
#pragma unroll 1
      for (int m = 0; m < 4; ++m) {
        dma(mx, voffX, rsX, sx); dma(mw, voffW, rsW, sw);
        mx += 4096u; sx += 32768u; mw += 1024u; sw += 8u * RS;
      }
    } else {
      const unsigned kcp = (unsigned)((2 * (f - 8) + (tq >> 1) + c.rot) & 7);
      unsigned sw = 64u * kcp * RS + wrow, mw = slot + (unsigned)tq * 4096u;
#pragma unroll 1
      for (int m = 0; m < 4; ++m) {
        dma(mw, voffW, rsW, sw + ua);
        if (ffn) dma(mw + 16384u, voffW, rsW, sw + ub);
        mw += 1024u; sw += 8u * RS;
      }
    }
  };
  fill(0);
  float st0;
  {
    asm volatile("s_waitcnt vmcnt(8)" : "+v"(r0), "+v"(r1) :: "memory");
    float v[2][8], mean[2], rs[2];
#pragma unroll
    for (int k = 0; k < 4; ++k) {
      v[0][2 * k] = u2f(r0[k] << 16); v[0][2 * k + 1] = u2f(r0[k] & 0xffff0000u);
      v[1][2 * k] = u2f(r1[k] << 16); v[1][2 * k + 1] = u2f(r1[k] & 0xffff0000u);
    }
#pragma unroll
    for (int i = 0; i < 2; ++i) mean[i] = wavesum(((v[i][0] + v[i][1]) + (v[i][2] + v[i][3])) + ((v[i][4] + v[i][5]) + (v[i][6] + v[i][7]))) * (1.0f / 512.0f);
#pragma unroll
    for (int i = 0; i < 2; ++i) {
      float q2 = 0.f;
#pragma unroll
      for (int k = 0; k < 8; ++k) { const float d = v[i][k] - mean[i]; q2 = __builtin_fmaf(d, d, q2); }
      rs[i] = __builtin_amdgcn_rsqf(wavesum(q2) * (1.0f / 512.0f) + 1e-5f);
    }
    st0 = (lane & 2) ? rs[1] : rs[0];
    st0 = (lane & 1) ? -((lane & 2) ? mean[1] : mean[0]) * st0 : st0;
    asm volatile("" : "+v"(st0));
  }
  fill(1);
  unsigned xa[4];
#pragma unroll
  for (int ks = 0; ks < 4; ++ks) xa[ks] = (unsigned)(32 * tq + t) * 128u + (unsigned)(((2 * ks + hh) ^ ((t >> 1) & 7)) * 16);
  v4u xr[32];
  u64 tag = 0;
  v4u gr;
  const unsigned token = (unsigned)(c.p * 128 + 32 * tq + t);
#pragma unroll
  for (int kc = 0; kc < 8; ++kc) {
    if (kc < 7 || ffn) VMW(8); else VMW(4);
    BAR();
    if (kc == 0) {
      tag = lds_tag();
      if (lane < 4)
        __hip_atomic_store((GAS u64*)(P.ws + WS_ST + tok * 16u + (unsigned)lane * 8u), ((u64)(unsigned)tag << 32) | f2u(st0), __ATOMIC_RELAXED, __HIP_MEMORY_SCOPE_AGENT);
    }
    if (kc == 5) {
      const char* gp = P.ws + WS_ST + token * 16u;
      asm volatile("global_load_dwordx4 %0, %1, off sc1" : "=&v"(gr) : "v"(gp) : "memory");
    }
    fill(kc + 2);
    const unsigned sb = (unsigned)(kc & 3) * SLOT;
#pragma unroll
    for (int ks = 0; ks < 4; ++ks) xr[4 * kc + ks] = lds128(xa[ks] + sb);
    asm volatile("" : "+v"(xr[4 * kc]), "+v"(xr[4 * kc + 1]), "+v"(xr[4 * kc + 2]), "+v"(xr[4 * kc + 3]));
  }
  asm volatile("" : "+v"(gr));
  STAMP(2);
  BAR();
  if (__builtin_amdgcn_ballot_w64(gr.y != (unsigned)tag || gr.w != (unsigned)tag) != 0) {
    const __amdgpu_buffer_rsrc_t rs = __builtin_amdgcn_make_buffer_rsrc((void*)(P.ws + WS_ST), 0, 32768, 0x00020000);
#pragma unroll 1
    for (unsigned spin = 0; spin < kSpin; ++spin) {
      gr = __builtin_amdgcn_raw_buffer_load_b128(rs, token * 16u, 0, 16);
      if (__builtin_amdgcn_ballot_w64(gr.y != (unsigned)tag || gr.w != (unsigned)tag) == 0) break;
      __builtin_amdgcn_s_sleep(1);
    }
  }
  const float rstd = u2f(gr.x), mr = u2f(gr.z);
  if (hh == 0) *(LAS f32x2*)(size_t)(L_STATS + (unsigned)(32 * tq + t) * 8u) = f32x2{rstd, mr};
  if (ffn) VMW(8); else VMW(4);
  BAR();
  const int cb = (lane >> 4) & 1, q = (lane >> 2) & 3, pp = lane & 3, sw = (q >> 1) & 1;
  unsigned wl[2];
#pragma unroll
  for (int h = 0; h < 2; ++h) wl[h] = (unsigned)(8 * hh + q) * 128u + (unsigned)((4 * (h ^ sw) + 2 * cb + (pp >> 1)) * 16 + 8 * (pp & 1));
  const __amdgpu_buffer_rsrc_t rsHB = __builtin_amdgcn_make_buffer_rsrc(P.ws + WS_HB, 0, HB_BYTES, 0x00020000);
  const unsigned T32 = (unsigned)(c.p * 4 + tq);
  f32x4 cs = {0.f, 0.f, 0.f, 0.f};
  auto cs_sync = [&](int tile, bool wr) AI {
    const float s = xhalf_add(cs[0]);
    if (hh == 0 && wr) *(LAS float*)(size_t)(L_CS1 + (unsigned)tile * 128u + (unsigned)t * 4u) = s;
    asm volatile("s_waitcnt lgkmcnt(0)" ::: "memory");
    lds_inc(L_CNT + C_CS1 * 4, lane);
    lds_wait_ge(L_CNT + C_CS1 * 4, 4);
  };
  if constexpr (ffn) {
    const unsigned w2a = ((g & 1) ? wl[1] : wl[0]) + 16384u;
    int ti[3];
    unsigned wb[3];
    f32x16 acc[3];
#pragma unroll
    for (int i = 0; i < 3; ++i) { ti[i] = (i + tq) % 3; wb[i] = ti[i] == 0 ? wl[0] : (ti[i] == 1 ? wl[1] : w2a); acc[i] = zero16(); }
    pass1<3, 8>(P, wid, amp_a, amp_b, fill, xr, acc, cs, wb, lane);
    cs_sync(ti[0], tq < 3);
    STAMP(3);
    if (PRIOL) __builtin_amdgcn_s_setprio(3);
    f32x4 csv[3][4], bsv[3][4];
#pragma unroll
    for (int i = 0; i < 3; ++i) { load16(L_CS1 + (unsigned)ti[i] * 128u, hh, csv[i]); load16(L_B1 + (unsigned)(4 + ti[i]) * 128u, hh, bsv[i]); }
#pragma unroll
    for (int i = 0; i < 3; ++i)
      ffn_tile(acc[i], rstd, mr, csv[i], bsv[i], rsHB, (unsigned)lane * 16u + (T32 * 144u + (unsigned)(64 + 6 * g + 2 * ti[i])) * 1024u);
    publish_last(L_CNT + C_LATE * 4, 4, P.ws, FL_L + (unsigned)(c.p * 8 + g) * 128u, tag, lane);
    if (PRIOL) __builtin_amdgcn_s_setprio(0);
  } else {
    int ti[2];
    unsigned wb[2];
    f32x16 acc[2];
#pragma unroll
    for (int i = 0; i < 2; ++i) { ti[i] = (i + tq) & 1; wb[i] = ti[i] == 0 ? wl[0] : wl[1]; acc[i] = zero16(); }
    pass1<2, 4>(P, wid, amp_a, amp_b, fill, xr, acc, cs, wb, lane);
    cs_sync(ti[0], tq < 2);
    STAMP(3);
    v4u pq[4][2];
#pragma unroll
    for (int hd = 0; hd < 4; ++hd) {
      float y[8];
      fold8(acc[hd >> 1], 8 * (hd & 1), rstd, mr, L_CS1 + (unsigned)ti[hd >> 1] * 128u, hh, y);
      hedge(mfma(wphi_frag(2 * ti[hd >> 1] + (hd & 1), lane), pack8(y), zero16()), pq[hd][0], pq[hd][1]);
    }
    asm volatile("" : "+v"(pq[0][0]), "+v"(pq[0][1]), "+v"(pq[1][0]), "+v"(pq[1][1]), "+v"(pq[2][0]), "+v"(pq[2][1]), "+v"(pq[3][0]), "+v"(pq[3][1]));
    STAMP(4);
    if (PRIO) __builtin_amdgcn_s_setprio(3);
    if (tq == 0) { poll_flags(P.ws, FL_K + (unsigned)g * 16u * 128u, 16, tag, lane); lds_set(L_CNT + C_KVRDY * 4, 1); }
    else lds_wait_ge(L_CNT + C_KVRDY * 4, 1);
    STAMP(5);
    {
      const __amdgpu_buffer_rsrc_t rsKV = __builtin_amdgcn_make_buffer_rsrc(P.ws + WS_KVP, 0, 524288 + 65536, 0x00020000);
      const unsigned hbase = (unsigned)(g * 4 + tq) * 16u;
      v4u pv[16];
#pragma unroll
      for (int i = 0; i < 16; ++i) pv[i] = __builtin_amdgcn_raw_buffer_load_b128(rsKV, (unsigned)lane * 16u, (hbase + (unsigned)i) * 1024u, 16);
      const v4u z0 = __builtin_amdgcn_raw_buffer_load_b128(rsKV, (unsigned)lane * 16u, 524288u + hbase * 128u, 16);
      const v4u z1 = __builtin_amdgcn_raw_buffer_load_b128(rsKV, (unsigned)lane * 16u, 524288u + hbase * 128u + 1024u, 16);
      const u64 lf = __hip_atomic_load((const GAS u64*)(P.ws + FL_L + (unsigned)(c.tt >> 1) * 8u * 128u + (unsigned)(lane & 7) * 128u), __ATOMIC_RELAXED, __HIP_MEMORY_SCOPE_AGENT);
      __builtin_amdgcn_sched_barrier(0);
      v4u idf;
      {
        const int m = lane & 31, h2 = lane >> 5;
        const int j = (m < 16 && ((m >> 2) & 1) == h2) ? (((m >> 3) << 2) | (m & 3)) : -1;
#pragma unroll
        for (int k = 0; k < 4; ++k) idf[k] = (j == 2 * k ? 0x3F80u : 0u) | (j == 2 * k + 1 ? 0x3F800000u : 0u);
      }
      f32x16 sum = zero16();
#pragma unroll
      for (int i = 0; i < 16; ++i) sum = mfma(idf, pv[i], sum);
      asm volatile("" : "+v"(sum));
      lds_inc(L_CNT + C_KVIN * 4, lane);
      if (!ORD && tq == 0 && __builtin_amdgcn_ballot_w64(lf != tag) == 0) lds_set(L_CNT + C_LATERDY * 4, 1);
      const float s[8] = {sum[0], sum[1], sum[2], sum[3], sum[4], sum[5], sum[6], sum[7]};
      const unsigned afr = L_AFR + (unsigned)tq * 2048u;
      *(LAS v4u*)(size_t)(afr + (unsigned)((lane >> 4) & 1) * 1024u + (unsigned)((lane & 15) + 32 * (lane >> 5)) * 16u) = pack8(s);
      const unsigned hh2 = (unsigned)(lane >> 4) & 1u, c16 = (unsigned)lane & 15u;
      float zz[4];
#pragma unroll
      for (int k = 0; k < 4; ++k) {
        float v = dppadd<0x128>(u2f(z0[k]) + u2f(z1[k]));
        auto r = __builtin_amdgcn_permlane16_swap(f2u(v), f2u(v), false, false);
        zz[k] = xhalf_add(u2f(r[0]) + u2f(r[1]));
      }
      if (lane < 8) *(LAS f32x4*)(size_t)(L_ZT + (unsigned)tq * 128u + (unsigned)lane * 16u) = f32x4{zz[0], zz[1], zz[2], zz[3]};
      const unsigned f0 = 16u * (unsigned)(lane >> 5) + 4u * hh2;
      const f32x4 za = lds128f(L_ZT + (unsigned)tq * 128u + f0 * 4u), zb = lds128f(L_ZT + (unsigned)tq * 128u + (f0 + 8u) * 4u);
      *(LAS v4u*)(size_t)(afr + (unsigned)(lane >> 5) * 1024u + (16u + c16 + 32u * hh2) * 16u) = v4u{pk2(za[0], za[1]), pk2(za[2], za[3]), pk2(zb[0], zb[1]), pk2(zb[2], zb[3])};
      asm volatile("s_waitcnt lgkmcnt(0)" ::: "memory");
      lds_inc(L_CNT + C_AFR * 4, lane);
      lds_wait_ge(L_CNT + C_AFR * 4, 4);
    }
#pragma unroll
    for (int hd = 0; hd < 4; ++hd) {
      const int head = 2 * ti[hd >> 1] + (hd & 1);
      f32x16 d = mfma(lds128(L_AFR + (unsigned)head * 2048u + (unsigned)lane * 16u), pq[hd][0], zero16());
      d = mfma(lds128(L_AFR + (unsigned)head * 2048u + 1024u + (unsigned)lane * 16u), pq[hd][1], d);
      const float r = __builtin_amdgcn_rcpf(d[8] + 1e-6f);
      const v4u o = v4u{pk2(d[0] * r, d[1] * r), pk2(d[2] * r, d[3] * r), pk2(d[4] * r, d[5] * r), pk2(d[6] * r, d[7] * r)};
      st128(o, rsHB, (unsigned)lane * 16u + (T32 * 144u + (unsigned)(112 + 4 * g + head)) * 1024u);
    }
    publish_last(L_CNT + C_O * 4, 4, P.ws, FL_O + (unsigned)(c.p * 8 + g) * 128u, tag, lane);
    if (PRIO) __builtin_amdgcn_s_setprio(0);
  }
  STAMP(6);
  return tag;
}

DI void role_b(const Params& P, const Ctx& c, int wid, u64 amp_a0) {
  u64 amp_a = amp_a0, amp_b = 0; (void)amp_a; (void)amp_b;
  const int lane = c.lane, tq = c.tq;
  const u64 tag = c.ffn ? role_b_head<true>(P, c, wid) : role_b_head<false>(P, c, wid);
  const int pan = c.tt >> 1;
  const int tb = tq * 64 + lane;
  v4u xres[2], bres;
#pragma unroll
  for (int m = 0; m < 2; ++m) {
    const int idx = tb + 256 * m;
    xres[m] = *(const GAS v4u*)(P.x + (size_t)(64 * c.tt + (idx >> 3)) * 512 + 64 * c.ct + 8 * (idx & 7));
  }
  bres = *(const GAS v4u*)(P.b2 + 64 * c.ct + 8 * (tb & 7));
  if (tq == 0) {
    poll_flags(P.ws, (ORD ? FL_O : FL_L) + (unsigned)pan * 8u * 128u, 8, tag, lane);
    lds_set(L_CNT + (ORD ? C_ORDY : C_LATERDY) * 4, 1);
    poll_flags(P.ws, (ORD ? FL_L : FL_O) + (unsigned)pan * 8u * 128u, 8, tag, lane);
    lds_set(L_CNT + (ORD ? C_LATERDY : C_ORDY) * 4, 1);
  } else if (tq == 1 && __builtin_amdgcn_workgroup_id_x() == 0) {
    poll_flags(P.ws, FL_E, 64, tag, lane); poll_flags(P.ws, FL_E + 64 * 128, 64, tag, lane);
    poll_flags(P.ws, FL_K, 64, tag, lane); poll_flags(P.ws, FL_K + 64 * 128, 64, tag, lane);
    if (lane == 0) __hip_atomic_store((GAS u64*)(P.ws + WS_EPOCH), tag, __ATOMIC_RELAXED, __HIP_MEMORY_SCOPE_AGENT);
  }
  BAR();
#pragma unroll
  for (int m = 0; m < 2; ++m) {
    const int idx = tb + 256 * m;
    const unsigned tok = (unsigned)(idx >> 3), cg = (unsigned)(idx & 7);
    const unsigned a = (tok >> 5) * SLOT + (tok & 31u) * 256u, c0 = ((2u * cg) ^ (tok & 15u)) * 16u, c1 = ((2u * cg + 1u) ^ (tok & 15u)) * 16u;
    f32x4 lo = lds128f(a + c0), hi = lds128f(a + c1);
#pragma unroll
    for (unsigned w = 1; w < 4; ++w) { lo += lds128f(w * 8192u + a + c0); hi += lds128f(w * 8192u + a + c1); }
    v4u o;
#pragma unroll
    for (int k = 0; k < 4; ++k) {
      const float a0 = k < 2 ? lo[2 * k] : hi[2 * k - 4], a1 = k < 2 ? lo[2 * k + 1] : hi[2 * k - 3];
      const float x0 = u2f(xres[m][k] << 16), x1 = u2f(xres[m][k] & 0xffff0000u);
      const float b0 = u2f(bres[k] << 16), b1 = u2f(bres[k] & 0xffff0000u);
      o[k] = pk2(a0 + x0 + b0, a1 + x1 + b1);
    }
    *(GAS v4u*)(P.out + (size_t)(64 * c.tt + (idx >> 3)) * 512 + 64 * c.ct + 8 * (idx & 7)) = o;
  }
  STAMP(7);
}

__global__ __launch_bounds__(512, 2) NOPK void fused_kernel(Params P) {
  __shared__ __attribute__((aligned(1024))) char smem[LDS_BYTES];
  asm volatile("" ::"v"((unsigned)(size_t)(LAS char*)smem) : "memory");
  u64 pc;
  asm volatile("s_getpc_b64 %0" : "=s"(pc));
  const int tid = __builtin_amdgcn_workitem_id_x(), lane = tid & 63;
  const int wid = __builtin_amdgcn_readfirstlane(tid >> 6);
  u64 amp_a = 0, amp_b = 0;
  STAMP(0);
  Ctx c;
  c.lane = lane; c.tq = wid & 3; c.hh = lane >> 5;
  const int blk = __builtin_amdgcn_workgroup_id_x(), xcd = blk & 7, j = blk >> 3, ja = j & 7, jb = j >> 3;
  c.p = (xcd & 1) * 8 + ja;
  c.G = (xcd >> 1) * 4 + jb;
  c.ffn = !(c.G & 1);
  c.gi = c.G >> 1;
  c.rot = ROT1 == 0 ? 0 : (ROT1 == 1 ? (((ja + jb) % 3) * 3) & 7 : (ROT1 == 2 ? (ja + 2 * jb) & 7 : ((ja + jb) & 1) * 4));
  c.tt = (xcd >> 1) * 8 + ja; c.ct = (xcd & 1) * 4 + jb; c.ja = ja; c.jb = jb;
  c.rotE = ROT2 ? (2 * ja + 4 * jb) & 15 : 0; c.rotL = ROT2 ? (ja + 3 * jb) % 12 : 0; c.rotO = ROT2 ? (ja + 2 * jb) & 7 : 0;
  if (wid < 4) {
    const unsigned kb0 = (unsigned)(j + 32 * (wid & 1)), kb = kb0 < CODE_KB ? kb0 : 0u;
    const unsigned pf = CODEPF ? *(const GAS unsigned*)((pc & ~255ull) + kb * 1024u + (unsigned)lane * 16u) : 0u;
    const u64 e = __hip_atomic_load((const GAS u64*)(P.ws + WS_EPOCH), __ATOMIC_RELAXED, __HIP_MEMORY_SCOPE_AGENT);
    if (c.ffn) {
      const int tc = tid < 224 ? tid : 0;
      const int t7 = tc >> 5, n = tc & 31, g = c.gi;
      const int cA = 224 * g + 32 * (g & 1);
      const int col = t7 < 4 ? cA + 32 * t7 : (t7 < 6 ? cA + 128 + 32 * (t7 - 4) : ((g & 1) ? 224 * g : 224 * g + 192));
      const unsigned bv = P.b1[col + n];
      *(LAS float*)(size_t)(L_B1 + (unsigned)tid * 4u) = u2f(bv << 16);
    } else {
      const v4u wv = *(const GAS v4u*)(P.wphi + (size_t)c.gi * 1024 + (tid & 127) * 8);
      *(LAS v4u*)(size_t)(L_WPHI + (unsigned)(tid & 127) * 16u) = wv;
    }
    if (tid < 32) *(LAS unsigned*)(size_t)(L_CNT + (unsigned)tid * 4u) = 0u;
    const u64 tag = e + 1u;
    if (tid == 0) *(LAS v2u*)(size_t)L_TAG = v2u{(unsigned)tag, (unsigned)(tag >> 32)};
    asm volatile("" ::"v"(pf));
    STAMP(1);
    role_a(P, c, wid, amp_a, amp_b);
  } else {
    STAMP(1);
    role_b(P, c, wid, amp_a);
  }
}

}

extern "C" void kernel_launch(void* const* d_in, const int* in_sizes, int n_in, void* d_out, int out_size, void* d_ws, size_t ws_size,
                              hipStream_t stream) {
  Params P;
  P.x = (const u16*)d_in[0]; P.wqkv = (const u16*)d_in[1]; P.wphi = (const u16*)d_in[2]; P.wo = (const u16*)d_in[3];
  P.w1 = (const u16*)d_in[5]; P.b1 = (const u16*)d_in[6]; P.w2 = (const u16*)d_in[7]; P.b2 = (const u16*)d_in[8];
  P.out = (u16*)d_out; P.ws = (char*)d_ws;
  fused_kernel<<<256, 512, 0, stream>>>(P);
}
```

```cpp
#include <hip/hip_runtime.h>
#include <stdint.h>
#include <type_traits>

namespace {

typedef unsigned short u16;
typedef unsigned long long u64;
typedef unsigned v4u __attribute__((ext_vector_type(4)));
typedef unsigned v2u __attribute__((ext_vector_type(2)));
typedef float f32x16 __attribute__((ext_vector_type(16)));
typedef float f32x4 __attribute__((ext_vector_type(4)));
typedef float f32x2 __attribute__((ext_vector_type(2)));
typedef __bf16 bf16x8 __attribute__((ext_vector_type(8)));
typedef __bf16 bf16x2 __attribute__((ext_vector_type(2)));
typedef short s16x4 __attribute__((ext_vector_type(4)));
#define LAS __attribute__((address_space(3)))
#define GAS __attribute__((address_space(1)))
#if defined(__HIP_DEVICE_COMPILE__)
#define NOPK __attribute__((target("no-packed-fp32-ops")))
#else
#define NOPK
#endif
#define DI __device__ __forceinline__ NOPK
#define AI __attribute__((always_inline)) NOPK

#define STAMPS 3
#define AMPI 0
#define AMPJ 1
#define AMPX 20
#define AMPB 0
#define ROT1 1
#define ROT2 0
#define STR2(x) #x
#define STR(x) STR2(x)
#define W1 8
#define W2 4
#define PRIO 1
#define SLP 1
#define KVFIRST 0
#define PRIOL 0
#define ORD 0
#define CODEPF 1
#define WARM2 1
#define CODE_KB 36

constexpr unsigned WS_EPOCH = 0;
constexpr unsigned FL_E = 4096;
constexpr unsigned FL_L = FL_E + 128 * 128;
constexpr unsigned FL_K = FL_L + 128 * 128;
constexpr unsigned FL_O = FL_K + 128 * 128;
constexpr unsigned WS_ST = 73728;
constexpr unsigned WS_KVP = 131072;
constexpr unsigned WS_ZP = WS_KVP + 524288;
constexpr unsigned WS_HB = 1048576;
constexpr unsigned HB_BYTES = 64u * 144u * 1024u;
constexpr unsigned WS_STAMP = 16u << 20;

constexpr unsigned SLOT = 32768;
constexpr unsigned L_MISC = 131072;
constexpr unsigned L_STATS = L_MISC;
constexpr unsigned L_CS0 = L_MISC + 1024;
constexpr unsigned L_CS1 = L_MISC + 1536;
constexpr unsigned L_B1 = L_MISC + 2048;
constexpr unsigned L_WPHI = L_MISC + 3072;
constexpr unsigned L_CNT = L_MISC + 5120;
constexpr unsigned L_TAG = L_MISC + 5248;
constexpr unsigned L_ZT = L_MISC + 5376;
constexpr unsigned L_AFR = L_MISC + 8192;
constexpr unsigned LDS_BYTES = 163840;
enum { C_CS1 = 0, C_LATE = 1, C_KV = 2, C_O = 3, C_AFR = 4, C_KVRDY = 5, C_LATERDY = 6, C_ORDY = 7, C_FREE = 8  , C_LAND = 12  , C_EARLY = 16, C_IMG = 17, C_KVIN = 18, C_IMGDONE = 19 };

constexpr unsigned kSpin = 1u << 16;

struct Params {
  const u16* x; const u16* wqkv; const u16* wphi; const u16* wo;
  const u16* w1; const u16* b1; const u16* w2; const u16* b2;
  u16* out; char* ws;
};

DI unsigned f2u(float f) { return __builtin_bit_cast(unsigned, f); }
DI float u2f(unsigned u) { return __builtin_bit_cast(float, u); }
DI float fmx(float a, float b) { return __builtin_fmaxf(a, b); }
DI f32x16 mfma(v4u a, v4u b, f32x16 c) {
  return __builtin_amdgcn_mfma_f32_32x32x16_bf16(__builtin_bit_cast(bf16x8, a), __builtin_bit_cast(bf16x8, b), c, 0, 0, 0);
}
DI f32x16 zero16() { f32x16 z; for (int r = 0; r < 16; ++r) z[r] = 0.f; return z; }
DI v4u lds128(unsigned a) { return *(const LAS v4u*)(size_t)a; }
DI f32x4 lds128f(unsigned a) { return *(const LAS f32x4*)(size_t)a; }
DI v2u tr64(unsigned a) { return __builtin_bit_cast(v2u, __builtin_amdgcn_ds_read_tr16_b64_v4i16((LAS s16x4*)(size_t)a)); }
DI v4u tr2(unsigned a0, unsigned a1) { v2u l = tr64(a0), h = tr64(a1); return v4u{l.x, l.y, h.x, h.y}; }
DI unsigned pk2(float a, float b) { return __builtin_bit_cast(unsigned, __builtin_convertvector(f32x2{a, b}, bf16x2)); }
DI float xhalf_add(float v) { auto r = __builtin_amdgcn_permlane32_swap(f2u(v), f2u(v), false, false); return u2f(r[0]) + u2f(r[1]); }
DI float xhalf_max(float v) { auto r = __builtin_amdgcn_permlane32_swap(f2u(v), f2u(v), false, false); return fmx(u2f(r[0]), u2f(r[1])); }
DI float shx(float v, int m, int lane) { return u2f((unsigned)__builtin_amdgcn_ds_bpermute((lane ^ m) << 2, (int)f2u(v))); }
DI f32x4 lanesum(v4u f, f32x4 c) {
  const s16x4 one = {0x3F80, 0x3F80, 0x3F80, 0x3F80};
  c = __builtin_amdgcn_mfma_f32_4x4x4bf16_1k(one, __builtin_bit_cast(s16x4, v2u{f.x, f.y}), c, 0, 0, 0);
  return __builtin_amdgcn_mfma_f32_4x4x4bf16_1k(one, __builtin_bit_cast(s16x4, v2u{f.z, f.w}), c, 0, 0, 0);
}
template <int CTRL> DI float dppadd(float v) { return v + u2f((unsigned)__builtin_amdgcn_update_dpp(0, (int)f2u(v), CTRL, 0xf, 0xf, true)); }
DI float wavesum(float v) {
  v = dppadd<0xB1>(v); v = dppadd<0x4E>(v); v = dppadd<0x141>(v); v = dppadd<0x140>(v);
  auto r = __builtin_amdgcn_permlane16_swap(f2u(v), f2u(v), false, false);
  return xhalf_add(u2f(r[0]) + u2f(r[1]));
}
DI void dma(unsigned m0v, unsigned voff, __amdgpu_buffer_rsrc_t rs, unsigned soff) {
  asm volatile("s_mov_b32 m0, %0\n\ts_nop 0\n\tbuffer_load_dwordx4 %1, %2, %3 offen lds" ::"s"(m0v), "v"(voff), "s"(rs), "s"(soff) : "memory");
}
DI void st128(v4u v, __amdgpu_buffer_rsrc_t rs, unsigned voff) { __builtin_amdgcn_raw_buffer_store_b128(v, rs, voff, 0, 16); }
#define BAR() do { __builtin_amdgcn_sched_barrier(0); asm volatile("s_waitcnt lgkmcnt(0)\n\ts_barrier" ::: "memory"); __builtin_amdgcn_sched_barrier(0); } while (0)
#define VMW(n) asm volatile("s_waitcnt vmcnt(" #n ")" ::: "memory")
constexpr unsigned ONES = 0x3F803F80u;

#define STAMP(i) do { if ((i) == AMPI) amp_a = __builtin_amdgcn_s_memrealtime(); if ((i) == AMPJ) amp_b = __builtin_amdgcn_s_memrealtime(); } while (0)

DI void poll_flags(const char* ws, unsigned base, int n, u64 tag, int lane) {
  const GAS u64* f = (const GAS u64*)(ws + base + (unsigned)(lane < n ? lane : 0) * 128u);
#pragma unroll 1
  for (unsigned spin = 0; spin < kSpin; ++spin) {
    const u64 v = __hip_atomic_load(f, __ATOMIC_RELAXED, __HIP_MEMORY_SCOPE_AGENT);
    if (__builtin_amdgcn_ballot_w64(v != tag) == 0) break;
    __builtin_amdgcn_s_sleep(1);
  }
  __builtin_amdgcn_fence(__ATOMIC_ACQUIRE, "wavefront");
  asm volatile("" ::: "memory");
}
DI void lds_wait_ge(unsigned addr, unsigned want) {
#pragma unroll 1
  for (unsigned spin = 0; spin < (kSpin << 4); ++spin) {
    const unsigned v = *(volatile LAS unsigned*)(size_t)addr;
    if ((unsigned)__builtin_amdgcn_readfirstlane(v) >= want) break;
    if (SLP) __builtin_amdgcn_s_sleep(SLP);
  }
  asm volatile("" ::: "memory");
}
DI void lds_set(unsigned addr, unsigned v) { *(volatile LAS unsigned*)(size_t)addr = v; }
DI void lds_inc(unsigned addr, int lane) { if (lane == 0) __hip_atomic_fetch_add((LAS unsigned*)(size_t)addr, 1u, __ATOMIC_RELAXED, __HIP_MEMORY_SCOPE_WORKGROUP); }
DI void publish_last(unsigned cnt_addr, unsigned n, char* ws, unsigned flag_off, u64 tag, int lane) {
  VMW(0);
  if (lane == 0) {
    const unsigned old = __hip_atomic_fetch_add((LAS unsigned*)(size_t)cnt_addr, 1u, __ATOMIC_RELAXED, __HIP_MEMORY_SCOPE_WORKGROUP);
    if (old == n - 1) __hip_atomic_store((GAS u64*)(ws + flag_off), tag, __ATOMIC_RELAXED, __HIP_MEMORY_SCOPE_AGENT);
  }
}
DI u64 lds_tag() { const v2u t = *(const LAS v2u*)(size_t)L_TAG; return ((u64)(unsigned)__builtin_amdgcn_readfirstlane(t.y) << 32) | (unsigned)__builtin_amdgcn_readfirstlane(t.x); }

DI void fold8(const f32x16& acc, int r0, float rstd, float mr, unsigned cs, int hh, float* y) {
#pragma unroll
  for (int g = 0; g < 2; ++g) {
    const f32x4 t = lds128f(cs + (unsigned)((r0 >> 2) + g) * 32u + (unsigned)hh * 16u) * mr;
#pragma unroll
    for (int k = 0; k < 4; ++k) y[4 * g + k] = __builtin_fmaf(acc[r0 + 4 * g + k], rstd, t[k]);
  }
}
DI void load16(unsigned a, int hh, f32x4* v) {
#pragma unroll
  for (int g = 0; g < 4; ++g) v[g] = lds128f(a + (unsigned)g * 32u + (unsigned)hh * 16u);
}
DI void ffn_tile(const f32x16& acc, float rstd, float mr, const f32x4* cs, const f32x4* bs, __amdgpu_buffer_rsrc_t rsHB, unsigned voff) {
  f32x2 y[8], d[8];
#pragma unroll
  for (int g = 0; g < 4; ++g) {
    const f32x4 t = cs[g] * mr + bs[g];
    y[2 * g] = f32x2{acc[4 * g], acc[4 * g + 1]} * rstd + f32x2{t[0], t[1]};
    y[2 * g + 1] = f32x2{acc[4 * g + 2], acc[4 * g + 3]} * rstd + f32x2{t[2], t[3]};
  }
#pragma unroll
  for (int k = 0; k < 8; ++k) d[k] = y[k] * ((y[k] * y[k]) * (-0.10294324f) + (-2.30220819f));
#pragma unroll
  for (int k = 0; k < 8; ++k) { d[k].x = __builtin_amdgcn_exp2f(d[k].x); d[k].y = __builtin_amdgcn_exp2f(d[k].y); }
#pragma unroll
  for (int k = 0; k < 8; ++k) d[k] += 1.0f;
#pragma unroll
  for (int k = 0; k < 8; ++k) { d[k].x = __builtin_amdgcn_rcpf(d[k].x); d[k].y = __builtin_amdgcn_rcpf(d[k].y); }
  v4u o0, o1;
#pragma unroll
  for (int k = 0; k < 4; ++k) {
    const f32x2 g0 = y[k] * d[k], g1 = y[4 + k] * d[4 + k];
    o0[k] = pk2(g0.x, g0.y); o1[k] = pk2(g1.x, g1.y);
  }
  st128(o0, rsHB, voff); st128(o1, rsHB, voff + 1024u);
}
DI void hedge(const f32x16& d, v4u& o0, v4u& o1) {
  constexpr float L = 1.4426950408889634f;
  float m0 = fmx(fmx(fmx(d[0], d[1]), fmx(d[2], d[3])), fmx(fmx(d[4], d[5]), fmx(d[6], d[7])));
  float m1 = fmx(fmx(fmx(d[8], d[9]), fmx(d[10], d[11])), fmx(fmx(d[12], d[13]), fmx(d[14], d[15])));
  m0 = xhalf_max(m0) * -L; m1 = xhalf_max(m1) * -L;
  float e[16];
#pragma unroll
  for (int i = 0; i < 8; ++i) { e[i] = __builtin_amdgcn_exp2f(__builtin_fmaf(d[i], L, m0)); e[8 + i] = __builtin_amdgcn_exp2f(__builtin_fmaf(d[8 + i], L, m1)); }
  float s0 = ((e[0] + e[1]) + (e[2] + e[3])) + ((e[4] + e[5]) + (e[6] + e[7]));
  float s1 = ((e[8] + e[9]) + (e[10] + e[11])) + ((e[12] + e[13]) + (e[14] + e[15]));
  s0 = __builtin_amdgcn_rcpf(xhalf_add(s0)); s1 = __builtin_amdgcn_rcpf(xhalf_add(s1));
#pragma unroll
  for (int k = 0; k < 4; ++k) { o0[k] = pk2(e[2 * k] * s0, e[2 * k + 1] * s0); o1[k] = pk2(e[8 + 2 * k] * s1, e[8 + 2 * k + 1] * s1); }
}
DI v4u wphi_frag(int hd, int lane) {
  const int hh = lane >> 5, q = (lane >> 2) & 3, pp = lane & 3;
  const unsigned a = L_WPHI + (unsigned)hd * 512u + (unsigned)(4 * hh + q) * 32u + (unsigned)pp * 8u;
  v4u f = tr2(a, a + 256u);
  const unsigned sg = (lane & 16) ? 0x80008000u : 0u;
  f.x ^= sg; f.y ^= sg; f.z ^= sg; f.w ^= sg;
  return f;
}
DI v4u selv(bool s, v4u a, v4u b) { const unsigned m = s ? 0xffffffffu : 0u; return v4u{(a.x & m) | (b.x & ~m), (a.y & m) | (b.y & ~m), (a.z & m) | (b.z & ~m), (a.w & m) | (b.w & ~m)}; }
DI v4u pack8(const float* y) { return v4u{pk2(y[0], y[1]), pk2(y[2], y[3]), pk2(y[4], y[5]), pk2(y[6], y[7])}; }

template <int NI> DI void warm_w2(const Params& P, int ct, int ja, int tq, int i0, int lane, v4u* d) {
#pragma unroll
  for (int i = 0; i < NI; ++i) {
    const int row = 288 * ja + 72 * tq + 8 * (i0 + i);
    const u64 a = row < 1792 ? (u64)(size_t)P.w2 + (u64)(unsigned)row * 1024u : (u64)(size_t)P.wo + (u64)(unsigned)(row - 1792) * 1024u;
    const u64 base = ((u64)(unsigned)__builtin_amdgcn_readfirstlane((unsigned)(a >> 32)) << 32) | (unsigned)__builtin_amdgcn_readfirstlane((unsigned)a);
    const unsigned voff = (unsigned)(lane >> 3) * 1024u + (unsigned)(lane & 7) * 16u + (unsigned)ct * 128u;
    asm volatile("s_nop 4\n\tglobal_load_dwordx4 %0, %1, %2" : "=&v"(d[i]) : "v"(voff), "s"(base) : "memory");
  }
}
struct Ctx {
  int lane, tq, hh;
  int p, gi, G;
  int rot;
  int tt, ct, ja, jb;
  int rotE, rotL, rotO;
  bool ffn;
};

DI void role_a(const Params& P, const Ctx& c, int wid, u64 amp_a, u64 amp_b) {
  const int lane = c.lane, tq = c.tq, hh = c.hh, t = lane & 31;
  const int cb = (lane >> 4) & 1, q = (lane >> 2) & 3, pp = lane & 3, sw = (q >> 1) & 1;
  const int th = tq & 1, up = tq >> 1;
  unsigned xa[4];
#pragma unroll
  for (int ks = 0; ks < 4; ++ks) xa[ks] = (unsigned)(64 * th + t) * 128u + (unsigned)(((2 * ks + hh) ^ ((t >> 1) & 7)) * 16);
  const unsigned wl = 16384u + (unsigned)(8 * hh + q) * 128u + (unsigned)((4 * (up ^ sw) + 2 * cb + (pp >> 1)) * 16 + 8 * (pp & 1));
  f32x16 acc[2][2];
#pragma unroll
  for (int i = 0; i < 2; ++i) { acc[i][0] = zero16(); acc[i][1] = zero16(); }
  f32x4 cs = {0.f, 0.f, 0.f, 0.f};
  unsigned x0 = xa[0], x1 = xa[1], x2 = xa[2], x3 = xa[3], w0 = wl;
  v4u xf[4][2], wf[4][2];
  auto rd = [&](int ks, v4u* X, v4u* W) AI {
    const unsigned xb = ks == 0 ? x0 : (ks == 1 ? x1 : (ks == 2 ? x2 : x3));
    X[0] = lds128(xb); X[1] = lds128(xb + 4096u);
    const unsigned a = w0 + (unsigned)(ks * 2048);
    W[0] = tr2(a, a + 512u); W[1] = tr2(a + 8192u, a + 8704u);
  };
  f32x4 cs2 = {0.f, 0.f, 0.f, 0.f};
  BAR();
  rd(0, xf[0], wf[0]); rd(1, xf[1], wf[1]);
  auto pass0 = [&](auto TH) AI {
    constexpr int CT = decltype(TH)::value;
    const s16x4 one = {0x3F80, 0x3F80, 0x3F80, 0x3F80};
#pragma unroll 1
    for (int kc = 0; kc < 8; ++kc) {
#pragma unroll
      for (int ks = 0; ks < 4; ++ks) {
        if (ks == 2) {
          __builtin_amdgcn_sched_barrier(0);
          asm volatile("s_barrier" ::: "memory");
          __builtin_amdgcn_sched_barrier(0);
          const unsigned sb = (unsigned)((kc + 1) & 3) * SLOT;
          x0 = xa[0] + sb; x1 = xa[1] + sb; w0 = wl + sb;
          asm volatile("" : "+v"(x0), "+v"(x1), "+v"(w0));
        }
        if (ks == 0) {
          const unsigned sb = (unsigned)(kc & 3) * SLOT;
          x2 = xa[2] + sb; x3 = xa[3] + sb;
          asm volatile("" : "+v"(x2), "+v"(x3));
        }
        rd((ks + 2) & 3, xf[(ks + 2) & 3], wf[(ks + 2) & 3]);
        acc[0][0] = mfma(wf[ks][0], xf[ks][0], acc[0][0]); acc[0][1] = mfma(wf[ks][0], xf[ks][1], acc[0][1]);
        acc[1][0] = mfma(wf[ks][1], xf[ks][0], acc[1][0]); acc[1][1] = mfma(wf[ks][1], xf[ks][1], acc[1][1]);
        cs = __builtin_amdgcn_mfma_f32_4x4x4bf16_1k(one, __builtin_bit_cast(s16x4, v2u{wf[ks][CT].x, wf[ks][CT].y}), cs, 0, 0, 0);
        cs2 = __builtin_amdgcn_mfma_f32_4x4x4bf16_1k(one, __builtin_bit_cast(s16x4, v2u{wf[ks][CT].z, wf[ks][CT].w}), cs2, 0, 0, 0);
        __builtin_amdgcn_sched_group_barrier(0x008, 1, 0);
        __builtin_amdgcn_sched_group_barrier(0x100, 2, 0);
        __builtin_amdgcn_sched_group_barrier(0x008, 1, 0);
        __builtin_amdgcn_sched_group_barrier(0x100, 2, 0);
        __builtin_amdgcn_sched_group_barrier(0x008, 1, 0);
        __builtin_amdgcn_sched_group_barrier(0x100, 2, 0);
        __builtin_amdgcn_sched_group_barrier(0x008, 3, 0);
        __builtin_amdgcn_sched_barrier(0);
      }
      asm volatile("" : "+v"(acc[0][0]), "+v"(acc[0][1]), "+v"(acc[1][0]), "+v"(acc[1][1]), "+v"(cs), "+v"(cs2));
    }
  };
  if (th) pass0(std::integral_constant<int, 1>{}); else pass0(std::integral_constant<int, 0>{});
  cs[0] += cs2[0];
  STAMP(2);
  {
    const float s = xhalf_add(cs[0]);
    if (hh == 0) *(LAS float*)(size_t)(L_CS0 + (unsigned)(up + 2 * th) * 128u + (unsigned)t * 4u) = s;
  }
  BAR();
  const u64 tag = lds_tag();
  float rstd[2], mr[2];
#pragma unroll
  for (int b = 0; b < 2; ++b) {
    const f32x2 st = *(const LAS f32x2*)(size_t)(L_STATS + (unsigned)(64 * th + 32 * b + t) * 8u);
    rstd[b] = st.x; mr[b] = st.y;
  }
  const __amdgpu_buffer_rsrc_t rsHB = __builtin_amdgcn_make_buffer_rsrc(P.ws + WS_HB, 0, HB_BYTES, 0x00020000);
  v4u wm[9];
  if (c.ffn) {
    f32x4 csv[2][4], bsv[2][4];
#pragma unroll
    for (int i = 0; i < 2; ++i) { load16(L_CS0 + (unsigned)(up + 2 * i) * 128u, hh, csv[i]); load16(L_B1 + (unsigned)(up + 2 * i) * 128u, hh, bsv[i]); }
#pragma unroll
    for (int i = 0; i < 2; ++i)
#pragma unroll
      for (int b = 0; b < 2; ++b) {
        const unsigned tile = (unsigned)(up + 2 * i), T32 = (unsigned)(c.p * 4 + 2 * th + b);
        ffn_tile(acc[i][b], rstd[b], mr[b], csv[i], bsv[i], rsHB, (unsigned)lane * 16u + (T32 * 144u + (unsigned)(8 * c.gi) + 2u * tile) * 1024u);
        if (WARM2 && 2 * i + b < 3) warm_w2<3>(P, c.ct, c.ja, tq, 3 * (2 * i + b), lane, wm + 3 * (2 * i + b));
      }
    STAMP(8);
    publish_last(L_CNT + C_EARLY * 4, 4, P.ws, FL_E + (unsigned)(c.p * 8 + c.gi) * 128u, tag, lane);
    if (WARM2) asm volatile("" :: "v"(wm[0]), "v"(wm[1]), "v"(wm[2]), "v"(wm[3]), "v"(wm[4]), "v"(wm[5]), "v"(wm[6]), "v"(wm[7]), "v"(wm[8]));
  } else {
#pragma unroll
    for (int b = 0; b < 2; ++b)
#pragma unroll
      for (int hf = 0; hf < 2; ++hf) {
        const int hd = 2 * up + hf;
        float y[8];
        fold8(acc[0][b], 8 * hf, rstd[b], mr[b], L_CS0 + (unsigned)up * 128u, hh, y);
        v4u o0, o1;
        hedge(mfma(wphi_frag(hd, lane), pack8(y), zero16()), o0, o1);
        const unsigned img = (unsigned)hd * SLOT + 16384u;
        const unsigned row = (unsigned)(64 * th + 32 * b + t);
        const unsigned g = (row >> 1) & 7u;
        *(LAS v2u*)(size_t)(img + row * 64u + (((unsigned)(0 + hh)) ^ g) * 8u) = v2u{o0.x, o0.y};
        *(LAS v2u*)(size_t)(img + row * 64u + (((unsigned)(2 + hh)) ^ g) * 8u) = v2u{o0.z, o0.w};
        *(LAS v2u*)(size_t)(img + row * 64u + (((unsigned)(4 + hh)) ^ g) * 8u) = v2u{o1.x, o1.y};
        *(LAS v2u*)(size_t)(img + row * 64u + (((unsigned)(6 + hh)) ^ g) * 8u) = v2u{o1.z, o1.w};
        fold8(acc[1][b], 8 * hf, rstd[b], mr[b], L_CS0 + (unsigned)(2 + up) * 128u, hh, y);
        const unsigned gv = (row >> 2) & 3u;
        *(LAS v2u*)(size_t)(img + 8192u + row * 32u + (((unsigned)(0 + hh)) ^ gv) * 8u) = v2u{pk2(y[0], y[1]), pk2(y[2], y[3])};
        *(LAS v2u*)(size_t)(img + 8192u + row * 32u + (((unsigned)(2 + hh)) ^ gv) * 8u) = v2u{pk2(y[4], y[5]), pk2(y[6], y[7])};
        if (WARM2 && 2 * b + hf < 3) warm_w2<3>(P, c.ct, c.ja, tq, 3 * (2 * b + hf), lane, wm + 3 * (2 * b + hf));
      }
    STAMP(8);
    asm volatile("s_waitcnt lgkmcnt(0)" ::: "memory");
    lds_inc(L_CNT + C_IMG * 4, lane);
    lds_wait_ge(L_CNT + C_IMG * 4, 4);
    const unsigned img = (unsigned)tq * SLOT + 16384u;
    f32x16 kv = zero16();
    unsigned pa[2], va[2];
#pragma unroll
    for (int i2 = 0; i2 < 2; ++i2) {
      const unsigned r = (unsigned)(8 * hh + 4 * i2 + q);
      pa[i2] = img + r * 64u + (((unsigned)(4 * cb + pp)) ^ ((r >> 1) & 7u)) * 8u;
      va[i2] = img + 8192u + r * 32u + (((unsigned)pp) ^ ((r >> 2) & 3u)) * 8u;
    }
#pragma unroll
    for (int ks = 0; ks < 8; ++ks) {
      const v4u af = tr2(pa[0] + (unsigned)ks * 1024u, pa[1] + (unsigned)ks * 1024u);
      v4u bf = tr2(va[0] + (unsigned)ks * 512u, va[1] + (unsigned)ks * 512u);
      if (cb) bf = v4u{ONES, ONES, ONES, ONES};
      kv = mfma(af, bf, kv);
    }
    asm volatile("" : "+v"(kv) :: "memory");
    lds_inc(L_CNT + C_IMGDONE * 4, lane);
    const unsigned hidx = (unsigned)(c.gi * 4 + tq) * 16u + (unsigned)c.p;
    const __amdgpu_buffer_rsrc_t rsKV = __builtin_amdgcn_make_buffer_rsrc(P.ws + WS_KVP, 0, 524288 + 65536, 0x00020000);
    if (cb == 0) {
#pragma unroll
      for (int s = 0; s < 2; ++s) {
        const v4u o = v4u{pk2(kv[8 * s], kv[8 * s + 1]), pk2(kv[8 * s + 2], kv[8 * s + 3]), pk2(kv[8 * s + 4], kv[8 * s + 5]), pk2(kv[8 * s + 6], kv[8 * s + 7])};
        st128(o, rsKV, (unsigned)((16 * s + (lane & 15) + 32 * hh) * 16) + hidx * 1024u);
      }
    } else if ((lane & 15) == 0) {
#pragma unroll
      for (int g4 = 0; g4 < 4; ++g4) {
        const v4u o = v4u{f2u(kv[4 * g4]), f2u(kv[4 * g4 + 1]), f2u(kv[4 * g4 + 2]), f2u(kv[4 * g4 + 3])};
        st128(o, rsKV, (unsigned)((8 * g4 + 4 * hh) * 4) + 524288u + hidx * 128u);
      }
    }
    publish_last(L_CNT + C_KV * 4, 4, P.ws, FL_K + (unsigned)(c.gi * 16 + c.p) * 128u, tag, lane);
    if (WARM2) asm volatile("" :: "v"(wm[0]), "v"(wm[1]), "v"(wm[2]), "v"(wm[3]), "v"(wm[4]), "v"(wm[5]), "v"(wm[6]), "v"(wm[7]), "v"(wm[8]));
  }
  STAMP(3);
  unsigned tabB, tabWlo, tabWhi;
  {
    const int jj = lane < 36 ? lane : 35;
    constexpr int J2 = ORD ? 24 : 28;
    int sg;
    auto perm = [&](int i, int n) AI {
      if (!ROT2) return i;
      const int b = c.jb;
      if (i == 0) return b;
      const int m = (i - 1) >> 2, r = (i - 1) & 3;
      if (m < n - 1) return r == 0 ? 4 * (m + 1) + b : 4 * m + ((b + r) & 3);
      return 4 * m + ((b + 1 + r) & 3);
    };
    if (jj < 16) sg = 4 * perm(jj, 4) + tq;
    else if ((jj < J2) != (ORD != 0)) sg = 64 + 4 * perm(jj - (ORD ? 24 : 16), 3) + tq;
    else sg = 112 + 4 * perm(jj - (ORD ? 16 : 28), 2) + tq;
    int row;
    if (sg < 64) { const int g = sg >> 3; row = 224 * g + 32 * (g & 1) + 16 * (sg & 7); }
    else if (sg < 112) {
      const int e = sg - 64, g = e / 6, i = e - 6 * g;
      row = i < 4 ? 224 * g + 32 * (g & 1) + 128 + 16 * i : ((g & 1) ? 224 * g : 224 * g + 192) + 16 * (i - 4);
    } else row = 16 * (sg - 112);
    const u64 a = (u64)(size_t)(sg < 112 ? P.w2 : P.wo) + (u64)row * 1024u + (u64)c.ct * 128u;
    tabB = (unsigned)sg * 1024u; tabWlo = (unsigned)a; tabWhi = (unsigned)(a >> 32);
  }
  const unsigned vw0 = (unsigned)(lane >> 3) * 1024u + (unsigned)(((lane & 7) ^ (((lane >> 4) & 1) << 2)) * 16), vw1 = vw0 + 8192u;
  const unsigned ring = (unsigned)tq * 8192u;
  auto pofs = [&](int r) AI { return (unsigned)((r >> 2) & 3) * SLOT + (unsigned)(r & 3) * 2048u; };
  auto wdma = [&](int j) AI {
    const unsigned lo = __builtin_amdgcn_readlane(tabWlo, j), hi = __builtin_amdgcn_readlane(tabWhi, j);
    const u64 b = ((u64)hi << 32) | lo;
    const unsigned m = ring + pofs(j);
    asm volatile("s_nop 4\n\ts_mov_b32 m0, %0\n\ts_nop 0\n\tglobal_load_lds_dwordx4 %1, %3\n\ts_add_u32 m0, m0, 0x400\n\ts_nop 0\n\tglobal_load_lds_dwordx4 %2, %3"
                 ::"s"(m), "v"(vw0), "v"(vw1), "s"(b) : "memory", "scc");
  };
  const unsigned vb0 = (unsigned)lane * 16u + (unsigned)(2 * c.tt) * 144u * 1024u, vb1 = vb0 + 144u * 1024u;
  constexpr int NB = 4;
  v4u br[NB][2];
#pragma unroll
  for (int u = 0; u < NB; ++u) { br[u][0] = v4u{0u, 0u, 0u, 0u}; br[u][1] = br[u][0]; }
  auto bload = [&](unsigned so, v4u& d0, v4u& d1) AI {
    asm volatile("s_nop 4\n\tbuffer_load_dwordx4 %0, %2, %4, %5 offen sc1\n\tbuffer_load_dwordx4 %1, %3, %4, %5 offen sc1"
                 : "+v"(d0), "+v"(d1) : "v"(vb0), "v"(vb1), "s"(rsHB), "s"(so) : "memory");
  };
  if (!c.ffn && tq >= 2) lds_wait_ge(L_CNT + C_IMGDONE * 4, 4);
#pragma unroll 1
  for (int s = 0; s < 3; ++s) {
    lds_wait_ge(L_CNT + (C_FREE + s) * 4, 4);
#pragma unroll 1
    for (int j = 4 * s; j < (s < 2 ? 4 * s + 4 : 10); ++j) wdma(j);
  }
  STAMP(4);
  const int pan = c.tt >> 1;
  poll_flags(P.ws, FL_E + (unsigned)pan * 8u * 128u, 8, tag, lane);
  if (KVFIRST && !c.ffn) lds_wait_ge(L_CNT + C_KVIN * 4, 4);
  STAMP(5);
  bload(__builtin_amdgcn_readlane(tabB, 0), br[0][0], br[0][1]);
  bload(__builtin_amdgcn_readlane(tabB, 1), br[1][0], br[1][1]);
  wdma(10);
  bload(__builtin_amdgcn_readlane(tabB, 2), br[2][0], br[2][1]);
  wdma(11);
  bload(__builtin_amdgcn_readlane(tabB, 3), br[3][0], br[3][1]);
  unsigned w2l[2];
#pragma unroll
  for (int h = 0; h < 2; ++h) w2l[h] = ring + (unsigned)(4 * hh + q) * 128u + (unsigned)((4 * (h ^ sw) + 2 * cb + (pp >> 1)) * 16 + 8 * (pp & 1));
  f32x16 o[2][2];
#pragma unroll
  for (int a = 0; a < 2; ++a)
#pragma unroll
    for (int b = 0; b < 2; ++b) o[a][b] = zero16();
  v4u af[2];
  asm volatile("s_waitcnt vmcnt(10)" : "+v"(br[0][0]), "+v"(br[0][1]) :: "memory");
  af[0] = tr2(w2l[0], w2l[0] + 1024u); af[1] = tr2(w2l[1], w2l[1] + 1024u);
  auto step = [&](int j, int u, bool refill, bool first) AI {
    v4u an[2];
    if (first) asm volatile("s_waitcnt vmcnt(" STR(W1) ")" : "+v"(br[(u + 1) % NB][0]), "+v"(br[(u + 1) % NB][1]) :: "memory");
    else asm volatile("s_waitcnt vmcnt(" STR(W2) ")" : "+v"(br[(u + 1) % NB][0]), "+v"(br[(u + 1) % NB][1]) :: "memory");
    unsigned p0 = w2l[0] + pofs(j + 1), p1 = w2l[1] + pofs(j + 1);
    asm volatile("" : "+v"(p0), "+v"(p1));
    an[0] = tr2(p0, p0 + 1024u); an[1] = tr2(p1, p1 + 1024u);
    o[0][0] = mfma(af[0], br[u][0], o[0][0]); o[0][1] = mfma(af[0], br[u][1], o[0][1]);
    o[1][0] = mfma(af[1], br[u][0], o[1][0]); o[1][1] = mfma(af[1], br[u][1], o[1][1]);
    asm volatile("" : "+v"(o[0][0]), "+v"(o[0][1]), "+v"(o[1][0]), "+v"(o[1][1]) :: "memory");
    if (refill) wdma(j + 12);
    const int jn = j + NB;
    if (u == 0) {
      if (jn == 16) lds_wait_ge(L_CNT + (ORD ? C_ORDY : C_LATERDY) * 4, 1);
      if (jn == (ORD ? 24 : 28)) lds_wait_ge(L_CNT + (ORD ? C_LATERDY : C_ORDY) * 4, 1);
    }
    bload(jn < 36 ? (unsigned)__builtin_amdgcn_readlane(tabB, jn) : 0x7F000000u, br[u][0], br[u][1]);
    af[0] = an[0]; af[1] = an[1];
  };
  lds_wait_ge(L_CNT + (C_FREE + 3) * 4, 4);
#pragma unroll 1
  for (int it = 0; it < 6; ++it) {
    if (it == 3) STAMP(10);
#pragma unroll
    for (int u = 0; u < NB; ++u) step(NB * it + u, u, true, true);
  }
  STAMP(11);
#pragma unroll 1
  for (int it = 6; it < 9; ++it) {
#pragma unroll
    for (int u = 0; u < NB; ++u) step(NB * it + u, u, false, false);
  }
  asm volatile("s_waitcnt vmcnt(0)" : "+v"(br[0][0]), "+v"(br[0][1]), "+v"(br[1][0]), "+v"(br[1][1]), "+v"(br[2][0]), "+v"(br[2][1]), "+v"(br[3][0]), "+v"(br[3][1]) :: "memory");
  STAMP(6);
#pragma unroll
  for (int a = 0; a < 2; ++a)
#pragma unroll
    for (int b = 0; b < 2; ++b)
#pragma unroll
      for (int g4 = 0; g4 < 4; ++g4)
        *(LAS f32x4*)(size_t)(ring + (unsigned)b * SLOT + (unsigned)t * 256u + (unsigned)((8 * a + 2 * g4 + hh) ^ (t & 15)) * 16u) =
            f32x4{o[a][b][4 * g4], o[a][b][4 * g4 + 1], o[a][b][4 * g4 + 2], o[a][b][4 * g4 + 3]};
  VMW(0);
  BAR();
  STAMP(7);
  if (wid == 0 && __builtin_amdgcn_workgroup_id_x() == AMPB) {
    const u64 d = (amp_b - amp_a) * AMPX, t1 = __builtin_amdgcn_s_memrealtime();
    while (__builtin_amdgcn_s_memrealtime() - t1 < d) __builtin_amdgcn_s_sleep(4);
  }
}

template <int NT, int NF, class Fill> DI void pass1(const Params& P, int wid, u64& amp_a, u64& amp_b, Fill& fill, const v4u (&xr)[32], f32x16 (&acc)[NT], f32x4& cs, const unsigned (&wb)[NT], int lane) {
  const s16x4 one = {0x3F80, 0x3F80, 0x3F80, 0x3F80};
  f32x4 csa = {0.f, 0.f, 0.f, 0.f}, csb = csa;
#pragma unroll
  for (int qq = 0; qq < 4; ++qq) {
    if (qq > 0) lds_wait_ge(L_CNT + (C_LAND + qq) * 4, 4);
    if (qq < 2) fill(10 + qq);
    unsigned a[NT];
#pragma unroll
    for (int i = 0; i < NT; ++i) { a[i] = wb[i] + (unsigned)qq * SLOT; asm volatile("" : "+v"(a[i])); }
    v4u f[3][NT];
    auto rd = [&](int k8, v4u* W) AI {
      const unsigned o = (unsigned)((k8 >> 2) * 8192 + (k8 & 3) * 2048);
#pragma unroll
      for (int i = 0; i < NT; ++i) W[i] = tr2(a[i] + o, a[i] + o + 512u);
    };
    rd(0, f[0]); rd(1, f[1]);
    __builtin_amdgcn_sched_barrier(0);
#pragma unroll
    for (int k8 = 0; k8 < 8; ++k8) {
      if (k8 == 4 && qq < 3) {
        if (qq < 2) { if (NF == 8) VMW(8); else VMW(4); } else VMW(0);
        lds_inc(L_CNT + (C_LAND + qq + 1) * 4, lane);
      }
      if (k8 < 6) rd(k8 + 2, f[(k8 + 2) % 3]);
      const v4u xf = xr[8 * qq + k8];
#pragma unroll
      for (int i = 0; i < NT; ++i) acc[i] = mfma(f[k8 % 3][i], xf, acc[i]);
      csa = __builtin_amdgcn_mfma_f32_4x4x4bf16_1k(one, __builtin_bit_cast(s16x4, v2u{f[k8 % 3][0].x, f[k8 % 3][0].y}), csa, 0, 0, 0);
      csb = __builtin_amdgcn_mfma_f32_4x4x4bf16_1k(one, __builtin_bit_cast(s16x4, v2u{f[k8 % 3][0].z, f[k8 % 3][0].w}), csb, 0, 0, 0);
      if (k8 < 6) {
#pragma unroll
        for (int i = 0; i < NT; ++i) {
          __builtin_amdgcn_sched_group_barrier(0x008, 1, 0);
          __builtin_amdgcn_sched_group_barrier(0x100, 2, 0);
        }
        __builtin_amdgcn_sched_group_barrier(0x008, 2, 0);
      }
      __builtin_amdgcn_sched_barrier(0);
    }
    asm volatile("" : "+v"(acc[0]), "+v"(acc[1]), "+v"(acc[NT - 1]), "+v"(csa), "+v"(csb) :: "memory");
    lds_inc(L_CNT + (C_FREE + qq) * 4, lane);
  }
  cs[0] = csa[0] + csb[0];
}

template <bool ffn> DI u64 role_b_head(const Params& P, const Ctx& c, int wid) {
  u64 amp_a = 0, amp_b = 0; (void)amp_a; (void)amp_b;
  const int lane = c.lane, tq = c.tq, hh = c.hh, t = lane & 31;
  const int g = c.gi;
  const unsigned tok = (unsigned)(c.p * 128 + 8 * c.G + 2 * tq);
  v4u r0, r1;
  {
    const u16* rp = P.x + (size_t)tok * 512 + lane * 8;
    asm volatile("global_load_dwordx4 %0, %2, off\n\tglobal_load_dwordx4 %1, %2, off offset:1024" : "=&v"(r0), "=&v"(r1) : "v"(rp) : "memory");
  }
  const __amdgpu_buffer_rsrc_t rsX = __builtin_amdgcn_make_buffer_rsrc((void*)P.x, 0, 2048 * 1024, 0x00020000);
  const __amdgpu_buffer_rsrc_t rsW = __builtin_amdgcn_make_buffer_rsrc((void*)(ffn ? P.w1 : P.wqkv), 0, ffn ? 512 * 3584 : 512 * 3072, 0x00020000);
  constexpr unsigned RS = ffn ? 3584u : 3072u;
  const unsigned cA = (unsigned)(224 * g + 32 * (g & 1));
  const unsigned u0 = ffn ? cA * 2u : (unsigned)(512 + 64 * g) * 2u, u1 = ffn ? cA * 2u + 128u : (unsigned)(1024 + 64 * g) * 2u;
  const unsigned ua = ffn ? (cA + 128u) * 2u : (unsigned)(64 * g) * 2u;
  const unsigned ub = (unsigned)((g & 1) ? 224 * g - 32 : 224 * g + 192) * 2u;
  const unsigned voffX = (unsigned)(lane >> 3) * 1024u + (unsigned)(((lane & 7) ^ ((4 * (tq & 1) + (lane >> 4)) & 7)) * 16);
  const unsigned voffW = (unsigned)(lane >> 3) * RS + (unsigned)(((lane & 7) ^ (((lane >> 4) & 1) << 2)) * 16);
  const unsigned soffX = (unsigned)(c.p * 128 + 8 * tq) * 1024u;
  const unsigned wrow = 32u * (unsigned)(tq & 1) * RS;
  auto fill = [&](int f) AI {
    const unsigned slot = (unsigned)(f & 3) * SLOT;
    if (f < 8) {
      const unsigned kcp = (unsigned)((f + c.rot) & 7);
      unsigned sx = soffX + kcp * 128u, sw = 64u * kcp * RS + wrow + ((tq >> 1) ? u1 : u0);
      unsigned mx = slot + (unsigned)tq * 1024u, mw = slot + 16384u + (unsigned)tq * 4096u;
#pragma unroll 1
      for (int m = 0; m < 4; ++m) {
        dma(mx, voffX, rsX, sx); dma(mw, voffW, rsW, sw);
        mx += 4096u; sx += 32768u; mw += 1024u; sw += 8u * RS;
      }
    } else {
      const unsigned kcp = (unsigned)((2 * (f - 8) + (tq >> 1) + c.rot) & 7);
      unsigned sw = 64u * kcp * RS + wrow, mw = slot + (unsigned)tq * 4096u;
#pragma unroll 1
      for (int m = 0; m < 4; ++m) {
        dma(mw, voffW, rsW, sw + ua);
        if (ffn) dma(mw + 16384u, voffW, rsW, sw + ub);
        mw += 1024u; sw += 8u * RS;
      }
    }
  };
  fill(0);
  float st0;
  {
    asm volatile("s_waitcnt vmcnt(8)" : "+v"(r0), "+v"(r1) :: "memory");
    float v[2][8], mean[2], rs[2];
#pragma unroll
    for (int k = 0; k < 4; ++k) {
      v[0][2 * k] = u2f(r0[k] << 16); v[0][2 * k + 1] = u2f(r0[k] & 0xffff0000u);
      v[1][2 * k] = u2f(r1[k] << 16); v[1][2 * k + 1] = u2f(r1[k] & 0xffff0000u);
    }
#pragma unroll
    for (int i = 0; i < 2; ++i) mean[i] = wavesum(((v[i][0] + v[i][1]) + (v[i][2] + v[i][3])) + ((v[i][4] + v[i][5]) + (v[i][6] + v[i][7]))) * (1.0f / 512.0f);
#pragma unroll
    for (int i = 0; i < 2; ++i) {
      float q2 = 0.f;
#pragma unroll
      for (int k = 0; k < 8; ++k) { const float d = v[i][k] - mean[i]; q2 = __builtin_fmaf(d, d, q2); }
      rs[i] = __builtin_amdgcn_rsqf(wavesum(q2) * (1.0f / 512.0f) + 1e-5f);
    }
    st0 = (lane & 2) ? rs[1] : rs[0];
    st0 = (lane & 1) ? -((lane & 2) ? mean[1] : mean[0]) * st0 : st0;
    asm volatile("" : "+v"(st0));
  }
  fill(1);
  unsigned xa[4];
#pragma unroll
  for (int ks = 0; ks < 4; ++ks) xa[ks] = (unsigned)(32 * tq + t) * 128u + (unsigned)(((2 * ks + hh) ^ ((t >> 1) & 7)) * 16);
  v4u xr[32];
  u64 tag = 0;
  v4u gr;
  const unsigned token = (unsigned)(c.p * 128 + 32 * tq + t);
#pragma unroll
  for (int kc = 0; kc < 8; ++kc) {
    if (kc < 7 || ffn) VMW(8); else VMW(4);
    BAR();
    if (kc == 0) {
      tag = lds_tag();
      if (lane < 4)
        __hip_atomic_store((GAS u64*)(P.ws + WS_ST + tok * 16u + (unsigned)lane * 8u), ((u64)(unsigned)tag << 32) | f2u(st0), __ATOMIC_RELAXED, __HIP_MEMORY_SCOPE_AGENT);
    }
    if (kc == 5) {
      const char* gp = P.ws + WS_ST + token * 16u;
      asm volatile("global_load_dwordx4 %0, %1, off sc1" : "=&v"(gr) : "v"(gp) : "memory");
    }
    fill(kc + 2);
    const unsigned sb = (unsigned)(kc & 3) * SLOT;
#pragma unroll
    for (int ks = 0; ks < 4; ++ks) xr[4 * kc + ks] = lds128(xa[ks] + sb);
    asm volatile("" : "+v"(xr[4 * kc]), "+v"(xr[4 * kc + 1]), "+v"(xr[4 * kc + 2]), "+v"(xr[4 * kc + 3]));
  }
  asm volatile("" : "+v"(gr));
  STAMP(2);
  BAR();
  if (__builtin_amdgcn_ballot_w64(gr.y != (unsigned)tag || gr.w != (unsigned)tag) != 0) {
    const __amdgpu_buffer_rsrc_t rs = __builtin_amdgcn_make_buffer_rsrc((void*)(P.ws + WS_ST), 0, 32768, 0x00020000);
#pragma unroll 1
    for (unsigned spin = 0; spin < kSpin; ++spin) {
      gr = __builtin_amdgcn_raw_buffer_load_b128(rs, token * 16u, 0, 16);
      if (__builtin_amdgcn_ballot_w64(gr.y != (unsigned)tag || gr.w != (unsigned)tag) == 0) break;
      __builtin_amdgcn_s_sleep(1);
    }
  }
  const float rstd = u2f(gr.x), mr = u2f(gr.z);
  if (hh == 0) *(LAS f32x2*)(size_t)(L_STATS + (unsigned)(32 * tq + t) * 8u) = f32x2{rstd, mr};
  if (ffn) VMW(8); else VMW(4);
  BAR();
  const int cb = (lane >> 4) & 1, q = (lane >> 2) & 3, pp = lane & 3, sw = (q >> 1) & 1;
  unsigned wl[2];
#pragma unroll
  for (int h = 0; h < 2; ++h) wl[h] = (unsigned)(8 * hh + q) * 128u + (unsigned)((4 * (h ^ sw) + 2 * cb + (pp >> 1)) * 16 + 8 * (pp & 1));
  const __amdgpu_buffer_rsrc_t rsHB = __builtin_amdgcn_make_buffer_rsrc(P.ws + WS_HB, 0, HB_BYTES, 0x00020000);
  const unsigned T32 = (unsigned)(c.p * 4 + tq);
  f32x4 cs = {0.f, 0.f, 0.f, 0.f};
  auto cs_sync = [&](int tile, bool wr) AI {
    const float s = xhalf_add(cs[0]);
    if (hh == 0 && wr) *(LAS float*)(size_t)(L_CS1 + (unsigned)tile * 128u + (unsigned)t * 4u) = s;
    asm volatile("s_waitcnt lgkmcnt(0)" ::: "memory");
    lds_inc(L_CNT + C_CS1 * 4, lane);
    lds_wait_ge(L_CNT + C_CS1 * 4, 4);
  };
  if constexpr (ffn) {
    const unsigned w2a = ((g & 1) ? wl[1] : wl[0]) + 16384u;
    int ti[3];
    unsigned wb[3];
    f32x16 acc[3];
#pragma unroll
    for (int i = 0; i < 3; ++i) { ti[i] = (i + tq) % 3; wb[i] = ti[i] == 0 ? wl[0] : (ti[i] == 1 ? wl[1] : w2a); acc[i] = zero16(); }
    pass1<3, 8>(P, wid, amp_a, amp_b, fill, xr, acc, cs, wb, lane);
    cs_sync(ti[0], tq < 3);
    STAMP(3);
    if (PRIOL) __builtin_amdgcn_s_setprio(3);
    f32x4 csv[3][4], bsv[3][4];
#pragma unroll
    for (int i = 0; i < 3; ++i) { load16(L_CS1 + (unsigned)ti[i] * 128u, hh, csv[i]); load16(L_B1 + (unsigned)(4 + ti[i]) * 128u, hh, bsv[i]); }
#pragma unroll
    for (int i = 0; i < 3; ++i)
      ffn_tile(acc[i], rstd, mr, csv[i], bsv[i], rsHB, (unsigned)lane * 16u + (T32 * 144u + (unsigned)(64 + 6 * g + 2 * ti[i])) * 1024u);
    publish_last(L_CNT + C_LATE * 4, 4, P.ws, FL_L + (unsigned)(c.p * 8 + g) * 128u, tag, lane);
    if (PRIOL) __builtin_amdgcn_s_setprio(0);
  } else {
    int ti[2];
    unsigned wb[2];
    f32x16 acc[2];
#pragma unroll
    for (int i = 0; i < 2; ++i) { ti[i] = (i + tq) & 1; wb[i] = ti[i] == 0 ? wl[0] : wl[1]; acc[i] = zero16(); }
    pass1<2, 4>(P, wid, amp_a, amp_b, fill, xr, acc, cs, wb, lane);
    cs_sync(ti[0], tq < 2);
    STAMP(3);
    v4u pq[4][2];
#pragma unroll
    for (int hd = 0; hd < 4; ++hd) {
      float y[8];
      fold8(acc[hd >> 1], 8 * (hd & 1), rstd, mr, L_CS1 + (unsigned)ti[hd >> 1] * 128u, hh, y);
      hedge(mfma(wphi_frag(2 * ti[hd >> 1] + (hd & 1), lane), pack8(y), zero16()), pq[hd][0], pq[hd][1]);
    }
    asm volatile("" : "+v"(pq[0][0]), "+v"(pq[0][1]), "+v"(pq[1][0]), "+v"(pq[1][1]), "+v"(pq[2][0]), "+v"(pq[2][1]), "+v"(pq[3][0]), "+v"(pq[3][1]));
    STAMP(4);
    if (PRIO) __builtin_amdgcn_s_setprio(3);
    if (tq == 0) { poll_flags(P.ws, FL_K + (unsigned)g * 16u * 128u, 16, tag, lane); lds_set(L_CNT + C_KVRDY * 4, 1); }
    else lds_wait_ge(L_CNT + C_KVRDY * 4, 1);
    STAMP(5);
    {
      const __amdgpu_buffer_rsrc_t rsKV = __builtin_amdgcn_make_buffer_rsrc(P.ws + WS_KVP, 0, 524288 + 65536, 0x00020000);
      const unsigned hbase = (unsigned)(g * 4 + tq) * 16u;
      v4u pv[16];
#pragma unroll
      for (int i = 0; i < 16; ++i) pv[i] = __builtin_amdgcn_raw_buffer_load_b128(rsKV, (unsigned)lane * 16u, (hbase + (unsigned)i) * 1024u, 16);
      const v4u z0 = __builtin_amdgcn_raw_buffer_load_b128(rsKV, (unsigned)lane * 16u, 524288u + hbase * 128u, 16);
      const v4u z1 = __builtin_amdgcn_raw_buffer_load_b128(rsKV, (unsigned)lane * 16u, 524288u + hbase * 128u + 1024u, 16);
      const u64 lf = __hip_atomic_load((const GAS u64*)(P.ws + FL_L + (unsigned)(c.tt >> 1) * 8u * 128u + (unsigned)(lane & 7) * 128u), __ATOMIC_RELAXED, __HIP_MEMORY_SCOPE_AGENT);
      __builtin_amdgcn_sched_barrier(0);
      v4u idf;
      {
        const int m = lane & 31, h2 = lane >> 5;
        const int j = (m < 16 && ((m >> 2) & 1) == h2) ? (((m >> 3) << 2) | (m & 3)) : -1;
#pragma unroll
        for (int k = 0; k < 4; ++k) idf[k] = (j == 2 * k ? 0x3F80u : 0u) | (j == 2 * k + 1 ? 0x3F800000u : 0u);
      }
      f32x16 sum = zero16();
#pragma unroll
      for (int i = 0; i < 16; ++i) sum = mfma(idf, pv[i], sum);
      asm volatile("" : "+v"(sum));
      lds_inc(L_CNT + C_KVIN * 4, lane);
      if (!ORD && tq == 0 && __builtin_amdgcn_ballot_w64(lf != tag) == 0) lds_set(L_CNT + C_LATERDY * 4, 1);
      const float s[8] = {sum[0], sum[1], sum[2], sum[3], sum[4], sum[5], sum[6], sum[7]};
      const unsigned afr = L_AFR + (unsigned)tq * 2048u;
      *(LAS v4u*)(size_t)(afr + (unsigned)((lane >> 4) & 1) * 1024u + (unsigned)((lane & 15) + 32 * (lane >> 5)) * 16u) = pack8(s);
      const unsigned hh2 = (unsigned)(lane >> 4) & 1u, c16 = (unsigned)lane & 15u;
      float zz[4];
#pragma unroll
      for (int k = 0; k < 4; ++k) {
        float v = dppadd<0x128>(u2f(z0[k]) + u2f(z1[k]));
        auto r = __builtin_amdgcn_permlane16_swap(f2u(v), f2u(v), false, false);
        zz[k] = xhalf_add(u2f(r[0]) + u2f(r[1]));
      }
      if (lane < 8) *(LAS f32x4*)(size_t)(L_ZT + (unsigned)tq * 128u + (unsigned)lane * 16u) = f32x4{zz[0], zz[1], zz[2], zz[3]};
      const unsigned f0 = 16u * (unsigned)(lane >> 5) + 4u * hh2;
      const f32x4 za = lds128f(L_ZT + (unsigned)tq * 128u + f0 * 4u), zb = lds128f(L_ZT + (unsigned)tq * 128u + (f0 + 8u) * 4u);
      *(LAS v4u*)(size_t)(afr + (unsigned)(lane >> 5) * 1024u + (16u + c16 + 32u * hh2) * 16u) = v4u{pk2(za[0], za[1]), pk2(za[2], za[3]), pk2(zb[0], zb[1]), pk2(zb[2], zb[3])};
      asm volatile("s_waitcnt lgkmcnt(0)" ::: "memory");
      lds_inc(L_CNT + C_AFR * 4, lane);
      lds_wait_ge(L_CNT + C_AFR * 4, 4);
    }
#pragma unroll
    for (int hd = 0; hd < 4; ++hd) {
      const int head = 2 * ti[hd >> 1] + (hd & 1);
      f32x16 d = mfma(lds128(L_AFR + (unsigned)head * 2048u + (unsigned)lane * 16u), pq[hd][0], zero16());
      d = mfma(lds128(L_AFR + (unsigned)head * 2048u + 1024u + (unsigned)lane * 16u), pq[hd][1], d);
      const float r = __builtin_amdgcn_rcpf(d[8] + 1e-6f);
      const v4u o = v4u{pk2(d[0] * r, d[1] * r), pk2(d[2] * r, d[3] * r), pk2(d[4] * r, d[5] * r), pk2(d[6] * r, d[7] * r)};
      st128(o, rsHB, (unsigned)lane * 16u + (T32 * 144u + (unsigned)(112 + 4 * g + head)) * 1024u);
    }
    publish_last(L_CNT + C_O * 4, 4, P.ws, FL_O + (unsigned)(c.p * 8 + g) * 128u, tag, lane);
    if (PRIO) __builtin_amdgcn_s_setprio(0);
  }
  STAMP(6);
  return tag;
}

DI void role_b(const Params& P, const Ctx& c, int wid, u64 amp_a0) {
  u64 amp_a = amp_a0, amp_b = 0; (void)amp_a; (void)amp_b;
  const int lane = c.lane, tq = c.tq;
  const u64 tag = c.ffn ? role_b_head<true>(P, c, wid) : role_b_head<false>(P, c, wid);
  const int pan = c.tt >> 1;
  const int tb = tq * 64 + lane;
  v4u xres[2], bres;
#pragma unroll
  for (int m = 0; m < 2; ++m) {
    const int idx = tb + 256 * m;
    xres[m] = *(const GAS v4u*)(P.x + (size_t)(64 * c.tt + (idx >> 3)) * 512 + 64 * c.ct + 8 * (idx & 7));
  }
  bres = *(const GAS v4u*)(P.b2 + 64 * c.ct + 8 * (tb & 7));
  if (tq == 0) {
    poll_flags(P.ws, (ORD ? FL_O : FL_L) + (unsigned)pan * 8u * 128u, 8, tag, lane);
    lds_set(L_CNT + (ORD ? C_ORDY : C_LATERDY) * 4, 1);
    poll_flags(P.ws, (ORD ? FL_L : FL_O) + (unsigned)pan * 8u * 128u, 8, tag, lane);
    lds_set(L_CNT + (ORD ? C_LATERDY : C_ORDY) * 4, 1);
  } else if (tq == 1 && __builtin_amdgcn_workgroup_id_x() == 0) {
    poll_flags(P.ws, FL_E, 64, tag, lane); poll_flags(P.ws, FL_E + 64 * 128, 64, tag, lane);
    poll_flags(P.ws, FL_K, 64, tag, lane); poll_flags(P.ws, FL_K + 64 * 128, 64, tag, lane);
    if (lane == 0) __hip_atomic_store((GAS u64*)(P.ws + WS_EPOCH), tag, __ATOMIC_RELAXED, __HIP_MEMORY_SCOPE_AGENT);
  }
  BAR();
#pragma unroll
  for (int m = 0; m < 2; ++m) {
    const int idx = tb + 256 * m;
    const unsigned tok = (unsigned)(idx >> 3), cg = (unsigned)(idx & 7);
    const unsigned a = (tok >> 5) * SLOT + (tok & 31u) * 256u, c0 = ((2u * cg) ^ (tok & 15u)) * 16u, c1 = ((2u * cg + 1u) ^ (tok & 15u)) * 16u;
    f32x4 lo = lds128f(a + c0), hi = lds128f(a + c1);
#pragma unroll
    for (unsigned w = 1; w < 4; ++w) { lo += lds128f(w * 8192u + a + c0); hi += lds128f(w * 8192u + a + c1); }
    v4u o;
#pragma unroll
    for (int k = 0; k < 4; ++k) {
      const float a0 = k < 2 ? lo[2 * k] : hi[2 * k - 4], a1 = k < 2 ? lo[2 * k + 1] : hi[2 * k - 3];
      const float x0 = u2f(xres[m][k] << 16), x1 = u2f(xres[m][k] & 0xffff0000u);
      const float b0 = u2f(bres[k] << 16), b1 = u2f(bres[k] & 0xffff0000u);
      o[k] = pk2(a0 + x0 + b0, a1 + x1 + b1);
    }
    *(GAS v4u*)(P.out + (size_t)(64 * c.tt + (idx >> 3)) * 512 + 64 * c.ct + 8 * (idx & 7)) = o;
  }
  STAMP(7);
}

__global__ __launch_bounds__(512, 2) NOPK void fused_kernel(Params P) {
  __shared__ __attribute__((aligned(1024))) char smem[LDS_BYTES];
  asm volatile("" ::"v"((unsigned)(size_t)(LAS char*)smem) : "memory");
  u64 pc;
  asm volatile("s_getpc_b64 %0" : "=s"(pc));
  const int tid = __builtin_amdgcn_workitem_id_x(), lane = tid & 63;
  const int wid = __builtin_amdgcn_readfirstlane(tid >> 6);
  u64 amp_a = 0, amp_b = 0;
  STAMP(0);
  Ctx c;
  c.lane = lane; c.tq = wid & 3; c.hh = lane >> 5;
  const int blk = __builtin_amdgcn_workgroup_id_x(), xcd = blk & 7, j = blk >> 3, ja = j & 7, jb = j >> 3;
  c.p = (xcd & 1) * 8 + ja;
  c.G = (xcd >> 1) * 4 + jb;
  c.ffn = !(c.G & 1);
  c.gi = c.G >> 1;
  c.rot = ROT1 == 0 ? 0 : (ROT1 == 1 ? (((ja + jb) % 3) * 3) & 7 : (ROT1 == 2 ? (ja + 2 * jb) & 7 : ((ja + jb) & 1) * 4));
  c.tt = (xcd >> 1) * 8 + ja; c.ct = (xcd & 1) * 4 + jb; c.ja = ja; c.jb = jb;
  c.rotE = ROT2 ? (2 * ja + 4 * jb) & 15 : 0; c.rotL = ROT2 ? (ja + 3 * jb) % 12 : 0; c.rotO = ROT2 ? (ja + 2 * jb) & 7 : 0;
  if (wid < 4) {
    const unsigned kb0 = (unsigned)(j + 32 * (wid & 1)), kb = kb0 < CODE_KB ? kb0 : 0u;
    const unsigned pf = CODEPF ? *(const GAS unsigned*)((pc & ~255ull) + kb * 1024u + (unsigned)lane * 16u) : 0u;
    const u64 e = __hip_atomic_load((const GAS u64*)(P.ws + WS_EPOCH), __ATOMIC_RELAXED, __HIP_MEMORY_SCOPE_AGENT);
    if (c.ffn) {
      const int tc = tid < 224 ? tid : 0;
      const int t7 = tc >> 5, n = tc & 31, g = c.gi;
      const int cA = 224 * g + 32 * (g & 1);
      const int col = t7 < 4 ? cA + 32 * t7 : (t7 < 6 ? cA + 128 + 32 * (t7 - 4) : ((g & 1) ? 224 * g : 224 * g + 192));
      const unsigned bv = P.b1[col + n];
      *(LAS float*)(size_t)(L_B1 + (unsigned)tid * 4u) = u2f(bv << 16);
    } else {
      const v4u wv = *(const GAS v4u*)(P.wphi + (size_t)c.gi * 1024 + (tid & 127) * 8);
      *(LAS v4u*)(size_t)(L_WPHI + (unsigned)(tid & 127) * 16u) = wv;
    }
    if (tid < 32) *(LAS unsigned*)(size_t)(L_CNT + (unsigned)tid * 4u) = 0u;
    const u64 tag = e + 1u;
    if (tid == 0) *(LAS v2u*)(size_t)L_TAG = v2u{(unsigned)tag, (unsigned)(tag >> 32)};
    asm volatile("" ::"v"(pf));
    STAMP(1);
    role_a(P, c, wid, amp_a, amp_b);
  } else {
    STAMP(1);
    role_b(P, c, wid, amp_a);
  }
}

}

extern "C" void kernel_launch(void* const* d_in, const int* in_sizes, int n_in, void* d_out, int out_size, void* d_ws, size_t ws_size,
                              hipStream_t stream) {
  Params P;
  P.x = (const u16*)d_in[0]; P.wqkv = (const u16*)d_in[1]; P.wphi = (const u16*)d_in[2]; P.wo = (const u16*)d_in[3];
  P.w1 = (const u16*)d_in[5]; P.b1 = (const u16*)d_in[6]; P.w2 = (const u16*)d_in[7]; P.b2 = (const u16*)d_in[8];
  P.out = (u16*)d_out; P.ws = (char*)d_ws;
  fused_kernel<<<256, 512, 0, stream>>>(P);
}
```

```cpp
#include <hip/hip_runtime.h>
#include <stdint.h>
#include <type_traits>

namespace {

typedef unsigned short u16;
typedef unsigned long long u64;
typedef unsigned v4u __attribute__((ext_vector_type(4)));
typedef unsigned v2u __attribute__((ext_vector_type(2)));
typedef float f32x16 __attribute__((ext_vector_type(16)));
typedef float f32x4 __attribute__((ext_vector_type(4)));
typedef float f32x2 __attribute__((ext_vector_type(2)));
typedef __bf16 bf16x8 __attribute__((ext_vector_type(8)));
typedef __bf16 bf16x2 __attribute__((ext_vector_type(2)));
typedef short s16x4 __attribute__((ext_vector_type(4)));
#define LAS __attribute__((address_space(3)))
#define GAS __attribute__((address_space(1)))
#if defined(__HIP_DEVICE_COMPILE__)
#define NOPK __attribute__((target("no-packed-fp32-ops")))
#else
#define NOPK
#endif
#define DI __device__ __forceinline__ NOPK
#define AI __attribute__((always_inline)) NOPK

#define STAMPS 3
#define AMPI 0
#define AMPJ 99
#define AMPX 20
#define AMPB 0
#define SPANMODE 0
#define ROT1 1
#define ROT2 0
#define STR2(x) #x
#define STR(x) STR2(x)
#define W1 8
#define W2 4
#define PRIO 1
#define SLP 1
#define KVFIRST 0
#define PRIOL 0
#define ORD 0
#define CODEPF 1
#define WARM2 1
#define CODE_KB 36

constexpr unsigned WS_EPOCH = 0;
constexpr unsigned FL_E = 4096;
constexpr unsigned FL_L = FL_E + 128 * 128;
constexpr unsigned FL_K = FL_L + 128 * 128;
constexpr unsigned FL_O = FL_K + 128 * 128;
constexpr unsigned WS_ST = 73728;
constexpr unsigned WS_KVP = 131072;
constexpr unsigned WS_ZP = WS_KVP + 524288;
constexpr unsigned WS_HB = 1048576;
constexpr unsigned HB_BYTES = 64u * 144u * 1024u;
constexpr unsigned WS_STAMP = 16u << 20;

constexpr unsigned SLOT = 32768;
constexpr unsigned L_MISC = 131072;
constexpr unsigned L_STATS = L_MISC;
constexpr unsigned L_CS0 = L_MISC + 1024;
constexpr unsigned L_CS1 = L_MISC + 1536;
constexpr unsigned L_B1 = L_MISC + 2048;
constexpr unsigned L_WPHI = L_MISC + 3072;
constexpr unsigned L_CNT = L_MISC + 5120;
constexpr unsigned L_TAG = L_MISC + 5248;
constexpr unsigned L_ZT = L_MISC + 5376;
constexpr unsigned L_AFR = L_MISC + 8192;
constexpr unsigned LDS_BYTES = 163840;
enum { C_CS1 = 0, C_LATE = 1, C_KV = 2, C_O = 3, C_AFR = 4, C_KVRDY = 5, C_LATERDY = 6, C_ORDY = 7, C_FREE = 8  , C_LAND = 12  , C_EARLY = 16, C_IMG = 17, C_KVIN = 18, C_IMGDONE = 19 };

constexpr unsigned kSpin = 1u << 16;

struct Params {
  const u16* x; const u16* wqkv; const u16* wphi; const u16* wo;
  const u16* w1; const u16* b1; const u16* w2; const u16* b2;
  u16* out; char* ws;
};

DI unsigned f2u(float f) { return __builtin_bit_cast(unsigned, f); }
DI float u2f(unsigned u) { return __builtin_bit_cast(float, u); }
DI float fmx(float a, float b) { return __builtin_fmaxf(a, b); }
DI f32x16 mfma(v4u a, v4u b, f32x16 c) {
  return __builtin_amdgcn_mfma_f32_32x32x16_bf16(__builtin_bit_cast(bf16x8, a), __builtin_bit_cast(bf16x8, b), c, 0, 0, 0);
}
DI f32x16 zero16() { f32x16 z; for (int r = 0; r < 16; ++r) z[r] = 0.f; return z; }
DI v4u lds128(unsigned a) { return *(const LAS v4u*)(size_t)a; }
DI f32x4 lds128f(unsigned a) { return *(const LAS f32x4*)(size_t)a; }
DI v2u tr64(unsigned a) { return __builtin_bit_cast(v2u, __builtin_amdgcn_ds_read_tr16_b64_v4i16((LAS s16x4*)(size_t)a)); }
DI v4u tr2(unsigned a0, unsigned a1) { v2u l = tr64(a0), h = tr64(a1); return v4u{l.x, l.y, h.x, h.y}; }
DI unsigned pk2(float a, float b) { return __builtin_bit_cast(unsigned, __builtin_convertvector(f32x2{a, b}, bf16x2)); }
DI float xhalf_add(float v) { auto r = __builtin_amdgcn_permlane32_swap(f2u(v), f2u(v), false, false); return u2f(r[0]) + u2f(r[1]); }
DI float xhalf_max(float v) { auto r = __builtin_amdgcn_permlane32_swap(f2u(v), f2u(v), false, false); return fmx(u2f(r[0]), u2f(r[1])); }
DI float shx(float v, int m, int lane) { return u2f((unsigned)__builtin_amdgcn_ds_bpermute((lane ^ m) << 2, (int)f2u(v))); }
DI f32x4 lanesum(v4u f, f32x4 c) {
  const s16x4 one = {0x3F80, 0x3F80, 0x3F80, 0x3F80};
  c = __builtin_amdgcn_mfma_f32_4x4x4bf16_1k(one, __builtin_bit_cast(s16x4, v2u{f.x, f.y}), c, 0, 0, 0);
  return __builtin_amdgcn_mfma_f32_4x4x4bf16_1k(one, __builtin_bit_cast(s16x4, v2u{f.z, f.w}), c, 0, 0, 0);
}
template <int CTRL> DI float dppadd(float v) { return v + u2f((unsigned)__builtin_amdgcn_update_dpp(0, (int)f2u(v), CTRL, 0xf, 0xf, true)); }
DI float wavesum(float v) {
  v = dppadd<0xB1>(v); v = dppadd<0x4E>(v); v = dppadd<0x141>(v); v = dppadd<0x140>(v);
  auto r = __builtin_amdgcn_permlane16_swap(f2u(v), f2u(v), false, false);
  return xhalf_add(u2f(r[0]) + u2f(r[1]));
}
DI void dma(unsigned m0v, unsigned voff, __amdgpu_buffer_rsrc_t rs, unsigned soff) {
  asm volatile("s_mov_b32 m0, %0\n\ts_nop 0\n\tbuffer_load_dwordx4 %1, %2, %3 offen lds" ::"s"(m0v), "v"(voff), "s"(rs), "s"(soff) : "memory");
}
DI void st128(v4u v, __amdgpu_buffer_rsrc_t rs, unsigned voff) { __builtin_amdgcn_raw_buffer_store_b128(v, rs, voff, 0, 16); }
#define BAR() do { __builtin_amdgcn_sched_barrier(0); asm volatile("s_waitcnt lgkmcnt(0)\n\ts_barrier" ::: "memory"); __builtin_amdgcn_sched_barrier(0); } while (0)
#define VMW(n) asm volatile("s_waitcnt vmcnt(" #n ")" ::: "memory")
constexpr unsigned ONES = 0x3F803F80u;

#define STAMP(i) do { if ((i) == AMPI) amp_a = __builtin_amdgcn_s_memrealtime(); if ((i) == AMPJ) amp_b = __builtin_amdgcn_s_memrealtime(); } while (0)

DI void poll_flags(const char* ws, unsigned base, int n, u64 tag, int lane) {
  const GAS u64* f = (const GAS u64*)(ws + base + (unsigned)(lane < n ? lane : 0) * 128u);
#pragma unroll 1
  for (unsigned spin = 0; spin < kSpin; ++spin) {
    const u64 v = __hip_atomic_load(f, __ATOMIC_RELAXED, __HIP_MEMORY_SCOPE_AGENT);
    if (__builtin_amdgcn_ballot_w64(v != tag) == 0) break;
    __builtin_amdgcn_s_sleep(1);
  }
  __builtin_amdgcn_fence(__ATOMIC_ACQUIRE, "wavefront");
  asm volatile("" ::: "memory");
}
DI void lds_wait_ge(unsigned addr, unsigned want) {
#pragma unroll 1
  for (unsigned spin = 0; spin < (kSpin << 4); ++spin) {
    const unsigned v = *(volatile LAS unsigned*)(size_t)addr;
    if ((unsigned)__builtin_amdgcn_readfirstlane(v) >= want) break;
    if (SLP) __builtin_amdgcn_s_sleep(SLP);
  }
  asm volatile("" ::: "memory");
}
DI void lds_set(unsigned addr, unsigned v) { *(volatile LAS unsigned*)(size_t)addr = v; }
DI void lds_inc(unsigned addr, int lane) { if (lane == 0) __hip_atomic_fetch_add((LAS unsigned*)(size_t)addr, 1u, __ATOMIC_RELAXED, __HIP_MEMORY_SCOPE_WORKGROUP); }
DI void publish_last(unsigned cnt_addr, unsigned n, char* ws, unsigned flag_off, u64 tag, int lane) {
  VMW(0);
  if (lane == 0) {
    const unsigned old = __hip_atomic_fetch_add((LAS unsigned*)(size_t)cnt_addr, 1u, __ATOMIC_RELAXED, __HIP_MEMORY_SCOPE_WORKGROUP);
    if (old == n - 1) __hip_atomic_store((GAS u64*)(ws + flag_off), tag, __ATOMIC_RELAXED, __HIP_MEMORY_SCOPE_AGENT);
  }
}
DI u64 lds_tag() { const v2u t = *(const LAS v2u*)(size_t)L_TAG; return ((u64)(unsigned)__builtin_amdgcn_readfirstlane(t.y) << 32) | (unsigned)__builtin_amdgcn_readfirstlane(t.x); }

DI void fold8(const f32x16& acc, int r0, float rstd, float mr, unsigned cs, int hh, float* y) {
#pragma unroll
  for (int g = 0; g < 2; ++g) {
    const f32x4 t = lds128f(cs + (unsigned)((r0 >> 2) + g) * 32u + (unsigned)hh * 16u) * mr;
#pragma unroll
    for (int k = 0; k < 4; ++k) y[4 * g + k] = __builtin_fmaf(acc[r0 + 4 * g + k], rstd, t[k]);
  }
}
DI void load16(unsigned a, int hh, f32x4* v) {
#pragma unroll
  for (int g = 0; g < 4; ++g) v[g] = lds128f(a + (unsigned)g * 32u + (unsigned)hh * 16u);
}
DI void ffn_tile(const f32x16& acc, float rstd, float mr, const f32x4* cs, const f32x4* bs, __amdgpu_buffer_rsrc_t rsHB, unsigned voff) {
  f32x2 y[8], d[8];
#pragma unroll
  for (int g = 0; g < 4; ++g) {
    const f32x4 t = cs[g] * mr + bs[g];
    y[2 * g] = f32x2{acc[4 * g], acc[4 * g + 1]} * rstd + f32x2{t[0], t[1]};
    y[2 * g + 1] = f32x2{acc[4 * g + 2], acc[4 * g + 3]} * rstd + f32x2{t[2], t[3]};
  }
#pragma unroll
  for (int k = 0; k < 8; ++k) d[k] = y[k] * ((y[k] * y[k]) * (-0.10294324f) + (-2.30220819f));
#pragma unroll
  for (int k = 0; k < 8; ++k) { d[k].x = __builtin_amdgcn_exp2f(d[k].x); d[k].y = __builtin_amdgcn_exp2f(d[k].y); }
#pragma unroll
  for (int k = 0; k < 8; ++k) d[k] += 1.0f;
#pragma unroll
  for (int k = 0; k < 8; ++k) { d[k].x = __builtin_amdgcn_rcpf(d[k].x); d[k].y = __builtin_amdgcn_rcpf(d[k].y); }
  v4u o0, o1;
#pragma unroll
  for (int k = 0; k < 4; ++k) {
    const f32x2 g0 = y[k] * d[k], g1 = y[4 + k] * d[4 + k];
    o0[k] = pk2(g0.x, g0.y); o1[k] = pk2(g1.x, g1.y);
  }
  st128(o0, rsHB, voff); st128(o1, rsHB, voff + 1024u);
}
DI void hedge(const f32x16& d, v4u& o0, v4u& o1) {
  constexpr float L = 1.4426950408889634f;
  float m0 = fmx(fmx(fmx(d[0], d[1]), fmx(d[2], d[3])), fmx(fmx(d[4], d[5]), fmx(d[6], d[7])));
  float m1 = fmx(fmx(fmx(d[8], d[9]), fmx(d[10], d[11])), fmx(fmx(d[12], d[13]), fmx(d[14], d[15])));
  m0 = xhalf_max(m0) * -L; m1 = xhalf_max(m1) * -L;
  float e[16];
#pragma unroll
  for (int i = 0; i < 8; ++i) { e[i] = __builtin_amdgcn_exp2f(__builtin_fmaf(d[i], L, m0)); e[8 + i] = __builtin_amdgcn_exp2f(__builtin_fmaf(d[8 + i], L, m1)); }
  float s0 = ((e[0] + e[1]) + (e[2] + e[3])) + ((e[4] + e[5]) + (e[6] + e[7]));
  float s1 = ((e[8] + e[9]) + (e[10] + e[11])) + ((e[12] + e[13]) + (e[14] + e[15]));
  s0 = __builtin_amdgcn_rcpf(xhalf_add(s0)); s1 = __builtin_amdgcn_rcpf(xhalf_add(s1));
#pragma unroll
  for (int k = 0; k < 4; ++k) { o0[k] = pk2(e[2 * k] * s0, e[2 * k + 1] * s0); o1[k] = pk2(e[8 + 2 * k] * s1, e[8 + 2 * k + 1] * s1); }
}
DI v4u wphi_frag(int hd, int lane) {
  const int hh = lane >> 5, q = (lane >> 2) & 3, pp = lane & 3;
  const unsigned a = L_WPHI + (unsigned)hd * 512u + (unsigned)(4 * hh + q) * 32u + (unsigned)pp * 8u;
  v4u f = tr2(a, a + 256u);
  const unsigned sg = (lane & 16) ? 0x80008000u : 0u;
  f.x ^= sg; f.y ^= sg; f.z ^= sg; f.w ^= sg;
  return f;
}
DI v4u selv(bool s, v4u a, v4u b) { const unsigned m = s ? 0xffffffffu : 0u; return v4u{(a.x & m) | (b.x & ~m), (a.y & m) | (b.y & ~m), (a.z & m) | (b.z & ~m), (a.w & m) | (b.w & ~m)}; }
DI v4u pack8(const float* y) { return v4u{pk2(y[0], y[1]), pk2(y[2], y[3]), pk2(y[4], y[5]), pk2(y[6], y[7])}; }

template <int NI> DI void warm_w2(const Params& P, int ct, int ja, int tq, int i0, int lane, v4u* d) {
#pragma unroll
  for (int i = 0; i < NI; ++i) {
    const int row = 288 * ja + 72 * tq + 8 * (i0 + i);
    const u64 a = row < 1792 ? (u64)(size_t)P.w2 + (u64)(unsigned)row * 1024u : (u64)(size_t)P.wo + (u64)(unsigned)(row - 1792) * 1024u;
    const u64 base = ((u64)(unsigned)__builtin_amdgcn_readfirstlane((unsigned)(a >> 32)) << 32) | (unsigned)__builtin_amdgcn_readfirstlane((unsigned)a);
    const unsigned voff = (unsigned)(lane >> 3) * 1024u + (unsigned)(lane & 7) * 16u + (unsigned)ct * 128u;
    asm volatile("s_nop 4\n\tglobal_load_dwordx4 %0, %1, %2" : "=&v"(d[i]) : "v"(voff), "s"(base) : "memory");
  }
}
struct Ctx {
  int lane, tq, hh;
  int p, gi, G;
  int rot;
  int tt, ct, ja, jb;
  int rotE, rotL, rotO;
  bool ffn;
};

DI void role_a(const Params& P, const Ctx& c, int wid, u64 amp_a, u64 amp_b) {
  const int lane = c.lane, tq = c.tq, hh = c.hh, t = lane & 31;
  const int cb = (lane >> 4) & 1, q = (lane >> 2) & 3, pp = lane & 3, sw = (q >> 1) & 1;
  const int th = tq & 1, up = tq >> 1;
  unsigned xa[4];
#pragma unroll
  for (int ks = 0; ks < 4; ++ks) xa[ks] = (unsigned)(64 * th + t) * 128u + (unsigned)(((2 * ks + hh) ^ ((t >> 1) & 7)) * 16);
  const unsigned wl = 16384u + (unsigned)(8 * hh + q) * 128u + (unsigned)((4 * (up ^ sw) + 2 * cb + (pp >> 1)) * 16 + 8 * (pp & 1));
  f32x16 acc[2][2];
#pragma unroll
  for (int i = 0; i < 2; ++i) { acc[i][0] = zero16(); acc[i][1] = zero16(); }
  f32x4 cs = {0.f, 0.f, 0.f, 0.f};
  unsigned x0 = xa[0], x1 = xa[1], x2 = xa[2], x3 = xa[3], w0 = wl;
  v4u xf[4][2], wf[4][2];
  auto rd = [&](int ks, v4u* X, v4u* W) AI {
    const unsigned xb = ks == 0 ? x0 : (ks == 1 ? x1 : (ks == 2 ? x2 : x3));
    X[0] = lds128(xb); X[1] = lds128(xb + 4096u);
    const unsigned a = w0 + (unsigned)(ks * 2048);
    W[0] = tr2(a, a + 512u); W[1] = tr2(a + 8192u, a + 8704u);
  };
  f32x4 cs2 = {0.f, 0.f, 0.f, 0.f};
  BAR();
  rd(0, xf[0], wf[0]); rd(1, xf[1], wf[1]);
  auto pass0 = [&](auto TH) AI {
    constexpr int CT = decltype(TH)::value;
    const s16x4 one = {0x3F80, 0x3F80, 0x3F80, 0x3F80};
#pragma unroll 1
    for (int kc = 0; kc < 8; ++kc) {
#pragma unroll
      for (int ks = 0; ks < 4; ++ks) {
        if (ks == 2) {
          __builtin_amdgcn_sched_barrier(0);
          asm volatile("s_barrier" ::: "memory");
          __builtin_amdgcn_sched_barrier(0);
          const unsigned sb = (unsigned)((kc + 1) & 3) * SLOT;
          x0 = xa[0] + sb; x1 = xa[1] + sb; w0 = wl + sb;
          asm volatile("" : "+v"(x0), "+v"(x1), "+v"(w0));
        }
        if (ks == 0) {
          const unsigned sb = (unsigned)(kc & 3) * SLOT;
          x2 = xa[2] + sb; x3 = xa[3] + sb;
          asm volatile("" : "+v"(x2), "+v"(x3));
        }
        rd((ks + 2) & 3, xf[(ks + 2) & 3], wf[(ks + 2) & 3]);
        acc[0][0] = mfma(wf[ks][0], xf[ks][0], acc[0][0]); acc[0][1] = mfma(wf[ks][0], xf[ks][1], acc[0][1]);
        acc[1][0] = mfma(wf[ks][1], xf[ks][0], acc[1][0]); acc[1][1] = mfma(wf[ks][1], xf[ks][1], acc[1][1]);
        cs = __builtin_amdgcn_mfma_f32_4x4x4bf16_1k(one, __builtin_bit_cast(s16x4, v2u{wf[ks][CT].x, wf[ks][CT].y}), cs, 0, 0, 0);
        cs2 = __builtin_amdgcn_mfma_f32_4x4x4bf16_1k(one, __builtin_bit_cast(s16x4, v2u{wf[ks][CT].z, wf[ks][CT].w}), cs2, 0, 0, 0);
        __builtin_amdgcn_sched_group_barrier(0x008, 1, 0);
        __builtin_amdgcn_sched_group_barrier(0x100, 2, 0);
        __builtin_amdgcn_sched_group_barrier(0x008, 1, 0);
        __builtin_amdgcn_sched_group_barrier(0x100, 2, 0);
        __builtin_amdgcn_sched_group_barrier(0x008, 1, 0);
        __builtin_amdgcn_sched_group_barrier(0x100, 2, 0);
        __builtin_amdgcn_sched_group_barrier(0x008, 3, 0);
        __builtin_amdgcn_sched_barrier(0);
      }
      asm volatile("" : "+v"(acc[0][0]), "+v"(acc[0][1]), "+v"(acc[1][0]), "+v"(acc[1][1]), "+v"(cs), "+v"(cs2));
    }
  };
  if (th) pass0(std::integral_constant<int, 1>{}); else pass0(std::integral_constant<int, 0>{});
  cs[0] += cs2[0];
  STAMP(2);
  {
    const float s = xhalf_add(cs[0]);
    if (hh == 0) *(LAS float*)(size_t)(L_CS0 + (unsigned)(up + 2 * th) * 128u + (unsigned)t * 4u) = s;
  }
  BAR();
  const u64 tag = lds_tag();
  float rstd[2], mr[2];
#pragma unroll
  for (int b = 0; b < 2; ++b) {
    const f32x2 st = *(const LAS f32x2*)(size_t)(L_STATS + (unsigned)(64 * th + 32 * b + t) * 8u);
    rstd[b] = st.x; mr[b] = st.y;
  }
  const __amdgpu_buffer_rsrc_t rsHB = __builtin_amdgcn_make_buffer_rsrc(P.ws + WS_HB, 0, HB_BYTES, 0x00020000);
  v4u wm[9];
  if (c.ffn) {
    f32x4 csv[2][4], bsv[2][4];
#pragma unroll
    for (int i = 0; i < 2; ++i) { load16(L_CS0 + (unsigned)(up + 2 * i) * 128u, hh, csv[i]); load16(L_B1 + (unsigned)(up + 2 * i) * 128u, hh, bsv[i]); }
#pragma unroll
    for (int i = 0; i < 2; ++i)
#pragma unroll
      for (int b = 0; b < 2; ++b) {
        const unsigned tile = (unsigned)(up + 2 * i), T32 = (unsigned)(c.p * 4 + 2 * th + b);
        ffn_tile(acc[i][b], rstd[b], mr[b], csv[i], bsv[i], rsHB, (unsigned)lane * 16u + (T32 * 144u + (unsigned)(8 * c.gi) + 2u * tile) * 1024u);
        if (WARM2 && 2 * i + b < 3) warm_w2<3>(P, c.ct, c.ja, tq, 3 * (2 * i + b), lane, wm + 3 * (2 * i + b));
      }
    STAMP(8);
    publish_last(L_CNT + C_EARLY * 4, 4, P.ws, FL_E + (unsigned)(c.p * 8 + c.gi) * 128u, tag, lane);
    if (WARM2) asm volatile("" :: "v"(wm[0]), "v"(wm[1]), "v"(wm[2]), "v"(wm[3]), "v"(wm[4]), "v"(wm[5]), "v"(wm[6]), "v"(wm[7]), "v"(wm[8]));
  } else {
#pragma unroll
    for (int b = 0; b < 2; ++b)
#pragma unroll
      for (int hf = 0; hf < 2; ++hf) {
        const int hd = 2 * up + hf;
        float y[8];
        fold8(acc[0][b], 8 * hf, rstd[b], mr[b], L_CS0 + (unsigned)up * 128u, hh, y);
        v4u o0, o1;
        hedge(mfma(wphi_frag(hd, lane), pack8(y), zero16()), o0, o1);
        const unsigned img = (unsigned)hd * SLOT + 16384u;
        const unsigned row = (unsigned)(64 * th + 32 * b + t);
        const unsigned g = (row >> 1) & 7u;
        *(LAS v2u*)(size_t)(img + row * 64u + (((unsigned)(0 + hh)) ^ g) * 8u) = v2u{o0.x, o0.y};
        *(LAS v2u*)(size_t)(img + row * 64u + (((unsigned)(2 + hh)) ^ g) * 8u) = v2u{o0.z, o0.w};
        *(LAS v2u*)(size_t)(img + row * 64u + (((unsigned)(4 + hh)) ^ g) * 8u) = v2u{o1.x, o1.y};
        *(LAS v2u*)(size_t)(img + row * 64u + (((unsigned)(6 + hh)) ^ g) * 8u) = v2u{o1.z, o1.w};
        fold8(acc[1][b], 8 * hf, rstd[b], mr[b], L_CS0 + (unsigned)(2 + up) * 128u, hh, y);
        const unsigned gv = (row >> 2) & 3u;
        *(LAS v2u*)(size_t)(img + 8192u + row * 32u + (((unsigned)(0 + hh)) ^ gv) * 8u) = v2u{pk2(y[0], y[1]), pk2(y[2], y[3])};
        *(LAS v2u*)(size_t)(img + 8192u + row * 32u + (((unsigned)(2 + hh)) ^ gv) * 8u) = v2u{pk2(y[4], y[5]), pk2(y[6], y[7])};
        if (WARM2 && 2 * b + hf < 3) warm_w2<3>(P, c.ct, c.ja, tq, 3 * (2 * b + hf), lane, wm + 3 * (2 * b + hf));
      }
    STAMP(8);
    asm volatile("s_waitcnt lgkmcnt(0)" ::: "memory");
    lds_inc(L_CNT + C_IMG * 4, lane);
    lds_wait_ge(L_CNT + C_IMG * 4, 4);
    const unsigned img = (unsigned)tq * SLOT + 16384u;
    f32x16 kv = zero16();
    unsigned pa[2], va[2];
#pragma unroll
    for (int i2 = 0; i2 < 2; ++i2) {
      const unsigned r = (unsigned)(8 * hh + 4 * i2 + q);
      pa[i2] = img + r * 64u + (((unsigned)(4 * cb + pp)) ^ ((r >> 1) & 7u)) * 8u;
      va[i2] = img + 8192u + r * 32u + (((unsigned)pp) ^ ((r >> 2) & 3u)) * 8u;
    }
#pragma unroll
    for (int ks = 0; ks < 8; ++ks) {
      const v4u af = tr2(pa[0] + (unsigned)ks * 1024u, pa[1] + (unsigned)ks * 1024u);
      v4u bf = tr2(va[0] + (unsigned)ks * 512u, va[1] + (unsigned)ks * 512u);
      if (cb) bf = v4u{ONES, ONES, ONES, ONES};
      kv = mfma(af, bf, kv);
    }
    asm volatile("" : "+v"(kv) :: "memory");
    lds_inc(L_CNT + C_IMGDONE * 4, lane);
    const unsigned hidx = (unsigned)(c.gi * 4 + tq) * 16u + (unsigned)c.p;
    const __amdgpu_buffer_rsrc_t rsKV = __builtin_amdgcn_make_buffer_rsrc(P.ws + WS_KVP, 0, 524288 + 65536, 0x00020000);
    if (cb == 0) {
#pragma unroll
      for (int s = 0; s < 2; ++s) {
        const v4u o = v4u{pk2(kv[8 * s], kv[8 * s + 1]), pk2(kv[8 * s + 2], kv[8 * s + 3]), pk2(kv[8 * s + 4], kv[8 * s + 5]), pk2(kv[8 * s + 6], kv[8 * s + 7])};
        st128(o, rsKV, (unsigned)((16 * s + (lane & 15) + 32 * hh) * 16) + hidx * 1024u);
      }
    } else if ((lane & 15) == 0) {
#pragma unroll
      for (int g4 = 0; g4 < 4; ++g4) {
        const v4u o = v4u{f2u(kv[4 * g4]), f2u(kv[4 * g4 + 1]), f2u(kv[4 * g4 + 2]), f2u(kv[4 * g4 + 3])};
        st128(o, rsKV, (unsigned)((8 * g4 + 4 * hh) * 4) + 524288u + hidx * 128u);
      }
    }
    publish_last(L_CNT + C_KV * 4, 4, P.ws, FL_K + (unsigned)(c.gi * 16 + c.p) * 128u, tag, lane);
    if (WARM2) asm volatile("" :: "v"(wm[0]), "v"(wm[1]), "v"(wm[2]), "v"(wm[3]), "v"(wm[4]), "v"(wm[5]), "v"(wm[6]), "v"(wm[7]), "v"(wm[8]));
  }
  STAMP(3);
  unsigned tabB, tabWlo, tabWhi;
  {
    const int jj = lane < 36 ? lane : 35;
    constexpr int J2 = ORD ? 24 : 28;
    int sg;
    auto perm = [&](int i, int n) AI {
      if (!ROT2) return i;
      const int b = c.jb;
      if (i == 0) return b;
      const int m = (i - 1) >> 2, r = (i - 1) & 3;
      if (m < n - 1) return r == 0 ? 4 * (m + 1) + b : 4 * m + ((b + r) & 3);
      return 4 * m + ((b + 1 + r) & 3);
    };
    if (jj < 16) sg = 4 * perm(jj, 4) + tq;
    else if ((jj < J2) != (ORD != 0)) sg = 64 + 4 * perm(jj - (ORD ? 24 : 16), 3) + tq;
    else sg = 112 + 4 * perm(jj - (ORD ? 16 : 28), 2) + tq;
    int row;
    if (sg < 64) { const int g = sg >> 3; row = 224 * g + 32 * (g & 1) + 16 * (sg & 7); }
    else if (sg < 112) {
      const int e = sg - 64, g = e / 6, i = e - 6 * g;
      row = i < 4 ? 224 * g + 32 * (g & 1) + 128 + 16 * i : ((g & 1) ? 224 * g : 224 * g + 192) + 16 * (i - 4);
    } else row = 16 * (sg - 112);
    const u64 a = (u64)(size_t)(sg < 112 ? P.w2 : P.wo) + (u64)row * 1024u + (u64)c.ct * 128u;
    tabB = (unsigned)sg * 1024u; tabWlo = (unsigned)a; tabWhi = (unsigned)(a >> 32);
  }
  const unsigned vw0 = (unsigned)(lane >> 3) * 1024u + (unsigned)(((lane & 7) ^ (((lane >> 4) & 1) << 2)) * 16), vw1 = vw0 + 8192u;
  const unsigned ring = (unsigned)tq * 8192u;
  auto pofs = [&](int r) AI { return (unsigned)((r >> 2) & 3) * SLOT + (unsigned)(r & 3) * 2048u; };
  auto wdma = [&](int j) AI {
    const unsigned lo = __builtin_amdgcn_readlane(tabWlo, j), hi = __builtin_amdgcn_readlane(tabWhi, j);
    const u64 b = ((u64)hi << 32) | lo;
    const unsigned m = ring + pofs(j);
    asm volatile("s_nop 4\n\ts_mov_b32 m0, %0\n\ts_nop 0\n\tglobal_load_lds_dwordx4 %1, %3\n\ts_add_u32 m0, m0, 0x400\n\ts_nop 0\n\tglobal_load_lds_dwordx4 %2, %3"
                 ::"s"(m), "v"(vw0), "v"(vw1), "s"(b) : "memory", "scc");
  };
  const unsigned vb0 = (unsigned)lane * 16u + (unsigned)(2 * c.tt) * 144u * 1024u, vb1 = vb0 + 144u * 1024u;
  constexpr int NB = 4;
  v4u br[NB][2];
#pragma unroll
  for (int u = 0; u < NB; ++u) { br[u][0] = v4u{0u, 0u, 0u, 0u}; br[u][1] = br[u][0]; }
  auto bload = [&](unsigned so, v4u& d0, v4u& d1) AI {
    asm volatile("s_nop 4\n\tbuffer_load_dwordx4 %0, %2, %4, %5 offen sc1\n\tbuffer_load_dwordx4 %1, %3, %4, %5 offen sc1"
                 : "+v"(d0), "+v"(d1) : "v"(vb0), "v"(vb1), "s"(rsHB), "s"(so) : "memory");
  };
  if (!c.ffn && tq >= 2) lds_wait_ge(L_CNT + C_IMGDONE * 4, 4);
#pragma unroll 1
  for (int s = 0; s < 3; ++s) {
    lds_wait_ge(L_CNT + (C_FREE + s) * 4, 4);
#pragma unroll 1
    for (int j = 4 * s; j < (s < 2 ? 4 * s + 4 : 10); ++j) wdma(j);
  }
  STAMP(4);
  const int pan = c.tt >> 1;
  poll_flags(P.ws, FL_E + (unsigned)pan * 8u * 128u, 8, tag, lane);
  if (KVFIRST && !c.ffn) lds_wait_ge(L_CNT + C_KVIN * 4, 4);
  STAMP(5);
  bload(__builtin_amdgcn_readlane(tabB, 0), br[0][0], br[0][1]);
  bload(__builtin_amdgcn_readlane(tabB, 1), br[1][0], br[1][1]);
  wdma(10);
  bload(__builtin_amdgcn_readlane(tabB, 2), br[2][0], br[2][1]);
  wdma(11);
  bload(__builtin_amdgcn_readlane(tabB, 3), br[3][0], br[3][1]);
  unsigned w2l[2];
#pragma unroll
  for (int h = 0; h < 2; ++h) w2l[h] = ring + (unsigned)(4 * hh + q) * 128u + (unsigned)((4 * (h ^ sw) + 2 * cb + (pp >> 1)) * 16 + 8 * (pp & 1));
  f32x16 o[2][2];
#pragma unroll
  for (int a = 0; a < 2; ++a)
#pragma unroll
    for (int b = 0; b < 2; ++b) o[a][b] = zero16();
  v4u af[2];
  asm volatile("s_waitcnt vmcnt(10)" : "+v"(br[0][0]), "+v"(br[0][1]) :: "memory");
  af[0] = tr2(w2l[0], w2l[0] + 1024u); af[1] = tr2(w2l[1], w2l[1] + 1024u);
  auto step = [&](int j, int u, bool refill, bool first) AI {
    v4u an[2];
    if (first) asm volatile("s_waitcnt vmcnt(" STR(W1) ")" : "+v"(br[(u + 1) % NB][0]), "+v"(br[(u + 1) % NB][1]) :: "memory");
    else asm volatile("s_waitcnt vmcnt(" STR(W2) ")" : "+v"(br[(u + 1) % NB][0]), "+v"(br[(u + 1) % NB][1]) :: "memory");
    unsigned p0 = w2l[0] + pofs(j + 1), p1 = w2l[1] + pofs(j + 1);
    asm volatile("" : "+v"(p0), "+v"(p1));
    an[0] = tr2(p0, p0 + 1024u); an[1] = tr2(p1, p1 + 1024u);
    o[0][0] = mfma(af[0], br[u][0], o[0][0]); o[0][1] = mfma(af[0], br[u][1], o[0][1]);
    o[1][0] = mfma(af[1], br[u][0], o[1][0]); o[1][1] = mfma(af[1], br[u][1], o[1][1]);
    asm volatile("" : "+v"(o[0][0]), "+v"(o[0][1]), "+v"(o[1][0]), "+v"(o[1][1]) :: "memory");
    if (refill) wdma(j + 12);
    const int jn = j + NB;
    if (u == 0) {
      if (jn == 16) lds_wait_ge(L_CNT + (ORD ? C_ORDY : C_LATERDY) * 4, 1);
      if (jn == (ORD ? 24 : 28)) lds_wait_ge(L_CNT + (ORD ? C_LATERDY : C_ORDY) * 4, 1);
    }
    bload(jn < 36 ? (unsigned)__builtin_amdgcn_readlane(tabB, jn) : 0x7F000000u, br[u][0], br[u][1]);
    af[0] = an[0]; af[1] = an[1];
  };
  lds_wait_ge(L_CNT + (C_FREE + 3) * 4, 4);
#pragma unroll 1
  for (int it = 0; it < 6; ++it) {
    if (it == 3) STAMP(10);
#pragma unroll
    for (int u = 0; u < NB; ++u) step(NB * it + u, u, true, true);
  }
  STAMP(11);
#pragma unroll 1
  for (int it = 6; it < 9; ++it) {
#pragma unroll
    for (int u = 0; u < NB; ++u) step(NB * it + u, u, false, false);
  }
  asm volatile("s_waitcnt vmcnt(0)" : "+v"(br[0][0]), "+v"(br[0][1]), "+v"(br[1][0]), "+v"(br[1][1]), "+v"(br[2][0]), "+v"(br[2][1]), "+v"(br[3][0]), "+v"(br[3][1]) :: "memory");
  STAMP(6);
#pragma unroll
  for (int a = 0; a < 2; ++a)
#pragma unroll
    for (int b = 0; b < 2; ++b)
#pragma unroll
      for (int g4 = 0; g4 < 4; ++g4)
        *(LAS f32x4*)(size_t)(ring + (unsigned)b * SLOT + (unsigned)t * 256u + (unsigned)((8 * a + 2 * g4 + hh) ^ (t & 15)) * 16u) =
            f32x4{o[a][b][4 * g4], o[a][b][4 * g4 + 1], o[a][b][4 * g4 + 2], o[a][b][4 * g4 + 3]};
  VMW(0);
  BAR();
  STAMP(7);
}

template <int NT, int NF, class Fill> DI void pass1(const Params& P, int wid, u64& amp_a, u64& amp_b, Fill& fill, const v4u (&xr)[32], f32x16 (&acc)[NT], f32x4& cs, const unsigned (&wb)[NT], int lane) {
  const s16x4 one = {0x3F80, 0x3F80, 0x3F80, 0x3F80};
  f32x4 csa = {0.f, 0.f, 0.f, 0.f}, csb = csa;
#pragma unroll
  for (int qq = 0; qq < 4; ++qq) {
    if (qq > 0) lds_wait_ge(L_CNT + (C_LAND + qq) * 4, 4);
    if (qq < 2) fill(10 + qq);
    unsigned a[NT];
#pragma unroll
    for (int i = 0; i < NT; ++i) { a[i] = wb[i] + (unsigned)qq * SLOT; asm volatile("" : "+v"(a[i])); }
    v4u f[3][NT];
    auto rd = [&](int k8, v4u* W) AI {
      const unsigned o = (unsigned)((k8 >> 2) * 8192 + (k8 & 3) * 2048);
#pragma unroll
      for (int i = 0; i < NT; ++i) W[i] = tr2(a[i] + o, a[i] + o + 512u);
    };
    rd(0, f[0]); rd(1, f[1]);
    __builtin_amdgcn_sched_barrier(0);
#pragma unroll
    for (int k8 = 0; k8 < 8; ++k8) {
      if (k8 == 4 && qq < 3) {
        if (qq < 2) { if (NF == 8) VMW(8); else VMW(4); } else VMW(0);
        lds_inc(L_CNT + (C_LAND + qq + 1) * 4, lane);
      }
      if (k8 < 6) rd(k8 + 2, f[(k8 + 2) % 3]);
      const v4u xf = xr[8 * qq + k8];
#pragma unroll
      for (int i = 0; i < NT; ++i) acc[i] = mfma(f[k8 % 3][i], xf, acc[i]);
      csa = __builtin_amdgcn_mfma_f32_4x4x4bf16_1k(one, __builtin_bit_cast(s16x4, v2u{f[k8 % 3][0].x, f[k8 % 3][0].y}), csa, 0, 0, 0);
      csb = __builtin_amdgcn_mfma_f32_4x4x4bf16_1k(one, __builtin_bit_cast(s16x4, v2u{f[k8 % 3][0].z, f[k8 % 3][0].w}), csb, 0, 0, 0);
      if (k8 < 6) {
#pragma unroll
        for (int i = 0; i < NT; ++i) {
          __builtin_amdgcn_sched_group_barrier(0x008, 1, 0);
          __builtin_amdgcn_sched_group_barrier(0x100, 2, 0);
        }
        __builtin_amdgcn_sched_group_barrier(0x008, 2, 0);
      }
      __builtin_amdgcn_sched_barrier(0);
    }
    asm volatile("" : "+v"(acc[0]), "+v"(acc[1]), "+v"(acc[NT - 1]), "+v"(csa), "+v"(csb) :: "memory");
    lds_inc(L_CNT + (C_FREE + qq) * 4, lane);
  }
  cs[0] = csa[0] + csb[0];
}

template <bool ffn> DI u64 role_b_head(const Params& P, const Ctx& c, int wid) {
  u64 amp_a = 0, amp_b = 0; (void)amp_a; (void)amp_b;
  const int lane = c.lane, tq = c.tq, hh = c.hh, t = lane & 31;
  const int g = c.gi;
  const unsigned tok = (unsigned)(c.p * 128 + 8 * c.G + 2 * tq);
  v4u r0, r1;
  {
    const u16* rp = P.x + (size_t)tok * 512 + lane * 8;
    asm volatile("global_load_dwordx4 %0, %2, off\n\tglobal_load_dwordx4 %1, %2, off offset:1024" : "=&v"(r0), "=&v"(r1) : "v"(rp) : "memory");
  }
  const __amdgpu_buffer_rsrc_t rsX = __builtin_amdgcn_make_buffer_rsrc((void*)P.x, 0, 2048 * 1024, 0x00020000);
  const __amdgpu_buffer_rsrc_t rsW = __builtin_amdgcn_make_buffer_rsrc((void*)(ffn ? P.w1 : P.wqkv), 0, ffn ? 512 * 3584 : 512 * 3072, 0x00020000);
  constexpr unsigned RS = ffn ? 3584u : 3072u;
  const unsigned cA = (unsigned)(224 * g + 32 * (g & 1));
  const unsigned u0 = ffn ? cA * 2u : (unsigned)(512 + 64 * g) * 2u, u1 = ffn ? cA * 2u + 128u : (unsigned)(1024 + 64 * g) * 2u;
  const unsigned ua = ffn ? (cA + 128u) * 2u : (unsigned)(64 * g) * 2u;
  const unsigned ub = (unsigned)((g & 1) ? 224 * g - 32 : 224 * g + 192) * 2u;
  const unsigned voffX = (unsigned)(lane >> 3) * 1024u + (unsigned)(((lane & 7) ^ ((4 * (tq & 1) + (lane >> 4)) & 7)) * 16);
  const unsigned voffW = (unsigned)(lane >> 3) * RS + (unsigned)(((lane & 7) ^ (((lane >> 4) & 1) << 2)) * 16);
  const unsigned soffX = (unsigned)(c.p * 128 + 8 * tq) * 1024u;
  const unsigned wrow = 32u * (unsigned)(tq & 1) * RS;
  auto fill = [&](int f) AI {
    const unsigned slot = (unsigned)(f & 3) * SLOT;
    if (f < 8) {
      const unsigned kcp = (unsigned)((f + c.rot) & 7);
      unsigned sx = soffX + kcp * 128u, sw = 64u * kcp * RS + wrow + ((tq >> 1) ? u1 : u0);
      unsigned mx = slot + (unsigned)tq * 1024u, mw = slot + 16384u + (unsigned)tq * 4096u;
#pragma unroll 1
      for (int m = 0; m < 4; ++m) {
        dma(mx, voffX, rsX, sx); dma(mw, voffW, rsW, sw);
        mx += 4096u; sx += 32768u; mw += 1024u; sw += 8u * RS;
      }
    } else {
      const unsigned kcp = (unsigned)((2 * (f - 8) + (tq >> 1) + c.rot) & 7);
      unsigned sw = 64u * kcp * RS + wrow, mw = slot + (unsigned)tq * 4096u;
#pragma unroll 1
      for (int m = 0; m < 4; ++m) {
        dma(mw, voffW, rsW, sw + ua);
        if (ffn) dma(mw + 16384u, voffW, rsW, sw + ub);
        mw += 1024u; sw += 8u * RS;
      }
    }
  };
  fill(0);
  float st0;
  {
    asm volatile("s_waitcnt vmcnt(8)" : "+v"(r0), "+v"(r1) :: "memory");
    float v[2][8], mean[2], rs[2];
#pragma unroll
    for (int k = 0; k < 4; ++k) {
      v[0][2 * k] = u2f(r0[k] << 16); v[0][2 * k + 1] = u2f(r0[k] & 0xffff0000u);
      v[1][2 * k] = u2f(r1[k] << 16); v[1][2 * k + 1] = u2f(r1[k] & 0xffff0000u);
    }
#pragma unroll
    for (int i = 0; i < 2; ++i) mean[i] = wavesum(((v[i][0] + v[i][1]) + (v[i][2] + v[i][3])) + ((v[i][4] + v[i][5]) + (v[i][6] + v[i][7]))) * (1.0f / 512.0f);
#pragma unroll
    for (int i = 0; i < 2; ++i) {
      float q2 = 0.f;
#pragma unroll
      for (int k = 0; k < 8; ++k) { const float d = v[i][k] - mean[i]; q2 = __builtin_fmaf(d, d, q2); }
      rs[i] = __builtin_amdgcn_rsqf(wavesum(q2) * (1.0f / 512.0f) + 1e-5f);
    }
    st0 = (lane & 2) ? rs[1] : rs[0];
    st0 = (lane & 1) ? -((lane & 2) ? mean[1] : mean[0]) * st0 : st0;
    asm volatile("" : "+v"(st0));
  }
  fill(1);
  unsigned xa[4];
#pragma unroll
  for (int ks = 0; ks < 4; ++ks) xa[ks] = (unsigned)(32 * tq + t) * 128u + (unsigned)(((2 * ks + hh) ^ ((t >> 1) & 7)) * 16);
  v4u xr[32];
  u64 tag = 0;
  v4u gr;
  const unsigned token = (unsigned)(c.p * 128 + 32 * tq + t);
#pragma unroll
  for (int kc = 0; kc < 8; ++kc) {
    if (kc < 7 || ffn) VMW(8); else VMW(4);
    BAR();
    if (kc == 0) {
      tag = lds_tag();
      if (lane < 4)
        __hip_atomic_store((GAS u64*)(P.ws + WS_ST + tok * 16u + (unsigned)lane * 8u), ((u64)(unsigned)tag << 32) | f2u(st0), __ATOMIC_RELAXED, __HIP_MEMORY_SCOPE_AGENT);
    }
    if (kc == 5) {
      const char* gp = P.ws + WS_ST + token * 16u;
      asm volatile("global_load_dwordx4 %0, %1, off sc1" : "=&v"(gr) : "v"(gp) : "memory");
    }
    fill(kc + 2);
    const unsigned sb = (unsigned)(kc & 3) * SLOT;
#pragma unroll
    for (int ks = 0; ks < 4; ++ks) xr[4 * kc + ks] = lds128(xa[ks] + sb);
    asm volatile("" : "+v"(xr[4 * kc]), "+v"(xr[4 * kc + 1]), "+v"(xr[4 * kc + 2]), "+v"(xr[4 * kc + 3]));
  }
  asm volatile("" : "+v"(gr));
  STAMP(2);
  BAR();
  if (__builtin_amdgcn_ballot_w64(gr.y != (unsigned)tag || gr.w != (unsigned)tag) != 0) {
    const __amdgpu_buffer_rsrc_t rs = __builtin_amdgcn_make_buffer_rsrc((void*)(P.ws + WS_ST), 0, 32768, 0x00020000);
#pragma unroll 1
    for (unsigned spin = 0; spin < kSpin; ++spin) {
      gr = __builtin_amdgcn_raw_buffer_load_b128(rs, token * 16u, 0, 16);
      if (__builtin_amdgcn_ballot_w64(gr.y != (unsigned)tag || gr.w != (unsigned)tag) == 0) break;
      __builtin_amdgcn_s_sleep(1);
    }
  }
  const float rstd = u2f(gr.x), mr = u2f(gr.z);
  if (hh == 0) *(LAS f32x2*)(size_t)(L_STATS + (unsigned)(32 * tq + t) * 8u) = f32x2{rstd, mr};
  if (ffn) VMW(8); else VMW(4);
  BAR();
  const int cb = (lane >> 4) & 1, q = (lane >> 2) & 3, pp = lane & 3, sw = (q >> 1) & 1;
  unsigned wl[2];
#pragma unroll
  for (int h = 0; h < 2; ++h) wl[h] = (unsigned)(8 * hh + q) * 128u + (unsigned)((4 * (h ^ sw) + 2 * cb + (pp >> 1)) * 16 + 8 * (pp & 1));
  const __amdgpu_buffer_rsrc_t rsHB = __builtin_amdgcn_make_buffer_rsrc(P.ws + WS_HB, 0, HB_BYTES, 0x00020000);
  const unsigned T32 = (unsigned)(c.p * 4 + tq);
  f32x4 cs = {0.f, 0.f, 0.f, 0.f};
  auto cs_sync = [&](int tile, bool wr) AI {
    const float s = xhalf_add(cs[0]);
    if (hh == 0 && wr) *(LAS float*)(size_t)(L_CS1 + (unsigned)tile * 128u + (unsigned)t * 4u) = s;
    asm volatile("s_waitcnt lgkmcnt(0)" ::: "memory");
    lds_inc(L_CNT + C_CS1 * 4, lane);
    lds_wait_ge(L_CNT + C_CS1 * 4, 4);
  };
  if constexpr (ffn) {
    const unsigned w2a = ((g & 1) ? wl[1] : wl[0]) + 16384u;
    int ti[3];
    unsigned wb[3];
    f32x16 acc[3];
#pragma unroll
    for (int i = 0; i < 3; ++i) { ti[i] = (i + tq) % 3; wb[i] = ti[i] == 0 ? wl[0] : (ti[i] == 1 ? wl[1] : w2a); acc[i] = zero16(); }
    pass1<3, 8>(P, wid, amp_a, amp_b, fill, xr, acc, cs, wb, lane);
    cs_sync(ti[0], tq < 3);
    STAMP(3);
    if (PRIOL) __builtin_amdgcn_s_setprio(3);
    f32x4 csv[3][4], bsv[3][4];
#pragma unroll
    for (int i = 0; i < 3; ++i) { load16(L_CS1 + (unsigned)ti[i] * 128u, hh, csv[i]); load16(L_B1 + (unsigned)(4 + ti[i]) * 128u, hh, bsv[i]); }
#pragma unroll
    for (int i = 0; i < 3; ++i)
      ffn_tile(acc[i], rstd, mr, csv[i], bsv[i], rsHB, (unsigned)lane * 16u + (T32 * 144u + (unsigned)(64 + 6 * g + 2 * ti[i])) * 1024u);
    publish_last(L_CNT + C_LATE * 4, 4, P.ws, FL_L + (unsigned)(c.p * 8 + g) * 128u, tag, lane);
    if (PRIOL) __builtin_amdgcn_s_setprio(0);
  } else {
    int ti[2];
    unsigned wb[2];
    f32x16 acc[2];
#pragma unroll
    for (int i = 0; i < 2; ++i) { ti[i] = (i + tq) & 1; wb[i] = ti[i] == 0 ? wl[0] : wl[1]; acc[i] = zero16(); }
    pass1<2, 4>(P, wid, amp_a, amp_b, fill, xr, acc, cs, wb, lane);
    cs_sync(ti[0], tq < 2);
    STAMP(3);
    v4u pq[4][2];
#pragma unroll
    for (int hd = 0; hd < 4; ++hd) {
      float y[8];
      fold8(acc[hd >> 1], 8 * (hd & 1), rstd, mr, L_CS1 + (unsigned)ti[hd >> 1] * 128u, hh, y);
      hedge(mfma(wphi_frag(2 * ti[hd >> 1] + (hd & 1), lane), pack8(y), zero16()), pq[hd][0], pq[hd][1]);
    }
    asm volatile("" : "+v"(pq[0][0]), "+v"(pq[0][1]), "+v"(pq[1][0]), "+v"(pq[1][1]), "+v"(pq[2][0]), "+v"(pq[2][1]), "+v"(pq[3][0]), "+v"(pq[3][1]));
    STAMP(4);
    if (PRIO) __builtin_amdgcn_s_setprio(3);
    if (tq == 0) { poll_flags(P.ws, FL_K + (unsigned)g * 16u * 128u, 16, tag, lane); lds_set(L_CNT + C_KVRDY * 4, 1); }
    else lds_wait_ge(L_CNT + C_KVRDY * 4, 1);
    STAMP(5);
    {
      const __amdgpu_buffer_rsrc_t rsKV = __builtin_amdgcn_make_buffer_rsrc(P.ws + WS_KVP, 0, 524288 + 65536, 0x00020000);
      const unsigned hbase = (unsigned)(g * 4 + tq) * 16u;
      v4u pv[16];
#pragma unroll
      for (int i = 0; i < 16; ++i) pv[i] = __builtin_amdgcn_raw_buffer_load_b128(rsKV, (unsigned)lane * 16u, (hbase + (unsigned)i) * 1024u, 16);
      const v4u z0 = __builtin_amdgcn_raw_buffer_load_b128(rsKV, (unsigned)lane * 16u, 524288u + hbase * 128u, 16);
      const v4u z1 = __builtin_amdgcn_raw_buffer_load_b128(rsKV, (unsigned)lane * 16u, 524288u + hbase * 128u + 1024u, 16);
      const u64 lf = __hip_atomic_load((const GAS u64*)(P.ws + FL_L + (unsigned)(c.tt >> 1) * 8u * 128u + (unsigned)(lane & 7) * 128u), __ATOMIC_RELAXED, __HIP_MEMORY_SCOPE_AGENT);
      __builtin_amdgcn_sched_barrier(0);
      v4u idf;
      {
        const int m = lane & 31, h2 = lane >> 5;
        const int j = (m < 16 && ((m >> 2) & 1) == h2) ? (((m >> 3) << 2) | (m & 3)) : -1;
#pragma unroll
        for (int k = 0; k < 4; ++k) idf[k] = (j == 2 * k ? 0x3F80u : 0u) | (j == 2 * k + 1 ? 0x3F800000u : 0u);
      }
      f32x16 sum = zero16();
#pragma unroll
      for (int i = 0; i < 16; ++i) sum = mfma(idf, pv[i], sum);
      asm volatile("" : "+v"(sum));
      lds_inc(L_CNT + C_KVIN * 4, lane);
      if (!ORD && tq == 0 && __builtin_amdgcn_ballot_w64(lf != tag) == 0) lds_set(L_CNT + C_LATERDY * 4, 1);
      const float s[8] = {sum[0], sum[1], sum[2], sum[3], sum[4], sum[5], sum[6], sum[7]};
      const unsigned afr = L_AFR + (unsigned)tq * 2048u;
      *(LAS v4u*)(size_t)(afr + (unsigned)((lane >> 4) & 1) * 1024u + (unsigned)((lane & 15) + 32 * (lane >> 5)) * 16u) = pack8(s);
      const unsigned hh2 = (unsigned)(lane >> 4) & 1u, c16 = (unsigned)lane & 15u;
      float zz[4];
#pragma unroll
      for (int k = 0; k < 4; ++k) {
        float v = dppadd<0x128>(u2f(z0[k]) + u2f(z1[k]));
        auto r = __builtin_amdgcn_permlane16_swap(f2u(v), f2u(v), false, false);
        zz[k] = xhalf_add(u2f(r[0]) + u2f(r[1]));
      }
      if (lane < 8) *(LAS f32x4*)(size_t)(L_ZT + (unsigned)tq * 128u + (unsigned)lane * 16u) = f32x4{zz[0], zz[1], zz[2], zz[3]};
      const unsigned f0 = 16u * (unsigned)(lane >> 5) + 4u * hh2;
      const f32x4 za = lds128f(L_ZT + (unsigned)tq * 128u + f0 * 4u), zb = lds128f(L_ZT + (unsigned)tq * 128u + (f0 + 8u) * 4u);
      *(LAS v4u*)(size_t)(afr + (unsigned)(lane >> 5) * 1024u + (16u + c16 + 32u * hh2) * 16u) = v4u{pk2(za[0], za[1]), pk2(za[2], za[3]), pk2(zb[0], zb[1]), pk2(zb[2], zb[3])};
      asm volatile("s_waitcnt lgkmcnt(0)" ::: "memory");
      lds_inc(L_CNT + C_AFR * 4, lane);
      lds_wait_ge(L_CNT + C_AFR * 4, 4);
    }
#pragma unroll
    for (int hd = 0; hd < 4; ++hd) {
      const int head = 2 * ti[hd >> 1] + (hd & 1);
      f32x16 d = mfma(lds128(L_AFR + (unsigned)head * 2048u + (unsigned)lane * 16u), pq[hd][0], zero16());
      d = mfma(lds128(L_AFR + (unsigned)head * 2048u + 1024u + (unsigned)lane * 16u), pq[hd][1], d);
      const float r = __builtin_amdgcn_rcpf(d[8] + 1e-6f);
      const v4u o = v4u{pk2(d[0] * r, d[1] * r), pk2(d[2] * r, d[3] * r), pk2(d[4] * r, d[5] * r), pk2(d[6] * r, d[7] * r)};
      st128(o, rsHB, (unsigned)lane * 16u + (T32 * 144u + (unsigned)(112 + 4 * g + head)) * 1024u);
    }
    publish_last(L_CNT + C_O * 4, 4, P.ws, FL_O + (unsigned)(c.p * 8 + g) * 128u, tag, lane);
    if (PRIO) __builtin_amdgcn_s_setprio(0);
  }
  STAMP(6);
  return tag;
}

DI void role_b(const Params& P, const Ctx& c, int wid, u64 amp_a0) {
  u64 amp_a = amp_a0, amp_b = 0; (void)amp_a; (void)amp_b;
  const int lane = c.lane, tq = c.tq;
  const u64 tag = c.ffn ? role_b_head<true>(P, c, wid) : role_b_head<false>(P, c, wid);
  const int pan = c.tt >> 1;
  const int tb = tq * 64 + lane;
  v4u xres[2], bres;
#pragma unroll
  for (int m = 0; m < 2; ++m) {
    const int idx = tb + 256 * m;
    xres[m] = *(const GAS v4u*)(P.x + (size_t)(64 * c.tt + (idx >> 3)) * 512 + 64 * c.ct + 8 * (idx & 7));
  }
  bres = *(const GAS v4u*)(P.b2 + 64 * c.ct + 8 * (tb & 7));
  if (tq == 0) {
    poll_flags(P.ws, (ORD ? FL_O : FL_L) + (unsigned)pan * 8u * 128u, 8, tag, lane);
    lds_set(L_CNT + (ORD ? C_ORDY : C_LATERDY) * 4, 1);
    poll_flags(P.ws, (ORD ? FL_L : FL_O) + (unsigned)pan * 8u * 128u, 8, tag, lane);
    lds_set(L_CNT + (ORD ? C_LATERDY : C_ORDY) * 4, 1);
  } else if (tq == 1 && __builtin_amdgcn_workgroup_id_x() == 0) {
    poll_flags(P.ws, FL_E, 64, tag, lane); poll_flags(P.ws, FL_E + 64 * 128, 64, tag, lane);
    poll_flags(P.ws, FL_K, 64, tag, lane); poll_flags(P.ws, FL_K + 64 * 128, 64, tag, lane);
    if (lane == 0) __hip_atomic_store((GAS u64*)(P.ws + WS_EPOCH), tag, __ATOMIC_RELAXED, __HIP_MEMORY_SCOPE_AGENT);
  }
  BAR();
#pragma unroll
  for (int m = 0; m < 2; ++m) {
    const int idx = tb + 256 * m;
    const unsigned tok = (unsigned)(idx >> 3), cg = (unsigned)(idx & 7);
    const unsigned a = (tok >> 5) * SLOT + (tok & 31u) * 256u, c0 = ((2u * cg) ^ (tok & 15u)) * 16u, c1 = ((2u * cg + 1u) ^ (tok & 15u)) * 16u;
    f32x4 lo = lds128f(a + c0), hi = lds128f(a + c1);
#pragma unroll
    for (unsigned w = 1; w < 4; ++w) { lo += lds128f(w * 8192u + a + c0); hi += lds128f(w * 8192u + a + c1); }
    v4u o;
#pragma unroll
    for (int k = 0; k < 4; ++k) {
      const float a0 = k < 2 ? lo[2 * k] : hi[2 * k - 4], a1 = k < 2 ? lo[2 * k + 1] : hi[2 * k - 3];
      const float x0 = u2f(xres[m][k] << 16), x1 = u2f(xres[m][k] & 0xffff0000u);
      const float b0 = u2f(bres[k] << 16), b1 = u2f(bres[k] & 0xffff0000u);
      o[k] = pk2(a0 + x0 + b0, a1 + x1 + b1);
    }
    *(GAS v4u*)(P.out + (size_t)(64 * c.tt + (idx >> 3)) * 512 + 64 * c.ct + 8 * (idx & 7)) = o;
  }
  STAMP(7);

  VMW(0);
  if (wid == 4) {
    const unsigned blk = __builtin_amdgcn_workgroup_id_x();
    const u64 tend = __builtin_amdgcn_s_memrealtime();
    if (lane == 0) {
      __hip_atomic_store((GAS u64*)(P.ws + (24u << 20) + blk * 128u), amp_a, __ATOMIC_RELAXED, __HIP_MEMORY_SCOPE_AGENT);
      __hip_atomic_store((GAS u64*)(P.ws + (24u << 20) + blk * 128u + 8u), tend, __ATOMIC_RELAXED, __HIP_MEMORY_SCOPE_AGENT);
    }
    VMW(0);
    if (lane == 0) __hip_atomic_store((GAS u64*)(P.ws + (25u << 20) + blk * 128u), tag, __ATOMIC_RELAXED, __HIP_MEMORY_SCOPE_AGENT);
    if (blk == 0) {
      for (int r = 0; r < 4; ++r) poll_flags(P.ws, (25u << 20) + r * 64u * 128u, 64, tag, lane);
      u64 mns = ~0ull, mxs = 0, mxe = 0; unsigned arg = 0, args = 0;
      for (int r = 0; r < 4; ++r) {
        const u64 s = __hip_atomic_load((const GAS u64*)(P.ws + (24u << 20) + (r * 64u + lane) * 128u), __ATOMIC_RELAXED, __HIP_MEMORY_SCOPE_AGENT);
        const u64 e = __hip_atomic_load((const GAS u64*)(P.ws + (24u << 20) + (r * 64u + lane) * 128u + 8u), __ATOMIC_RELAXED, __HIP_MEMORY_SCOPE_AGENT);
        if (s < mns) mns = s;
        if (s > mxs) { mxs = s; args = r * 64 + lane; }
        if (e > mxe) { mxe = e; arg = r * 64 + lane; }
      }
      u64 gmns = ~0ull, gmxs = 0, gmxe = 0; unsigned garg = 0, gargs = 0;
      for (int l = 0; l < 64; ++l) {
        const u64 a = ((u64)(unsigned)__builtin_amdgcn_readlane((unsigned)(mns >> 32), l) << 32) | (unsigned)__builtin_amdgcn_readlane((unsigned)mns, l);
        const u64 b = ((u64)(unsigned)__builtin_amdgcn_readlane((unsigned)(mxs >> 32), l) << 32) | (unsigned)__builtin_amdgcn_readlane((unsigned)mxs, l);
        const u64 c2 = ((u64)(unsigned)__builtin_amdgcn_readlane((unsigned)(mxe >> 32), l) << 32) | (unsigned)__builtin_amdgcn_readlane((unsigned)mxe, l);
        if (a < gmns) gmns = a;
        if (b > gmxs) { gmxs = b; gargs = __builtin_amdgcn_readlane(args, l); }
        if (c2 > gmxe) { gmxe = c2; garg = __builtin_amdgcn_readlane(arg, l); }
      }
      u64 d = 0;
      if (SPANMODE == 1) d = (gmxe - gmns) * 20;
      if (SPANMODE == 2) d = (gmxs - gmns) * 20;
      if (SPANMODE == 3) d = (u64)garg * 100;
      if (SPANMODE == 4) d = (gmxe - tend) * 20;
      if (SPANMODE == 5) d = (amp_a - gmns) * 20;
      if (SPANMODE == 6) d = (u64)gargs * 100;
      const u64 t1 = __builtin_amdgcn_s_memrealtime();
      while (__builtin_amdgcn_s_memrealtime() - t1 < d) __builtin_amdgcn_s_sleep(4);
    }
  }
}

__global__ __launch_bounds__(512, 2) NOPK void fused_kernel(Params P) {
  __shared__ __attribute__((aligned(1024))) char smem[LDS_BYTES];
  asm volatile("" ::"v"((unsigned)(size_t)(LAS char*)smem) : "memory");
  u64 pc;
  asm volatile("s_getpc_b64 %0" : "=s"(pc));
  const int tid = __builtin_amdgcn_workitem_id_x(), lane = tid & 63;
  const int wid = __builtin_amdgcn_readfirstlane(tid >> 6);
  u64 amp_a = 0, amp_b = 0;
  STAMP(0);
  Ctx c;
  c.lane = lane; c.tq = wid & 3; c.hh = lane >> 5;
  const int blk = __builtin_amdgcn_workgroup_id_x(), xcd = blk & 7, j = blk >> 3, ja = j & 7, jb = j >> 3;
  c.p = (xcd & 1) * 8 + ja;
  c.G = (xcd >> 1) * 4 + jb;
  c.ffn = !(c.G & 1);
  c.gi = c.G >> 1;
  c.rot = ROT1 == 0 ? 0 : (ROT1 == 1 ? (((ja + jb) % 3) * 3) & 7 : (ROT1 == 2 ? (ja + 2 * jb) & 7 : ((ja + jb) & 1) * 4));
  c.tt = (xcd >> 1) * 8 + ja; c.ct = (xcd & 1) * 4 + jb; c.ja = ja; c.jb = jb;
  c.rotE = ROT2 ? (2 * ja + 4 * jb) & 15 : 0; c.rotL = ROT2 ? (ja + 3 * jb) % 12 : 0; c.rotO = ROT2 ? (ja + 2 * jb) & 7 : 0;
  if (wid < 4) {
    const unsigned kb0 = (unsigned)(j + 32 * (wid & 1)), kb = kb0 < CODE_KB ? kb0 : 0u;
    const unsigned pf = CODEPF ? *(const GAS unsigned*)((pc & ~255ull) + kb * 1024u + (unsigned)lane * 16u) : 0u;
    const u64 e = __hip_atomic_load((const GAS u64*)(P.ws + WS_EPOCH), __ATOMIC_RELAXED, __HIP_MEMORY_SCOPE_AGENT);
    if (c.ffn) {
      const int tc = tid < 224 ? tid : 0;
      const int t7 = tc >> 5, n = tc & 31, g = c.gi;
      const int cA = 224 * g + 32 * (g & 1);
      const int col = t7 < 4 ? cA + 32 * t7 : (t7 < 6 ? cA + 128 + 32 * (t7 - 4) : ((g & 1) ? 224 * g : 224 * g + 192));
      const unsigned bv = P.b1[col + n];
      *(LAS float*)(size_t)(L_B1 + (unsigned)tid * 4u) = u2f(bv << 16);
    } else {
      const v4u wv = *(const GAS v4u*)(P.wphi + (size_t)c.gi * 1024 + (tid & 127) * 8);
      *(LAS v4u*)(size_t)(L_WPHI + (unsigned)(tid & 127) * 16u) = wv;
    }
    if (tid < 32) *(LAS unsigned*)(size_t)(L_CNT + (unsigned)tid * 4u) = 0u;
    const u64 tag = e + 1u;
    if (tid == 0) *(LAS v2u*)(size_t)L_TAG = v2u{(unsigned)tag, (unsigned)(tag >> 32)};
    asm volatile("" ::"v"(pf));
    STAMP(1);
    role_a(P, c, wid, amp_a, amp_b);
  } else {
    STAMP(1);
    role_b(P, c, wid, amp_a);
  }
}

}

extern "C" void kernel_launch(void* const* d_in, const int* in_sizes, int n_in, void* d_out, int out_size, void* d_ws, size_t ws_size,
                              hipStream_t stream) {
  Params P;
  P.x = (const u16*)d_in[0]; P.wqkv = (const u16*)d_in[1]; P.wphi = (const u16*)d_in[2]; P.wo = (const u16*)d_in[3];
  P.w1 = (const u16*)d_in[5]; P.b1 = (const u16*)d_in[6]; P.w2 = (const u16*)d_in[7]; P.b2 = (const u16*)d_in[8];
  P.out = (u16*)d_out; P.ws = (char*)d_ws;
  fused_kernel<<<256, 512, 0, stream>>>(P);
}
```

```cpp
#include <hip/hip_runtime.h>
#include <stdint.h>
#include <type_traits>

namespace {

typedef unsigned short u16;
typedef unsigned long long u64;
typedef unsigned v4u __attribute__((ext_vector_type(4)));
typedef unsigned v2u __attribute__((ext_vector_type(2)));
typedef float f32x16 __attribute__((ext_vector_type(16)));
typedef float f32x4 __attribute__((ext_vector_type(4)));
typedef float f32x2 __attribute__((ext_vector_type(2)));
typedef __bf16 bf16x8 __attribute__((ext_vector_type(8)));
typedef __bf16 bf16x2 __attribute__((ext_vector_type(2)));
typedef short s16x4 __attribute__((ext_vector_type(4)));
#define LAS __attribute__((address_space(3)))
#define GAS __attribute__((address_space(1)))
#if defined(__HIP_DEVICE_COMPILE__)
#define NOPK __attribute__((target("no-packed-fp32-ops")))
#else
#define NOPK
#endif
#define DI __device__ __forceinline__ NOPK
#define AI __attribute__((always_inline)) NOPK

#define STAMPS 0
#define ROT1 1
#define ROT2 0
#define STR2(x) #x
#define STR(x) STR2(x)
#define W1 8
#define W2 4
#define PRIO 1
#define SLP 1
#define KVFIRST 0
#define PRIOL 0
#define AMPX2 0
#define ORD 0
#define CODEPF 1
#define WARM2 1
#define CODE_KB 36

constexpr unsigned WS_EPOCH = 0;
constexpr unsigned FL_E = 4096;
constexpr unsigned FL_L = FL_E + 128 * 128;
constexpr unsigned FL_K = FL_L + 128 * 128;
constexpr unsigned FL_O = FL_K + 128 * 128;
constexpr unsigned WS_ST = 73728;
constexpr unsigned WS_KVP = 131072;
constexpr unsigned WS_ZP = WS_KVP + 524288;
constexpr unsigned WS_HB = 1048576;
constexpr unsigned HB_BYTES = 64u * 144u * 1024u;
constexpr unsigned WS_STAMP = 16u << 20;

constexpr unsigned SLOT = 32768;
constexpr unsigned L_MISC = 131072;
constexpr unsigned L_STATS = L_MISC;
constexpr unsigned L_CS0 = L_MISC + 1024;
constexpr unsigned L_CS1 = L_MISC + 1536;
constexpr unsigned L_B1 = L_MISC + 2048;
constexpr unsigned L_WPHI = L_MISC + 3072;
constexpr unsigned L_CNT = L_MISC + 5120;
constexpr unsigned L_TAG = L_MISC + 5248;
constexpr unsigned L_ZT = L_MISC + 5376;
constexpr unsigned L_AFR = L_MISC + 8192;
constexpr unsigned LDS_BYTES = 163840;
enum { C_CS1 = 0, C_LATE = 1, C_KV = 2, C_O = 3, C_AFR = 4, C_KVRDY = 5, C_LATERDY = 6, C_ORDY = 7, C_FREE = 8  , C_LAND = 12  , C_EARLY = 16, C_IMG = 17, C_KVIN = 18, C_IMGDONE = 19 };

constexpr unsigned kSpin = 1u << 16;

struct Params {
  const u16* x; const u16* wqkv; const u16* wphi; const u16* wo;
  const u16* w1; const u16* b1; const u16* w2; const u16* b2;
  u16* out; char* ws;
};

DI unsigned f2u(float f) { return __builtin_bit_cast(unsigned, f); }
DI float u2f(unsigned u) { return __builtin_bit_cast(float, u); }
DI float fmx(float a, float b) { return __builtin_fmaxf(a, b); }
DI f32x16 mfma(v4u a, v4u b, f32x16 c) {
  return __builtin_amdgcn_mfma_f32_32x32x16_bf16(__builtin_bit_cast(bf16x8, a), __builtin_bit_cast(bf16x8, b), c, 0, 0, 0);
}
DI f32x16 zero16() { f32x16 z; for (int r = 0; r < 16; ++r) z[r] = 0.f; return z; }
DI v4u lds128(unsigned a) { return *(const LAS v4u*)(size_t)a; }
DI f32x4 lds128f(unsigned a) { return *(const LAS f32x4*)(size_t)a; }
DI v2u tr64(unsigned a) { return __builtin_bit_cast(v2u, __builtin_amdgcn_ds_read_tr16_b64_v4i16((LAS s16x4*)(size_t)a)); }
DI v4u tr2(unsigned a0, unsigned a1) { v2u l = tr64(a0), h = tr64(a1); return v4u{l.x, l.y, h.x, h.y}; }
DI unsigned pk2(float a, float b) { return __builtin_bit_cast(unsigned, __builtin_convertvector(f32x2{a, b}, bf16x2)); }
DI float xhalf_add(float v) { auto r = __builtin_amdgcn_permlane32_swap(f2u(v), f2u(v), false, false); return u2f(r[0]) + u2f(r[1]); }
DI float xhalf_max(float v) { auto r = __builtin_amdgcn_permlane32_swap(f2u(v), f2u(v), false, false); return fmx(u2f(r[0]), u2f(r[1])); }
DI float shx(float v, int m, int lane) { return u2f((unsigned)__builtin_amdgcn_ds_bpermute((lane ^ m) << 2, (int)f2u(v))); }
DI f32x4 lanesum(v4u f, f32x4 c) {
  const s16x4 one = {0x3F80, 0x3F80, 0x3F80, 0x3F80};
  c = __builtin_amdgcn_mfma_f32_4x4x4bf16_1k(one, __builtin_bit_cast(s16x4, v2u{f.x, f.y}), c, 0, 0, 0);
  return __builtin_amdgcn_mfma_f32_4x4x4bf16_1k(one, __builtin_bit_cast(s16x4, v2u{f.z, f.w}), c, 0, 0, 0);
}
template <int CTRL> DI float dppadd(float v) { return v + u2f((unsigned)__builtin_amdgcn_update_dpp(0, (int)f2u(v), CTRL, 0xf, 0xf, true)); }
DI float wavesum(float v) {
  v = dppadd<0xB1>(v); v = dppadd<0x4E>(v); v = dppadd<0x141>(v); v = dppadd<0x140>(v);
  auto r = __builtin_amdgcn_permlane16_swap(f2u(v), f2u(v), false, false);
  return xhalf_add(u2f(r[0]) + u2f(r[1]));
}
DI void dma(unsigned m0v, unsigned voff, __amdgpu_buffer_rsrc_t rs, unsigned soff) {
  asm volatile("s_mov_b32 m0, %0\n\ts_nop 0\n\tbuffer_load_dwordx4 %1, %2, %3 offen lds" ::"s"(m0v), "v"(voff), "s"(rs), "s"(soff) : "memory");
}
DI void st128(v4u v, __amdgpu_buffer_rsrc_t rs, unsigned voff) { __builtin_amdgcn_raw_buffer_store_b128(v, rs, voff, 0, 16); }
#define BAR() do { __builtin_amdgcn_sched_barrier(0); asm volatile("s_waitcnt lgkmcnt(0)\n\ts_barrier" ::: "memory"); __builtin_amdgcn_sched_barrier(0); } while (0)
#define VMW(n) asm volatile("s_waitcnt vmcnt(" #n ")" ::: "memory")
constexpr unsigned ONES = 0x3F803F80u;

#define STAMP(i) do { } while (0)

DI void poll_flags(const char* ws, unsigned base, int n, u64 tag, int lane) {
  const GAS u64* f = (const GAS u64*)(ws + base + (unsigned)(lane < n ? lane : 0) * 128u);
#pragma unroll 1
  for (unsigned spin = 0; spin < kSpin; ++spin) {
    const u64 v = __hip_atomic_load(f, __ATOMIC_RELAXED, __HIP_MEMORY_SCOPE_AGENT);
    if (__builtin_amdgcn_ballot_w64(v != tag) == 0) break;
    __builtin_amdgcn_s_sleep(1);
  }
  __builtin_amdgcn_fence(__ATOMIC_ACQUIRE, "wavefront");
  asm volatile("" ::: "memory");
}
DI void lds_wait_ge(unsigned addr, unsigned want) {
#pragma unroll 1
  for (unsigned spin = 0; spin < (kSpin << 4); ++spin) {
    const unsigned v = *(volatile LAS unsigned*)(size_t)addr;
    if ((unsigned)__builtin_amdgcn_readfirstlane(v) >= want) break;
    if (SLP) __builtin_amdgcn_s_sleep(SLP);
  }
  asm volatile("" ::: "memory");
}
DI void lds_set(unsigned addr, unsigned v) { *(volatile LAS unsigned*)(size_t)addr = v; }
DI void lds_inc(unsigned addr, int lane) { if (lane == 0) __hip_atomic_fetch_add((LAS unsigned*)(size_t)addr, 1u, __ATOMIC_RELAXED, __HIP_MEMORY_SCOPE_WORKGROUP); }
DI void publish_last(unsigned cnt_addr, unsigned n, char* ws, unsigned flag_off, u64 tag, int lane) {
  VMW(0);
  if (lane == 0) {
    const unsigned old = __hip_atomic_fetch_add((LAS unsigned*)(size_t)cnt_addr, 1u, __ATOMIC_RELAXED, __HIP_MEMORY_SCOPE_WORKGROUP);
    if (old == n - 1) __hip_atomic_store((GAS u64*)(ws + flag_off), tag, __ATOMIC_RELAXED, __HIP_MEMORY_SCOPE_AGENT);
  }
}
DI u64 lds_tag() { const v2u t = *(const LAS v2u*)(size_t)L_TAG; return ((u64)(unsigned)__builtin_amdgcn_readfirstlane(t.y) << 32) | (unsigned)__builtin_amdgcn_readfirstlane(t.x); }

DI void fold8(const f32x16& acc, int r0, float rstd, float mr, unsigned cs, int hh, float* y) {
#pragma unroll
  for (int g = 0; g < 2; ++g) {
    const f32x4 t = lds128f(cs + (unsigned)((r0 >> 2) + g) * 32u + (unsigned)hh * 16u) * mr;
#pragma unroll
    for (int k = 0; k < 4; ++k) y[4 * g + k] = __builtin_fmaf(acc[r0 + 4 * g + k], rstd, t[k]);
  }
}
DI void load16(unsigned a, int hh, f32x4* v) {
#pragma unroll
  for (int g = 0; g < 4; ++g) v[g] = lds128f(a + (unsigned)g * 32u + (unsigned)hh * 16u);
}
DI void ffn_tile(const f32x16& acc, float rstd, float mr, const f32x4* cs, const f32x4* bs, __amdgpu_buffer_rsrc_t rsHB, unsigned voff) {
  f32x2 y[8], d[8];
#pragma unroll
  for (int g = 0; g < 4; ++g) {
    const f32x4 t = cs[g] * mr + bs[g];
    y[2 * g] = f32x2{acc[4 * g], acc[4 * g + 1]} * rstd + f32x2{t[0], t[1]};
    y[2 * g + 1] = f32x2{acc[4 * g + 2], acc[4 * g + 3]} * rstd + f32x2{t[2], t[3]};
  }
#pragma unroll
  for (int k = 0; k < 8; ++k) d[k] = y[k] * ((y[k] * y[k]) * (-0.10294324f) + (-2.30220819f));
#pragma unroll
  for (int k = 0; k < 8; ++k) { d[k].x = __builtin_amdgcn_exp2f(d[k].x); d[k].y = __builtin_amdgcn_exp2f(d[k].y); }
#pragma unroll
  for (int k = 0; k < 8; ++k) d[k] += 1.0f;
#pragma unroll
  for (int k = 0; k < 8; ++k) { d[k].x = __builtin_amdgcn_rcpf(d[k].x); d[k].y = __builtin_amdgcn_rcpf(d[k].y); }
  v4u o0, o1;
#pragma unroll
  for (int k = 0; k < 4; ++k) {
    const f32x2 g0 = y[k] * d[k], g1 = y[4 + k] * d[4 + k];
    o0[k] = pk2(g0.x, g0.y); o1[k] = pk2(g1.x, g1.y);
  }
  st128(o0, rsHB, voff); st128(o1, rsHB, voff + 1024u);
}
DI void hedge(const f32x16& d, v4u& o0, v4u& o1) {
  constexpr float L = 1.4426950408889634f;
  float m0 = fmx(fmx(fmx(d[0], d[1]), fmx(d[2], d[3])), fmx(fmx(d[4], d[5]), fmx(d[6], d[7])));
  float m1 = fmx(fmx(fmx(d[8], d[9]), fmx(d[10], d[11])), fmx(fmx(d[12], d[13]), fmx(d[14], d[15])));
  m0 = xhalf_max(m0) * -L; m1 = xhalf_max(m1) * -L;
  float e[16];
#pragma unroll
  for (int i = 0; i < 8; ++i) { e[i] = __builtin_amdgcn_exp2f(__builtin_fmaf(d[i], L, m0)); e[8 + i] = __builtin_amdgcn_exp2f(__builtin_fmaf(d[8 + i], L, m1)); }
  float s0 = ((e[0] + e[1]) + (e[2] + e[3])) + ((e[4] + e[5]) + (e[6] + e[7]));
  float s1 = ((e[8] + e[9]) + (e[10] + e[11])) + ((e[12] + e[13]) + (e[14] + e[15]));
  s0 = __builtin_amdgcn_rcpf(xhalf_add(s0)); s1 = __builtin_amdgcn_rcpf(xhalf_add(s1));
#pragma unroll
  for (int k = 0; k < 4; ++k) { o0[k] = pk2(e[2 * k] * s0, e[2 * k + 1] * s0); o1[k] = pk2(e[8 + 2 * k] * s1, e[8 + 2 * k + 1] * s1); }
}
DI v4u wphi_frag(int hd, int lane) {
  const int hh = lane >> 5, q = (lane >> 2) & 3, pp = lane & 3;
  const unsigned a = L_WPHI + (unsigned)hd * 512u + (unsigned)(4 * hh + q) * 32u + (unsigned)pp * 8u;
  v4u f = tr2(a, a + 256u);
  const unsigned sg = (lane & 16) ? 0x80008000u : 0u;
  f.x ^= sg; f.y ^= sg; f.z ^= sg; f.w ^= sg;
  return f;
}
DI v4u selv(bool s, v4u a, v4u b) { const unsigned m = s ? 0xffffffffu : 0u; return v4u{(a.x & m) | (b.x & ~m), (a.y & m) | (b.y & ~m), (a.z & m) | (b.z & ~m), (a.w & m) | (b.w & ~m)}; }
DI v4u pack8(const float* y) { return v4u{pk2(y[0], y[1]), pk2(y[2], y[3]), pk2(y[4], y[5]), pk2(y[6], y[7])}; }

template <int NI> DI void warm_w2(const Params& P, int ct, int ja, int tq, int i0, int lane, v4u* d) {
#pragma unroll
  for (int i = 0; i < NI; ++i) {
    const int row = 288 * ja + 72 * tq + 8 * (i0 + i);
    const u64 a = row < 1792 ? (u64)(size_t)P.w2 + (u64)(unsigned)row * 1024u : (u64)(size_t)P.wo + (u64)(unsigned)(row - 1792) * 1024u;
    const u64 base = ((u64)(unsigned)__builtin_amdgcn_readfirstlane((unsigned)(a >> 32)) << 32) | (unsigned)__builtin_amdgcn_readfirstlane((unsigned)a);
    const unsigned voff = (unsigned)(lane >> 3) * 1024u + (unsigned)(lane & 7) * 16u + (unsigned)ct * 128u;
    asm volatile("s_nop 4\n\tglobal_load_dwordx4 %0, %1, %2" : "=&v"(d[i]) : "v"(voff), "s"(base) : "memory");
  }
}
struct Ctx {
  int lane, tq, hh;
  int p, gi, G;
  int rot;
  int tt, ct, ja, jb;
  int rotE, rotL, rotO;
  bool ffn;
};

DI void role_a(const Params& P, const Ctx& c, int wid, u64 amp_a, u64 amp_b) {
  const int lane = c.lane, tq = c.tq, hh = c.hh, t = lane & 31;
  const int cb = (lane >> 4) & 1, q = (lane >> 2) & 3, pp = lane & 3, sw = (q >> 1) & 1;
  const int th = tq & 1, up = tq >> 1;
  unsigned xa[4];
#pragma unroll
  for (int ks = 0; ks < 4; ++ks) xa[ks] = (unsigned)(64 * th + t) * 128u + (unsigned)(((2 * ks + hh) ^ ((t >> 1) & 7)) * 16);
  const unsigned wl = 16384u + (unsigned)(8 * hh + q) * 128u + (unsigned)((4 * (up ^ sw) + 2 * cb + (pp >> 1)) * 16 + 8 * (pp & 1));
  f32x16 acc[2][2];
#pragma unroll
  for (int i = 0; i < 2; ++i) { acc[i][0] = zero16(); acc[i][1] = zero16(); }
  f32x4 cs = {0.f, 0.f, 0.f, 0.f};
  unsigned x0 = xa[0], x1 = xa[1], x2 = xa[2], x3 = xa[3], w0 = wl;
  v4u xf[4][2], wf[4][2];
  auto rd = [&](int ks, v4u* X, v4u* W) AI {
    const unsigned xb = ks == 0 ? x0 : (ks == 1 ? x1 : (ks == 2 ? x2 : x3));
    X[0] = lds128(xb); X[1] = lds128(xb + 4096u);
    const unsigned a = w0 + (unsigned)(ks * 2048);
    W[0] = tr2(a, a + 512u); W[1] = tr2(a + 8192u, a + 8704u);
  };
  f32x4 cs2 = {0.f, 0.f, 0.f, 0.f};
  BAR();
  rd(0, xf[0], wf[0]); rd(1, xf[1], wf[1]);
  auto pass0 = [&](auto TH) AI {
    constexpr int CT = decltype(TH)::value;
    const s16x4 one = {0x3F80, 0x3F80, 0x3F80, 0x3F80};
#pragma unroll 1
    for (int kc = 0; kc < 8; ++kc) {
#pragma unroll
      for (int ks = 0; ks < 4; ++ks) {
        if (ks == 2) {
          __builtin_amdgcn_sched_barrier(0);
          asm volatile("s_barrier" ::: "memory");
          __builtin_amdgcn_sched_barrier(0);
          const unsigned sb = (unsigned)((kc + 1) & 3) * SLOT;
          x0 = xa[0] + sb; x1 = xa[1] + sb; w0 = wl + sb;
          asm volatile("" : "+v"(x0), "+v"(x1), "+v"(w0));
        }
        if (ks == 0) {
          const unsigned sb = (unsigned)(kc & 3) * SLOT;
          x2 = xa[2] + sb; x3 = xa[3] + sb;
          asm volatile("" : "+v"(x2), "+v"(x3));
        }
        rd((ks + 2) & 3, xf[(ks + 2) & 3], wf[(ks + 2) & 3]);
        acc[0][0] = mfma(wf[ks][0], xf[ks][0], acc[0][0]); acc[0][1] = mfma(wf[ks][0], xf[ks][1], acc[0][1]);
        acc[1][0] = mfma(wf[ks][1], xf[ks][0], acc[1][0]); acc[1][1] = mfma(wf[ks][1], xf[ks][1], acc[1][1]);
        cs = __builtin_amdgcn_mfma_f32_4x4x4bf16_1k(one, __builtin_bit_cast(s16x4, v2u{wf[ks][CT].x, wf[ks][CT].y}), cs, 0, 0, 0);
        cs2 = __builtin_amdgcn_mfma_f32_4x4x4bf16_1k(one, __builtin_bit_cast(s16x4, v2u{wf[ks][CT].z, wf[ks][CT].w}), cs2, 0, 0, 0);
        __builtin_amdgcn_sched_group_barrier(0x008, 1, 0);
        __builtin_amdgcn_sched_group_barrier(0x100, 2, 0);
        __builtin_amdgcn_sched_group_barrier(0x008, 1, 0);
        __builtin_amdgcn_sched_group_barrier(0x100, 2, 0);
        __builtin_amdgcn_sched_group_barrier(0x008, 1, 0);
        __builtin_amdgcn_sched_group_barrier(0x100, 2, 0);
        __builtin_amdgcn_sched_group_barrier(0x008, 3, 0);
        __builtin_amdgcn_sched_barrier(0);
      }
      asm volatile("" : "+v"(acc[0][0]), "+v"(acc[0][1]), "+v"(acc[1][0]), "+v"(acc[1][1]), "+v"(cs), "+v"(cs2));
    }
  };
  if (th) pass0(std::integral_constant<int, 1>{}); else pass0(std::integral_constant<int, 0>{});
  cs[0] += cs2[0];
  STAMP(2);
  {
    const float s = xhalf_add(cs[0]);
    if (hh == 0) *(LAS float*)(size_t)(L_CS0 + (unsigned)(up + 2 * th) * 128u + (unsigned)t * 4u) = s;
  }
  BAR();
  const u64 tag = lds_tag();
  float rstd[2], mr[2];
#pragma unroll
  for (int b = 0; b < 2; ++b) {
    const f32x2 st = *(const LAS f32x2*)(size_t)(L_STATS + (unsigned)(64 * th + 32 * b + t) * 8u);
    rstd[b] = st.x; mr[b] = st.y;
  }
  const __amdgpu_buffer_rsrc_t rsHB = __builtin_amdgcn_make_buffer_rsrc(P.ws + WS_HB, 0, HB_BYTES, 0x00020000);
  v4u wm[9];
  if (c.ffn) {
    f32x4 csv[2][4], bsv[2][4];
#pragma unroll
    for (int i = 0; i < 2; ++i) { load16(L_CS0 + (unsigned)(up + 2 * i) * 128u, hh, csv[i]); load16(L_B1 + (unsigned)(up + 2 * i) * 128u, hh, bsv[i]); }
#pragma unroll
    for (int i = 0; i < 2; ++i)
#pragma unroll
      for (int b = 0; b < 2; ++b) {
        const unsigned tile = (unsigned)(up + 2 * i), T32 = (unsigned)(c.p * 4 + 2 * th + b);
        ffn_tile(acc[i][b], rstd[b], mr[b], csv[i], bsv[i], rsHB, (unsigned)lane * 16u + (T32 * 144u + (unsigned)(8 * c.gi) + 2u * tile) * 1024u);
        if (WARM2 && 2 * i + b < 3) warm_w2<3>(P, c.ct, c.ja, tq, 3 * (2 * i + b), lane, wm + 3 * (2 * i + b));
      }
    STAMP(8);
    publish_last(L_CNT + C_EARLY * 4, 4, P.ws, FL_E + (unsigned)(c.p * 8 + c.gi) * 128u, tag, lane);
    if (WARM2) asm volatile("" :: "v"(wm[0]), "v"(wm[1]), "v"(wm[2]), "v"(wm[3]), "v"(wm[4]), "v"(wm[5]), "v"(wm[6]), "v"(wm[7]), "v"(wm[8]));
  } else {
#pragma unroll
    for (int b = 0; b < 2; ++b)
#pragma unroll
      for (int hf = 0; hf < 2; ++hf) {
        const int hd = 2 * up + hf;
        float y[8];
        fold8(acc[0][b], 8 * hf, rstd[b], mr[b], L_CS0 + (unsigned)up * 128u, hh, y);
        v4u o0, o1;
        hedge(mfma(wphi_frag(hd, lane), pack8(y), zero16()), o0, o1);
        const unsigned img = (unsigned)hd * SLOT + 16384u;
        const unsigned row = (unsigned)(64 * th + 32 * b + t);
        const unsigned g = (row >> 1) & 7u;
        *(LAS v2u*)(size_t)(img + row * 64u + (((unsigned)(0 + hh)) ^ g) * 8u) = v2u{o0.x, o0.y};
        *(LAS v2u*)(size_t)(img + row * 64u + (((unsigned)(2 + hh)) ^ g) * 8u) = v2u{o0.z, o0.w};
        *(LAS v2u*)(size_t)(img + row * 64u + (((unsigned)(4 + hh)) ^ g) * 8u) = v2u{o1.x, o1.y};
        *(LAS v2u*)(size_t)(img + row * 64u + (((unsigned)(6 + hh)) ^ g) * 8u) = v2u{o1.z, o1.w};
        fold8(acc[1][b], 8 * hf, rstd[b], mr[b], L_CS0 + (unsigned)(2 + up) * 128u, hh, y);
        const unsigned gv = (row >> 2) & 3u;
        *(LAS v2u*)(size_t)(img + 8192u + row * 32u + (((unsigned)(0 + hh)) ^ gv) * 8u) = v2u{pk2(y[0], y[1]), pk2(y[2], y[3])};
        *(LAS v2u*)(size_t)(img + 8192u + row * 32u + (((unsigned)(2 + hh)) ^ gv) * 8u) = v2u{pk2(y[4], y[5]), pk2(y[6], y[7])};
        if (WARM2 && 2 * b + hf < 3) warm_w2<3>(P, c.ct, c.ja, tq, 3 * (2 * b + hf), lane, wm + 3 * (2 * b + hf));
      }
    STAMP(8);
    asm volatile("s_waitcnt lgkmcnt(0)" ::: "memory");
    lds_inc(L_CNT + C_IMG * 4, lane);
    lds_wait_ge(L_CNT + C_IMG * 4, 4);
    const unsigned img = (unsigned)tq * SLOT + 16384u;
    f32x16 kv = zero16();
    unsigned pa[2], va[2];
#pragma unroll
    for (int i2 = 0; i2 < 2; ++i2) {
      const unsigned r = (unsigned)(8 * hh + 4 * i2 + q);
      pa[i2] = img + r * 64u + (((unsigned)(4 * cb + pp)) ^ ((r >> 1) & 7u)) * 8u;
      va[i2] = img + 8192u + r * 32u + (((unsigned)pp) ^ ((r >> 2) & 3u)) * 8u;
    }
#pragma unroll
    for (int ks = 0; ks < 8; ++ks) {
      const v4u af = tr2(pa[0] + (unsigned)ks * 1024u, pa[1] + (unsigned)ks * 1024u);
      v4u bf = tr2(va[0] + (unsigned)ks * 512u, va[1] + (unsigned)ks * 512u);
      if (cb) bf = v4u{ONES, ONES, ONES, ONES};
      kv = mfma(af, bf, kv);
    }
    asm volatile("" : "+v"(kv) :: "memory");
    lds_inc(L_CNT + C_IMGDONE * 4, lane);
    const unsigned hidx = (unsigned)(c.gi * 4 + tq) * 16u + (unsigned)c.p;
    const __amdgpu_buffer_rsrc_t rsKV = __builtin_amdgcn_make_buffer_rsrc(P.ws + WS_KVP, 0, 524288 + 65536, 0x00020000);
    if (cb == 0) {
#pragma unroll
      for (int s = 0; s < 2; ++s) {
        const v4u o = v4u{pk2(kv[8 * s], kv[8 * s + 1]), pk2(kv[8 * s + 2], kv[8 * s + 3]), pk2(kv[8 * s + 4], kv[8 * s + 5]), pk2(kv[8 * s + 6], kv[8 * s + 7])};
        st128(o, rsKV, (unsigned)((16 * s + (lane & 15) + 32 * hh) * 16) + hidx * 1024u);
      }
    } else if ((lane & 15) == 0) {
#pragma unroll
      for (int g4 = 0; g4 < 4; ++g4) {
        const v4u o = v4u{f2u(kv[4 * g4]), f2u(kv[4 * g4 + 1]), f2u(kv[4 * g4 + 2]), f2u(kv[4 * g4 + 3])};
        st128(o, rsKV, (unsigned)((8 * g4 + 4 * hh) * 4) + 524288u + hidx * 128u);
      }
    }
    publish_last(L_CNT + C_KV * 4, 4, P.ws, FL_K + (unsigned)(c.gi * 16 + c.p) * 128u, tag, lane);
    if (WARM2) asm volatile("" :: "v"(wm[0]), "v"(wm[1]), "v"(wm[2]), "v"(wm[3]), "v"(wm[4]), "v"(wm[5]), "v"(wm[6]), "v"(wm[7]), "v"(wm[8]));
  }
  STAMP(3);
  unsigned tabB, tabWlo, tabWhi;
  {
    const int jj = lane < 36 ? lane : 35;
    constexpr int J2 = ORD ? 24 : 28;
    int sg;
    auto perm = [&](int i, int n) AI {
      if (!ROT2) return i;
      const int b = c.jb;
      if (i == 0) return b;
      const int m = (i - 1) >> 2, r = (i - 1) & 3;
      if (m < n - 1) return r == 0 ? 4 * (m + 1) + b : 4 * m + ((b + r) & 3);
      return 4 * m + ((b + 1 + r) & 3);
    };
    if (jj < 16) sg = 4 * perm(jj, 4) + tq;
    else if ((jj < J2) != (ORD != 0)) sg = 64 + 4 * perm(jj - (ORD ? 24 : 16), 3) + tq;
    else sg = 112 + 4 * perm(jj - (ORD ? 16 : 28), 2) + tq;
    int row;
    if (sg < 64) { const int g = sg >> 3; row = 224 * g + 32 * (g & 1) + 16 * (sg & 7); }
    else if (sg < 112) {
      const int e = sg - 64, g = e / 6, i = e - 6 * g;
      row = i < 4 ? 224 * g + 32 * (g & 1) + 128 + 16 * i : ((g & 1) ? 224 * g : 224 * g + 192) + 16 * (i - 4);
    } else row = 16 * (sg - 112);
    const u64 a = (u64)(size_t)(sg < 112 ? P.w2 : P.wo) + (u64)row * 1024u + (u64)c.ct * 128u;
    tabB = (unsigned)sg * 1024u; tabWlo = (unsigned)a; tabWhi = (unsigned)(a >> 32);
  }
  const unsigned vw0 = (unsigned)(lane >> 3) * 1024u + (unsigned)(((lane & 7) ^ (((lane >> 4) & 1) << 2)) * 16), vw1 = vw0 + 8192u;
  const unsigned ring = (unsigned)tq * 8192u;
  auto pofs = [&](int r) AI { return (unsigned)((r >> 2) & 3) * SLOT + (unsigned)(r & 3) * 2048u; };
  auto wdma = [&](int j) AI {
    const unsigned lo = __builtin_amdgcn_readlane(tabWlo, j), hi = __builtin_amdgcn_readlane(tabWhi, j);
    const u64 b = ((u64)hi << 32) | lo;
    const unsigned m = ring + pofs(j);
    asm volatile("s_nop 4\n\ts_mov_b32 m0, %0\n\ts_nop 0\n\tglobal_load_lds_dwordx4 %1, %3\n\ts_add_u32 m0, m0, 0x400\n\ts_nop 0\n\tglobal_load_lds_dwordx4 %2, %3"
                 ::"s"(m), "v"(vw0), "v"(vw1), "s"(b) : "memory", "scc");
  };
  const unsigned vb0 = (unsigned)lane * 16u + (unsigned)(2 * c.tt) * 144u * 1024u, vb1 = vb0 + 144u * 1024u;
  constexpr int NB = 4;
  v4u br[NB][2];
#pragma unroll
  for (int u = 0; u < NB; ++u) { br[u][0] = v4u{0u, 0u, 0u, 0u}; br[u][1] = br[u][0]; }
  auto bload = [&](unsigned so, v4u& d0, v4u& d1) AI {
    asm volatile("s_nop 4\n\tbuffer_load_dwordx4 %0, %2, %4, %5 offen sc1\n\tbuffer_load_dwordx4 %1, %3, %4, %5 offen sc1"
                 : "+v"(d0), "+v"(d1) : "v"(vb0), "v"(vb1), "s"(rsHB), "s"(so) : "memory");
  };
  if (!c.ffn && tq >= 2) lds_wait_ge(L_CNT + C_IMGDONE * 4, 4);
#pragma unroll 1
  for (int s = 0; s < 3; ++s) {
    lds_wait_ge(L_CNT + (C_FREE + s) * 4, 4);
#pragma unroll 1
    for (int j = 4 * s; j < (s < 2 ? 4 * s + 4 : 10); ++j) wdma(j);
  }
  STAMP(4);
  const int pan = c.tt >> 1;
  poll_flags(P.ws, FL_E + (unsigned)pan * 8u * 128u, 8, tag, lane);
  if (KVFIRST && !c.ffn) lds_wait_ge(L_CNT + C_KVIN * 4, 4);
  STAMP(5);
  bload(__builtin_amdgcn_readlane(tabB, 0), br[0][0], br[0][1]);
  bload(__builtin_amdgcn_readlane(tabB, 1), br[1][0], br[1][1]);
  wdma(10);
  bload(__builtin_amdgcn_readlane(tabB, 2), br[2][0], br[2][1]);
  wdma(11);
  bload(__builtin_amdgcn_readlane(tabB, 3), br[3][0], br[3][1]);
  unsigned w2l[2];
#pragma unroll
  for (int h = 0; h < 2; ++h) w2l[h] = ring + (unsigned)(4 * hh + q) * 128u + (unsigned)((4 * (h ^ sw) + 2 * cb + (pp >> 1)) * 16 + 8 * (pp & 1));
  f32x16 o[2][2];
#pragma unroll
  for (int a = 0; a < 2; ++a)
#pragma unroll
    for (int b = 0; b < 2; ++b) o[a][b] = zero16();
  v4u af[2];
  asm volatile("s_waitcnt vmcnt(10)" : "+v"(br[0][0]), "+v"(br[0][1]) :: "memory");
  af[0] = tr2(w2l[0], w2l[0] + 1024u); af[1] = tr2(w2l[1], w2l[1] + 1024u);
  auto step = [&](int j, int u, bool refill, bool first) AI {
    v4u an[2];
    if (first) asm volatile("s_waitcnt vmcnt(" STR(W1) ")" : "+v"(br[(u + 1) % NB][0]), "+v"(br[(u + 1) % NB][1]) :: "memory");
    else asm volatile("s_waitcnt vmcnt(" STR(W2) ")" : "+v"(br[(u + 1) % NB][0]), "+v"(br[(u + 1) % NB][1]) :: "memory");
    unsigned p0 = w2l[0] + pofs(j + 1), p1 = w2l[1] + pofs(j + 1);
    asm volatile("" : "+v"(p0), "+v"(p1));
    an[0] = tr2(p0, p0 + 1024u); an[1] = tr2(p1, p1 + 1024u);
    o[0][0] = mfma(af[0], br[u][0], o[0][0]); o[0][1] = mfma(af[0], br[u][1], o[0][1]);
    o[1][0] = mfma(af[1], br[u][0], o[1][0]); o[1][1] = mfma(af[1], br[u][1], o[1][1]);
    asm volatile("" : "+v"(o[0][0]), "+v"(o[0][1]), "+v"(o[1][0]), "+v"(o[1][1]) :: "memory");
    if (refill) wdma(j + 12);
    const int jn = j + NB;
    if (u == 0) {
      if (jn == 16) lds_wait_ge(L_CNT + (ORD ? C_ORDY : C_LATERDY) * 4, 1);
      if (jn == (ORD ? 24 : 28)) lds_wait_ge(L_CNT + (ORD ? C_LATERDY : C_ORDY) * 4, 1);
    }
    bload(jn < 36 ? (unsigned)__builtin_amdgcn_readlane(tabB, jn) : 0x7F000000u, br[u][0], br[u][1]);
    af[0] = an[0]; af[1] = an[1];
  };
  lds_wait_ge(L_CNT + (C_FREE + 3) * 4, 4);
#pragma unroll 1
  for (int it = 0; it < 6; ++it) {
    if (it == 3) STAMP(10);
#pragma unroll
    for (int u = 0; u < NB; ++u) step(NB * it + u, u, true, true);
  }
  STAMP(11);
#pragma unroll 1
  for (int it = 6; it < 9; ++it) {
#pragma unroll
    for (int u = 0; u < NB; ++u) step(NB * it + u, u, false, false);
  }
  asm volatile("s_waitcnt vmcnt(0)" : "+v"(br[0][0]), "+v"(br[0][1]), "+v"(br[1][0]), "+v"(br[1][1]), "+v"(br[2][0]), "+v"(br[2][1]), "+v"(br[3][0]), "+v"(br[3][1]) :: "memory");
  STAMP(6);
#pragma unroll
  for (int a = 0; a < 2; ++a)
#pragma unroll
    for (int b = 0; b < 2; ++b)
#pragma unroll
      for (int g4 = 0; g4 < 4; ++g4)
        *(LAS f32x4*)(size_t)(ring + (unsigned)b * SLOT + (unsigned)t * 256u + (unsigned)((8 * a + 2 * g4 + hh) ^ (t & 15)) * 16u) =
            f32x4{o[a][b][4 * g4], o[a][b][4 * g4 + 1], o[a][b][4 * g4 + 2], o[a][b][4 * g4 + 3]};
  VMW(0);
  BAR();
  STAMP(7);
}

template <int NT, int NF, class Fill> DI void pass1(const Params& P, int wid, u64& amp_a, u64& amp_b, Fill& fill, const v4u (&xr)[32], f32x16 (&acc)[NT], f32x4& cs, const unsigned (&wb)[NT], int lane) {
  const s16x4 one = {0x3F80, 0x3F80, 0x3F80, 0x3F80};
  f32x4 csa = {0.f, 0.f, 0.f, 0.f}, csb = csa;
#pragma unroll
  for (int qq = 0; qq < 4; ++qq) {
    if (qq > 0) lds_wait_ge(L_CNT + (C_LAND + qq) * 4, 4);
    if (qq < 2) fill(10 + qq);
    unsigned a[NT];
#pragma unroll
    for (int i = 0; i < NT; ++i) { a[i] = wb[i] + (unsigned)qq * SLOT; asm volatile("" : "+v"(a[i])); }
    v4u f[3][NT];
    auto rd = [&](int k8, v4u* W) AI {
      const unsigned o = (unsigned)((k8 >> 2) * 8192 + (k8 & 3) * 2048);
#pragma unroll
      for (int i = 0; i < NT; ++i) W[i] = tr2(a[i] + o, a[i] + o + 512u);
    };
    rd(0, f[0]); rd(1, f[1]);
    __builtin_amdgcn_sched_barrier(0);
#pragma unroll
    for (int k8 = 0; k8 < 8; ++k8) {
      if (k8 == 4 && qq < 3) {
        if (qq < 2) { if (NF == 8) VMW(8); else VMW(4); } else VMW(0);
        lds_inc(L_CNT + (C_LAND + qq + 1) * 4, lane);
      }
      if (k8 < 6) rd(k8 + 2, f[(k8 + 2) % 3]);
      const v4u xf = xr[8 * qq + k8];
#pragma unroll
      for (int i = 0; i < NT; ++i) acc[i] = mfma(f[k8 % 3][i], xf, acc[i]);
      csa = __builtin_amdgcn_mfma_f32_4x4x4bf16_1k(one, __builtin_bit_cast(s16x4, v2u{f[k8 % 3][0].x, f[k8 % 3][0].y}), csa, 0, 0, 0);
      csb = __builtin_amdgcn_mfma_f32_4x4x4bf16_1k(one, __builtin_bit_cast(s16x4, v2u{f[k8 % 3][0].z, f[k8 % 3][0].w}), csb, 0, 0, 0);
      if (k8 < 6) {
#pragma unroll
        for (int i = 0; i < NT; ++i) {
          __builtin_amdgcn_sched_group_barrier(0x008, 1, 0);
          __builtin_amdgcn_sched_group_barrier(0x100, 2, 0);
        }
        __builtin_amdgcn_sched_group_barrier(0x008, 2, 0);
      }
      __builtin_amdgcn_sched_barrier(0);
    }
    asm volatile("" : "+v"(acc[0]), "+v"(acc[1]), "+v"(acc[NT - 1]), "+v"(csa), "+v"(csb) :: "memory");
    lds_inc(L_CNT + (C_FREE + qq) * 4, lane);
  }
  cs[0] = csa[0] + csb[0];
}

template <bool ffn> DI u64 role_b_head(const Params& P, const Ctx& c, int wid) {
  u64 amp_a = 0, amp_b = 0; (void)amp_a; (void)amp_b;
  const int lane = c.lane, tq = c.tq, hh = c.hh, t = lane & 31;
  const int g = c.gi;
  const unsigned tok = (unsigned)(c.p * 128 + 8 * c.G + 2 * tq);
  v4u r0, r1;
  {
    const u16* rp = P.x + (size_t)tok * 512 + lane * 8;
    asm volatile("global_load_dwordx4 %0, %2, off\n\tglobal_load_dwordx4 %1, %2, off offset:1024" : "=&v"(r0), "=&v"(r1) : "v"(rp) : "memory");
  }
  const __amdgpu_buffer_rsrc_t rsX = __builtin_amdgcn_make_buffer_rsrc((void*)P.x, 0, 2048 * 1024, 0x00020000);
  const __amdgpu_buffer_rsrc_t rsW = __builtin_amdgcn_make_buffer_rsrc((void*)(ffn ? P.w1 : P.wqkv), 0, ffn ? 512 * 3584 : 512 * 3072, 0x00020000);
  constexpr unsigned RS = ffn ? 3584u : 3072u;
  const unsigned cA = (unsigned)(224 * g + 32 * (g & 1));
  const unsigned u0 = ffn ? cA * 2u : (unsigned)(512 + 64 * g) * 2u, u1 = ffn ? cA * 2u + 128u : (unsigned)(1024 + 64 * g) * 2u;
  const unsigned ua = ffn ? (cA + 128u) * 2u : (unsigned)(64 * g) * 2u;
  const unsigned ub = (unsigned)((g & 1) ? 224 * g - 32 : 224 * g + 192) * 2u;
  const unsigned voffX = (unsigned)(lane >> 3) * 1024u + (unsigned)(((lane & 7) ^ ((4 * (tq & 1) + (lane >> 4)) & 7)) * 16);
  const unsigned voffW = (unsigned)(lane >> 3) * RS + (unsigned)(((lane & 7) ^ (((lane >> 4) & 1) << 2)) * 16);
  const unsigned soffX = (unsigned)(c.p * 128 + 8 * tq) * 1024u;
  const unsigned wrow = 32u * (unsigned)(tq & 1) * RS;
  auto fill = [&](int f) AI {
    const unsigned slot = (unsigned)(f & 3) * SLOT;
    if (f < 8) {
      const unsigned kcp = (unsigned)((f + c.rot) & 7);
      unsigned sx = soffX + kcp * 128u, sw = 64u * kcp * RS + wrow + ((tq >> 1) ? u1 : u0);
      unsigned mx = slot + (unsigned)tq * 1024u, mw = slot + 16384u + (unsigned)tq * 4096u;
#pragma unroll 1
      for (int m = 0; m < 4; ++m) {
        dma(mx, voffX, rsX, sx); dma(mw, voffW, rsW, sw);
        mx += 4096u; sx += 32768u; mw += 1024u; sw += 8u * RS;
      }
    } else {
      const unsigned kcp = (unsigned)((2 * (f - 8) + (tq >> 1) + c.rot) & 7);
      unsigned sw = 64u * kcp * RS + wrow, mw = slot + (unsigned)tq * 4096u;
#pragma unroll 1
      for (int m = 0; m < 4; ++m) {
        dma(mw, voffW, rsW, sw + ua);
        if (ffn) dma(mw + 16384u, voffW, rsW, sw + ub);
        mw += 1024u; sw += 8u * RS;
      }
    }
  };
  fill(0);
  float st0;
  {
    asm volatile("s_waitcnt vmcnt(8)" : "+v"(r0), "+v"(r1) :: "memory");
    float v[2][8], mean[2], rs[2];
#pragma unroll
    for (int k = 0; k < 4; ++k) {
      v[0][2 * k] = u2f(r0[k] << 16); v[0][2 * k + 1] = u2f(r0[k] & 0xffff0000u);
      v[1][2 * k] = u2f(r1[k] << 16); v[1][2 * k + 1] = u2f(r1[k] & 0xffff0000u);
    }
#pragma unroll
    for (int i = 0; i < 2; ++i) mean[i] = wavesum(((v[i][0] + v[i][1]) + (v[i][2] + v[i][3])) + ((v[i][4] + v[i][5]) + (v[i][6] + v[i][7]))) * (1.0f / 512.0f);
#pragma unroll
    for (int i = 0; i < 2; ++i) {
      float q2 = 0.f;
#pragma unroll
      for (int k = 0; k < 8; ++k) { const float d = v[i][k] - mean[i]; q2 = __builtin_fmaf(d, d, q2); }
      rs[i] = __builtin_amdgcn_rsqf(wavesum(q2) * (1.0f / 512.0f) + 1e-5f);
    }
    st0 = (lane & 2) ? rs[1] : rs[0];
    st0 = (lane & 1) ? -((lane & 2) ? mean[1] : mean[0]) * st0 : st0;
    asm volatile("" : "+v"(st0));
  }
  fill(1);
  unsigned xa[4];
#pragma unroll
  for (int ks = 0; ks < 4; ++ks) xa[ks] = (unsigned)(32 * tq + t) * 128u + (unsigned)(((2 * ks + hh) ^ ((t >> 1) & 7)) * 16);
  v4u xr[32];
  u64 tag = 0;
  v4u gr;
  const unsigned token = (unsigned)(c.p * 128 + 32 * tq + t);
#pragma unroll
  for (int kc = 0; kc < 8; ++kc) {
    if (kc < 7 || ffn) VMW(8); else VMW(4);
    BAR();
    if (kc == 0) {
      tag = lds_tag();
      if (lane < 4)
        __hip_atomic_store((GAS u64*)(P.ws + WS_ST + tok * 16u + (unsigned)lane * 8u), ((u64)(unsigned)tag << 32) | f2u(st0), __ATOMIC_RELAXED, __HIP_MEMORY_SCOPE_AGENT);
    }
    if (kc == 5) {
      const char* gp = P.ws + WS_ST + token * 16u;
      asm volatile("global_load_dwordx4 %0, %1, off sc1" : "=&v"(gr) : "v"(gp) : "memory");
    }
    fill(kc + 2);
    const unsigned sb = (unsigned)(kc & 3) * SLOT;
#pragma unroll
    for (int ks = 0; ks < 4; ++ks) xr[4 * kc + ks] = lds128(xa[ks] + sb);
    asm volatile("" : "+v"(xr[4 * kc]), "+v"(xr[4 * kc + 1]), "+v"(xr[4 * kc + 2]), "+v"(xr[4 * kc + 3]));
  }
  asm volatile("" : "+v"(gr));
  STAMP(2);
  BAR();
  if (__builtin_amdgcn_ballot_w64(gr.y != (unsigned)tag || gr.w != (unsigned)tag) != 0) {
    const __amdgpu_buffer_rsrc_t rs = __builtin_amdgcn_make_buffer_rsrc((void*)(P.ws + WS_ST), 0, 32768, 0x00020000);
#pragma unroll 1
    for (unsigned spin = 0; spin < kSpin; ++spin) {
      gr = __builtin_amdgcn_raw_buffer_load_b128(rs, token * 16u, 0, 16);
      if (__builtin_amdgcn_ballot_w64(gr.y != (unsigned)tag || gr.w != (unsigned)tag) == 0) break;
      __builtin_amdgcn_s_sleep(1);
    }
  }
  const float rstd = u2f(gr.x), mr = u2f(gr.z);
  if (hh == 0) *(LAS f32x2*)(size_t)(L_STATS + (unsigned)(32 * tq + t) * 8u) = f32x2{rstd, mr};
  if (ffn) VMW(8); else VMW(4);
  BAR();
  const int cb = (lane >> 4) & 1, q = (lane >> 2) & 3, pp = lane & 3, sw = (q >> 1) & 1;
  unsigned wl[2];
#pragma unroll
  for (int h = 0; h < 2; ++h) wl[h] = (unsigned)(8 * hh + q) * 128u + (unsigned)((4 * (h ^ sw) + 2 * cb + (pp >> 1)) * 16 + 8 * (pp & 1));
  const __amdgpu_buffer_rsrc_t rsHB = __builtin_amdgcn_make_buffer_rsrc(P.ws + WS_HB, 0, HB_BYTES, 0x00020000);
  const unsigned T32 = (unsigned)(c.p * 4 + tq);
  f32x4 cs = {0.f, 0.f, 0.f, 0.f};
  auto cs_sync = [&](int tile, bool wr) AI {
    const float s = xhalf_add(cs[0]);
    if (hh == 0 && wr) *(LAS float*)(size_t)(L_CS1 + (unsigned)tile * 128u + (unsigned)t * 4u) = s;
    asm volatile("s_waitcnt lgkmcnt(0)" ::: "memory");
    lds_inc(L_CNT + C_CS1 * 4, lane);
    lds_wait_ge(L_CNT + C_CS1 * 4, 4);
  };
  if constexpr (ffn) {
    const unsigned w2a = ((g & 1) ? wl[1] : wl[0]) + 16384u;
    int ti[3];
    unsigned wb[3];
    f32x16 acc[3];
#pragma unroll
    for (int i = 0; i < 3; ++i) { ti[i] = (i + tq) % 3; wb[i] = ti[i] == 0 ? wl[0] : (ti[i] == 1 ? wl[1] : w2a); acc[i] = zero16(); }
    pass1<3, 8>(P, wid, amp_a, amp_b, fill, xr, acc, cs, wb, lane);
    cs_sync(ti[0], tq < 3);
    STAMP(3);
    if (PRIOL) __builtin_amdgcn_s_setprio(3);
    f32x4 csv[3][4], bsv[3][4];
#pragma unroll
    for (int i = 0; i < 3; ++i) { load16(L_CS1 + (unsigned)ti[i] * 128u, hh, csv[i]); load16(L_B1 + (unsigned)(4 + ti[i]) * 128u, hh, bsv[i]); }
#pragma unroll
    for (int i = 0; i < 3; ++i)
      ffn_tile(acc[i], rstd, mr, csv[i], bsv[i], rsHB, (unsigned)lane * 16u + (T32 * 144u + (unsigned)(64 + 6 * g + 2 * ti[i])) * 1024u);
    publish_last(L_CNT + C_LATE * 4, 4, P.ws, FL_L + (unsigned)(c.p * 8 + g) * 128u, tag, lane);
    if (PRIOL) __builtin_amdgcn_s_setprio(0);
  } else {
    int ti[2];
    unsigned wb[2];
    f32x16 acc[2];
#pragma unroll
    for (int i = 0; i < 2; ++i) { ti[i] = (i + tq) & 1; wb[i] = ti[i] == 0 ? wl[0] : wl[1]; acc[i] = zero16(); }
    pass1<2, 4>(P, wid, amp_a, amp_b, fill, xr, acc, cs, wb, lane);
    cs_sync(ti[0], tq < 2);
    STAMP(3);
    v4u pq[4][2];
#pragma unroll
    for (int hd = 0; hd < 4; ++hd) {
      float y[8];
      fold8(acc[hd >> 1], 8 * (hd & 1), rstd, mr, L_CS1 + (unsigned)ti[hd >> 1] * 128u, hh, y);
      hedge(mfma(wphi_frag(2 * ti[hd >> 1] + (hd & 1), lane), pack8(y), zero16()), pq[hd][0], pq[hd][1]);
    }
    asm volatile("" : "+v"(pq[0][0]), "+v"(pq[0][1]), "+v"(pq[1][0]), "+v"(pq[1][1]), "+v"(pq[2][0]), "+v"(pq[2][1]), "+v"(pq[3][0]), "+v"(pq[3][1]));
    STAMP(4);
    if (PRIO) __builtin_amdgcn_s_setprio(3);
    if (tq == 0) { poll_flags(P.ws, FL_K + (unsigned)g * 16u * 128u, 16, tag, lane); lds_set(L_CNT + C_KVRDY * 4, 1); }
    else lds_wait_ge(L_CNT + C_KVRDY * 4, 1);
    STAMP(5);
    {
      const __amdgpu_buffer_rsrc_t rsKV = __builtin_amdgcn_make_buffer_rsrc(P.ws + WS_KVP, 0, 524288 + 65536, 0x00020000);
      const unsigned hbase = (unsigned)(g * 4 + tq) * 16u;
      v4u pv[16];
#pragma unroll
      for (int i = 0; i < 16; ++i) pv[i] = __builtin_amdgcn_raw_buffer_load_b128(rsKV, (unsigned)lane * 16u, (hbase + (unsigned)i) * 1024u, 16);
      const v4u z0 = __builtin_amdgcn_raw_buffer_load_b128(rsKV, (unsigned)lane * 16u, 524288u + hbase * 128u, 16);
      const v4u z1 = __builtin_amdgcn_raw_buffer_load_b128(rsKV, (unsigned)lane * 16u, 524288u + hbase * 128u + 1024u, 16);
      const u64 lf = __hip_atomic_load((const GAS u64*)(P.ws + FL_L + (unsigned)(c.tt >> 1) * 8u * 128u + (unsigned)(lane & 7) * 128u), __ATOMIC_RELAXED, __HIP_MEMORY_SCOPE_AGENT);
      __builtin_amdgcn_sched_barrier(0);
      v4u idf;
      {
        const int m = lane & 31, h2 = lane >> 5;
        const int j = (m < 16 && ((m >> 2) & 1) == h2) ? (((m >> 3) << 2) | (m & 3)) : -1;
#pragma unroll
        for (int k = 0; k < 4; ++k) idf[k] = (j == 2 * k ? 0x3F80u : 0u) | (j == 2 * k + 1 ? 0x3F800000u : 0u);
      }
      f32x16 sum = zero16();
#pragma unroll
      for (int i = 0; i < 16; ++i) sum = mfma(idf, pv[i], sum);
      asm volatile("" : "+v"(sum));
      lds_inc(L_CNT + C_KVIN * 4, lane);
      if (!ORD && tq == 0 && __builtin_amdgcn_ballot_w64(lf != tag) == 0) lds_set(L_CNT + C_LATERDY * 4, 1);
      const float s[8] = {sum[0], sum[1], sum[2], sum[3], sum[4], sum[5], sum[6], sum[7]};
      const unsigned afr = L_AFR + (unsigned)tq * 2048u;
      *(LAS v4u*)(size_t)(afr + (unsigned)((lane >> 4) & 1) * 1024u + (unsigned)((lane & 15) + 32 * (lane >> 5)) * 16u) = pack8(s);
      const unsigned hh2 = (unsigned)(lane >> 4) & 1u, c16 = (unsigned)lane & 15u;
      float zz[4];
#pragma unroll
      for (int k = 0; k < 4; ++k) {
        float v = dppadd<0x128>(u2f(z0[k]) + u2f(z1[k]));
        auto r = __builtin_amdgcn_permlane16_swap(f2u(v), f2u(v), false, false);
        zz[k] = xhalf_add(u2f(r[0]) + u2f(r[1]));
      }
      if (lane < 8) *(LAS f32x4*)(size_t)(L_ZT + (unsigned)tq * 128u + (unsigned)lane * 16u) = f32x4{zz[0], zz[1], zz[2], zz[3]};
      const unsigned f0 = 16u * (unsigned)(lane >> 5) + 4u * hh2;
      const f32x4 za = lds128f(L_ZT + (unsigned)tq * 128u + f0 * 4u), zb = lds128f(L_ZT + (unsigned)tq * 128u + (f0 + 8u) * 4u);
      *(LAS v4u*)(size_t)(afr + (unsigned)(lane >> 5) * 1024u + (16u + c16 + 32u * hh2) * 16u) = v4u{pk2(za[0], za[1]), pk2(za[2], za[3]), pk2(zb[0], zb[1]), pk2(zb[2], zb[3])};
      asm volatile("s_waitcnt lgkmcnt(0)" ::: "memory");
      lds_inc(L_CNT + C_AFR * 4, lane);
      lds_wait_ge(L_CNT + C_AFR * 4, 4);
    }
#pragma unroll
    for (int hd = 0; hd < 4; ++hd) {
      const int head = 2 * ti[hd >> 1] + (hd & 1);
      f32x16 d = mfma(lds128(L_AFR + (unsigned)head * 2048u + (unsigned)lane * 16u), pq[hd][0], zero16());
      d = mfma(lds128(L_AFR + (unsigned)head * 2048u + 1024u + (unsigned)lane * 16u), pq[hd][1], d);
      const float r = __builtin_amdgcn_rcpf(d[8] + 1e-6f);
      const v4u o = v4u{pk2(d[0] * r, d[1] * r), pk2(d[2] * r, d[3] * r), pk2(d[4] * r, d[5] * r), pk2(d[6] * r, d[7] * r)};
      st128(o, rsHB, (unsigned)lane * 16u + (T32 * 144u + (unsigned)(112 + 4 * g + head)) * 1024u);
    }
    publish_last(L_CNT + C_O * 4, 4, P.ws, FL_O + (unsigned)(c.p * 8 + g) * 128u, tag, lane);
    if (PRIO) __builtin_amdgcn_s_setprio(0);
  }
  STAMP(6);
  return tag;
}

DI void role_b(const Params& P, const Ctx& c, int wid, u64 amp_a0) {
  u64 amp_a = amp_a0, amp_b = 0; (void)amp_a; (void)amp_b;
  const int lane = c.lane, tq = c.tq;
  const u64 tag = c.ffn ? role_b_head<true>(P, c, wid) : role_b_head<false>(P, c, wid);
  const int pan = c.tt >> 1;
  const int tb = tq * 64 + lane;
  v4u xres[2], bres;
#pragma unroll
  for (int m = 0; m < 2; ++m) {
    const int idx = tb + 256 * m;
    xres[m] = *(const GAS v4u*)(P.x + (size_t)(64 * c.tt + (idx >> 3)) * 512 + 64 * c.ct + 8 * (idx & 7));
  }
  bres = *(const GAS v4u*)(P.b2 + 64 * c.ct + 8 * (tb & 7));
  if (tq == 0) {
    poll_flags(P.ws, (ORD ? FL_O : FL_L) + (unsigned)pan * 8u * 128u, 8, tag, lane);
    lds_set(L_CNT + (ORD ? C_ORDY : C_LATERDY) * 4, 1);
    poll_flags(P.ws, (ORD ? FL_L : FL_O) + (unsigned)pan * 8u * 128u, 8, tag, lane);
    lds_set(L_CNT + (ORD ? C_LATERDY : C_ORDY) * 4, 1);
  } else if (tq == 1 && __builtin_amdgcn_workgroup_id_x() == 0) {
    poll_flags(P.ws, FL_E, 64, tag, lane); poll_flags(P.ws, FL_E + 64 * 128, 64, tag, lane);
    poll_flags(P.ws, FL_K, 64, tag, lane); poll_flags(P.ws, FL_K + 64 * 128, 64, tag, lane);
    if (lane == 0) __hip_atomic_store((GAS u64*)(P.ws + WS_EPOCH), tag, __ATOMIC_RELAXED, __HIP_MEMORY_SCOPE_AGENT);
  }
  BAR();
#pragma unroll
  for (int m = 0; m < 2; ++m) {
    const int idx = tb + 256 * m;
    const unsigned tok = (unsigned)(idx >> 3), cg = (unsigned)(idx & 7);
    const unsigned a = (tok >> 5) * SLOT + (tok & 31u) * 256u, c0 = ((2u * cg) ^ (tok & 15u)) * 16u, c1 = ((2u * cg + 1u) ^ (tok & 15u)) * 16u;
    f32x4 lo = lds128f(a + c0), hi = lds128f(a + c1);
#pragma unroll
    for (unsigned w = 1; w < 4; ++w) { lo += lds128f(w * 8192u + a + c0); hi += lds128f(w * 8192u + a + c1); }
    v4u o;
#pragma unroll
    for (int k = 0; k < 4; ++k) {
      const float a0 = k < 2 ? lo[2 * k] : hi[2 * k - 4], a1 = k < 2 ? lo[2 * k + 1] : hi[2 * k - 3];
      const float x0 = u2f(xres[m][k] << 16), x1 = u2f(xres[m][k] & 0xffff0000u);
      const float b0 = u2f(bres[k] << 16), b1 = u2f(bres[k] & 0xffff0000u);
      o[k] = pk2(a0 + x0 + b0, a1 + x1 + b1);
    }
    *(GAS v4u*)(P.out + (size_t)(64 * c.tt + (idx >> 3)) * 512 + 64 * c.ct + 8 * (idx & 7)) = o;
  }
  VMW(0);
  if (wid == 4 && lane == 0) __hip_atomic_store((GAS u64*)(P.ws + (24u << 20) + __builtin_amdgcn_workgroup_id_x() * 8u), (u64)__builtin_amdgcn_s_memrealtime(), __ATOMIC_RELAXED, __HIP_MEMORY_SCOPE_AGENT);
  STAMP(7);

}

__global__ __launch_bounds__(512, 2) NOPK void fused_kernel(Params P) {
  __shared__ __attribute__((aligned(1024))) char smem[LDS_BYTES];
  asm volatile("" ::"v"((unsigned)(size_t)(LAS char*)smem) : "memory");
  u64 pc;
  asm volatile("s_getpc_b64 %0" : "=s"(pc));
  const int tid = __builtin_amdgcn_workitem_id_x(), lane = tid & 63;
  const int wid = __builtin_amdgcn_readfirstlane(tid >> 6);
  u64 amp_a = 0, amp_b = 0;
  STAMP(0);
  Ctx c;
  c.lane = lane; c.tq = wid & 3; c.hh = lane >> 5;
  const int blk = __builtin_amdgcn_workgroup_id_x(), xcd = blk & 7, j = blk >> 3, ja = j & 7, jb = j >> 3;
  c.p = (xcd & 1) * 8 + ja;
  c.G = (xcd >> 1) * 4 + jb;
  c.ffn = !(c.G & 1);
  c.gi = c.G >> 1;
  c.rot = ROT1 == 0 ? 0 : (ROT1 == 1 ? (((ja + jb) % 3) * 3) & 7 : (ROT1 == 2 ? (ja + 2 * jb) & 7 : ((ja + jb) & 1) * 4));
  c.tt = (xcd >> 1) * 8 + ja; c.ct = (xcd & 1) * 4 + jb; c.ja = ja; c.jb = jb;
  c.rotE = ROT2 ? (2 * ja + 4 * jb) & 15 : 0; c.rotL = ROT2 ? (ja + 3 * jb) % 12 : 0; c.rotO = ROT2 ? (ja + 2 * jb) & 7 : 0;
  if (wid < 4) {
    const unsigned kb0 = (unsigned)(j + 32 * (wid & 1)), kb = kb0 < CODE_KB ? kb0 : 0u;
    const unsigned pf = CODEPF ? *(const GAS unsigned*)((pc & ~255ull) + kb * 1024u + (unsigned)lane * 16u) : 0u;
    const u64 e = __hip_atomic_load((const GAS u64*)(P.ws + WS_EPOCH), __ATOMIC_RELAXED, __HIP_MEMORY_SCOPE_AGENT);
    if (c.ffn) {
      const int tc = tid < 224 ? tid : 0;
      const int t7 = tc >> 5, n = tc & 31, g = c.gi;
      const int cA = 224 * g + 32 * (g & 1);
      const int col = t7 < 4 ? cA + 32 * t7 : (t7 < 6 ? cA + 128 + 32 * (t7 - 4) : ((g & 1) ? 224 * g : 224 * g + 192));
      const unsigned bv = P.b1[col + n];
      *(LAS float*)(size_t)(L_B1 + (unsigned)tid * 4u) = u2f(bv << 16);
    } else {
      const v4u wv = *(const GAS v4u*)(P.wphi + (size_t)c.gi * 1024 + (tid & 127) * 8);
      *(LAS v4u*)(size_t)(L_WPHI + (unsigned)(tid & 127) * 16u) = wv;
    }
    if (tid < 32) *(LAS unsigned*)(size_t)(L_CNT + (unsigned)tid * 4u) = 0u;
    const u64 tag = e + 1u;
    if (tid == 0) *(LAS v2u*)(size_t)L_TAG = v2u{(unsigned)tag, (unsigned)(tag >> 32)};
    asm volatile("" ::"v"(pf));
    STAMP(1);
    role_a(P, c, wid, amp_a, amp_b);
  } else {
    STAMP(1);
    role_b(P, c, wid, amp_a);
  }
}

}

__global__ void k_t1(char* ws) {
  unsigned long long t0 = __builtin_amdgcn_s_memrealtime();
  unsigned long long m = 0;
  for (int i = 0; i < 256; ++i) { unsigned long long e = __hip_atomic_load((const __attribute__((address_space(1))) unsigned long long*)(ws + (24u << 20) + i * 8u), __ATOMIC_RELAXED, __HIP_MEMORY_SCOPE_AGENT); if (e > m) m = e; }
  unsigned long long d = (t0 - m) * AMPX2, t1 = __builtin_amdgcn_s_memrealtime();
  while (__builtin_amdgcn_s_memrealtime() - t1 < d) __builtin_amdgcn_s_sleep(4);
}
extern "C" void kernel_launch(void* const* d_in, const int* in_sizes, int n_in, void* d_out, int out_size, void* d_ws, size_t ws_size,
                              hipStream_t stream) {
  Params P;
  P.x = (const u16*)d_in[0]; P.wqkv = (const u16*)d_in[1]; P.wphi = (const u16*)d_in[2]; P.wo = (const u16*)d_in[3];
  P.w1 = (const u16*)d_in[5]; P.b1 = (const u16*)d_in[6]; P.w2 = (const u16*)d_in[7]; P.b2 = (const u16*)d_in[8];
  P.out = (u16*)d_out; P.ws = (char*)d_ws;
  fused_kernel<<<256, 512, 0, stream>>>(P);
  k_t1<<<1, 64, 0, stream>>>((char*)d_ws);
}
```

```cpp
#include <hip/hip_runtime.h>
#include <stdint.h>
#include <type_traits>

namespace {

typedef unsigned short u16;
typedef unsigned long long u64;
typedef unsigned v4u __attribute__((ext_vector_type(4)));
typedef unsigned v2u __attribute__((ext_vector_type(2)));
typedef float f32x16 __attribute__((ext_vector_type(16)));
typedef float f32x4 __attribute__((ext_vector_type(4)));
typedef float f32x2 __attribute__((ext_vector_type(2)));
typedef __bf16 bf16x8 __attribute__((ext_vector_type(8)));
typedef __bf16 bf16x2 __attribute__((ext_vector_type(2)));
typedef short s16x4 __attribute__((ext_vector_type(4)));
#define LAS __attribute__((address_space(3)))
#define GAS __attribute__((address_space(1)))
#if defined(__HIP_DEVICE_COMPILE__)
#define NOPK __attribute__((target("no-packed-fp32-ops")))
#else
#define NOPK
#endif
#define DI __device__ __forceinline__ NOPK
#define AI __attribute__((always_inline)) NOPK

#define STAMPS 0
#define ROT1 4
#define ROT2 0
#define STR2(x) #x
#define STR(x) STR2(x)
#define W1 8
#define W2 4
#define PRIO 1
#define SLP 1
#define KVFIRST 0
#define PRIOL 0
#define ORD 0
#define CODEPF 1
#define WARM2 1
#define CODE_KB 36

constexpr unsigned WS_EPOCH = 0;
constexpr unsigned FL_E = 4096;
constexpr unsigned FL_L = FL_E + 128 * 128;
constexpr unsigned FL_K = FL_L + 128 * 128;
constexpr unsigned FL_O = FL_K + 128 * 128;
constexpr unsigned WS_ST = 73728;
constexpr unsigned WS_KVP = 131072;
constexpr unsigned WS_ZP = WS_KVP + 524288;
constexpr unsigned WS_HB = 1048576;
constexpr unsigned HB_BYTES = 64u * 144u * 1024u;
constexpr unsigned WS_STAMP = 16u << 20;

constexpr unsigned SLOT = 32768;
constexpr unsigned L_MISC = 131072;
constexpr unsigned L_STATS = L_MISC;
constexpr unsigned L_CS0 = L_MISC + 1024;
constexpr unsigned L_CS1 = L_MISC + 1536;
constexpr unsigned L_B1 = L_MISC + 2048;
constexpr unsigned L_WPHI = L_MISC + 3072;
constexpr unsigned L_CNT = L_MISC + 5120;
constexpr unsigned L_TAG = L_MISC + 5248;
constexpr unsigned L_ZT = L_MISC + 5376;
constexpr unsigned L_AFR = L_MISC + 8192;
constexpr unsigned LDS_BYTES = 163840;
enum { C_CS1 = 0, C_LATE = 1, C_KV = 2, C_O = 3, C_AFR = 4, C_KVRDY = 5, C_LATERDY = 6, C_ORDY = 7, C_FREE = 8  , C_LAND = 12  , C_EARLY = 16, C_IMG = 17, C_KVIN = 18, C_IMGDONE = 19 };

constexpr unsigned kSpin = 1u << 16;

struct Params {
  const u16* x; const u16* wqkv; const u16* wphi; const u16* wo;
  const u16* w1; const u16* b1; const u16* w2; const u16* b2;
  u16* out; char* ws;
};

DI unsigned f2u(float f) { return __builtin_bit_cast(unsigned, f); }
DI float u2f(unsigned u) { return __builtin_bit_cast(float, u); }
DI float fmx(float a, float b) { return __builtin_fmaxf(a, b); }
DI f32x16 mfma(v4u a, v4u b, f32x16 c) {
  return __builtin_amdgcn_mfma_f32_32x32x16_bf16(__builtin_bit_cast(bf16x8, a), __builtin_bit_cast(bf16x8, b), c, 0, 0, 0);
}
DI f32x16 zero16() { f32x16 z; for (int r = 0; r < 16; ++r) z[r] = 0.f; return z; }
DI v4u lds128(unsigned a) { return *(const LAS v4u*)(size_t)a; }
DI f32x4 lds128f(unsigned a) { return *(const LAS f32x4*)(size_t)a; }
DI v2u tr64(unsigned a) { return __builtin_bit_cast(v2u, __builtin_amdgcn_ds_read_tr16_b64_v4i16((LAS s16x4*)(size_t)a)); }
DI v4u tr2(unsigned a0, unsigned a1) { v2u l = tr64(a0), h = tr64(a1); return v4u{l.x, l.y, h.x, h.y}; }
DI unsigned pk2(float a, float b) { return __builtin_bit_cast(unsigned, __builtin_convertvector(f32x2{a, b}, bf16x2)); }
DI float xhalf_add(float v) { auto r = __builtin_amdgcn_permlane32_swap(f2u(v), f2u(v), false, false); return u2f(r[0]) + u2f(r[1]); }
DI float xhalf_max(float v) { auto r = __builtin_amdgcn_permlane32_swap(f2u(v), f2u(v), false, false); return fmx(u2f(r[0]), u2f(r[1])); }
DI float shx(float v, int m, int lane) { return u2f((unsigned)__builtin_amdgcn_ds_bpermute((lane ^ m) << 2, (int)f2u(v))); }
DI f32x4 lanesum(v4u f, f32x4 c) {
  const s16x4 one = {0x3F80, 0x3F80, 0x3F80, 0x3F80};
  c = __builtin_amdgcn_mfma_f32_4x4x4bf16_1k(one, __builtin_bit_cast(s16x4, v2u{f.x, f.y}), c, 0, 0, 0);
  return __builtin_amdgcn_mfma_f32_4x4x4bf16_1k(one, __builtin_bit_cast(s16x4, v2u{f.z, f.w}), c, 0, 0, 0);
}
template <int CTRL> DI float dppadd(float v) { return v + u2f((unsigned)__builtin_amdgcn_update_dpp(0, (int)f2u(v), CTRL, 0xf, 0xf, true)); }
DI float wavesum(float v) {
  v = dppadd<0xB1>(v); v = dppadd<0x4E>(v); v = dppadd<0x141>(v); v = dppadd<0x140>(v);
  auto r = __builtin_amdgcn_permlane16_swap(f2u(v), f2u(v), false, false);
  return xhalf_add(u2f(r[0]) + u2f(r[1]));
}
DI void dma(unsigned m0v, unsigned voff, __amdgpu_buffer_rsrc_t rs, unsigned soff) {
  asm volatile("s_mov_b32 m0, %0\n\ts_nop 0\n\tbuffer_load_dwordx4 %1, %2, %3 offen lds" ::"s"(m0v), "v"(voff), "s"(rs), "s"(soff) : "memory");
}
DI void st128(v4u v, __amdgpu_buffer_rsrc_t rs, unsigned voff) { __builtin_amdgcn_raw_buffer_store_b128(v, rs, voff, 0, 16); }
#define BAR() do { __builtin_amdgcn_sched_barrier(0); asm volatile("s_waitcnt lgkmcnt(0)\n\ts_barrier" ::: "memory"); __builtin_amdgcn_sched_barrier(0); } while (0)
#define VMW(n) asm volatile("s_waitcnt vmcnt(" #n ")" ::: "memory")
constexpr unsigned ONES = 0x3F803F80u;

#define STAMP(i) do { } while (0)

DI void poll_flags(const char* ws, unsigned base, int n, u64 tag, int lane) {
  const GAS u64* f = (const GAS u64*)(ws + base + (unsigned)(lane < n ? lane : 0) * 128u);
#pragma unroll 1
  for (unsigned spin = 0; spin < kSpin; ++spin) {
    const u64 v = __hip_atomic_load(f, __ATOMIC_RELAXED, __HIP_MEMORY_SCOPE_AGENT);
    if (__builtin_amdgcn_ballot_w64(v != tag) == 0) break;
    __builtin_amdgcn_s_sleep(1);
  }
  __builtin_amdgcn_fence(__ATOMIC_ACQUIRE, "wavefront");
  asm volatile("" ::: "memory");
}
DI void lds_wait_ge(unsigned addr, unsigned want) {
#pragma unroll 1
  for (unsigned spin = 0; spin < (kSpin << 4); ++spin) {
    const unsigned v = *(volatile LAS unsigned*)(size_t)addr;
    if ((unsigned)__builtin_amdgcn_readfirstlane(v) >= want) break;
    if (SLP) __builtin_amdgcn_s_sleep(SLP);
  }
  asm volatile("" ::: "memory");
}
DI void lds_set(unsigned addr, unsigned v) { *(volatile LAS unsigned*)(size_t)addr = v; }
DI void lds_inc(unsigned addr, int lane) { if (lane == 0) __hip_atomic_fetch_add((LAS unsigned*)(size_t)addr, 1u, __ATOMIC_RELAXED, __HIP_MEMORY_SCOPE_WORKGROUP); }
DI void publish_last(unsigned cnt_addr, unsigned n, char* ws, unsigned flag_off, u64 tag, int lane) {
  VMW(0);
  if (lane == 0) {
    const unsigned old = __hip_atomic_fetch_add((LAS unsigned*)(size_t)cnt_addr, 1u, __ATOMIC_RELAXED, __HIP_MEMORY_SCOPE_WORKGROUP);
    if (old == n - 1) __hip_atomic_store((GAS u64*)(ws + flag_off), tag, __ATOMIC_RELAXED, __HIP_MEMORY_SCOPE_AGENT);
  }
}
DI u64 lds_tag() { const v2u t = *(const LAS v2u*)(size_t)L_TAG; return ((u64)(unsigned)__builtin_amdgcn_readfirstlane(t.y) << 32) | (unsigned)__builtin_amdgcn_readfirstlane(t.x); }

DI void fold8(const f32x16& acc, int r0, float rstd, float mr, unsigned cs, int hh, float* y) {
#pragma unroll
  for (int g = 0; g < 2; ++g) {
    const f32x4 t = lds128f(cs + (unsigned)((r0 >> 2) + g) * 32u + (unsigned)hh * 16u) * mr;
#pragma unroll
    for (int k = 0; k < 4; ++k) y[4 * g + k] = __builtin_fmaf(acc[r0 + 4 * g + k], rstd, t[k]);
  }
}
DI void load16(unsigned a, int hh, f32x4* v) {
#pragma unroll
  for (int g = 0; g < 4; ++g) v[g] = lds128f(a + (unsigned)g * 32u + (unsigned)hh * 16u);
}
DI void ffn_tile(const f32x16& acc, float rstd, float mr, const f32x4* cs, const f32x4* bs, __amdgpu_buffer_rsrc_t rsHB, unsigned voff) {
  f32x2 y[8], d[8];
#pragma unroll
  for (int g = 0; g < 4; ++g) {
    const f32x4 t = cs[g] * mr + bs[g];
    y[2 * g] = f32x2{acc[4 * g], acc[4 * g + 1]} * rstd + f32x2{t[0], t[1]};
    y[2 * g + 1] = f32x2{acc[4 * g + 2], acc[4 * g + 3]} * rstd + f32x2{t[2], t[3]};
  }
#pragma unroll
  for (int k = 0; k < 8; ++k) d[k] = y[k] * ((y[k] * y[k]) * (-0.10294324f) + (-2.30220819f));
#pragma unroll
  for (int k = 0; k < 8; ++k) { d[k].x = __builtin_amdgcn_exp2f(d[k].x); d[k].y = __builtin_amdgcn_exp2f(d[k].y); }
#pragma unroll
  for (int k = 0; k < 8; ++k) d[k] += 1.0f;
#pragma unroll
  for (int k = 0; k < 8; ++k) { d[k].x = __builtin_amdgcn_rcpf(d[k].x); d[k].y = __builtin_amdgcn_rcpf(d[k].y); }
  v4u o0, o1;
#pragma unroll
  for (int k = 0; k < 4; ++k) {
    const f32x2 g0 = y[k] * d[k], g1 = y[4 + k] * d[4 + k];
    o0[k] = pk2(g0.x, g0.y); o1[k] = pk2(g1.x, g1.y);
  }
  st128(o0, rsHB, voff); st128(o1, rsHB, voff + 1024u);
}
DI void hedge(const f32x16& d, v4u& o0, v4u& o1) {
  constexpr float L = 1.4426950408889634f;
  float m0 = fmx(fmx(fmx(d[0], d[1]), fmx(d[2], d[3])), fmx(fmx(d[4], d[5]), fmx(d[6], d[7])));
  float m1 = fmx(fmx(fmx(d[8], d[9]), fmx(d[10], d[11])), fmx(fmx(d[12], d[13]), fmx(d[14], d[15])));
  m0 = xhalf_max(m0) * -L; m1 = xhalf_max(m1) * -L;
  float e[16];
#pragma unroll
  for (int i = 0; i < 8; ++i) { e[i] = __builtin_amdgcn_exp2f(__builtin_fmaf(d[i], L, m0)); e[8 + i] = __builtin_amdgcn_exp2f(__builtin_fmaf(d[8 + i], L, m1)); }
  float s0 = ((e[0] + e[1]) + (e[2] + e[3])) + ((e[4] + e[5]) + (e[6] + e[7]));
  float s1 = ((e[8] + e[9]) + (e[10] + e[11])) + ((e[12] + e[13]) + (e[14] + e[15]));
  s0 = __builtin_amdgcn_rcpf(xhalf_add(s0)); s1 = __builtin_amdgcn_rcpf(xhalf_add(s1));
#pragma unroll
  for (int k = 0; k < 4; ++k) { o0[k] = pk2(e[2 * k] * s0, e[2 * k + 1] * s0); o1[k] = pk2(e[8 + 2 * k] * s1, e[8 + 2 * k + 1] * s1); }
}
DI v4u wphi_frag(int hd, int lane) {
  const int hh = lane >> 5, q = (lane >> 2) & 3, pp = lane & 3;
  const unsigned a = L_WPHI + (unsigned)hd * 512u + (unsigned)(4 * hh + q) * 32u + (unsigned)pp * 8u;
  v4u f = tr2(a, a + 256u);
  const unsigned sg = (lane & 16) ? 0x80008000u : 0u;
  f.x ^= sg; f.y ^= sg; f.z ^= sg; f.w ^= sg;
  return f;
}
DI v4u selv(bool s, v4u a, v4u b) { const unsigned m = s ? 0xffffffffu : 0u; return v4u{(a.x & m) | (b.x & ~m), (a.y & m) | (b.y & ~m), (a.z & m) | (b.z & ~m), (a.w & m) | (b.w & ~m)}; }
DI v4u pack8(const float* y) { return v4u{pk2(y[0], y[1]), pk2(y[2], y[3]), pk2(y[4], y[5]), pk2(y[6], y[7])}; }

template <int NI> DI void warm_w2(const Params& P, int ct, int ja, int tq, int i0, int lane, v4u* d) {
#pragma unroll
  for (int i = 0; i < NI; ++i) {
    const int row = 288 * ja + 72 * tq + 8 * (i0 + i);
    const u64 a = row < 1792 ? (u64)(size_t)P.w2 + (u64)(unsigned)row * 1024u : (u64)(size_t)P.wo + (u64)(unsigned)(row - 1792) * 1024u;
    const u64 base = ((u64)(unsigned)__builtin_amdgcn_readfirstlane((unsigned)(a >> 32)) << 32) | (unsigned)__builtin_amdgcn_readfirstlane((unsigned)a);
    const unsigned voff = (unsigned)(lane >> 3) * 1024u + (unsigned)(lane & 7) * 16u + (unsigned)ct * 128u;
    asm volatile("s_nop 4\n\tglobal_load_dwordx4 %0, %1, %2" : "=&v"(d[i]) : "v"(voff), "s"(base) : "memory");
  }
}
struct Ctx {
  int lane, tq, hh;
  int p, gi, G;
  int rot;
  int tt, ct, ja, jb;
  int rotE, rotL, rotO;
  bool ffn;
};

DI void role_a(const Params& P, const Ctx& c, int wid, u64 amp_a, u64 amp_b) {
  const int lane = c.lane, tq = c.tq, hh = c.hh, t = lane & 31;
  const int cb = (lane >> 4) & 1, q = (lane >> 2) & 3, pp = lane & 3, sw = (q >> 1) & 1;
  const int th = tq & 1, up = tq >> 1;
  unsigned xa[4];
#pragma unroll
  for (int ks = 0; ks < 4; ++ks) xa[ks] = (unsigned)(64 * th + t) * 128u + (unsigned)(((2 * ks + hh) ^ ((t >> 1) & 7)) * 16);
  const unsigned wl = 16384u + (unsigned)(8 * hh + q) * 128u + (unsigned)((4 * (up ^ sw) + 2 * cb + (pp >> 1)) * 16 + 8 * (pp & 1));
  f32x16 acc[2][2];
#pragma unroll
  for (int i = 0; i < 2; ++i) { acc[i][0] = zero16(); acc[i][1] = zero16(); }
  f32x4 cs = {0.f, 0.f, 0.f, 0.f};
  unsigned x0 = xa[0], x1 = xa[1], x2 = xa[2], x3 = xa[3], w0 = wl;
  v4u xf[4][2], wf[4][2];
  auto rd = [&](int ks, v4u* X, v4u* W) AI {
    const unsigned xb = ks == 0 ? x0 : (ks == 1 ? x1 : (ks == 2 ? x2 : x3));
    X[0] = lds128(xb); X[1] = lds128(xb + 4096u);
    const unsigned a = w0 + (unsigned)(ks * 2048);
    W[0] = tr2(a, a + 512u); W[1] = tr2(a + 8192u, a + 8704u);
  };
  f32x4 cs2 = {0.f, 0.f, 0.f, 0.f};
  BAR();
  rd(0, xf[0], wf[0]); rd(1, xf[1], wf[1]);
  auto pass0 = [&](auto TH) AI {
    constexpr int CT = decltype(TH)::value;
    const s16x4 one = {0x3F80, 0x3F80, 0x3F80, 0x3F80};
#pragma unroll 1
    for (int kc = 0; kc < 8; ++kc) {
#pragma unroll
      for (int ks = 0; ks < 4; ++ks) {
        if (ks == 2) {
          __builtin_amdgcn_sched_barrier(0);
          asm volatile("s_barrier" ::: "memory");
          __builtin_amdgcn_sched_barrier(0);
          const unsigned sb = (unsigned)((kc + 1) & 3) * SLOT;
          x0 = xa[0] + sb; x1 = xa[1] + sb; w0 = wl + sb;
          asm volatile("" : "+v"(x0), "+v"(x1), "+v"(w0));
        }
        if (ks == 0) {
          const unsigned sb = (unsigned)(kc & 3) * SLOT;
          x2 = xa[2] + sb; x3 = xa[3] + sb;
          asm volatile("" : "+v"(x2), "+v"(x3));
        }
        rd((ks + 2) & 3, xf[(ks + 2) & 3], wf[(ks + 2) & 3]);
        acc[0][0] = mfma(wf[ks][0], xf[ks][0], acc[0][0]); acc[0][1] = mfma(wf[ks][0], xf[ks][1], acc[0][1]);
        acc[1][0] = mfma(wf[ks][1], xf[ks][0], acc[1][0]); acc[1][1] = mfma(wf[ks][1], xf[ks][1], acc[1][1]);
        cs = __builtin_amdgcn_mfma_f32_4x4x4bf16_1k(one, __builtin_bit_cast(s16x4, v2u{wf[ks][CT].x, wf[ks][CT].y}), cs, 0, 0, 0);
        cs2 = __builtin_amdgcn_mfma_f32_4x4x4bf16_1k(one, __builtin_bit_cast(s16x4, v2u{wf[ks][CT].z, wf[ks][CT].w}), cs2, 0, 0, 0);
        __builtin_amdgcn_sched_group_barrier(0x008, 1, 0);
        __builtin_amdgcn_sched_group_barrier(0x100, 2, 0);
        __builtin_amdgcn_sched_group_barrier(0x008, 1, 0);
        __builtin_amdgcn_sched_group_barrier(0x100, 2, 0);
        __builtin_amdgcn_sched_group_barrier(0x008, 1, 0);
        __builtin_amdgcn_sched_group_barrier(0x100, 2, 0);
        __builtin_amdgcn_sched_group_barrier(0x008, 3, 0);
        __builtin_amdgcn_sched_barrier(0);
      }
      asm volatile("" : "+v"(acc[0][0]), "+v"(acc[0][1]), "+v"(acc[1][0]), "+v"(acc[1][1]), "+v"(cs), "+v"(cs2));
    }
  };
  if (th) pass0(std::integral_constant<int, 1>{}); else pass0(std::integral_constant<int, 0>{});
  cs[0] += cs2[0];
  STAMP(2);
  {
    const float s = xhalf_add(cs[0]);
    if (hh == 0) *(LAS float*)(size_t)(L_CS0 + (unsigned)(up + 2 * th) * 128u + (unsigned)t * 4u) = s;
  }
  BAR();
  const u64 tag = lds_tag();
  float rstd[2], mr[2];
#pragma unroll
  for (int b = 0; b < 2; ++b) {
    const f32x2 st = *(const LAS f32x2*)(size_t)(L_STATS + (unsigned)(64 * th + 32 * b + t) * 8u);
    rstd[b] = st.x; mr[b] = st.y;
  }
  const __amdgpu_buffer_rsrc_t rsHB = __builtin_amdgcn_make_buffer_rsrc(P.ws + WS_HB, 0, HB_BYTES, 0x00020000);
  v4u wm[9];
  if (c.ffn) {
    f32x4 csv[2][4], bsv[2][4];
#pragma unroll
    for (int i = 0; i < 2; ++i) { load16(L_CS0 + (unsigned)(up + 2 * i) * 128u, hh, csv[i]); load16(L_B1 + (unsigned)(up + 2 * i) * 128u, hh, bsv[i]); }
#pragma unroll
    for (int i = 0; i < 2; ++i)
#pragma unroll
      for (int b = 0; b < 2; ++b) {
        const unsigned tile = (unsigned)(up + 2 * i), T32 = (unsigned)(c.p * 4 + 2 * th + b);
        ffn_tile(acc[i][b], rstd[b], mr[b], csv[i], bsv[i], rsHB, (unsigned)lane * 16u + (T32 * 144u + (unsigned)(8 * c.gi) + 2u * tile) * 1024u);
        if (WARM2 && 2 * i + b < 3) warm_w2<3>(P, c.ct, c.ja, tq, 3 * (2 * i + b), lane, wm + 3 * (2 * i + b));
      }
    STAMP(8);
    publish_last(L_CNT + C_EARLY * 4, 4, P.ws, FL_E + (unsigned)(c.p * 8 + c.gi) * 128u, tag, lane);
    if (WARM2) asm volatile("" :: "v"(wm[0]), "v"(wm[1]), "v"(wm[2]), "v"(wm[3]), "v"(wm[4]), "v"(wm[5]), "v"(wm[6]), "v"(wm[7]), "v"(wm[8]));
  } else {
#pragma unroll
    for (int b = 0; b < 2; ++b)
#pragma unroll
      for (int hf = 0; hf < 2; ++hf) {
        const int hd = 2 * up + hf;
        float y[8];
        fold8(acc[0][b], 8 * hf, rstd[b], mr[b], L_CS0 + (unsigned)up * 128u, hh, y);
        v4u o0, o1;
        hedge(mfma(wphi_frag(hd, lane), pack8(y), zero16()), o0, o1);
        const unsigned img = (unsigned)hd * SLOT + 16384u;
        const unsigned row = (unsigned)(64 * th + 32 * b + t);
        const unsigned g = (row >> 1) & 7u;
        *(LAS v2u*)(size_t)(img + row * 64u + (((unsigned)(0 + hh)) ^ g) * 8u) = v2u{o0.x, o0.y};
        *(LAS v2u*)(size_t)(img + row * 64u + (((unsigned)(2 + hh)) ^ g) * 8u) = v2u{o0.z, o0.w};
        *(LAS v2u*)(size_t)(img + row * 64u + (((unsigned)(4 + hh)) ^ g) * 8u) = v2u{o1.x, o1.y};
        *(LAS v2u*)(size_t)(img + row * 64u + (((unsigned)(6 + hh)) ^ g) * 8u) = v2u{o1.z, o1.w};
        fold8(acc[1][b], 8 * hf, rstd[b], mr[b], L_CS0 + (unsigned)(2 + up) * 128u, hh, y);
        const unsigned gv = (row >> 2) & 3u;
        *(LAS v2u*)(size_t)(img + 8192u + row * 32u + (((unsigned)(0 + hh)) ^ gv) * 8u) = v2u{pk2(y[0], y[1]), pk2(y[2], y[3])};
        *(LAS v2u*)(size_t)(img + 8192u + row * 32u + (((unsigned)(2 + hh)) ^ gv) * 8u) = v2u{pk2(y[4], y[5]), pk2(y[6], y[7])};
        if (WARM2 && 2 * b + hf < 3) warm_w2<3>(P, c.ct, c.ja, tq, 3 * (2 * b + hf), lane, wm + 3 * (2 * b + hf));
      }
    STAMP(8);
    asm volatile("s_waitcnt lgkmcnt(0)" ::: "memory");
    lds_inc(L_CNT + C_IMG * 4, lane);
    lds_wait_ge(L_CNT + C_IMG * 4, 4);
    const unsigned img = (unsigned)tq * SLOT + 16384u;
    f32x16 kv = zero16();
    unsigned pa[2], va[2];
#pragma unroll
    for (int i2 = 0; i2 < 2; ++i2) {
      const unsigned r = (unsigned)(8 * hh + 4 * i2 + q);
      pa[i2] = img + r * 64u + (((unsigned)(4 * cb + pp)) ^ ((r >> 1) & 7u)) * 8u;
      va[i2] = img + 8192u + r * 32u + (((unsigned)pp) ^ ((r >> 2) & 3u)) * 8u;
    }
#pragma unroll
    for (int ks = 0; ks < 8; ++ks) {
      const v4u af = tr2(pa[0] + (unsigned)ks * 1024u, pa[1] + (unsigned)ks * 1024u);
      v4u bf = tr2(va[0] + (unsigned)ks * 512u, va[1] + (unsigned)ks * 512u);
      if (cb) bf = v4u{ONES, ONES, ONES, ONES};
      kv = mfma(af, bf, kv);
    }
    asm volatile("" : "+v"(kv) :: "memory");
    lds_inc(L_CNT + C_IMGDONE * 4, lane);
    const unsigned hidx = (unsigned)(c.gi * 4 + tq) * 16u + (unsigned)c.p;
    const __amdgpu_buffer_rsrc_t rsKV = __builtin_amdgcn_make_buffer_rsrc(P.ws + WS_KVP, 0, 524288 + 65536, 0x00020000);
    if (cb == 0) {
#pragma unroll
      for (int s = 0; s < 2; ++s) {
        const v4u o = v4u{pk2(kv[8 * s], kv[8 * s + 1]), pk2(kv[8 * s + 2], kv[8 * s + 3]), pk2(kv[8 * s + 4], kv[8 * s + 5]), pk2(kv[8 * s + 6], kv[8 * s + 7])};
        st128(o, rsKV, (unsigned)((16 * s + (lane & 15) + 32 * hh) * 16) + hidx * 1024u);
      }
    } else if ((lane & 15) == 0) {
#pragma unroll
      for (int g4 = 0; g4 < 4; ++g4) {
        const v4u o = v4u{f2u(kv[4 * g4]), f2u(kv[4 * g4 + 1]), f2u(kv[4 * g4 + 2]), f2u(kv[4 * g4 + 3])};
        st128(o, rsKV, (unsigned)((8 * g4 + 4 * hh) * 4) + 524288u + hidx * 128u);
      }
    }
    publish_last(L_CNT + C_KV * 4, 4, P.ws, FL_K + (unsigned)(c.gi * 16 + c.p) * 128u, tag, lane);
    if (WARM2) asm volatile("" :: "v"(wm[0]), "v"(wm[1]), "v"(wm[2]), "v"(wm[3]), "v"(wm[4]), "v"(wm[5]), "v"(wm[6]), "v"(wm[7]), "v"(wm[8]));
  }
  STAMP(3);
  unsigned tabB, tabWlo, tabWhi;
  {
    const int jj = lane < 36 ? lane : 35;
    constexpr int J2 = ORD ? 24 : 28;
    int sg;
    auto perm = [&](int i, int n) AI {
      if (!ROT2) return i;
      const int b = c.jb;
      if (i == 0) return b;
      const int m = (i - 1) >> 2, r = (i - 1) & 3;
      if (m < n - 1) return r == 0 ? 4 * (m + 1) + b : 4 * m + ((b + r) & 3);
      return 4 * m + ((b + 1 + r) & 3);
    };
    if (jj < 16) sg = 4 * perm(jj, 4) + tq;
    else if ((jj < J2) != (ORD != 0)) sg = 64 + 4 * perm(jj - (ORD ? 24 : 16), 3) + tq;
    else sg = 112 + 4 * perm(jj - (ORD ? 16 : 28), 2) + tq;
    int row;
    if (sg < 64) { const int g = sg >> 3; row = 224 * g + 32 * (g & 1) + 16 * (sg & 7); }
    else if (sg < 112) {
      const int e = sg - 64, g = e / 6, i = e - 6 * g;
      row = i < 4 ? 224 * g + 32 * (g & 1) + 128 + 16 * i : ((g & 1) ? 224 * g : 224 * g + 192) + 16 * (i - 4);
    } else row = 16 * (sg - 112);
    const u64 a = (u64)(size_t)(sg < 112 ? P.w2 : P.wo) + (u64)row * 1024u + (u64)c.ct * 128u;
    tabB = (unsigned)sg * 1024u; tabWlo = (unsigned)a; tabWhi = (unsigned)(a >> 32);
  }
  const unsigned vw0 = (unsigned)(lane >> 3) * 1024u + (unsigned)(((lane & 7) ^ (((lane >> 4) & 1) << 2)) * 16), vw1 = vw0 + 8192u;
  const unsigned ring = (unsigned)tq * 8192u;
  auto pofs = [&](int r) AI { return (unsigned)((r >> 2) & 3) * SLOT + (unsigned)(r & 3) * 2048u; };
  auto wdma = [&](int j) AI {
    const unsigned lo = __builtin_amdgcn_readlane(tabWlo, j), hi = __builtin_amdgcn_readlane(tabWhi, j);
    const u64 b = ((u64)hi << 32) | lo;
    const unsigned m = ring + pofs(j);
    asm volatile("s_nop 4\n\ts_mov_b32 m0, %0\n\ts_nop 0\n\tglobal_load_lds_dwordx4 %1, %3\n\ts_add_u32 m0, m0, 0x400\n\ts_nop 0\n\tglobal_load_lds_dwordx4 %2, %3"
                 ::"s"(m), "v"(vw0), "v"(vw1), "s"(b) : "memory", "scc");
  };
  const unsigned vb0 = (unsigned)lane * 16u + (unsigned)(2 * c.tt) * 144u * 1024u, vb1 = vb0 + 144u * 1024u;
  constexpr int NB = 4;
  v4u br[NB][2];
#pragma unroll
  for (int u = 0; u < NB; ++u) { br[u][0] = v4u{0u, 0u, 0u, 0u}; br[u][1] = br[u][0]; }
  auto bload = [&](unsigned so, v4u& d0, v4u& d1) AI {
    asm volatile("s_nop 4\n\tbuffer_load_dwordx4 %0, %2, %4, %5 offen sc1\n\tbuffer_load_dwordx4 %1, %3, %4, %5 offen sc1"
                 : "+v"(d0), "+v"(d1) : "v"(vb0), "v"(vb1), "s"(rsHB), "s"(so) : "memory");
  };
  if (!c.ffn && tq >= 2) lds_wait_ge(L_CNT + C_IMGDONE * 4, 4);
#pragma unroll 1
  for (int s = 0; s < 3; ++s) {
    lds_wait_ge(L_CNT + (C_FREE + s) * 4, 4);
#pragma unroll 1
    for (int j = 4 * s; j < (s < 2 ? 4 * s + 4 : 10); ++j) wdma(j);
  }
  STAMP(4);
  const int pan = c.tt >> 1;
  poll_flags(P.ws, FL_E + (unsigned)pan * 8u * 128u, 8, tag, lane);
  if (KVFIRST && !c.ffn) lds_wait_ge(L_CNT + C_KVIN * 4, 4);
  STAMP(5);
  bload(__builtin_amdgcn_readlane(tabB, 0), br[0][0], br[0][1]);
  bload(__builtin_amdgcn_readlane(tabB, 1), br[1][0], br[1][1]);
  wdma(10);
  bload(__builtin_amdgcn_readlane(tabB, 2), br[2][0], br[2][1]);
  wdma(11);
  bload(__builtin_amdgcn_readlane(tabB, 3), br[3][0], br[3][1]);
  unsigned w2l[2];
#pragma unroll
  for (int h = 0; h < 2; ++h) w2l[h] = ring + (unsigned)(4 * hh + q) * 128u + (unsigned)((4 * (h ^ sw) + 2 * cb + (pp >> 1)) * 16 + 8 * (pp & 1));
  f32x16 o[2][2];
#pragma unroll
  for (int a = 0; a < 2; ++a)
#pragma unroll
    for (int b = 0; b < 2; ++b) o[a][b] = zero16();
  v4u af[2];
  asm volatile("s_waitcnt vmcnt(10)" : "+v"(br[0][0]), "+v"(br[0][1]) :: "memory");
  af[0] = tr2(w2l[0], w2l[0] + 1024u); af[1] = tr2(w2l[1], w2l[1] + 1024u);
  auto step = [&](int j, int u, bool refill, bool first) AI {
    v4u an[2];
    if (first) asm volatile("s_waitcnt vmcnt(" STR(W1) ")" : "+v"(br[(u + 1) % NB][0]), "+v"(br[(u + 1) % NB][1]) :: "memory");
    else asm volatile("s_waitcnt vmcnt(" STR(W2) ")" : "+v"(br[(u + 1) % NB][0]), "+v"(br[(u + 1) % NB][1]) :: "memory");
    unsigned p0 = w2l[0] + pofs(j + 1), p1 = w2l[1] + pofs(j + 1);
    asm volatile("" : "+v"(p0), "+v"(p1));
    an[0] = tr2(p0, p0 + 1024u); an[1] = tr2(p1, p1 + 1024u);
    o[0][0] = mfma(af[0], br[u][0], o[0][0]); o[0][1] = mfma(af[0], br[u][1], o[0][1]);
    o[1][0] = mfma(af[1], br[u][0], o[1][0]); o[1][1] = mfma(af[1], br[u][1], o[1][1]);
    asm volatile("" : "+v"(o[0][0]), "+v"(o[0][1]), "+v"(o[1][0]), "+v"(o[1][1]) :: "memory");
    if (refill) wdma(j + 12);
    const int jn = j + NB;
    if (u == 0) {
      if (jn == 16) lds_wait_ge(L_CNT + (ORD ? C_ORDY : C_LATERDY) * 4, 1);
      if (jn == (ORD ? 24 : 28)) lds_wait_ge(L_CNT + (ORD ? C_LATERDY : C_ORDY) * 4, 1);
    }
    bload(jn < 36 ? (unsigned)__builtin_amdgcn_readlane(tabB, jn) : 0x7F000000u, br[u][0], br[u][1]);
    af[0] = an[0]; af[1] = an[1];
  };
  lds_wait_ge(L_CNT + (C_FREE + 3) * 4, 4);
#pragma unroll 1
  for (int it = 0; it < 6; ++it) {
    if (it == 3) STAMP(10);
#pragma unroll
    for (int u = 0; u < NB; ++u) step(NB * it + u, u, true, true);
  }
  STAMP(11);
#pragma unroll 1
  for (int it = 6; it < 9; ++it) {
#pragma unroll
    for (int u = 0; u < NB; ++u) step(NB * it + u, u, false, false);
  }
  asm volatile("s_waitcnt vmcnt(0)" : "+v"(br[0][0]), "+v"(br[0][1]), "+v"(br[1][0]), "+v"(br[1][1]), "+v"(br[2][0]), "+v"(br[2][1]), "+v"(br[3][0]), "+v"(br[3][1]) :: "memory");
  STAMP(6);
#pragma unroll
  for (int a = 0; a < 2; ++a)
#pragma unroll
    for (int b = 0; b < 2; ++b)
#pragma unroll
      for (int g4 = 0; g4 < 4; ++g4)
        *(LAS f32x4*)(size_t)(ring + (unsigned)b * SLOT + (unsigned)t * 256u + (unsigned)((8 * a + 2 * g4 + hh) ^ (t & 15)) * 16u) =
            f32x4{o[a][b][4 * g4], o[a][b][4 * g4 + 1], o[a][b][4 * g4 + 2], o[a][b][4 * g4 + 3]};
  VMW(0);
  BAR();
  STAMP(7);
}

template <int NT, int NF, class Fill> DI void pass1(const Params& P, int wid, u64& amp_a, u64& amp_b, Fill& fill, const v4u (&xr)[32], f32x16 (&acc)[NT], f32x4& cs, const unsigned (&wb)[NT], int lane) {
  const s16x4 one = {0x3F80, 0x3F80, 0x3F80, 0x3F80};
  f32x4 csa = {0.f, 0.f, 0.f, 0.f}, csb = csa;
#pragma unroll
  for (int qq = 0; qq < 4; ++qq) {
    if (qq > 0) lds_wait_ge(L_CNT + (C_LAND + qq) * 4, 4);
    if (qq < 2) fill(10 + qq);
    unsigned a[NT];
#pragma unroll
    for (int i = 0; i < NT; ++i) { a[i] = wb[i] + (unsigned)qq * SLOT; asm volatile("" : "+v"(a[i])); }
    v4u f[3][NT];
    auto rd = [&](int k8, v4u* W) AI {
      const unsigned o = (unsigned)((k8 >> 2) * 8192 + (k8 & 3) * 2048);
#pragma unroll
      for (int i = 0; i < NT; ++i) W[i] = tr2(a[i] + o, a[i] + o + 512u);
    };
    rd(0, f[0]); rd(1, f[1]);
    __builtin_amdgcn_sched_barrier(0);
#pragma unroll
    for (int k8 = 0; k8 < 8; ++k8) {
      if (k8 == 4 && qq < 3) {
        if (qq < 2) { if (NF == 8) VMW(8); else VMW(4); } else VMW(0);
        lds_inc(L_CNT + (C_LAND + qq + 1) * 4, lane);
      }
      if (k8 < 6) rd(k8 + 2, f[(k8 + 2) % 3]);
      const v4u xf = xr[8 * qq + k8];
#pragma unroll
      for (int i = 0; i < NT; ++i) acc[i] = mfma(f[k8 % 3][i], xf, acc[i]);
      csa = __builtin_amdgcn_mfma_f32_4x4x4bf16_1k(one, __builtin_bit_cast(s16x4, v2u{f[k8 % 3][0].x, f[k8 % 3][0].y}), csa, 0, 0, 0);
      csb = __builtin_amdgcn_mfma_f32_4x4x4bf16_1k(one, __builtin_bit_cast(s16x4, v2u{f[k8 % 3][0].z, f[k8 % 3][0].w}), csb, 0, 0, 0);
      if (k8 < 6) {
#pragma unroll
        for (int i = 0; i < NT; ++i) {
          __builtin_amdgcn_sched_group_barrier(0x008, 1, 0);
          __builtin_amdgcn_sched_group_barrier(0x100, 2, 0);
        }
        __builtin_amdgcn_sched_group_barrier(0x008, 2, 0);
      }
      __builtin_amdgcn_sched_barrier(0);
    }
    asm volatile("" : "+v"(acc[0]), "+v"(acc[1]), "+v"(acc[NT - 1]), "+v"(csa), "+v"(csb) :: "memory");
    lds_inc(L_CNT + (C_FREE + qq) * 4, lane);
  }
  cs[0] = csa[0] + csb[0];
}

template <bool ffn> DI u64 role_b_head(const Params& P, const Ctx& c, int wid) {
  u64 amp_a = 0, amp_b = 0; (void)amp_a; (void)amp_b;
  const int lane = c.lane, tq = c.tq, hh = c.hh, t = lane & 31;
  const int g = c.gi;
  const unsigned tok = (unsigned)(c.p * 128 + 8 * c.G + 2 * tq);
  v4u r0, r1;
  {
    const u16* rp = P.x + (size_t)tok * 512 + lane * 8;
    asm volatile("global_load_dwordx4 %0, %2, off\n\tglobal_load_dwordx4 %1, %2, off offset:1024" : "=&v"(r0), "=&v"(r1) : "v"(rp) : "memory");
  }
  const __amdgpu_buffer_rsrc_t rsX = __builtin_amdgcn_make_buffer_rsrc((void*)P.x, 0, 2048 * 1024, 0x00020000);
  const __amdgpu_buffer_rsrc_t rsW = __builtin_amdgcn_make_buffer_rsrc((void*)(ffn ? P.w1 : P.wqkv), 0, ffn ? 512 * 3584 : 512 * 3072, 0x00020000);
  constexpr unsigned RS = ffn ? 3584u : 3072u;
  const unsigned cA = (unsigned)(224 * g + 32 * (g & 1));
  const unsigned u0 = ffn ? cA * 2u : (unsigned)(512 + 64 * g) * 2u, u1 = ffn ? cA * 2u + 128u : (unsigned)(1024 + 64 * g) * 2u;
  const unsigned ua = ffn ? (cA + 128u) * 2u : (unsigned)(64 * g) * 2u;
  const unsigned ub = (unsigned)((g & 1) ? 224 * g - 32 : 224 * g + 192) * 2u;
  const unsigned voffX = (unsigned)(lane >> 3) * 1024u + (unsigned)(((lane & 7) ^ ((4 * (tq & 1) + (lane >> 4)) & 7)) * 16);
  const unsigned voffW = (unsigned)(lane >> 3) * RS + (unsigned)(((lane & 7) ^ (((lane >> 4) & 1) << 2)) * 16);
  const unsigned soffX = (unsigned)(c.p * 128 + 8 * tq) * 1024u;
  const unsigned wrow = 32u * (unsigned)(tq & 1) * RS;
  auto fill = [&](int f) AI {
    const unsigned slot = (unsigned)(f & 3) * SLOT;
    if (f < 8) {
      const unsigned kcp = (unsigned)((f + c.rot) & 7);
      unsigned sx = soffX + kcp * 128u, sw = 64u * kcp * RS + wrow + ((tq >> 1) ? u1 : u0);
      unsigned mx = slot + (unsigned)tq * 1024u, mw = slot + 16384u + (unsigned)tq * 4096u;
#pragma unroll 1
      for (int m = 0; m < 4; ++m) {
        dma(mx, voffX, rsX, sx); dma(mw, voffW, rsW, sw);
        mx += 4096u; sx += 32768u; mw += 1024u; sw += 8u * RS;
      }
    } else {
      const unsigned kcp = (unsigned)((2 * (f - 8) + (tq >> 1) + c.rot) & 7);
      unsigned sw = 64u * kcp * RS + wrow, mw = slot + (unsigned)tq * 4096u;
#pragma unroll 1
      for (int m = 0; m < 4; ++m) {
        dma(mw, voffW, rsW, sw + ua);
        if (ffn) dma(mw + 16384u, voffW, rsW, sw + ub);
        mw += 1024u; sw += 8u * RS;
      }
    }
  };
  fill(0);
  float st0;
  {
    asm volatile("s_waitcnt vmcnt(8)" : "+v"(r0), "+v"(r1) :: "memory");
    float v[2][8], mean[2], rs[2];
#pragma unroll
    for (int k = 0; k < 4; ++k) {
      v[0][2 * k] = u2f(r0[k] << 16); v[0][2 * k + 1] = u2f(r0[k] & 0xffff0000u);
      v[1][2 * k] = u2f(r1[k] << 16); v[1][2 * k + 1] = u2f(r1[k] & 0xffff0000u);
    }
#pragma unroll
    for (int i = 0; i < 2; ++i) mean[i] = wavesum(((v[i][0] + v[i][1]) + (v[i][2] + v[i][3])) + ((v[i][4] + v[i][5]) + (v[i][6] + v[i][7]))) * (1.0f / 512.0f);
#pragma unroll
    for (int i = 0; i < 2; ++i) {
      float q2 = 0.f;
#pragma unroll
      for (int k = 0; k < 8; ++k) { const float d = v[i][k] - mean[i]; q2 = __builtin_fmaf(d, d, q2); }
      rs[i] = __builtin_amdgcn_rsqf(wavesum(q2) * (1.0f / 512.0f) + 1e-5f);
    }
    st0 = (lane & 2) ? rs[1] : rs[0];
    st0 = (lane & 1) ? -((lane & 2) ? mean[1] : mean[0]) * st0 : st0;
    asm volatile("" : "+v"(st0));
  }
  fill(1);
  unsigned xa[4];
#pragma unroll
  for (int ks = 0; ks < 4; ++ks) xa[ks] = (unsigned)(32 * tq + t) * 128u + (unsigned)(((2 * ks + hh) ^ ((t >> 1) & 7)) * 16);
  v4u xr[32];
  u64 tag = 0;
  v4u gr;
  const unsigned token = (unsigned)(c.p * 128 + 32 * tq + t);
#pragma unroll
  for (int kc = 0; kc < 8; ++kc) {
    if (kc < 7 || ffn) VMW(8); else VMW(4);
    BAR();
    if (kc == 0) {
      tag = lds_tag();
      if (lane < 4)
        __hip_atomic_store((GAS u64*)(P.ws + WS_ST + tok * 16u + (unsigned)lane * 8u), ((u64)(unsigned)tag << 32) | f2u(st0), __ATOMIC_RELAXED, __HIP_MEMORY_SCOPE_AGENT);
    }
    if (kc == 5) {
      const char* gp = P.ws + WS_ST + token * 16u;
      asm volatile("global_load_dwordx4 %0, %1, off sc1" : "=&v"(gr) : "v"(gp) : "memory");
    }
    fill(kc + 2);
    const unsigned sb = (unsigned)(kc & 3) * SLOT;
#pragma unroll
    for (int ks = 0; ks < 4; ++ks) xr[4 * kc + ks] = lds128(xa[ks] + sb);
    asm volatile("" : "+v"(xr[4 * kc]), "+v"(xr[4 * kc + 1]), "+v"(xr[4 * kc + 2]), "+v"(xr[4 * kc + 3]));
  }
  asm volatile("" : "+v"(gr));
  STAMP(2);
  BAR();
  if (__builtin_amdgcn_ballot_w64(gr.y != (unsigned)tag || gr.w != (unsigned)tag) != 0) {
    const __amdgpu_buffer_rsrc_t rs = __builtin_amdgcn_make_buffer_rsrc((void*)(P.ws + WS_ST), 0, 32768, 0x00020000);
#pragma unroll 1
    for (unsigned spin = 0; spin < kSpin; ++spin) {
      gr = __builtin_amdgcn_raw_buffer_load_b128(rs, token * 16u, 0, 16);
      if (__builtin_amdgcn_ballot_w64(gr.y != (unsigned)tag || gr.w != (unsigned)tag) == 0) break;
      __builtin_amdgcn_s_sleep(1);
    }
  }
  const float rstd = u2f(gr.x), mr = u2f(gr.z);
  if (hh == 0) *(LAS f32x2*)(size_t)(L_STATS + (unsigned)(32 * tq + t) * 8u) = f32x2{rstd, mr};
  if (ffn) VMW(8); else VMW(4);
  BAR();
  const int cb = (lane >> 4) & 1, q = (lane >> 2) & 3, pp = lane & 3, sw = (q >> 1) & 1;
  unsigned wl[2];
#pragma unroll
  for (int h = 0; h < 2; ++h) wl[h] = (unsigned)(8 * hh + q) * 128u + (unsigned)((4 * (h ^ sw) + 2 * cb + (pp >> 1)) * 16 + 8 * (pp & 1));
  const __amdgpu_buffer_rsrc_t rsHB = __builtin_amdgcn_make_buffer_rsrc(P.ws + WS_HB, 0, HB_BYTES, 0x00020000);
  const unsigned T32 = (unsigned)(c.p * 4 + tq);
  f32x4 cs = {0.f, 0.f, 0.f, 0.f};
  auto cs_sync = [&](int tile, bool wr) AI {
    const float s = xhalf_add(cs[0]);
    if (hh == 0 && wr) *(LAS float*)(size_t)(L_CS1 + (unsigned)tile * 128u + (unsigned)t * 4u) = s;
    asm volatile("s_waitcnt lgkmcnt(0)" ::: "memory");
    lds_inc(L_CNT + C_CS1 * 4, lane);
    lds_wait_ge(L_CNT + C_CS1 * 4, 4);
  };
  if constexpr (ffn) {
    const unsigned w2a = ((g & 1) ? wl[1] : wl[0]) + 16384u;
    int ti[3];
    unsigned wb[3];
    f32x16 acc[3];
#pragma unroll
    for (int i = 0; i < 3; ++i) { ti[i] = (i + tq) % 3; wb[i] = ti[i] == 0 ? wl[0] : (ti[i] == 1 ? wl[1] : w2a); acc[i] = zero16(); }
    pass1<3, 8>(P, wid, amp_a, amp_b, fill, xr, acc, cs, wb, lane);
    cs_sync(ti[0], tq < 3);
    STAMP(3);
    if (PRIOL) __builtin_amdgcn_s_setprio(3);
    f32x4 csv[3][4], bsv[3][4];
#pragma unroll
    for (int i = 0; i < 3; ++i) { load16(L_CS1 + (unsigned)ti[i] * 128u, hh, csv[i]); load16(L_B1 + (unsigned)(4 + ti[i]) * 128u, hh, bsv[i]); }
#pragma unroll
    for (int i = 0; i < 3; ++i)
      ffn_tile(acc[i], rstd, mr, csv[i], bsv[i], rsHB, (unsigned)lane * 16u + (T32 * 144u + (unsigned)(64 + 6 * g + 2 * ti[i])) * 1024u);
    publish_last(L_CNT + C_LATE * 4, 4, P.ws, FL_L + (unsigned)(c.p * 8 + g) * 128u, tag, lane);
    if (PRIOL) __builtin_amdgcn_s_setprio(0);
  } else {
    int ti[2];
    unsigned wb[2];
    f32x16 acc[2];
#pragma unroll
    for (int i = 0; i < 2; ++i) { ti[i] = (i + tq) & 1; wb[i] = ti[i] == 0 ? wl[0] : wl[1]; acc[i] = zero16(); }
    pass1<2, 4>(P, wid, amp_a, amp_b, fill, xr, acc, cs, wb, lane);
    cs_sync(ti[0], tq < 2);
    STAMP(3);
    v4u pq[4][2];
#pragma unroll
    for (int hd = 0; hd < 4; ++hd) {
      float y[8];
      fold8(acc[hd >> 1], 8 * (hd & 1), rstd, mr, L_CS1 + (unsigned)ti[hd >> 1] * 128u, hh, y);
      hedge(mfma(wphi_frag(2 * ti[hd >> 1] + (hd & 1), lane), pack8(y), zero16()), pq[hd][0], pq[hd][1]);
    }
    asm volatile("" : "+v"(pq[0][0]), "+v"(pq[0][1]), "+v"(pq[1][0]), "+v"(pq[1][1]), "+v"(pq[2][0]), "+v"(pq[2][1]), "+v"(pq[3][0]), "+v"(pq[3][1]));
    STAMP(4);
    if (PRIO) __builtin_amdgcn_s_setprio(3);
    if (tq == 0) { poll_flags(P.ws, FL_K + (unsigned)g * 16u * 128u, 16, tag, lane); lds_set(L_CNT + C_KVRDY * 4, 1); }
    else lds_wait_ge(L_CNT + C_KVRDY * 4, 1);
    STAMP(5);
    {
      const __amdgpu_buffer_rsrc_t rsKV = __builtin_amdgcn_make_buffer_rsrc(P.ws + WS_KVP, 0, 524288 + 65536, 0x00020000);
      const unsigned hbase = (unsigned)(g * 4 + tq) * 16u;
      v4u pv[16];
#pragma unroll
      for (int i = 0; i < 16; ++i) pv[i] = __builtin_amdgcn_raw_buffer_load_b128(rsKV, (unsigned)lane * 16u, (hbase + (unsigned)i) * 1024u, 16);
      const v4u z0 = __builtin_amdgcn_raw_buffer_load_b128(rsKV, (unsigned)lane * 16u, 524288u + hbase * 128u, 16);
      const v4u z1 = __builtin_amdgcn_raw_buffer_load_b128(rsKV, (unsigned)lane * 16u, 524288u + hbase * 128u + 1024u, 16);
      const u64 lf = __hip_atomic_load((const GAS u64*)(P.ws + FL_L + (unsigned)(c.tt >> 1) * 8u * 128u + (unsigned)(lane & 7) * 128u), __ATOMIC_RELAXED, __HIP_MEMORY_SCOPE_AGENT);
      __builtin_amdgcn_sched_barrier(0);
      v4u idf;
      {
        const int m = lane & 31, h2 = lane >> 5;
        const int j = (m < 16 && ((m >> 2) & 1) == h2) ? (((m >> 3) << 2) | (m & 3)) : -1;
#pragma unroll
        for (int k = 0; k < 4; ++k) idf[k] = (j == 2 * k ? 0x3F80u : 0u) | (j == 2 * k + 1 ? 0x3F800000u : 0u);
      }
      f32x16 sum = zero16();
#pragma unroll
      for (int i = 0; i < 16; ++i) sum = mfma(idf, pv[i], sum);
      asm volatile("" : "+v"(sum));
      lds_inc(L_CNT + C_KVIN * 4, lane);
      if (!ORD && tq == 0 && __builtin_amdgcn_ballot_w64(lf != tag) == 0) lds_set(L_CNT + C_LATERDY * 4, 1);
      const float s[8] = {sum[0], sum[1], sum[2], sum[3], sum[4], sum[5], sum[6], sum[7]};
      const unsigned afr = L_AFR + (unsigned)tq * 2048u;
      *(LAS v4u*)(size_t)(afr + (unsigned)((lane >> 4) & 1) * 1024u + (unsigned)((lane & 15) + 32 * (lane >> 5)) * 16u) = pack8(s);
      const unsigned hh2 = (unsigned)(lane >> 4) & 1u, c16 = (unsigned)lane & 15u;
      float zz[4];
#pragma unroll
      for (int k = 0; k < 4; ++k) {
        float v = dppadd<0x128>(u2f(z0[k]) + u2f(z1[k]));
        auto r = __builtin_amdgcn_permlane16_swap(f2u(v), f2u(v), false, false);
        zz[k] = xhalf_add(u2f(r[0]) + u2f(r[1]));
      }
      if (lane < 8) *(LAS f32x4*)(size_t)(L_ZT + (unsigned)tq * 128u + (unsigned)lane * 16u) = f32x4{zz[0], zz[1], zz[2], zz[3]};
      const unsigned f0 = 16u * (unsigned)(lane >> 5) + 4u * hh2;
      const f32x4 za = lds128f(L_ZT + (unsigned)tq * 128u + f0 * 4u), zb = lds128f(L_ZT + (unsigned)tq * 128u + (f0 + 8u) * 4u);
      *(LAS v4u*)(size_t)(afr + (unsigned)(lane >> 5) * 1024u + (16u + c16 + 32u * hh2) * 16u) = v4u{pk2(za[0], za[1]), pk2(za[2], za[3]), pk2(zb[0], zb[1]), pk2(zb[2], zb[3])};
      asm volatile("s_waitcnt lgkmcnt(0)" ::: "memory");
      lds_inc(L_CNT + C_AFR * 4, lane);
      lds_wait_ge(L_CNT + C_AFR * 4, 4);
    }
#pragma unroll
    for (int hd = 0; hd < 4; ++hd) {
      const int head = 2 * ti[hd >> 1] + (hd & 1);
      f32x16 d = mfma(lds128(L_AFR + (unsigned)head * 2048u + (unsigned)lane * 16u), pq[hd][0], zero16());
      d = mfma(lds128(L_AFR + (unsigned)head * 2048u + 1024u + (unsigned)lane * 16u), pq[hd][1], d);
      const float r = __builtin_amdgcn_rcpf(d[8] + 1e-6f);
      const v4u o = v4u{pk2(d[0] * r, d[1] * r), pk2(d[2] * r, d[3] * r), pk2(d[4] * r, d[5] * r), pk2(d[6] * r, d[7] * r)};
      st128(o, rsHB, (unsigned)lane * 16u + (T32 * 144u + (unsigned)(112 + 4 * g + head)) * 1024u);
    }
    publish_last(L_CNT + C_O * 4, 4, P.ws, FL_O + (unsigned)(c.p * 8 + g) * 128u, tag, lane);
    if (PRIO) __builtin_amdgcn_s_setprio(0);
  }
  STAMP(6);
  return tag;
}

DI void role_b(const Params& P, const Ctx& c, int wid, u64 amp_a0) {
  u64 amp_a = amp_a0, amp_b = 0; (void)amp_a; (void)amp_b;
  const int lane = c.lane, tq = c.tq;
  const u64 tag = c.ffn ? role_b_head<true>(P, c, wid) : role_b_head<false>(P, c, wid);
  const int pan = c.tt >> 1;
  const int tb = tq * 64 + lane;
  v4u xres[2], bres;
#pragma unroll
  for (int m = 0; m < 2; ++m) {
    const int idx = tb + 256 * m;
    xres[m] = *(const GAS v4u*)(P.x + (size_t)(64 * c.tt + (idx >> 3)) * 512 + 64 * c.ct + 8 * (idx & 7));
  }
  bres = *(const GAS v4u*)(P.b2 + 64 * c.ct + 8 * (tb & 7));
  if (tq == 0) {
    poll_flags(P.ws, (ORD ? FL_O : FL_L) + (unsigned)pan * 8u * 128u, 8, tag, lane);
    lds_set(L_CNT + (ORD ? C_ORDY : C_LATERDY) * 4, 1);
    poll_flags(P.ws, (ORD ? FL_L : FL_O) + (unsigned)pan * 8u * 128u, 8, tag, lane);
    lds_set(L_CNT + (ORD ? C_LATERDY : C_ORDY) * 4, 1);
  } else if (tq == 1 && __builtin_amdgcn_workgroup_id_x() == 0) {
    poll_flags(P.ws, FL_E, 64, tag, lane); poll_flags(P.ws, FL_E + 64 * 128, 64, tag, lane);
    poll_flags(P.ws, FL_K, 64, tag, lane); poll_flags(P.ws, FL_K + 64 * 128, 64, tag, lane);
    if (lane == 0) __hip_atomic_store((GAS u64*)(P.ws + WS_EPOCH), tag, __ATOMIC_RELAXED, __HIP_MEMORY_SCOPE_AGENT);
  }
  BAR();
#pragma unroll
  for (int m = 0; m < 2; ++m) {
    const int idx = tb + 256 * m;
    const unsigned tok = (unsigned)(idx >> 3), cg = (unsigned)(idx & 7);
    const unsigned a = (tok >> 5) * SLOT + (tok & 31u) * 256u, c0 = ((2u * cg) ^ (tok & 15u)) * 16u, c1 = ((2u * cg + 1u) ^ (tok & 15u)) * 16u;
    f32x4 lo = lds128f(a + c0), hi = lds128f(a + c1);
#pragma unroll
    for (unsigned w = 1; w < 4; ++w) { lo += lds128f(w * 8192u + a + c0); hi += lds128f(w * 8192u + a + c1); }
    v4u o;
#pragma unroll
    for (int k = 0; k < 4; ++k) {
      const float a0 = k < 2 ? lo[2 * k] : hi[2 * k - 4], a1 = k < 2 ? lo[2 * k + 1] : hi[2 * k - 3];
      const float x0 = u2f(xres[m][k] << 16), x1 = u2f(xres[m][k] & 0xffff0000u);
      const float b0 = u2f(bres[k] << 16), b1 = u2f(bres[k] & 0xffff0000u);
      o[k] = pk2(a0 + x0 + b0, a1 + x1 + b1);
    }
    *(GAS v4u*)(P.out + (size_t)(64 * c.tt + (idx >> 3)) * 512 + 64 * c.ct + 8 * (idx & 7)) = o;
  }
  STAMP(7);

}

__global__ __launch_bounds__(512, 2) NOPK void fused_kernel(Params P) {
  __shared__ __attribute__((aligned(1024))) char smem[LDS_BYTES];
  asm volatile("" ::"v"((unsigned)(size_t)(LAS char*)smem) : "memory");
  u64 pc;
  asm volatile("s_getpc_b64 %0" : "=s"(pc));
  const int tid = __builtin_amdgcn_workitem_id_x(), lane = tid & 63;
  const int wid = __builtin_amdgcn_readfirstlane(tid >> 6);
  u64 amp_a = 0, amp_b = 0;
  STAMP(0);
  Ctx c;
  c.lane = lane; c.tq = wid & 3; c.hh = lane >> 5;
  const int blk = __builtin_amdgcn_workgroup_id_x(), xcd = blk & 7, j = blk >> 3, ja = j & 7, jb = j >> 3;
  c.p = (xcd & 1) * 8 + ja;
  c.G = (xcd >> 1) * 4 + jb;
  c.ffn = !(c.G & 1);
  c.gi = c.G >> 1;
  c.rot = ROT1 == 0 ? 0 : (ROT1 == 1 ? (((ja + jb) % 3) * 3) & 7 : (ROT1 == 2 ? (ja + 2 * jb) & 7 : (ROT1 == 3 ? ((ja + jb) & 1) * 4 : (ROT1 == 4 ? ((ja + jb) & 3) * 2 : ((ja + jb) & 3)))));
  c.tt = (xcd >> 1) * 8 + ja; c.ct = (xcd & 1) * 4 + jb; c.ja = ja; c.jb = jb;
  c.rotE = ROT2 ? (2 * ja + 4 * jb) & 15 : 0; c.rotL = ROT2 ? (ja + 3 * jb) % 12 : 0; c.rotO = ROT2 ? (ja + 2 * jb) & 7 : 0;
  if (wid < 4) {
    const unsigned kb0 = (unsigned)(j + 32 * (wid & 1)), kb = kb0 < CODE_KB ? kb0 : 0u;
    const unsigned pf = CODEPF ? *(const GAS unsigned*)((pc & ~255ull) + kb * 1024u + (unsigned)lane * 16u) : 0u;
    const u64 e = __hip_atomic_load((const GAS u64*)(P.ws + WS_EPOCH), __ATOMIC_RELAXED, __HIP_MEMORY_SCOPE_AGENT);
    if (c.ffn) {
      const int tc = tid < 224 ? tid : 0;
      const int t7 = tc >> 5, n = tc & 31, g = c.gi;
      const int cA = 224 * g + 32 * (g & 1);
      const int col = t7 < 4 ? cA + 32 * t7 : (t7 < 6 ? cA + 128 + 32 * (t7 - 4) : ((g & 1) ? 224 * g : 224 * g + 192));
      const unsigned bv = P.b1[col + n];
      *(LAS float*)(size_t)(L_B1 + (unsigned)tid * 4u) = u2f(bv << 16);
    } else {
      const v4u wv = *(const GAS v4u*)(P.wphi + (size_t)c.gi * 1024 + (tid & 127) * 8);
      *(LAS v4u*)(size_t)(L_WPHI + (unsigned)(tid & 127) * 16u) = wv;
    }
    if (tid < 32) *(LAS unsigned*)(size_t)(L_CNT + (unsigned)tid * 4u) = 0u;
    const u64 tag = e + 1u;
    if (tid == 0) *(LAS v2u*)(size_t)L_TAG = v2u{(unsigned)tag, (unsigned)(tag >> 32)};
    asm volatile("" ::"v"(pf));
    STAMP(1);
    role_a(P, c, wid, amp_a, amp_b);
  } else {
    STAMP(1);
    role_b(P, c, wid, amp_a);
  }
}

}

extern "C" void kernel_launch(void* const* d_in, const int* in_sizes, int n_in, void* d_out, int out_size, void* d_ws, size_t ws_size,
                              hipStream_t stream) {
  Params P;
  P.x = (const u16*)d_in[0]; P.wqkv = (const u16*)d_in[1]; P.wphi = (const u16*)d_in[2]; P.wo = (const u16*)d_in[3];
  P.w1 = (const u16*)d_in[5]; P.b1 = (const u16*)d_in[6]; P.w2 = (const u16*)d_in[7]; P.b2 = (const u16*)d_in[8];
  P.out = (u16*)d_out; P.ws = (char*)d_ws;
  fused_kernel<<<256, 512, 0, stream>>>(P);
}
```

```cpp
#include <hip/hip_runtime.h>
#include <stdint.h>
#include <type_traits>

namespace {

typedef unsigned short u16;
typedef unsigned long long u64;
typedef unsigned v4u __attribute__((ext_vector_type(4)));
typedef unsigned v2u __attribute__((ext_vector_type(2)));
typedef float f32x16 __attribute__((ext_vector_type(16)));
typedef float f32x4 __attribute__((ext_vector_type(4)));
typedef float f32x2 __attribute__((ext_vector_type(2)));
typedef __bf16 bf16x8 __attribute__((ext_vector_type(8)));
typedef __bf16 bf16x2 __attribute__((ext_vector_type(2)));
typedef short s16x4 __attribute__((ext_vector_type(4)));
#define LAS __attribute__((address_space(3)))
#define GAS __attribute__((address_space(1)))
#if defined(__HIP_DEVICE_COMPILE__) && !defined(USEPK)
#define NOPK __attribute__((target("no-packed-fp32-ops")))
#else
#define NOPK
#endif
#define DI __device__ __forceinline__ NOPK
#define AI __attribute__((always_inline)) NOPK

#define STAMPS 0
#define ROT1 1
#define ROT2 0
#define STR2(x) #x
#define STR(x) STR2(x)
#define W1 8
#define W2 4
#define PRIO 1
#define SLP 1
#define KVFIRST 0
#define PRIOL 0
#define MAP2 0
#define NBK 4
#define ORD 0
#define CODEPF 1
#define WARM2 1
#define CODE_KB 36

constexpr unsigned WS_EPOCH = 0;
constexpr unsigned FL_E = 4096;
constexpr unsigned FL_L = FL_E + 128 * 128;
constexpr unsigned FL_K = FL_L + 128 * 128;
constexpr unsigned FL_O = FL_K + 128 * 128;
constexpr unsigned WS_ST = 73728;
constexpr unsigned WS_KVP = 131072;
constexpr unsigned WS_ZP = WS_KVP + 524288;
constexpr unsigned WS_HB = 1048576;
constexpr unsigned HB_BYTES = 64u * 144u * 1024u;
constexpr unsigned WS_STAMP = 16u << 20;

constexpr unsigned SLOT = 32768;
constexpr unsigned L_MISC = 131072;
constexpr unsigned L_STATS = L_MISC;
constexpr unsigned L_CS0 = L_MISC + 1024;
constexpr unsigned L_CS1 = L_MISC + 1536;
constexpr unsigned L_B1 = L_MISC + 2048;
constexpr unsigned L_WPHI = L_MISC + 3072;
constexpr unsigned L_CNT = L_MISC + 5120;
constexpr unsigned L_TAG = L_MISC + 5248;
constexpr unsigned L_ZT = L_MISC + 5376;
constexpr unsigned L_AFR = L_MISC + 8192;
constexpr unsigned LDS_BYTES = 163840;
enum { C_CS1 = 0, C_LATE = 1, C_KV = 2, C_O = 3, C_AFR = 4, C_KVRDY = 5, C_LATERDY = 6, C_ORDY = 7, C_FREE = 8  , C_LAND = 12  , C_EARLY = 16, C_IMG = 17, C_KVIN = 18, C_IMGDONE = 19 };

constexpr unsigned kSpin = 1u << 16;

struct Params {
  const u16* x; const u16* wqkv; const u16* wphi; const u16* wo;
  const u16* w1; const u16* b1; const u16* w2; const u16* b2;
  u16* out; char* ws;
};

DI unsigned f2u(float f) { return __builtin_bit_cast(unsigned, f); }
DI float u2f(unsigned u) { return __builtin_bit_cast(float, u); }
DI float fmx(float a, float b) { return __builtin_fmaxf(a, b); }
DI f32x16 mfma(v4u a, v4u b, f32x16 c) {
  return __builtin_amdgcn_mfma_f32_32x32x16_bf16(__builtin_bit_cast(bf16x8, a), __builtin_bit_cast(bf16x8, b), c, 0, 0, 0);
}
DI f32x16 zero16() { f32x16 z; for (int r = 0; r < 16; ++r) z[r] = 0.f; return z; }
DI v4u lds128(unsigned a) { return *(const LAS v4u*)(size_t)a; }
DI f32x4 lds128f(unsigned a) { return *(const LAS f32x4*)(size_t)a; }
DI v2u tr64(unsigned a) { return __builtin_bit_cast(v2u, __builtin_amdgcn_ds_read_tr16_b64_v4i16((LAS s16x4*)(size_t)a)); }
DI v4u tr2(unsigned a0, unsigned a1) { v2u l = tr64(a0), h = tr64(a1); return v4u{l.x, l.y, h.x, h.y}; }
DI unsigned pk2(float a, float b) { return __builtin_bit_cast(unsigned, __builtin_convertvector(f32x2{a, b}, bf16x2)); }
DI float xhalf_add(float v) { auto r = __builtin_amdgcn_permlane32_swap(f2u(v), f2u(v), false, false); return u2f(r[0]) + u2f(r[1]); }
DI float xhalf_max(float v) { auto r = __builtin_amdgcn_permlane32_swap(f2u(v), f2u(v), false, false); return fmx(u2f(r[0]), u2f(r[1])); }
DI float shx(float v, int m, int lane) { return u2f((unsigned)__builtin_amdgcn_ds_bpermute((lane ^ m) << 2, (int)f2u(v))); }
DI f32x4 lanesum(v4u f, f32x4 c) {
  const s16x4 one = {0x3F80, 0x3F80, 0x3F80, 0x3F80};
  c = __builtin_amdgcn_mfma_f32_4x4x4bf16_1k(one, __builtin_bit_cast(s16x4, v2u{f.x, f.y}), c, 0, 0, 0);
  return __builtin_amdgcn_mfma_f32_4x4x4bf16_1k(one, __builtin_bit_cast(s16x4, v2u{f.z, f.w}), c, 0, 0, 0);
}
template <int CTRL> DI float dppadd(float v) { return v + u2f((unsigned)__builtin_amdgcn_update_dpp(0, (int)f2u(v), CTRL, 0xf, 0xf, true)); }
DI float wavesum(float v) {
  v = dppadd<0xB1>(v); v = dppadd<0x4E>(v); v = dppadd<0x141>(v); v = dppadd<0x140>(v);
  auto r = __builtin_amdgcn_permlane16_swap(f2u(v), f2u(v), false, false);
  return xhalf_add(u2f(r[0]) + u2f(r[1]));
}
DI void dma(unsigned m0v, unsigned voff, __amdgpu_buffer_rsrc_t rs, unsigned soff) {
  asm volatile("s_mov_b32 m0, %0\n\ts_nop 0\n\tbuffer_load_dwordx4 %1, %2, %3 offen lds" ::"s"(m0v), "v"(voff), "s"(rs), "s"(soff) : "memory");
}
DI void st128(v4u v, __amdgpu_buffer_rsrc_t rs, unsigned voff) { __builtin_amdgcn_raw_buffer_store_b128(v, rs, voff, 0, 16); }
#define BAR() do { __builtin_amdgcn_sched_barrier(0); asm volatile("s_waitcnt lgkmcnt(0)\n\ts_barrier" ::: "memory"); __builtin_amdgcn_sched_barrier(0); } while (0)
#define VMW(n) asm volatile("s_waitcnt vmcnt(" #n ")" ::: "memory")
constexpr unsigned ONES = 0x3F803F80u;

#define STAMP(i) do { } while (0)

DI void poll_flags(const char* ws, unsigned base, int n, u64 tag, int lane) {
  const GAS u64* f = (const GAS u64*)(ws + base + (unsigned)(lane < n ? lane : 0) * 128u);
#pragma unroll 1
  for (unsigned spin = 0; spin < kSpin; ++spin) {
    const u64 v = __hip_atomic_load(f, __ATOMIC_RELAXED, __HIP_MEMORY_SCOPE_AGENT);
    if (__builtin_amdgcn_ballot_w64(v != tag) == 0) break;
    __builtin_amdgcn_s_sleep(1);
  }
  __builtin_amdgcn_fence(__ATOMIC_ACQUIRE, "wavefront");
  asm volatile("" ::: "memory");
}
DI void lds_wait_ge(unsigned addr, unsigned want) {
#pragma unroll 1
  for (unsigned spin = 0; spin < (kSpin << 4); ++spin) {
    const unsigned v = *(volatile LAS unsigned*)(size_t)addr;
    if ((unsigned)__builtin_amdgcn_readfirstlane(v) >= want) break;
    if (SLP) __builtin_amdgcn_s_sleep(SLP);
  }
  asm volatile("" ::: "memory");
}
DI void lds_set(unsigned addr, unsigned v) { *(volatile LAS unsigned*)(size_t)addr = v; }
DI void lds_inc(unsigned addr, int lane) { if (lane == 0) __hip_atomic_fetch_add((LAS unsigned*)(size_t)addr, 1u, __ATOMIC_RELAXED, __HIP_MEMORY_SCOPE_WORKGROUP); }
DI void publish_last(unsigned cnt_addr, unsigned n, char* ws, unsigned flag_off, u64 tag, int lane) {
  VMW(0);
  if (lane == 0) {
    const unsigned old = __hip_atomic_fetch_add((LAS unsigned*)(size_t)cnt_addr, 1u, __ATOMIC_RELAXED, __HIP_MEMORY_SCOPE_WORKGROUP);
    if (old == n - 1) __hip_atomic_store((GAS u64*)(ws + flag_off), tag, __ATOMIC_RELAXED, __HIP_MEMORY_SCOPE_AGENT);
  }
}
DI u64 lds_tag() { const v2u t = *(const LAS v2u*)(size_t)L_TAG; return ((u64)(unsigned)__builtin_amdgcn_readfirstlane(t.y) << 32) | (unsigned)__builtin_amdgcn_readfirstlane(t.x); }

DI void fold8(const f32x16& acc, int r0, float rstd, float mr, unsigned cs, int hh, float* y) {
#pragma unroll
  for (int g = 0; g < 2; ++g) {
    const f32x4 t = lds128f(cs + (unsigned)((r0 >> 2) + g) * 32u + (unsigned)hh * 16u) * mr;
#pragma unroll
    for (int k = 0; k < 4; ++k) y[4 * g + k] = __builtin_fmaf(acc[r0 + 4 * g + k], rstd, t[k]);
  }
}
DI void load16(unsigned a, int hh, f32x4* v) {
#pragma unroll
  for (int g = 0; g < 4; ++g) v[g] = lds128f(a + (unsigned)g * 32u + (unsigned)hh * 16u);
}
DI void ffn_tile(const f32x16& acc, float rstd, float mr, const f32x4* cs, const f32x4* bs, __amdgpu_buffer_rsrc_t rsHB, unsigned voff) {
  f32x2 y[8], d[8];
#pragma unroll
  for (int g = 0; g < 4; ++g) {
    const f32x4 t = cs[g] * mr + bs[g];
    y[2 * g] = f32x2{acc[4 * g], acc[4 * g + 1]} * rstd + f32x2{t[0], t[1]};
    y[2 * g + 1] = f32x2{acc[4 * g + 2], acc[4 * g + 3]} * rstd + f32x2{t[2], t[3]};
  }
#pragma unroll
  for (int k = 0; k < 8; ++k) d[k] = y[k] * ((y[k] * y[k]) * (-0.10294324f) + (-2.30220819f));
#pragma unroll
  for (int k = 0; k < 8; ++k) { d[k].x = __builtin_amdgcn_exp2f(d[k].x); d[k].y = __builtin_amdgcn_exp2f(d[k].y); }
#pragma unroll
  for (int k = 0; k < 8; ++k) d[k] += 1.0f;
#pragma unroll
  for (int k = 0; k < 8; ++k) { d[k].x = __builtin_amdgcn_rcpf(d[k].x); d[k].y = __builtin_amdgcn_rcpf(d[k].y); }
  v4u o0, o1;
#pragma unroll
  for (int k = 0; k < 4; ++k) {
    const f32x2 g0 = y[k] * d[k], g1 = y[4 + k] * d[4 + k];
    o0[k] = pk2(g0.x, g0.y); o1[k] = pk2(g1.x, g1.y);
  }
  st128(o0, rsHB, voff); st128(o1, rsHB, voff + 1024u);
}
DI void hedge(const f32x16& d, v4u& o0, v4u& o1) {
  constexpr float L = 1.4426950408889634f;
  float m0 = fmx(fmx(fmx(d[0], d[1]), fmx(d[2], d[3])), fmx(fmx(d[4], d[5]), fmx(d[6], d[7])));
  float m1 = fmx(fmx(fmx(d[8], d[9]), fmx(d[10], d[11])), fmx(fmx(d[12], d[13]), fmx(d[14], d[15])));
  m0 = xhalf_max(m0) * -L; m1 = xhalf_max(m1) * -L;
  float e[16];
#pragma unroll
  for (int i = 0; i < 8; ++i) { e[i] = __builtin_amdgcn_exp2f(__builtin_fmaf(d[i], L, m0)); e[8 + i] = __builtin_amdgcn_exp2f(__builtin_fmaf(d[8 + i], L, m1)); }
  float s0 = ((e[0] + e[1]) + (e[2] + e[3])) + ((e[4] + e[5]) + (e[6] + e[7]));
  float s1 = ((e[8] + e[9]) + (e[10] + e[11])) + ((e[12] + e[13]) + (e[14] + e[15]));
  s0 = __builtin_amdgcn_rcpf(xhalf_add(s0)); s1 = __builtin_amdgcn_rcpf(xhalf_add(s1));
#pragma unroll
  for (int k = 0; k < 4; ++k) { o0[k] = pk2(e[2 * k] * s0, e[2 * k + 1] * s0); o1[k] = pk2(e[8 + 2 * k] * s1, e[8 + 2 * k + 1] * s1); }
}
DI v4u wphi_frag(int hd, int lane) {
  const int hh = lane >> 5, q = (lane >> 2) & 3, pp = lane & 3;
  const unsigned a = L_WPHI + (unsigned)hd * 512u + (unsigned)(4 * hh + q) * 32u + (unsigned)pp * 8u;
  v4u f = tr2(a, a + 256u);
  const unsigned sg = (lane & 16) ? 0x80008000u : 0u;
  f.x ^= sg; f.y ^= sg; f.z ^= sg; f.w ^= sg;
  return f;
}
DI v4u selv(bool s, v4u a, v4u b) { const unsigned m = s ? 0xffffffffu : 0u; return v4u{(a.x & m) | (b.x & ~m), (a.y & m) | (b.y & ~m), (a.z & m) | (b.z & ~m), (a.w & m) | (b.w & ~m)}; }
DI v4u pack8(const float* y) { return v4u{pk2(y[0], y[1]), pk2(y[2], y[3]), pk2(y[4], y[5]), pk2(y[6], y[7])}; }

constexpr int NWI = MAP2 ? 18 : 9;
template <int NI> DI void warm_w2(const Params& P, int ct, int ja, int tq, int i0, int lane, v4u* d) {
#pragma unroll
  for (int i = 0; i < NI; ++i) {
    const int row = 32 * NWI * ja + 8 * NWI * tq + 8 * (i0 + i);
    const u64 a = row < 1792 ? (u64)(size_t)P.w2 + (u64)(unsigned)row * 1024u : (u64)(size_t)P.wo + (u64)(unsigned)(row - 1792) * 1024u;
    const u64 base = ((u64)(unsigned)__builtin_amdgcn_readfirstlane((unsigned)(a >> 32)) << 32) | (unsigned)__builtin_amdgcn_readfirstlane((unsigned)a);
    const unsigned voff = (unsigned)(lane >> 3) * 1024u + (unsigned)(lane & 7) * 16u + (unsigned)ct * 128u;
    asm volatile("s_nop 4\n\tglobal_load_dwordx4 %0, %1, %2" : "+v"(d[i % 3]) : "v"(voff), "s"(base) : "memory");
  }
}
struct Ctx {
  int lane, tq, hh;
  int p, gi, G;
  int rot;
  int tt, ct, ja, jb;
  int rotE, rotL, rotO;
  bool ffn;
};

DI void role_a(const Params& P, const Ctx& c, int wid, u64 amp_a, u64 amp_b) {
  const int lane = c.lane, tq = c.tq, hh = c.hh, t = lane & 31;
  const int cb = (lane >> 4) & 1, q = (lane >> 2) & 3, pp = lane & 3, sw = (q >> 1) & 1;
  unsigned tabB, tabWlo, tabWhi;
  {
    const int jj = lane < 36 ? lane : 35;
    constexpr int J2 = ORD ? 24 : 28;
    int sg;
    auto perm = [&](int i, int n) AI {
      if (!ROT2) return i;
      const int b = c.jb;
      if (i == 0) return b;
      const int m = (i - 1) >> 2, r = (i - 1) & 3;
      if (m < n - 1) return r == 0 ? 4 * (m + 1) + b : 4 * m + ((b + r) & 3);
      return 4 * m + ((b + 1 + r) & 3);
    };
    if (jj < 16) sg = 4 * perm(jj, 4) + tq;
    else if ((jj < J2) != (ORD != 0)) sg = 64 + 4 * perm(jj - (ORD ? 24 : 16), 3) + tq;
    else sg = 112 + 4 * perm(jj - (ORD ? 16 : 28), 2) + tq;
    int row;
    if (sg < 64) { const int g = sg >> 3; row = 224 * g + 32 * (g & 1) + 16 * (sg & 7); }
    else if (sg < 112) {
      const int e = sg - 64, g = e / 6, i = e - 6 * g;
      row = i < 4 ? 224 * g + 32 * (g & 1) + 128 + 16 * i : ((g & 1) ? 224 * g : 224 * g + 192) + 16 * (i - 4);
    } else row = 16 * (sg - 112);
    const u64 a = (u64)(size_t)(sg < 112 ? P.w2 : P.wo) + (u64)row * 1024u + (u64)c.ct * 128u;
    tabB = (unsigned)sg * 1024u; tabWlo = (unsigned)a; tabWhi = (unsigned)(a >> 32);
  }
  asm volatile("" : "+v"(tabB), "+v"(tabWlo), "+v"(tabWhi));
  const int th = tq & 1, up = tq >> 1;
  unsigned xa[4];
#pragma unroll
  for (int ks = 0; ks < 4; ++ks) xa[ks] = (unsigned)(64 * th + t) * 128u + (unsigned)(((2 * ks + hh) ^ ((t >> 1) & 7)) * 16);
  const unsigned wl = 16384u + (unsigned)(8 * hh + q) * 128u + (unsigned)((4 * (up ^ sw) + 2 * cb + (pp >> 1)) * 16 + 8 * (pp & 1));
  f32x16 acc[2][2];
#pragma unroll
  for (int i = 0; i < 2; ++i) { acc[i][0] = zero16(); acc[i][1] = zero16(); }
  f32x4 cs = {0.f, 0.f, 0.f, 0.f};
  unsigned x0 = xa[0], x1 = xa[1], x2 = xa[2], x3 = xa[3], w0 = wl;
  v4u xf[4][2], wf[4][2];
  auto rd = [&](int ks, v4u* X, v4u* W) AI {
    const unsigned xb = ks == 0 ? x0 : (ks == 1 ? x1 : (ks == 2 ? x2 : x3));
    X[0] = lds128(xb); X[1] = lds128(xb + 4096u);
    const unsigned a = w0 + (unsigned)(ks * 2048);
    W[0] = tr2(a, a + 512u); W[1] = tr2(a + 8192u, a + 8704u);
  };
  f32x4 cs2 = {0.f, 0.f, 0.f, 0.f};
  BAR();
  rd(0, xf[0], wf[0]); rd(1, xf[1], wf[1]);
  auto pass0 = [&](auto TH) AI {
    constexpr int CT = decltype(TH)::value;
    const s16x4 one = {0x3F80, 0x3F80, 0x3F80, 0x3F80};
#pragma unroll 1
    for (int kc = 0; kc < 8; ++kc) {
#pragma unroll
      for (int ks = 0; ks < 4; ++ks) {
        if (ks == 2) {
          __builtin_amdgcn_sched_barrier(0);
          asm volatile("s_barrier" ::: "memory");
          __builtin_amdgcn_sched_barrier(0);
          const unsigned sb = (unsigned)((kc + 1) & 3) * SLOT;
          x0 = xa[0] + sb; x1 = xa[1] + sb; w0 = wl + sb;
          asm volatile("" : "+v"(x0), "+v"(x1), "+v"(w0));
        }
        if (ks == 0) {
          const unsigned sb = (unsigned)(kc & 3) * SLOT;
          x2 = xa[2] + sb; x3 = xa[3] + sb;
          asm volatile("" : "+v"(x2), "+v"(x3));
        }
        rd((ks + 2) & 3, xf[(ks + 2) & 3], wf[(ks + 2) & 3]);
        acc[0][0] = mfma(wf[ks][0], xf[ks][0], acc[0][0]); acc[0][1] = mfma(wf[ks][0], xf[ks][1], acc[0][1]);
        acc[1][0] = mfma(wf[ks][1], xf[ks][0], acc[1][0]); acc[1][1] = mfma(wf[ks][1], xf[ks][1], acc[1][1]);
        cs = __builtin_amdgcn_mfma_f32_4x4x4bf16_1k(one, __builtin_bit_cast(s16x4, v2u{wf[ks][CT].x, wf[ks][CT].y}), cs, 0, 0, 0);
        cs2 = __builtin_amdgcn_mfma_f32_4x4x4bf16_1k(one, __builtin_bit_cast(s16x4, v2u{wf[ks][CT].z, wf[ks][CT].w}), cs2, 0, 0, 0);
        __builtin_amdgcn_sched_group_barrier(0x008, 1, 0);
        __builtin_amdgcn_sched_group_barrier(0x100, 2, 0);
        __builtin_amdgcn_sched_group_barrier(0x008, 1, 0);
        __builtin_amdgcn_sched_group_barrier(0x100, 2, 0);
        __builtin_amdgcn_sched_group_barrier(0x008, 1, 0);
        __builtin_amdgcn_sched_group_barrier(0x100, 2, 0);
        __builtin_amdgcn_sched_group_barrier(0x008, 3, 0);
        __builtin_amdgcn_sched_barrier(0);
      }
      asm volatile("" : "+v"(acc[0][0]), "+v"(acc[0][1]), "+v"(acc[1][0]), "+v"(acc[1][1]), "+v"(cs), "+v"(cs2));
    }
  };
  if (th) pass0(std::integral_constant<int, 1>{}); else pass0(std::integral_constant<int, 0>{});
  cs[0] += cs2[0];
  STAMP(2);
  {
    const float s = xhalf_add(cs[0]);
    if (hh == 0) *(LAS float*)(size_t)(L_CS0 + (unsigned)(up + 2 * th) * 128u + (unsigned)t * 4u) = s;
  }
  BAR();
  const u64 tag = lds_tag();
  float rstd[2], mr[2];
#pragma unroll
  for (int b = 0; b < 2; ++b) {
    const f32x2 st = *(const LAS f32x2*)(size_t)(L_STATS + (unsigned)(64 * th + 32 * b + t) * 8u);
    rstd[b] = st.x; mr[b] = st.y;
  }
  const __amdgpu_buffer_rsrc_t rsHB = __builtin_amdgcn_make_buffer_rsrc(P.ws + WS_HB, 0, HB_BYTES, 0x00020000);
  v4u wm[3] = {v4u{0u, 0u, 0u, 0u}, v4u{0u, 0u, 0u, 0u}, v4u{0u, 0u, 0u, 0u}};
  if (c.ffn) {
    f32x4 csv[2][4], bsv[2][4];
#pragma unroll
    for (int i = 0; i < 2; ++i) { load16(L_CS0 + (unsigned)(up + 2 * i) * 128u, hh, csv[i]); load16(L_B1 + (unsigned)(up + 2 * i) * 128u, hh, bsv[i]); }
#pragma unroll
    for (int i = 0; i < 2; ++i)
#pragma unroll
      for (int b = 0; b < 2; ++b) {
        const unsigned tile = (unsigned)(up + 2 * i), T32 = (unsigned)(c.p * 4 + 2 * th + b);
        ffn_tile(acc[i][b], rstd[b], mr[b], csv[i], bsv[i], rsHB, (unsigned)lane * 16u + (T32 * 144u + (unsigned)(8 * c.gi) + 2u * tile) * 1024u);
        if (WARM2 && 2 * i + b < 3) warm_w2<NWI / 3>(P, c.ct, c.ja, tq, (NWI / 3) * (2 * i + b), lane, wm);
      }
    STAMP(8);
    publish_last(L_CNT + C_EARLY * 4, 4, P.ws, FL_E + (unsigned)(c.p * 8 + c.gi) * 128u, tag, lane);
    if (WARM2) asm volatile("" :: "v"(wm[0]), "v"(wm[1]), "v"(wm[2]));
  } else {
#pragma unroll
    for (int b = 0; b < 2; ++b)
#pragma unroll
      for (int hf = 0; hf < 2; ++hf) {
        const int hd = 2 * up + hf;
        float y[8];
        fold8(acc[0][b], 8 * hf, rstd[b], mr[b], L_CS0 + (unsigned)up * 128u, hh, y);
        v4u o0, o1;
        hedge(mfma(wphi_frag(hd, lane), pack8(y), zero16()), o0, o1);
        const unsigned img = (unsigned)hd * SLOT + 16384u;
        const unsigned row = (unsigned)(64 * th + 32 * b + t);
        const unsigned g = (row >> 1) & 7u;
        *(LAS v2u*)(size_t)(img + row * 64u + (((unsigned)(0 + hh)) ^ g) * 8u) = v2u{o0.x, o0.y};
        *(LAS v2u*)(size_t)(img + row * 64u + (((unsigned)(2 + hh)) ^ g) * 8u) = v2u{o0.z, o0.w};
        *(LAS v2u*)(size_t)(img + row * 64u + (((unsigned)(4 + hh)) ^ g) * 8u) = v2u{o1.x, o1.y};
        *(LAS v2u*)(size_t)(img + row * 64u + (((unsigned)(6 + hh)) ^ g) * 8u) = v2u{o1.z, o1.w};
        fold8(acc[1][b], 8 * hf, rstd[b], mr[b], L_CS0 + (unsigned)(2 + up) * 128u, hh, y);
        const unsigned gv = (row >> 2) & 3u;
        *(LAS v2u*)(size_t)(img + 8192u + row * 32u + (((unsigned)(0 + hh)) ^ gv) * 8u) = v2u{pk2(y[0], y[1]), pk2(y[2], y[3])};
        *(LAS v2u*)(size_t)(img + 8192u + row * 32u + (((unsigned)(2 + hh)) ^ gv) * 8u) = v2u{pk2(y[4], y[5]), pk2(y[6], y[7])};
        if (WARM2 && 2 * b + hf < 3) warm_w2<NWI / 3>(P, c.ct, c.ja, tq, (NWI / 3) * (2 * b + hf), lane, wm);
      }
    STAMP(8);
    asm volatile("s_waitcnt lgkmcnt(0)" ::: "memory");
    lds_inc(L_CNT + C_IMG * 4, lane);
    lds_wait_ge(L_CNT + C_IMG * 4, 4);
    const unsigned img = (unsigned)tq * SLOT + 16384u;
    f32x16 kv = zero16();
    unsigned pa[2], va[2];
#pragma unroll
    for (int i2 = 0; i2 < 2; ++i2) {
      const unsigned r = (unsigned)(8 * hh + 4 * i2 + q);
      pa[i2] = img + r * 64u + (((unsigned)(4 * cb + pp)) ^ ((r >> 1) & 7u)) * 8u;
      va[i2] = img + 8192u + r * 32u + (((unsigned)pp) ^ ((r >> 2) & 3u)) * 8u;
    }
#pragma unroll
    for (int ks = 0; ks < 8; ++ks) {
      const v4u af = tr2(pa[0] + (unsigned)ks * 1024u, pa[1] + (unsigned)ks * 1024u);
      v4u bf = tr2(va[0] + (unsigned)ks * 512u, va[1] + (unsigned)ks * 512u);
      if (cb) bf = v4u{ONES, ONES, ONES, ONES};
      kv = mfma(af, bf, kv);
    }
    asm volatile("" : "+v"(kv) :: "memory");
    lds_inc(L_CNT + C_IMGDONE * 4, lane);
    const unsigned hidx = (unsigned)(c.gi * 4 + tq) * 16u + (unsigned)c.p;
    const __amdgpu_buffer_rsrc_t rsKV = __builtin_amdgcn_make_buffer_rsrc(P.ws + WS_KVP, 0, 524288 + 65536, 0x00020000);
    if (cb == 0) {
#pragma unroll
      for (int s = 0; s < 2; ++s) {
        const v4u o = v4u{pk2(kv[8 * s], kv[8 * s + 1]), pk2(kv[8 * s + 2], kv[8 * s + 3]), pk2(kv[8 * s + 4], kv[8 * s + 5]), pk2(kv[8 * s + 6], kv[8 * s + 7])};
        st128(o, rsKV, (unsigned)((16 * s + (lane & 15) + 32 * hh) * 16) + hidx * 1024u);
      }
    } else if ((lane & 15) == 0) {
#pragma unroll
      for (int g4 = 0; g4 < 4; ++g4) {
        const v4u o = v4u{f2u(kv[4 * g4]), f2u(kv[4 * g4 + 1]), f2u(kv[4 * g4 + 2]), f2u(kv[4 * g4 + 3])};
        st128(o, rsKV, (unsigned)((8 * g4 + 4 * hh) * 4) + 524288u + hidx * 128u);
      }
    }
    publish_last(L_CNT + C_KV * 4, 4, P.ws, FL_K + (unsigned)(c.gi * 16 + c.p) * 128u, tag, lane);
    if (WARM2) asm volatile("" :: "v"(wm[0]), "v"(wm[1]), "v"(wm[2]));
  }
  STAMP(3);
  const unsigned vw0 = (unsigned)(lane >> 3) * 1024u + (unsigned)(((lane & 7) ^ (((lane >> 4) & 1) << 2)) * 16), vw1 = vw0 + 8192u;
  const unsigned ring = (unsigned)tq * 8192u;
  auto pofs = [&](int r) AI { return (unsigned)((r >> 2) & 3) * SLOT + (unsigned)(r & 3) * 2048u; };
  auto wdma = [&](int j) AI {
    const unsigned lo = __builtin_amdgcn_readlane(tabWlo, j), hi = __builtin_amdgcn_readlane(tabWhi, j);
    const u64 b = ((u64)hi << 32) | lo;
    const unsigned m = ring + pofs(j);
    asm volatile("s_nop 4\n\ts_mov_b32 m0, %0\n\ts_nop 0\n\tglobal_load_lds_dwordx4 %1, %3\n\ts_add_u32 m0, m0, 0x400\n\ts_nop 0\n\tglobal_load_lds_dwordx4 %2, %3"
                 ::"s"(m), "v"(vw0), "v"(vw1), "s"(b) : "memory", "scc");
  };
  const unsigned vb0 = (unsigned)lane * 16u + (unsigned)(2 * c.tt) * 144u * 1024u, vb1 = vb0 + 144u * 1024u;
  constexpr int NB = NBK;
  v4u br[NB][2];
  auto bload0 = [&](unsigned so, v4u& d0, v4u& d1) AI {
    asm volatile("s_nop 4\n\tbuffer_load_dwordx4 %0, %2, %4, %5 offen sc1\n\tbuffer_load_dwordx4 %1, %3, %4, %5 offen sc1"
                 : "=&v"(d0), "=&v"(d1) : "v"(vb0), "v"(vb1), "s"(rsHB), "s"(so) : "memory");
  };
  auto bload = [&](unsigned so, v4u& d0, v4u& d1) AI {
    asm volatile("s_nop 4\n\tbuffer_load_dwordx4 %0, %2, %4, %5 offen sc1\n\tbuffer_load_dwordx4 %1, %3, %4, %5 offen sc1"
                 : "+v"(d0), "+v"(d1) : "v"(vb0), "v"(vb1), "s"(rsHB), "s"(so) : "memory");
  };
  if (!c.ffn && tq >= 2) lds_wait_ge(L_CNT + C_IMGDONE * 4, 4);
  const int pan = c.tt >> 1;
  v2u ef;
  const char* efp = P.ws + FL_E + (unsigned)pan * 8u * 128u + (unsigned)(lane & 7) * 128u;
#pragma unroll 1
  for (int s = 0; s < 2; ++s) {
    lds_wait_ge(L_CNT + (C_FREE + s) * 4, 4);
#pragma unroll 1
    for (int j = 4 * s; j < 4 * s + 4; ++j) wdma(j);
  }
  lds_wait_ge(L_CNT + (C_FREE + 2) * 4, 4);
  asm volatile("global_load_dwordx2 %0, %1, off sc1" : "=&v"(ef) : "v"(efp) : "memory");
  if (NB == 4) { wdma(8); wdma(9); }
  STAMP(4);
  if (NB == 4) asm volatile("s_waitcnt vmcnt(4)" : "+v"(ef) :: "memory"); else asm volatile("s_waitcnt vmcnt(0)" : "+v"(ef) :: "memory");
  if (__builtin_amdgcn_ballot_w64(ef.x != (unsigned)tag || ef.y != (unsigned)(tag >> 32)) != 0) poll_flags(P.ws, FL_E + (unsigned)pan * 8u * 128u, 8, tag, lane);
  if (KVFIRST && !c.ffn) lds_wait_ge(L_CNT + C_KVIN * 4, 4);
  STAMP(5);
  bload0(__builtin_amdgcn_readlane(tabB, 0), br[0][0], br[0][1]);
  bload0(__builtin_amdgcn_readlane(tabB, 1), br[1][0], br[1][1]);
#pragma unroll
  for (int u = 2; u < NB; ++u) {
    wdma(12 - NB + u);
    bload0(__builtin_amdgcn_readlane(tabB, u), br[u][0], br[u][1]);
  }
  unsigned w2l[2];
#pragma unroll
  for (int h = 0; h < 2; ++h) w2l[h] = ring + (unsigned)(4 * hh + q) * 128u + (unsigned)((4 * (h ^ sw) + 2 * cb + (pp >> 1)) * 16 + 8 * (pp & 1));
  f32x16 o[2][2];
#pragma unroll
  for (int a = 0; a < 2; ++a)
#pragma unroll
    for (int b = 0; b < 2; ++b) o[a][b] = zero16();
  v4u af[2];
  if (NB == 4) asm volatile("s_waitcnt vmcnt(10)" : "+v"(br[0][0]), "+v"(br[0][1]) :: "memory");
  else asm volatile("s_waitcnt vmcnt(18)" : "+v"(br[0][0]), "+v"(br[0][1]) :: "memory");
  af[0] = tr2(w2l[0], w2l[0] + 1024u); af[1] = tr2(w2l[1], w2l[1] + 1024u);
  auto step = [&](int j, int u, bool refill, bool first) AI {
    v4u an[2];
    if (NB == 4) {
      if (first) asm volatile("s_waitcnt vmcnt(8)" : "+v"(br[(u + 1) % NB][0]), "+v"(br[(u + 1) % NB][1]) :: "memory");
      else asm volatile("s_waitcnt vmcnt(4)" : "+v"(br[(u + 1) % NB][0]), "+v"(br[(u + 1) % NB][1]) :: "memory");
    } else {
      if (first) asm volatile("s_waitcnt vmcnt(16)" : "+v"(br[(u + 1) % NB][0]), "+v"(br[(u + 1) % NB][1]) :: "memory");
      else asm volatile("s_waitcnt vmcnt(8)" : "+v"(br[(u + 1) % NB][0]), "+v"(br[(u + 1) % NB][1]) :: "memory");
    }
    unsigned p0 = w2l[0] + pofs(j + 1), p1 = w2l[1] + pofs(j + 1);
    asm volatile("" : "+v"(p0), "+v"(p1));
    an[0] = tr2(p0, p0 + 1024u); an[1] = tr2(p1, p1 + 1024u);
    o[0][0] = mfma(af[0], br[u][0], o[0][0]); o[0][1] = mfma(af[0], br[u][1], o[0][1]);
    o[1][0] = mfma(af[1], br[u][0], o[1][0]); o[1][1] = mfma(af[1], br[u][1], o[1][1]);
    asm volatile("" : "+v"(o[0][0]), "+v"(o[0][1]), "+v"(o[1][0]), "+v"(o[1][1]) :: "memory");
    if (refill) wdma(j + 12);
    const int jn = j + NB;
    constexpr int J3 = ORD ? 24 : 28;
    if (u == (16 - NB) % NB && jn == 16) lds_wait_ge(L_CNT + (ORD ? C_ORDY : C_LATERDY) * 4, 1);
    if (u == (J3 - NB) % NB && jn == J3) lds_wait_ge(L_CNT + (ORD ? C_LATERDY : C_ORDY) * 4, 1);
    bload(jn < 36 ? (unsigned)__builtin_amdgcn_readlane(tabB, jn) : 0x7F000000u, br[u][0], br[u][1]);
    af[0] = an[0]; af[1] = an[1];
  };
  lds_wait_ge(L_CNT + (C_FREE + 3) * 4, 4);
#pragma unroll 1
  for (int it = 0; it < 24 / NB; ++it) {
    if (it == 12 / NB) STAMP(10);
#pragma unroll
    for (int u = 0; u < NB; ++u) step(NB * it + u, u, true, true);
  }
  STAMP(11);
#pragma unroll 1
  for (int it = 24 / NB; it < 36 / NB; ++it) {
#pragma unroll
    for (int u = 0; u < NB; ++u) step(NB * it + u, u, false, false);
  }
  asm volatile("s_waitcnt vmcnt(0)" : "+v"(br[0][0]), "+v"(br[0][1]), "+v"(br[1][0]), "+v"(br[1][1]), "+v"(br[2][0]), "+v"(br[2][1]), "+v"(br[3][0]), "+v"(br[3][1]) :: "memory");
  asm volatile("" : "+v"(br[NB - 2][0]), "+v"(br[NB - 2][1]), "+v"(br[NB - 1][0]), "+v"(br[NB - 1][1]) :: "memory");
  STAMP(6);
#pragma unroll
  for (int a = 0; a < 2; ++a)
#pragma unroll
    for (int b = 0; b < 2; ++b)
#pragma unroll
      for (int g4 = 0; g4 < 4; ++g4)
        *(LAS f32x4*)(size_t)(ring + (unsigned)b * SLOT + (unsigned)t * 256u + (unsigned)((8 * a + 2 * g4 + hh) ^ (t & 15)) * 16u) =
            f32x4{o[a][b][4 * g4], o[a][b][4 * g4 + 1], o[a][b][4 * g4 + 2], o[a][b][4 * g4 + 3]};
  VMW(0);
  BAR();
  STAMP(7);
}

template <int NT, int NF, class Fill> DI void pass1(const Params& P, int wid, u64& amp_a, u64& amp_b, Fill& fill, const v4u (&xr)[32], f32x16 (&acc)[NT], f32x4& cs, const unsigned (&wb)[NT], int lane) {
  const s16x4 one = {0x3F80, 0x3F80, 0x3F80, 0x3F80};
  f32x4 csa = {0.f, 0.f, 0.f, 0.f}, csb = csa;
#pragma unroll
  for (int qq = 0; qq < 4; ++qq) {
    if (qq > 0) lds_wait_ge(L_CNT + (C_LAND + qq) * 4, 4);
    if (qq < 2) fill(10 + qq);
    unsigned a[NT];
#pragma unroll
    for (int i = 0; i < NT; ++i) { a[i] = wb[i] + (unsigned)qq * SLOT; asm volatile("" : "+v"(a[i])); }
    v4u f[3][NT];
    auto rd = [&](int k8, v4u* W) AI {
      const unsigned o = (unsigned)((k8 >> 2) * 8192 + (k8 & 3) * 2048);
#pragma unroll
      for (int i = 0; i < NT; ++i) W[i] = tr2(a[i] + o, a[i] + o + 512u);
    };
    rd(0, f[0]); rd(1, f[1]);
    __builtin_amdgcn_sched_barrier(0);
#pragma unroll
    for (int k8 = 0; k8 < 8; ++k8) {
      if (k8 == 4 && qq < 3) {
        if (qq < 2) { if (NF == 8) VMW(8); else VMW(4); } else VMW(0);
        lds_inc(L_CNT + (C_LAND + qq + 1) * 4, lane);
      }
      if (k8 < 6) rd(k8 + 2, f[(k8 + 2) % 3]);
      const v4u xf = xr[8 * qq + k8];
#pragma unroll
      for (int i = 0; i < NT; ++i) acc[i] = mfma(f[k8 % 3][i], xf, acc[i]);
      csa = __builtin_amdgcn_mfma_f32_4x4x4bf16_1k(one, __builtin_bit_cast(s16x4, v2u{f[k8 % 3][0].x, f[k8 % 3][0].y}), csa, 0, 0, 0);
      csb = __builtin_amdgcn_mfma_f32_4x4x4bf16_1k(one, __builtin_bit_cast(s16x4, v2u{f[k8 % 3][0].z, f[k8 % 3][0].w}), csb, 0, 0, 0);
      if (k8 < 6) {
#pragma unroll
        for (int i = 0; i < NT; ++i) {
          __builtin_amdgcn_sched_group_barrier(0x008, 1, 0);
          __builtin_amdgcn_sched_group_barrier(0x100, 2, 0);
        }
        __builtin_amdgcn_sched_group_barrier(0x008, 2, 0);
      }
      __builtin_amdgcn_sched_barrier(0);
    }
    asm volatile("" : "+v"(acc[0]), "+v"(acc[1]), "+v"(acc[NT - 1]), "+v"(csa), "+v"(csb) :: "memory");
    lds_inc(L_CNT + (C_FREE + qq) * 4, lane);
  }
  cs[0] = csa[0] + csb[0];
}

template <bool ffn> DI u64 role_b_head(const Params& P, const Ctx& c, int wid) {
  u64 amp_a = 0, amp_b = 0; (void)amp_a; (void)amp_b;
  const int lane = c.lane, tq = c.tq, hh = c.hh, t = lane & 31;
  const int g = c.gi;
  const unsigned tok = (unsigned)(c.p * 128 + 8 * c.G + 2 * tq);
  v4u r0, r1;
  {
    const u16* rp = P.x + (size_t)tok * 512 + lane * 8;
    asm volatile("global_load_dwordx4 %0, %2, off\n\tglobal_load_dwordx4 %1, %2, off offset:1024" : "=&v"(r0), "=&v"(r1) : "v"(rp) : "memory");
  }
  const __amdgpu_buffer_rsrc_t rsX = __builtin_amdgcn_make_buffer_rsrc((void*)P.x, 0, 2048 * 1024, 0x00020000);
  const __amdgpu_buffer_rsrc_t rsW = __builtin_amdgcn_make_buffer_rsrc((void*)(ffn ? P.w1 : P.wqkv), 0, ffn ? 512 * 3584 : 512 * 3072, 0x00020000);
  constexpr unsigned RS = ffn ? 3584u : 3072u;
  const unsigned cA = (unsigned)(224 * g + 32 * (g & 1));
  const unsigned u0 = ffn ? cA * 2u : (unsigned)(512 + 64 * g) * 2u, u1 = ffn ? cA * 2u + 128u : (unsigned)(1024 + 64 * g) * 2u;
  const unsigned ua = ffn ? (cA + 128u) * 2u : (unsigned)(64 * g) * 2u;
  const unsigned ub = (unsigned)((g & 1) ? 224 * g - 32 : 224 * g + 192) * 2u;
  const unsigned voffX = (unsigned)(lane >> 3) * 1024u + (unsigned)(((lane & 7) ^ ((4 * (tq & 1) + (lane >> 4)) & 7)) * 16);
  const unsigned voffW = (unsigned)(lane >> 3) * RS + (unsigned)(((lane & 7) ^ (((lane >> 4) & 1) << 2)) * 16);
  const unsigned soffX = (unsigned)(c.p * 128 + 8 * tq) * 1024u;
  const unsigned wrow = 32u * (unsigned)(tq & 1) * RS;
  auto fill = [&](int f) AI {
    const unsigned slot = (unsigned)(f & 3) * SLOT;
    if (f < 8) {
      const unsigned kcp = (unsigned)((f + c.rot) & 7);
      unsigned sx = soffX + kcp * 128u, sw = 64u * kcp * RS + wrow + ((tq >> 1) ? u1 : u0);
      unsigned mx = slot + (unsigned)tq * 1024u, mw = slot + 16384u + (unsigned)tq * 4096u;
#pragma unroll 1
      for (int m = 0; m < 4; ++m) {
        dma(mx, voffX, rsX, sx); dma(mw, voffW, rsW, sw);
        mx += 4096u; sx += 32768u; mw += 1024u; sw += 8u * RS;
      }
    } else {
      const unsigned kcp = (unsigned)((2 * (f - 8) + (tq >> 1) + c.rot) & 7);
      unsigned sw = 64u * kcp * RS + wrow, mw = slot + (unsigned)tq * 4096u;
#pragma unroll 1
      for (int m = 0; m < 4; ++m) {
        dma(mw, voffW, rsW, sw + ua);
        if (ffn) dma(mw + 16384u, voffW, rsW, sw + ub);
        mw += 1024u; sw += 8u * RS;
      }
    }
  };
  fill(0);
  fill(1);
  unsigned xa[4];
#pragma unroll
  for (int ks = 0; ks < 4; ++ks) xa[ks] = (unsigned)(32 * tq + t) * 128u + (unsigned)(((2 * ks + hh) ^ ((t >> 1) & 7)) * 16);
  v4u xr[32];
  u64 tag = 0;
  v4u gr;
  const unsigned token = (unsigned)(c.p * 128 + 32 * tq + t);
#pragma unroll
  for (int kc = 0; kc < 8; ++kc) {
    if (kc < 7 || ffn) VMW(8); else VMW(4);
    BAR();
    if (kc == 0) tag = lds_tag();
    if (kc == 5) {
      const char* gp = P.ws + WS_ST + token * 16u;
      asm volatile("global_load_dwordx4 %0, %1, off sc1" : "=&v"(gr) : "v"(gp) : "memory");
    }
    fill(kc + 2);
    const unsigned sb = (unsigned)(kc & 3) * SLOT;
#pragma unroll
    for (int ks = 0; ks < 4; ++ks) xr[4 * kc + ks] = lds128(xa[ks] + sb);
    asm volatile("" : "+v"(xr[4 * kc]), "+v"(xr[4 * kc + 1]), "+v"(xr[4 * kc + 2]), "+v"(xr[4 * kc + 3]));
    if (kc == 1) {
  float st0;
  {
    asm volatile("" : "+v"(r0), "+v"(r1));
    float v[2][8], mean[2], rs[2];
#pragma unroll
    for (int k = 0; k < 4; ++k) {
      v[0][2 * k] = u2f(r0[k] << 16); v[0][2 * k + 1] = u2f(r0[k] & 0xffff0000u);
      v[1][2 * k] = u2f(r1[k] << 16); v[1][2 * k + 1] = u2f(r1[k] & 0xffff0000u);
    }
#pragma unroll
    for (int i = 0; i < 2; ++i) mean[i] = wavesum(((v[i][0] + v[i][1]) + (v[i][2] + v[i][3])) + ((v[i][4] + v[i][5]) + (v[i][6] + v[i][7]))) * (1.0f / 512.0f);
#pragma unroll
    for (int i = 0; i < 2; ++i) {
      float q2 = 0.f;
#pragma unroll
      for (int k = 0; k < 8; ++k) { const float d = v[i][k] - mean[i]; q2 = __builtin_fmaf(d, d, q2); }
      rs[i] = __builtin_amdgcn_rsqf(wavesum(q2) * (1.0f / 512.0f) + 1e-5f);
    }
    st0 = (lane & 2) ? rs[1] : rs[0];
    st0 = (lane & 1) ? -((lane & 2) ? mean[1] : mean[0]) * st0 : st0;
    asm volatile("" : "+v"(st0));
  }
      if (lane < 4)
        __hip_atomic_store((GAS u64*)(P.ws + WS_ST + tok * 16u + (unsigned)lane * 8u), ((u64)(unsigned)tag << 32) | f2u(st0), __ATOMIC_RELAXED, __HIP_MEMORY_SCOPE_AGENT);
    }
  }
  asm volatile("" : "+v"(gr));
  STAMP(2);
  BAR();
  if (__builtin_amdgcn_ballot_w64(gr.y != (unsigned)tag || gr.w != (unsigned)tag) != 0) {
    const __amdgpu_buffer_rsrc_t rs = __builtin_amdgcn_make_buffer_rsrc((void*)(P.ws + WS_ST), 0, 32768, 0x00020000);
#pragma unroll 1
    for (unsigned spin = 0; spin < kSpin; ++spin) {
      gr = __builtin_amdgcn_raw_buffer_load_b128(rs, token * 16u, 0, 16);
      if (__builtin_amdgcn_ballot_w64(gr.y != (unsigned)tag || gr.w != (unsigned)tag) == 0) break;
      __builtin_amdgcn_s_sleep(1);
    }
  }
  const float rstd = u2f(gr.x), mr = u2f(gr.z);
  if (hh == 0) *(LAS f32x2*)(size_t)(L_STATS + (unsigned)(32 * tq + t) * 8u) = f32x2{rstd, mr};
  if (ffn) VMW(8); else VMW(4);
  BAR();
  const int cb = (lane >> 4) & 1, q = (lane >> 2) & 3, pp = lane & 3, sw = (q >> 1) & 1;
  unsigned wl[2];
#pragma unroll
  for (int h = 0; h < 2; ++h) wl[h] = (unsigned)(8 * hh + q) * 128u + (unsigned)((4 * (h ^ sw) + 2 * cb + (pp >> 1)) * 16 + 8 * (pp & 1));
  const __amdgpu_buffer_rsrc_t rsHB = __builtin_amdgcn_make_buffer_rsrc(P.ws + WS_HB, 0, HB_BYTES, 0x00020000);
  const unsigned T32 = (unsigned)(c.p * 4 + tq);
  f32x4 cs = {0.f, 0.f, 0.f, 0.f};
  auto cs_sync = [&](int tile, bool wr) AI {
    const float s = xhalf_add(cs[0]);
    if (hh == 0 && wr) *(LAS float*)(size_t)(L_CS1 + (unsigned)tile * 128u + (unsigned)t * 4u) = s;
    asm volatile("s_waitcnt lgkmcnt(0)" ::: "memory");
    lds_inc(L_CNT + C_CS1 * 4, lane);
    lds_wait_ge(L_CNT + C_CS1 * 4, 4);
  };
  if constexpr (ffn) {
    const unsigned w2a = ((g & 1) ? wl[1] : wl[0]) + 16384u;
    int ti[3];
    unsigned wb[3];
    f32x16 acc[3];
#pragma unroll
    for (int i = 0; i < 3; ++i) { ti[i] = (i + tq) % 3; wb[i] = ti[i] == 0 ? wl[0] : (ti[i] == 1 ? wl[1] : w2a); acc[i] = zero16(); }
    pass1<3, 8>(P, wid, amp_a, amp_b, fill, xr, acc, cs, wb, lane);
    cs_sync(ti[0], tq < 3);
    STAMP(3);
    if (PRIOL) __builtin_amdgcn_s_setprio(3);
    f32x4 csv[3][4], bsv[3][4];
#pragma unroll
    for (int i = 0; i < 3; ++i) { load16(L_CS1 + (unsigned)ti[i] * 128u, hh, csv[i]); load16(L_B1 + (unsigned)(4 + ti[i]) * 128u, hh, bsv[i]); }
#pragma unroll
    for (int i = 0; i < 3; ++i)
      ffn_tile(acc[i], rstd, mr, csv[i], bsv[i], rsHB, (unsigned)lane * 16u + (T32 * 144u + (unsigned)(64 + 6 * g + 2 * ti[i])) * 1024u);
    publish_last(L_CNT + C_LATE * 4, 4, P.ws, FL_L + (unsigned)(c.p * 8 + g) * 128u, tag, lane);
    if (PRIOL) __builtin_amdgcn_s_setprio(0);
  } else {
    int ti[2];
    unsigned wb[2];
    f32x16 acc[2];
#pragma unroll
    for (int i = 0; i < 2; ++i) { ti[i] = (i + tq) & 1; wb[i] = ti[i] == 0 ? wl[0] : wl[1]; acc[i] = zero16(); }
    pass1<2, 4>(P, wid, amp_a, amp_b, fill, xr, acc, cs, wb, lane);
    cs_sync(ti[0], tq < 2);
    STAMP(3);
    v4u pq[4][2];
#pragma unroll
    for (int hd = 0; hd < 4; ++hd) {
      float y[8];
      fold8(acc[hd >> 1], 8 * (hd & 1), rstd, mr, L_CS1 + (unsigned)ti[hd >> 1] * 128u, hh, y);
      hedge(mfma(wphi_frag(2 * ti[hd >> 1] + (hd & 1), lane), pack8(y), zero16()), pq[hd][0], pq[hd][1]);
    }
    asm volatile("" : "+v"(pq[0][0]), "+v"(pq[0][1]), "+v"(pq[1][0]), "+v"(pq[1][1]), "+v"(pq[2][0]), "+v"(pq[2][1]), "+v"(pq[3][0]), "+v"(pq[3][1]));
    STAMP(4);
    if (PRIO) __builtin_amdgcn_s_setprio(3);
    if (tq == 0) { poll_flags(P.ws, FL_K + (unsigned)g * 16u * 128u, 16, tag, lane); lds_set(L_CNT + C_KVRDY * 4, 1); }
    else lds_wait_ge(L_CNT + C_KVRDY * 4, 1);
    STAMP(5);
    {
      const __amdgpu_buffer_rsrc_t rsKV = __builtin_amdgcn_make_buffer_rsrc(P.ws + WS_KVP, 0, 524288 + 65536, 0x00020000);
      const unsigned hbase = (unsigned)(g * 4 + tq) * 16u;
      v4u pv[16];
#pragma unroll
      for (int i = 0; i < 16; ++i) pv[i] = __builtin_amdgcn_raw_buffer_load_b128(rsKV, (unsigned)lane * 16u, (hbase + (unsigned)i) * 1024u, 16);
      const v4u z0 = __builtin_amdgcn_raw_buffer_load_b128(rsKV, (unsigned)lane * 16u, 524288u + hbase * 128u, 16);
      const v4u z1 = __builtin_amdgcn_raw_buffer_load_b128(rsKV, (unsigned)lane * 16u, 524288u + hbase * 128u + 1024u, 16);
      const u64 lf = __hip_atomic_load((const GAS u64*)(P.ws + FL_L + (unsigned)(c.tt >> 1) * 8u * 128u + (unsigned)(lane & 7) * 128u), __ATOMIC_RELAXED, __HIP_MEMORY_SCOPE_AGENT);
      __builtin_amdgcn_sched_barrier(0);
      v4u idf;
      {
        const int m = lane & 31, h2 = lane >> 5;
        const int j = (m < 16 && ((m >> 2) & 1) == h2) ? (((m >> 3) << 2) | (m & 3)) : -1;
#pragma unroll
        for (int k = 0; k < 4; ++k) idf[k] = (j == 2 * k ? 0x3F80u : 0u) | (j == 2 * k + 1 ? 0x3F800000u : 0u);
      }
      f32x16 sum = zero16();
#pragma unroll
      for (int i = 0; i < 16; ++i) sum = mfma(idf, pv[i], sum);
      asm volatile("" : "+v"(sum));
      lds_inc(L_CNT + C_KVIN * 4, lane);
      if (!ORD && tq == 0 && __builtin_amdgcn_ballot_w64(lf != tag) == 0) lds_set(L_CNT + C_LATERDY * 4, 1);
      const float s[8] = {sum[0], sum[1], sum[2], sum[3], sum[4], sum[5], sum[6], sum[7]};
      const unsigned afr = L_AFR + (unsigned)tq * 2048u;
      *(LAS v4u*)(size_t)(afr + (unsigned)((lane >> 4) & 1) * 1024u + (unsigned)((lane & 15) + 32 * (lane >> 5)) * 16u) = pack8(s);
      const unsigned hh2 = (unsigned)(lane >> 4) & 1u, c16 = (unsigned)lane & 15u;
      float zz[4];
#pragma unroll
      for (int k = 0; k < 4; ++k) {
        float v = dppadd<0x128>(u2f(z0[k]) + u2f(z1[k]));
        auto r = __builtin_amdgcn_permlane16_swap(f2u(v), f2u(v), false, false);
        zz[k] = xhalf_add(u2f(r[0]) + u2f(r[1]));
      }
      if (lane < 8) *(LAS f32x4*)(size_t)(L_ZT + (unsigned)tq * 128u + (unsigned)lane * 16u) = f32x4{zz[0], zz[1], zz[2], zz[3]};
      const unsigned f0 = 16u * (unsigned)(lane >> 5) + 4u * hh2;
      const f32x4 za = lds128f(L_ZT + (unsigned)tq * 128u + f0 * 4u), zb = lds128f(L_ZT + (unsigned)tq * 128u + (f0 + 8u) * 4u);
      *(LAS v4u*)(size_t)(afr + (unsigned)(lane >> 5) * 1024u + (16u + c16 + 32u * hh2) * 16u) = v4u{pk2(za[0], za[1]), pk2(za[2], za[3]), pk2(zb[0], zb[1]), pk2(zb[2], zb[3])};
      asm volatile("s_waitcnt lgkmcnt(0)" ::: "memory");
      lds_inc(L_CNT + C_AFR * 4, lane);
      lds_wait_ge(L_CNT + C_AFR * 4, 4);
    }
#pragma unroll
    for (int hd = 0; hd < 4; ++hd) {
      const int head = 2 * ti[hd >> 1] + (hd & 1);
      f32x16 d = mfma(lds128(L_AFR + (unsigned)head * 2048u + (unsigned)lane * 16u), pq[hd][0], zero16());
      d = mfma(lds128(L_AFR + (unsigned)head * 2048u + 1024u + (unsigned)lane * 16u), pq[hd][1], d);
      const float r = __builtin_amdgcn_rcpf(d[8] + 1e-6f);
      const v4u o = v4u{pk2(d[0] * r, d[1] * r), pk2(d[2] * r, d[3] * r), pk2(d[4] * r, d[5] * r), pk2(d[6] * r, d[7] * r)};
      st128(o, rsHB, (unsigned)lane * 16u + (T32 * 144u + (unsigned)(112 + 4 * g + head)) * 1024u);
    }
    publish_last(L_CNT + C_O * 4, 4, P.ws, FL_O + (unsigned)(c.p * 8 + g) * 128u, tag, lane);
    if (PRIO) __builtin_amdgcn_s_setprio(0);
  }
  STAMP(6);
  return tag;
}

DI void role_b(const Params& P, const Ctx& c, int wid, u64 amp_a0) {
  u64 amp_a = amp_a0, amp_b = 0; (void)amp_a; (void)amp_b;
  const int lane = c.lane, tq = c.tq;
  const u64 tag = c.ffn ? role_b_head<true>(P, c, wid) : role_b_head<false>(P, c, wid);
  const int pan = c.tt >> 1;
  const int tb = tq * 64 + lane;
  v4u xres[2], bres;
#pragma unroll
  for (int m = 0; m < 2; ++m) {
    const int idx = tb + 256 * m;
    xres[m] = *(const GAS v4u*)(P.x + (size_t)(64 * c.tt + (idx >> 3)) * 512 + 64 * c.ct + 8 * (idx & 7));
  }
  bres = *(const GAS v4u*)(P.b2 + 64 * c.ct + 8 * (tb & 7));
  if (tq == 0) {
    poll_flags(P.ws, (ORD ? FL_O : FL_L) + (unsigned)pan * 8u * 128u, 8, tag, lane);
    lds_set(L_CNT + (ORD ? C_ORDY : C_LATERDY) * 4, 1);
    poll_flags(P.ws, (ORD ? FL_L : FL_O) + (unsigned)pan * 8u * 128u, 8, tag, lane);
    lds_set(L_CNT + (ORD ? C_LATERDY : C_ORDY) * 4, 1);
  } else if (tq == 1 && __builtin_amdgcn_workgroup_id_x() == 0) {
    poll_flags(P.ws, FL_E, 64, tag, lane); poll_flags(P.ws, FL_E + 64 * 128, 64, tag, lane);
    poll_flags(P.ws, FL_K, 64, tag, lane); poll_flags(P.ws, FL_K + 64 * 128, 64, tag, lane);
    if (lane == 0) __hip_atomic_store((GAS u64*)(P.ws + WS_EPOCH), tag, __ATOMIC_RELAXED, __HIP_MEMORY_SCOPE_AGENT);
  }
  BAR();
#pragma unroll
  for (int m = 0; m < 2; ++m) {
    const int idx = tb + 256 * m;
    const unsigned tok = (unsigned)(idx >> 3), cg = (unsigned)(idx & 7);
    const unsigned a = (tok >> 5) * SLOT + (tok & 31u) * 256u, c0 = ((2u * cg) ^ (tok & 15u)) * 16u, c1 = ((2u * cg + 1u) ^ (tok & 15u)) * 16u;
    f32x4 lo = lds128f(a + c0), hi = lds128f(a + c1);
#pragma unroll
    for (unsigned w = 1; w < 4; ++w) { lo += lds128f(w * 8192u + a + c0); hi += lds128f(w * 8192u + a + c1); }
    v4u o;
#pragma unroll
    for (int k = 0; k < 4; ++k) {
      const float a0 = k < 2 ? lo[2 * k] : hi[2 * k - 4], a1 = k < 2 ? lo[2 * k + 1] : hi[2 * k - 3];
      const float x0 = u2f(xres[m][k] << 16), x1 = u2f(xres[m][k] & 0xffff0000u);
      const float b0 = u2f(bres[k] << 16), b1 = u2f(bres[k] & 0xffff0000u);
      o[k] = pk2(a0 + x0 + b0, a1 + x1 + b1);
    }
    *(GAS v4u*)(P.out + (size_t)(64 * c.tt + (idx >> 3)) * 512 + 64 * c.ct + 8 * (idx & 7)) = o;
  }
  STAMP(7);

}

__global__ __launch_bounds__(512, 2) NOPK void fused_kernel(Params P) {
  __shared__ __attribute__((aligned(1024))) char smem[LDS_BYTES];
  asm volatile("" ::"v"((unsigned)(size_t)(LAS char*)smem) : "memory");
  u64 pc;
  asm volatile("s_getpc_b64 %0" : "=s"(pc));
  const int tid = __builtin_amdgcn_workitem_id_x(), lane = tid & 63;
  const int wid = __builtin_amdgcn_readfirstlane(tid >> 6);
  u64 amp_a = 0, amp_b = 0;
  STAMP(0);
  Ctx c;
  c.lane = lane; c.tq = wid & 3; c.hh = lane >> 5;
  const int blk = __builtin_amdgcn_workgroup_id_x(), xcd = blk & 7, j = blk >> 3, ja = j & 7, jb = j >> 3;
  c.p = (xcd & 1) * 8 + ja;
  c.G = (xcd >> 1) * 4 + jb;
  c.ffn = !(c.G & 1);
  c.gi = c.G >> 1;
  c.rot = ROT1 == 0 ? 0 : (ROT1 == 1 ? (((ja + jb) % 3) * 3) & 7 : (ROT1 == 2 ? (ja + 2 * jb) & 7 : (ROT1 == 3 ? ((ja + jb) & 1) * 4 : (ROT1 == 4 ? ((ja + jb) & 3) * 2 : ((ja + jb) & 3)))));
  if (MAP2) { c.tt = xcd * 4 + (j & 3); c.ct = j >> 2; c.ja = j & 3; c.jb = j >> 2; }
  else { c.tt = (xcd >> 1) * 8 + ja; c.ct = (xcd & 1) * 4 + jb; c.ja = ja; c.jb = jb; }
  c.rotE = ROT2 ? (2 * ja + 4 * jb) & 15 : 0; c.rotL = ROT2 ? (ja + 3 * jb) % 12 : 0; c.rotO = ROT2 ? (ja + 2 * jb) & 7 : 0;
  if (wid < 4) {
    const unsigned kb0 = (unsigned)(j + 32 * (wid & 1)), kb = kb0 < CODE_KB ? kb0 : 0u;
    const unsigned pf = CODEPF ? *(const GAS unsigned*)((pc & ~255ull) + kb * 1024u + (unsigned)lane * 16u) : 0u;
    const u64 e = __hip_atomic_load((const GAS u64*)(P.ws + WS_EPOCH), __ATOMIC_RELAXED, __HIP_MEMORY_SCOPE_AGENT);
    if (c.ffn) {
      const int tc = tid < 224 ? tid : 0;
      const int t7 = tc >> 5, n = tc & 31, g = c.gi;
      const int cA = 224 * g + 32 * (g & 1);
      const int col = t7 < 4 ? cA + 32 * t7 : (t7 < 6 ? cA + 128 + 32 * (t7 - 4) : ((g & 1) ? 224 * g : 224 * g + 192));
      const unsigned bv = P.b1[col + n];
      *(LAS float*)(size_t)(L_B1 + (unsigned)tid * 4u) = u2f(bv << 16);
    } else {
      const v4u wv = *(const GAS v4u*)(P.wphi + (size_t)c.gi * 1024 + (tid & 127) * 8);
      *(LAS v4u*)(size_t)(L_WPHI + (unsigned)(tid & 127) * 16u) = wv;
    }
    if (tid < 32) *(LAS unsigned*)(size_t)(L_CNT + (unsigned)tid * 4u) = 0u;
    const u64 tag = e + 1u;
    if (tid == 0) *(LAS v2u*)(size_t)L_TAG = v2u{(unsigned)tag, (unsigned)(tag >> 32)};
    asm volatile("" ::"v"(pf));
    STAMP(1);
    role_a(P, c, wid, amp_a, amp_b);
  } else {
    STAMP(1);
    role_b(P, c, wid, amp_a);
  }
}

}

extern "C" void kernel_launch(void* const* d_in, const int* in_sizes, int n_in, void* d_out, int out_size, void* d_ws, size_t ws_size,
                              hipStream_t stream) {
  Params P;
  P.x = (const u16*)d_in[0]; P.wqkv = (const u16*)d_in[1]; P.wphi = (const u16*)d_in[2]; P.wo = (const u16*)d_in[3];
  P.w1 = (const u16*)d_in[5]; P.b1 = (const u16*)d_in[6]; P.w2 = (const u16*)d_in[7]; P.b2 = (const u16*)d_in[8];
  P.out = (u16*)d_out; P.ws = (char*)d_ws;
  fused_kernel<<<256, 512, 0, stream>>>(P);
}
```

```cpp
#include <hip/hip_runtime.h>
#include <stdint.h>
#include <type_traits>

namespace {

typedef unsigned short u16;
typedef unsigned long long u64;
typedef unsigned v4u __attribute__((ext_vector_type(4)));
typedef unsigned v2u __attribute__((ext_vector_type(2)));
typedef float f32x16 __attribute__((ext_vector_type(16)));
typedef float f32x4 __attribute__((ext_vector_type(4)));
typedef float f32x2 __attribute__((ext_vector_type(2)));
typedef __bf16 bf16x8 __attribute__((ext_vector_type(8)));
typedef __bf16 bf16x2 __attribute__((ext_vector_type(2)));
typedef short s16x4 __attribute__((ext_vector_type(4)));
#define LAS __attribute__((address_space(3)))
#define GAS __attribute__((address_space(1)))
#if defined(__HIP_DEVICE_COMPILE__) && !defined(USEPK)
#define NOPK __attribute__((target("no-packed-fp32-ops")))
#else
#define NOPK
#endif
#define DI __device__ __forceinline__ NOPK
#define AI __attribute__((always_inline)) NOPK

#define STAMPS 0
#define ROT1 1
#define ROT2 0
#define STR2(x) #x
#define STR(x) STR2(x)
#define W1 8
#define W2 4
#define PRIO 1
#define SLP 1
#define KVFIRST 0
#define PRIOL 0
#define MAP2 0
#define NBK 4
#define PRIO1 0
#define PRIOA 0
#define ORD 0
#define CODEPF 1
#define WARM2 1
#define CODE_KB 36

constexpr unsigned WS_EPOCH = 0;
constexpr unsigned FL_E = 4096;
constexpr unsigned FL_L = FL_E + 128 * 128;
constexpr unsigned FL_K = FL_L + 128 * 128;
constexpr unsigned FL_O = FL_K + 128 * 128;
constexpr unsigned WS_ST = 73728;
constexpr unsigned WS_KVP = 131072;
constexpr unsigned WS_ZP = WS_KVP + 524288;
constexpr unsigned WS_HB = 1048576;
constexpr unsigned HB_BYTES = 64u * 144u * 1024u;
constexpr unsigned WS_STAMP = 16u << 20;

constexpr unsigned SLOT = 32768;
constexpr unsigned L_MISC = 131072;
constexpr unsigned L_STATS = L_MISC;
constexpr unsigned L_CS0 = L_MISC + 1024;
constexpr unsigned L_CS1 = L_MISC + 1536;
constexpr unsigned L_B1 = L_MISC + 2048;
constexpr unsigned L_WPHI = L_MISC + 3072;
constexpr unsigned L_CNT = L_MISC + 5120;
constexpr unsigned L_TAG = L_MISC + 5248;
constexpr unsigned L_ZT = L_MISC + 5376;
constexpr unsigned L_AFR = L_MISC + 8192;
constexpr unsigned LDS_BYTES = 163840;
enum { C_CS1 = 0, C_LATE = 1, C_KV = 2, C_O = 3, C_AFR = 4, C_KVRDY = 5, C_LATERDY = 6, C_ORDY = 7, C_FREE = 8  , C_LAND = 12  , C_EARLY = 16, C_IMG = 17, C_KVIN = 18, C_IMGDONE = 19 };

constexpr unsigned kSpin = 1u << 16;

struct Params {
  const u16* x; const u16* wqkv; const u16* wphi; const u16* wo;
  const u16* w1; const u16* b1; const u16* w2; const u16* b2;
  u16* out; char* ws;
};

DI unsigned f2u(float f) { return __builtin_bit_cast(unsigned, f); }
DI float u2f(unsigned u) { return __builtin_bit_cast(float, u); }
DI float fmx(float a, float b) { return __builtin_fmaxf(a, b); }
DI f32x16 mfma(v4u a, v4u b, f32x16 c) {
  return __builtin_amdgcn_mfma_f32_32x32x16_bf16(__builtin_bit_cast(bf16x8, a), __builtin_bit_cast(bf16x8, b), c, 0, 0, 0);
}
DI f32x16 zero16() { f32x16 z; for (int r = 0; r < 16; ++r) z[r] = 0.f; return z; }
DI v4u lds128(unsigned a) { return *(const LAS v4u*)(size_t)a; }
DI f32x4 lds128f(unsigned a) { return *(const LAS f32x4*)(size_t)a; }
DI v2u tr64(unsigned a) { return __builtin_bit_cast(v2u, __builtin_amdgcn_ds_read_tr16_b64_v4i16((LAS s16x4*)(size_t)a)); }
DI v4u tr2(unsigned a0, unsigned a1) { v2u l = tr64(a0), h = tr64(a1); return v4u{l.x, l.y, h.x, h.y}; }
DI unsigned pk2(float a, float b) { return __builtin_bit_cast(unsigned, __builtin_convertvector(f32x2{a, b}, bf16x2)); }
DI float xhalf_add(float v) { auto r = __builtin_amdgcn_permlane32_swap(f2u(v), f2u(v), false, false); return u2f(r[0]) + u2f(r[1]); }
DI float xhalf_max(float v) { auto r = __builtin_amdgcn_permlane32_swap(f2u(v), f2u(v), false, false); return fmx(u2f(r[0]), u2f(r[1])); }
DI float shx(float v, int m, int lane) { return u2f((unsigned)__builtin_amdgcn_ds_bpermute((lane ^ m) << 2, (int)f2u(v))); }
DI f32x4 lanesum(v4u f, f32x4 c) {
  const s16x4 one = {0x3F80, 0x3F80, 0x3F80, 0x3F80};
  c = __builtin_amdgcn_mfma_f32_4x4x4bf16_1k(one, __builtin_bit_cast(s16x4, v2u{f.x, f.y}), c, 0, 0, 0);
  return __builtin_amdgcn_mfma_f32_4x4x4bf16_1k(one, __builtin_bit_cast(s16x4, v2u{f.z, f.w}), c, 0, 0, 0);
}
template <int CTRL> DI float dppadd(float v) { return v + u2f((unsigned)__builtin_amdgcn_update_dpp(0, (int)f2u(v), CTRL, 0xf, 0xf, true)); }
DI float wavesum(float v) {
  v = dppadd<0xB1>(v); v = dppadd<0x4E>(v); v = dppadd<0x141>(v); v = dppadd<0x140>(v);
  auto r = __builtin_amdgcn_permlane16_swap(f2u(v), f2u(v), false, false);
  return xhalf_add(u2f(r[0]) + u2f(r[1]));
}
DI void dma(unsigned m0v, unsigned voff, __amdgpu_buffer_rsrc_t rs, unsigned soff) {
  asm volatile("s_mov_b32 m0, %0\n\ts_nop 0\n\tbuffer_load_dwordx4 %1, %2, %3 offen lds" ::"s"(m0v), "v"(voff), "s"(rs), "s"(soff) : "memory");
}
DI void st128(v4u v, __amdgpu_buffer_rsrc_t rs, unsigned voff) { __builtin_amdgcn_raw_buffer_store_b128(v, rs, voff, 0, 16); }
#define BAR() do { __builtin_amdgcn_sched_barrier(0); asm volatile("s_waitcnt lgkmcnt(0)\n\ts_barrier" ::: "memory"); __builtin_amdgcn_sched_barrier(0); } while (0)
#define VMW(n) asm volatile("s_waitcnt vmcnt(" #n ")" ::: "memory")
constexpr unsigned ONES = 0x3F803F80u;

#define STAMP(i) do { } while (0)

DI void poll_flags(const char* ws, unsigned base, int n, u64 tag, int lane) {
  const GAS u64* f = (const GAS u64*)(ws + base + (unsigned)(lane < n ? lane : 0) * 128u);
#pragma unroll 1
  for (unsigned spin = 0; spin < kSpin; ++spin) {
    const u64 v = __hip_atomic_load(f, __ATOMIC_RELAXED, __HIP_MEMORY_SCOPE_AGENT);
    if (__builtin_amdgcn_ballot_w64(v != tag) == 0) break;
    __builtin_amdgcn_s_sleep(1);
  }
  __builtin_amdgcn_fence(__ATOMIC_ACQUIRE, "wavefront");
  asm volatile("" ::: "memory");
}
DI void lds_wait_ge(unsigned addr, unsigned want) {
#pragma unroll 1
  for (unsigned spin = 0; spin < (kSpin << 4); ++spin) {
    const unsigned v = *(volatile LAS unsigned*)(size_t)addr;
    if ((unsigned)__builtin_amdgcn_readfirstlane(v) >= want) break;
    if (SLP) __builtin_amdgcn_s_sleep(SLP);
  }
  asm volatile("" ::: "memory");
}
DI void lds_set(unsigned addr, unsigned v) { *(volatile LAS unsigned*)(size_t)addr = v; }
DI void lds_inc(unsigned addr, int lane) { if (lane == 0) __hip_atomic_fetch_add((LAS unsigned*)(size_t)addr, 1u, __ATOMIC_RELAXED, __HIP_MEMORY_SCOPE_WORKGROUP); }
DI void publish_last(unsigned cnt_addr, unsigned n, char* ws, unsigned flag_off, u64 tag, int lane) {
  VMW(0);
  if (lane == 0) {
    const unsigned old = __hip_atomic_fetch_add((LAS unsigned*)(size_t)cnt_addr, 1u, __ATOMIC_RELAXED, __HIP_MEMORY_SCOPE_WORKGROUP);
    if (old == n - 1) __hip_atomic_store((GAS u64*)(ws + flag_off), tag, __ATOMIC_RELAXED, __HIP_MEMORY_SCOPE_AGENT);
  }
}
DI u64 lds_tag() { const v2u t = *(const LAS v2u*)(size_t)L_TAG; return ((u64)(unsigned)__builtin_amdgcn_readfirstlane(t.y) << 32) | (unsigned)__builtin_amdgcn_readfirstlane(t.x); }

DI void fold8(const f32x16& acc, int r0, float rstd, float mr, unsigned cs, int hh, float* y) {
#pragma unroll
  for (int g = 0; g < 2; ++g) {
    const f32x4 t = lds128f(cs + (unsigned)((r0 >> 2) + g) * 32u + (unsigned)hh * 16u) * mr;
#pragma unroll
    for (int k = 0; k < 4; ++k) y[4 * g + k] = __builtin_fmaf(acc[r0 + 4 * g + k], rstd, t[k]);
  }
}
DI void load16(unsigned a, int hh, f32x4* v) {
#pragma unroll
  for (int g = 0; g < 4; ++g) v[g] = lds128f(a + (unsigned)g * 32u + (unsigned)hh * 16u);
}
DI void ffn_tile(const f32x16& acc, float rstd, float mr, const f32x4* cs, const f32x4* bs, __amdgpu_buffer_rsrc_t rsHB, unsigned voff) {
  f32x2 y[8], d[8];
#pragma unroll
  for (int g = 0; g < 4; ++g) {
    const f32x4 t = cs[g] * mr + bs[g];
    y[2 * g] = f32x2{acc[4 * g], acc[4 * g + 1]} * rstd + f32x2{t[0], t[1]};
    y[2 * g + 1] = f32x2{acc[4 * g + 2], acc[4 * g + 3]} * rstd + f32x2{t[2], t[3]};
  }
#pragma unroll
  for (int k = 0; k < 8; ++k) d[k] = y[k] * ((y[k] * y[k]) * (-0.10294324f) + (-2.30220819f));
#pragma unroll
  for (int k = 0; k < 8; ++k) { d[k].x = __builtin_amdgcn_exp2f(d[k].x); d[k].y = __builtin_amdgcn_exp2f(d[k].y); }
#pragma unroll
  for (int k = 0; k < 8; ++k) d[k] += 1.0f;
#pragma unroll
  for (int k = 0; k < 8; ++k) { d[k].x = __builtin_amdgcn_rcpf(d[k].x); d[k].y = __builtin_amdgcn_rcpf(d[k].y); }
  v4u o0, o1;
#pragma unroll
  for (int k = 0; k < 4; ++k) {
    const f32x2 g0 = y[k] * d[k], g1 = y[4 + k] * d[4 + k];
    o0[k] = pk2(g0.x, g0.y); o1[k] = pk2(g1.x, g1.y);
  }
  st128(o0, rsHB, voff); st128(o1, rsHB, voff + 1024u);
}
DI void hedge(const f32x16& d, v4u& o0, v4u& o1) {
  constexpr float L = 1.4426950408889634f;
  float m0 = fmx(fmx(fmx(d[0], d[1]), fmx(d[2], d[3])), fmx(fmx(d[4], d[5]), fmx(d[6], d[7])));
  float m1 = fmx(fmx(fmx(d[8], d[9]), fmx(d[10], d[11])), fmx(fmx(d[12], d[13]), fmx(d[14], d[15])));
  m0 = xhalf_max(m0) * -L; m1 = xhalf_max(m1) * -L;
  float e[16];
#pragma unroll
  for (int i = 0; i < 8; ++i) { e[i] = __builtin_amdgcn_exp2f(__builtin_fmaf(d[i], L, m0)); e[8 + i] = __builtin_amdgcn_exp2f(__builtin_fmaf(d[8 + i], L, m1)); }
  float s0 = ((e[0] + e[1]) + (e[2] + e[3])) + ((e[4] + e[5]) + (e[6] + e[7]));
  float s1 = ((e[8] + e[9]) + (e[10] + e[11])) + ((e[12] + e[13]) + (e[14] + e[15]));
  s0 = __builtin_amdgcn_rcpf(xhalf_add(s0)); s1 = __builtin_amdgcn_rcpf(xhalf_add(s1));
#pragma unroll
  for (int k = 0; k < 4; ++k) { o0[k] = pk2(e[2 * k] * s0, e[2 * k + 1] * s0); o1[k] = pk2(e[8 + 2 * k] * s1, e[8 + 2 * k + 1] * s1); }
}
DI v4u wphi_frag(int hd, int lane) {
  const int hh = lane >> 5, q = (lane >> 2) & 3, pp = lane & 3;
  const unsigned a = L_WPHI + (unsigned)hd * 512u + (unsigned)(4 * hh + q) * 32u + (unsigned)pp * 8u;
  v4u f = tr2(a, a + 256u);
  const unsigned sg = (lane & 16) ? 0x80008000u : 0u;
  f.x ^= sg; f.y ^= sg; f.z ^= sg; f.w ^= sg;
  return f;
}
DI v4u selv(bool s, v4u a, v4u b) { const unsigned m = s ? 0xffffffffu : 0u; return v4u{(a.x & m) | (b.x & ~m), (a.y & m) | (b.y & ~m), (a.z & m) | (b.z & ~m), (a.w & m) | (b.w & ~m)}; }
DI v4u pack8(const float* y) { return v4u{pk2(y[0], y[1]), pk2(y[2], y[3]), pk2(y[4], y[5]), pk2(y[6], y[7])}; }

constexpr int NWI = MAP2 ? 18 : 9;
template <int NI> DI void warm_w2(const Params& P, int ct, int ja, int tq, int i0, int lane, v4u* d) {
#pragma unroll
  for (int i = 0; i < NI; ++i) {
    const int row = 32 * NWI * ja + 8 * NWI * tq + 8 * (i0 + i);
    const u64 a = row < 1792 ? (u64)(size_t)P.w2 + (u64)(unsigned)row * 1024u : (u64)(size_t)P.wo + (u64)(unsigned)(row - 1792) * 1024u;
    const u64 base = ((u64)(unsigned)__builtin_amdgcn_readfirstlane((unsigned)(a >> 32)) << 32) | (unsigned)__builtin_amdgcn_readfirstlane((unsigned)a);
    const unsigned voff = (unsigned)(lane >> 3) * 1024u + (unsigned)(lane & 7) * 16u + (unsigned)ct * 128u;
    asm volatile("s_nop 4\n\tglobal_load_dwordx4 %0, %1, %2" : "+v"(d[i % 3]) : "v"(voff), "s"(base) : "memory");
  }
}
struct Ctx {
  int lane, tq, hh;
  int p, gi, G;
  int rot;
  int tt, ct, ja, jb;
  int rotE, rotL, rotO;
  bool ffn;
};

DI void role_a(const Params& P, const Ctx& c, int wid, u64 amp_a, u64 amp_b) {
  const int lane = c.lane, tq = c.tq, hh = c.hh, t = lane & 31;
  const int cb = (lane >> 4) & 1, q = (lane >> 2) & 3, pp = lane & 3, sw = (q >> 1) & 1;
  unsigned tabB, tabWlo, tabWhi;
  {
    const int jj = lane < 36 ? lane : 35;
    constexpr int J2 = ORD ? 24 : 28;
    int sg;
    auto perm = [&](int i, int n4) AI {
      if (!ROT2) return i;
      constexpr int NO = MAP2 ? 4 : 2;
      const int b = (c.jb >> 1) & (NO - 1), ng = 4 * n4 / NO;
      if (i < 2) return NO * i + b;
      const int m = (i - 2) / NO, r = (i - 2) % NO;
      if (m + 2 < ng) return r < NO - 1 ? NO * m + ((b + 1 + r) & (NO - 1)) : NO * (m + 2) + b;
      const int e = i - 2 - NO * (ng - 2);
      return NO * (ng - 2 + e / (NO - 1)) + ((b + 1 + e % (NO - 1)) & (NO - 1));
    };
    if (jj < 16) sg = 4 * perm(jj, 4) + tq;
    else if ((jj < J2) != (ORD != 0)) sg = 64 + 4 * perm(jj - (ORD ? 24 : 16), 3) + tq;
    else sg = 112 + 4 * perm(jj - (ORD ? 16 : 28), 2) + tq;
    int row;
    if (sg < 64) { const int g = sg >> 3; row = 224 * g + 32 * (g & 1) + 16 * (sg & 7); }
    else if (sg < 112) {
      const int e = sg - 64, g = e / 6, i = e - 6 * g;
      row = i < 4 ? 224 * g + 32 * (g & 1) + 128 + 16 * i : ((g & 1) ? 224 * g : 224 * g + 192) + 16 * (i - 4);
    } else row = 16 * (sg - 112);
    const u64 a = (u64)(size_t)(sg < 112 ? P.w2 : P.wo) + (u64)row * 1024u + (u64)c.ct * 128u;
    tabB = (unsigned)sg * 1024u; tabWlo = (unsigned)a; tabWhi = (unsigned)(a >> 32);
  }
  asm volatile("" : "+v"(tabB), "+v"(tabWlo), "+v"(tabWhi));
  const int th = tq & 1, up = tq >> 1;
  unsigned xa[4];
#pragma unroll
  for (int ks = 0; ks < 4; ++ks) xa[ks] = (unsigned)(64 * th + t) * 128u + (unsigned)(((2 * ks + hh) ^ ((t >> 1) & 7)) * 16);
  const unsigned wl = 16384u + (unsigned)(8 * hh + q) * 128u + (unsigned)((4 * (up ^ sw) + 2 * cb + (pp >> 1)) * 16 + 8 * (pp & 1));
  f32x16 acc[2][2];
#pragma unroll
  for (int i = 0; i < 2; ++i) { acc[i][0] = zero16(); acc[i][1] = zero16(); }
  f32x4 cs = {0.f, 0.f, 0.f, 0.f};
  unsigned x0 = xa[0], x1 = xa[1], x2 = xa[2], x3 = xa[3], w0 = wl;
  v4u xf[4][2], wf[4][2];
  auto rd = [&](int ks, v4u* X, v4u* W) AI {
    const unsigned xb = ks == 0 ? x0 : (ks == 1 ? x1 : (ks == 2 ? x2 : x3));
    X[0] = lds128(xb); X[1] = lds128(xb + 4096u);
    const unsigned a = w0 + (unsigned)(ks * 2048);
    W[0] = tr2(a, a + 512u); W[1] = tr2(a + 8192u, a + 8704u);
  };
  f32x4 cs2 = {0.f, 0.f, 0.f, 0.f};
  BAR();
  rd(0, xf[0], wf[0]); rd(1, xf[1], wf[1]);
  auto pass0 = [&](auto TH) AI {
    constexpr int CT = decltype(TH)::value;
    const s16x4 one = {0x3F80, 0x3F80, 0x3F80, 0x3F80};
#pragma unroll 1
    for (int kc = 0; kc < 8; ++kc) {
#pragma unroll
      for (int ks = 0; ks < 4; ++ks) {
        if (ks == 2) {
          __builtin_amdgcn_sched_barrier(0);
          asm volatile("s_barrier" ::: "memory");
          __builtin_amdgcn_sched_barrier(0);
          const unsigned sb = (unsigned)((kc + 1) & 3) * SLOT;
          x0 = xa[0] + sb; x1 = xa[1] + sb; w0 = wl + sb;
          asm volatile("" : "+v"(x0), "+v"(x1), "+v"(w0));
        }
        if (ks == 0) {
          const unsigned sb = (unsigned)(kc & 3) * SLOT;
          x2 = xa[2] + sb; x3 = xa[3] + sb;
          asm volatile("" : "+v"(x2), "+v"(x3));
        }
        rd((ks + 2) & 3, xf[(ks + 2) & 3], wf[(ks + 2) & 3]);
        acc[0][0] = mfma(wf[ks][0], xf[ks][0], acc[0][0]); acc[0][1] = mfma(wf[ks][0], xf[ks][1], acc[0][1]);
        acc[1][0] = mfma(wf[ks][1], xf[ks][0], acc[1][0]); acc[1][1] = mfma(wf[ks][1], xf[ks][1], acc[1][1]);
        cs = __builtin_amdgcn_mfma_f32_4x4x4bf16_1k(one, __builtin_bit_cast(s16x4, v2u{wf[ks][CT].x, wf[ks][CT].y}), cs, 0, 0, 0);
        cs2 = __builtin_amdgcn_mfma_f32_4x4x4bf16_1k(one, __builtin_bit_cast(s16x4, v2u{wf[ks][CT].z, wf[ks][CT].w}), cs2, 0, 0, 0);
        __builtin_amdgcn_sched_group_barrier(0x008, 1, 0);
        __builtin_amdgcn_sched_group_barrier(0x100, 2, 0);
        __builtin_amdgcn_sched_group_barrier(0x008, 1, 0);
        __builtin_amdgcn_sched_group_barrier(0x100, 2, 0);
        __builtin_amdgcn_sched_group_barrier(0x008, 1, 0);
        __builtin_amdgcn_sched_group_barrier(0x100, 2, 0);
        __builtin_amdgcn_sched_group_barrier(0x008, 3, 0);
        __builtin_amdgcn_sched_barrier(0);
      }
      asm volatile("" : "+v"(acc[0][0]), "+v"(acc[0][1]), "+v"(acc[1][0]), "+v"(acc[1][1]), "+v"(cs), "+v"(cs2));
    }
  };
  if (th) pass0(std::integral_constant<int, 1>{}); else pass0(std::integral_constant<int, 0>{});
  cs[0] += cs2[0];
  STAMP(2);
  {
    const float s = xhalf_add(cs[0]);
    if (hh == 0) *(LAS float*)(size_t)(L_CS0 + (unsigned)(up + 2 * th) * 128u + (unsigned)t * 4u) = s;
  }
  BAR();
  const u64 tag = lds_tag();
  float rstd[2], mr[2];
#pragma unroll
  for (int b = 0; b < 2; ++b) {
    const f32x2 st = *(const LAS f32x2*)(size_t)(L_STATS + (unsigned)(64 * th + 32 * b + t) * 8u);
    rstd[b] = st.x; mr[b] = st.y;
  }
  const __amdgpu_buffer_rsrc_t rsHB = __builtin_amdgcn_make_buffer_rsrc(P.ws + WS_HB, 0, HB_BYTES, 0x00020000);
  v4u wm[3] = {v4u{0u, 0u, 0u, 0u}, v4u{0u, 0u, 0u, 0u}, v4u{0u, 0u, 0u, 0u}};
  if (c.ffn) {
    f32x4 csv[2][4], bsv[2][4];
#pragma unroll
    for (int i = 0; i < 2; ++i) { load16(L_CS0 + (unsigned)(up + 2 * i) * 128u, hh, csv[i]); load16(L_B1 + (unsigned)(up + 2 * i) * 128u, hh, bsv[i]); }
#pragma unroll
    for (int i = 0; i < 2; ++i)
#pragma unroll
      for (int b = 0; b < 2; ++b) {
        const unsigned tile = (unsigned)(up + 2 * i), T32 = (unsigned)(c.p * 4 + 2 * th + b);
        ffn_tile(acc[i][b], rstd[b], mr[b], csv[i], bsv[i], rsHB, (unsigned)lane * 16u + (T32 * 144u + (unsigned)(8 * c.gi) + 2u * tile) * 1024u);
        if (WARM2 && 2 * i + b < 3) warm_w2<NWI / 3>(P, c.ct, c.ja, tq, (NWI / 3) * (2 * i + b), lane, wm);
      }
    STAMP(8);
    publish_last(L_CNT + C_EARLY * 4, 4, P.ws, FL_E + (unsigned)(c.p * 8 + c.gi) * 128u, tag, lane);
    if (WARM2) asm volatile("" :: "v"(wm[0]), "v"(wm[1]), "v"(wm[2]));
  } else {
#pragma unroll
    for (int b = 0; b < 2; ++b)
#pragma unroll
      for (int hf = 0; hf < 2; ++hf) {
        const int hd = 2 * up + hf;
        float y[8];
        fold8(acc[0][b], 8 * hf, rstd[b], mr[b], L_CS0 + (unsigned)up * 128u, hh, y);
        v4u o0, o1;
        hedge(mfma(wphi_frag(hd, lane), pack8(y), zero16()), o0, o1);
        const unsigned img = (unsigned)hd * SLOT + 16384u;
        const unsigned row = (unsigned)(64 * th + 32 * b + t);
        const unsigned g = (row >> 1) & 7u;
        *(LAS v2u*)(size_t)(img + row * 64u + (((unsigned)(0 + hh)) ^ g) * 8u) = v2u{o0.x, o0.y};
        *(LAS v2u*)(size_t)(img + row * 64u + (((unsigned)(2 + hh)) ^ g) * 8u) = v2u{o0.z, o0.w};
        *(LAS v2u*)(size_t)(img + row * 64u + (((unsigned)(4 + hh)) ^ g) * 8u) = v2u{o1.x, o1.y};
        *(LAS v2u*)(size_t)(img + row * 64u + (((unsigned)(6 + hh)) ^ g) * 8u) = v2u{o1.z, o1.w};
        fold8(acc[1][b], 8 * hf, rstd[b], mr[b], L_CS0 + (unsigned)(2 + up) * 128u, hh, y);
        const unsigned gv = (row >> 2) & 3u;
        *(LAS v2u*)(size_t)(img + 8192u + row * 32u + (((unsigned)(0 + hh)) ^ gv) * 8u) = v2u{pk2(y[0], y[1]), pk2(y[2], y[3])};
        *(LAS v2u*)(size_t)(img + 8192u + row * 32u + (((unsigned)(2 + hh)) ^ gv) * 8u) = v2u{pk2(y[4], y[5]), pk2(y[6], y[7])};
        if (WARM2 && 2 * b + hf < 3) warm_w2<NWI / 3>(P, c.ct, c.ja, tq, (NWI / 3) * (2 * b + hf), lane, wm);
      }
    STAMP(8);
    asm volatile("s_waitcnt lgkmcnt(0)" ::: "memory");
    lds_inc(L_CNT + C_IMG * 4, lane);
    lds_wait_ge(L_CNT + C_IMG * 4, 4);
    const unsigned img = (unsigned)tq * SLOT + 16384u;
    f32x16 kv = zero16();
    unsigned pa[2], va[2];
#pragma unroll
    for (int i2 = 0; i2 < 2; ++i2) {
      const unsigned r = (unsigned)(8 * hh + 4 * i2 + q);
      pa[i2] = img + r * 64u + (((unsigned)(4 * cb + pp)) ^ ((r >> 1) & 7u)) * 8u;
      va[i2] = img + 8192u + r * 32u + (((unsigned)pp) ^ ((r >> 2) & 3u)) * 8u;
    }
#pragma unroll
    for (int ks = 0; ks < 8; ++ks) {
      const v4u af = tr2(pa[0] + (unsigned)ks * 1024u, pa[1] + (unsigned)ks * 1024u);
      v4u bf = tr2(va[0] + (unsigned)ks * 512u, va[1] + (unsigned)ks * 512u);
      if (cb) bf = v4u{ONES, ONES, ONES, ONES};
      kv = mfma(af, bf, kv);
    }
    asm volatile("" : "+v"(kv) :: "memory");
    lds_inc(L_CNT + C_IMGDONE * 4, lane);
    const unsigned hidx = (unsigned)(c.gi * 4 + tq) * 16u + (unsigned)c.p;
    const __amdgpu_buffer_rsrc_t rsKV = __builtin_amdgcn_make_buffer_rsrc(P.ws + WS_KVP, 0, 524288 + 65536, 0x00020000);
    if (cb == 0) {
#pragma unroll
      for (int s = 0; s < 2; ++s) {
        const v4u o = v4u{pk2(kv[8 * s], kv[8 * s + 1]), pk2(kv[8 * s + 2], kv[8 * s + 3]), pk2(kv[8 * s + 4], kv[8 * s + 5]), pk2(kv[8 * s + 6], kv[8 * s + 7])};
        st128(o, rsKV, (unsigned)((16 * s + (lane & 15) + 32 * hh) * 16) + hidx * 1024u);
      }
    } else if ((lane & 15) == 0) {
#pragma unroll
      for (int g4 = 0; g4 < 4; ++g4) {
        const v4u o = v4u{f2u(kv[4 * g4]), f2u(kv[4 * g4 + 1]), f2u(kv[4 * g4 + 2]), f2u(kv[4 * g4 + 3])};
        st128(o, rsKV, (unsigned)((8 * g4 + 4 * hh) * 4) + 524288u + hidx * 128u);
      }
    }
    publish_last(L_CNT + C_KV * 4, 4, P.ws, FL_K + (unsigned)(c.gi * 16 + c.p) * 128u, tag, lane);
    if (WARM2) asm volatile("" :: "v"(wm[0]), "v"(wm[1]), "v"(wm[2]));
  }
  STAMP(3);
  const unsigned vw0 = (unsigned)(lane >> 3) * 1024u + (unsigned)(((lane & 7) ^ (((lane >> 4) & 1) << 2)) * 16), vw1 = vw0 + 8192u;
  const unsigned ring = (unsigned)tq * 8192u;
  auto pofs = [&](int r) AI { return (unsigned)((r >> 2) & 3) * SLOT + (unsigned)(r & 3) * 2048u; };
  const unsigned vb0 = (unsigned)lane * 16u + (unsigned)(2 * c.tt) * 144u * 1024u, vb1 = vb0 + 144u * 1024u;
  auto wbase = [&](int j) AI { const unsigned lo = __builtin_amdgcn_readlane(tabWlo, j), hi = __builtin_amdgcn_readlane(tabWhi, j); return ((u64)hi << 32) | lo; };
  auto wdma1 = [&](u64 b, unsigned m, unsigned v) AI {
    asm volatile("s_mov_b32 m0, %0\n\ts_nop 0\n\tglobal_load_lds_dwordx4 %1, %2" ::"s"(m), "v"(v), "s"(b) : "memory");
  };
  auto wdma = [&](int j) AI {
    const u64 b = wbase(j);
    const unsigned m = ring + pofs(j);
    asm volatile("s_nop 4" ::: "memory");
    wdma1(b, m, vw0); wdma1(b, m + 1024u, vw1);
  };
  constexpr int NS = 6, LB = NS - 1;
  v4u br[NS][2];
  auto bload1 = [&](unsigned so, v4u& d, unsigned v) AI {
    asm volatile("buffer_load_dwordx4 %0, %1, %2, %3 offen sc1" : "+v"(d) : "v"(v), "s"(rsHB), "s"(so) : "memory");
  };
  auto bload0 = [&](unsigned so, v4u& d0, v4u& d1) AI {
    asm volatile("s_nop 4\n\tbuffer_load_dwordx4 %0, %2, %4, %5 offen sc1\n\tbuffer_load_dwordx4 %1, %3, %4, %5 offen sc1"
                 : "=&v"(d0), "=&v"(d1) : "v"(vb0), "v"(vb1), "s"(rsHB), "s"(so) : "memory");
  };
  if (!c.ffn && tq >= 2) lds_wait_ge(L_CNT + C_IMGDONE * 4, 4);
  const int pan = c.tt >> 1;
  v2u ef;
  const char* efp = P.ws + FL_E + (unsigned)pan * 8u * 128u + (unsigned)(lane & 7) * 128u;
#pragma unroll 1
  for (int s = 0; s < 2; ++s) {
    lds_wait_ge(L_CNT + (C_FREE + s) * 4, 4);
#pragma unroll 1
    for (int j = 4 * s; j < 4 * s + 4; ++j) wdma(j);
  }
  lds_wait_ge(L_CNT + (C_FREE + 2) * 4, 4);
  asm volatile("global_load_dwordx2 %0, %1, off sc1" : "=&v"(ef) : "v"(efp) : "memory");
  wdma(8);
  STAMP(4);
  asm volatile("s_waitcnt vmcnt(2)" : "+v"(ef) :: "memory");
  if (__builtin_amdgcn_ballot_w64(ef.x != (unsigned)tag || ef.y != (unsigned)(tag >> 32)) != 0) poll_flags(P.ws, FL_E + (unsigned)pan * 8u * 128u, 8, tag, lane);
  STAMP(5);
  bload0(__builtin_amdgcn_readlane(tabB, 0), br[0][0], br[0][1]);
  bload0(__builtin_amdgcn_readlane(tabB, 1), br[1][0], br[1][1]);
#pragma unroll
  for (int u = 2; u < LB; ++u) {
    wdma(7 + u);
    bload0(__builtin_amdgcn_readlane(tabB, u), br[u][0], br[u][1]);
  }
  br[LB][0] = v4u{0u, 0u, 0u, 0u}; br[LB][1] = br[LB][0];
  unsigned w2l[2];
#pragma unroll
  for (int h = 0; h < 2; ++h) w2l[h] = ring + (unsigned)(4 * hh + q) * 128u + (unsigned)((4 * (h ^ sw) + 2 * cb + (pp >> 1)) * 16 + 8 * (pp & 1));
  f32x16 o[2][2];
#pragma unroll
  for (int a = 0; a < 2; ++a)
#pragma unroll
    for (int b = 0; b < 2; ++b) o[a][b] = zero16();
  v4u af[2];
  asm volatile("s_waitcnt vmcnt(14)" : "+v"(br[0][0]), "+v"(br[0][1]) :: "memory");
  af[0] = tr2(w2l[0], w2l[0] + 1024u); af[1] = tr2(w2l[1], w2l[1] + 1024u);
  auto step = [&](int j, int u, bool refill) AI {
    v4u an[2];
    if (refill) asm volatile("s_waitcnt vmcnt(12)" : "+v"(br[(u + 1) % NS][0]), "+v"(br[(u + 1) % NS][1]) :: "memory");
    else asm volatile("s_waitcnt vmcnt(6)" : "+v"(br[(u + 1) % NS][0]), "+v"(br[(u + 1) % NS][1]) :: "memory");
    const int jn = j + LB;
    constexpr int J3 = ORD ? 24 : 28;
    if (u == (16 - LB) % NS && jn == 16) lds_wait_ge(L_CNT + (ORD ? C_ORDY : C_LATERDY) * 4, 1);
    if (u == (J3 - LB) % NS && jn == J3) lds_wait_ge(L_CNT + (ORD ? C_LATERDY : C_ORDY) * 4, 1);
    unsigned p0 = w2l[0] + pofs(j + 1), p1 = w2l[1] + pofs(j + 1);
    asm volatile("" : "+v"(p0), "+v"(p1));
    const unsigned so = jn < 36 ? (unsigned)__builtin_amdgcn_readlane(tabB, jn) : 0x7F000000u;
    u64 wb2 = 0;
    if (refill) wb2 = wbase(j + 12);
    const unsigned m = ring + pofs(j + 12);
    an[0] = tr2(p0, p0 + 1024u); an[1] = tr2(p1, p1 + 1024u);
    asm volatile("s_nop 2" ::"s"(so), "s"(wb2) : "memory");
    __builtin_amdgcn_sched_barrier(0);
    o[0][0] = mfma(af[0], br[u][0], o[0][0]);
    __builtin_amdgcn_sched_barrier(0);
    if (refill) wdma1(wb2, m, vw0);
    __builtin_amdgcn_sched_barrier(0);
    o[0][1] = mfma(af[0], br[u][1], o[0][1]);
    __builtin_amdgcn_sched_barrier(0);
    if (refill) wdma1(wb2, m + 1024u, vw1);
    __builtin_amdgcn_sched_barrier(0);
    o[1][0] = mfma(af[1], br[u][0], o[1][0]);
    __builtin_amdgcn_sched_barrier(0);
    bload1(so, br[(u + LB) % NS][0], vb0);
    __builtin_amdgcn_sched_barrier(0);
    o[1][1] = mfma(af[1], br[u][1], o[1][1]);
    __builtin_amdgcn_sched_barrier(0);
    bload1(so, br[(u + LB) % NS][1], vb1);
    __builtin_amdgcn_sched_barrier(0);
    af[0] = an[0]; af[1] = an[1];
  };
  lds_wait_ge(L_CNT + (C_FREE + 3) * 4, 4);
#pragma unroll 1
  for (int it = 0; it < 4; ++it) {
    if (it == 2) STAMP(10);
#pragma unroll
    for (int u = 0; u < NS; ++u) step(NS * it + u, u, true);
  }
  STAMP(11);
#pragma unroll 1
  for (int it = 4; it < 6; ++it) {
#pragma unroll
    for (int u = 0; u < NS; ++u) step(NS * it + u, u, false);
  }
  asm volatile("s_waitcnt vmcnt(0)" : "+v"(br[0][0]), "+v"(br[0][1]), "+v"(br[1][0]), "+v"(br[1][1]), "+v"(br[2][0]), "+v"(br[2][1]),
               "+v"(br[3][0]), "+v"(br[3][1]), "+v"(br[4][0]), "+v"(br[4][1]), "+v"(br[5][0]), "+v"(br[5][1]) :: "memory");
  STAMP(6);
#pragma unroll
  for (int a = 0; a < 2; ++a)
#pragma unroll
    for (int b = 0; b < 2; ++b)
#pragma unroll
      for (int g4 = 0; g4 < 4; ++g4)
        *(LAS f32x4*)(size_t)(ring + (unsigned)b * SLOT + (unsigned)t * 256u + (unsigned)((8 * a + 2 * g4 + hh) ^ (t & 15)) * 16u) =
            f32x4{o[a][b][4 * g4], o[a][b][4 * g4 + 1], o[a][b][4 * g4 + 2], o[a][b][4 * g4 + 3]};
  VMW(0);
  BAR();
  STAMP(7);
}

template <int NT, int NF, class Fill> DI void pass1(const Params& P, int wid, u64& amp_a, u64& amp_b, Fill& fill, const v4u (&xr)[32], f32x16 (&acc)[NT], f32x4& cs, const unsigned (&wb)[NT], int lane) {
  const s16x4 one = {0x3F80, 0x3F80, 0x3F80, 0x3F80};
  f32x4 csa = {0.f, 0.f, 0.f, 0.f}, csb = csa;
#pragma unroll
  for (int qq = 0; qq < 4; ++qq) {
    if (qq > 0) lds_wait_ge(L_CNT + (C_LAND + qq) * 4, 4);
    if (qq < 2) fill(10 + qq);
    unsigned a[NT];
#pragma unroll
    for (int i = 0; i < NT; ++i) { a[i] = wb[i] + (unsigned)qq * SLOT; asm volatile("" : "+v"(a[i])); }
    v4u f[3][NT];
    auto rd = [&](int k8, v4u* W) AI {
      const unsigned o = (unsigned)((k8 >> 2) * 8192 + (k8 & 3) * 2048);
#pragma unroll
      for (int i = 0; i < NT; ++i) W[i] = tr2(a[i] + o, a[i] + o + 512u);
    };
    rd(0, f[0]); rd(1, f[1]);
    __builtin_amdgcn_sched_barrier(0);
#pragma unroll
    for (int k8 = 0; k8 < 8; ++k8) {
      if (k8 == 4 && qq < 3) {
        if (qq < 2) { if (NF == 8) VMW(8); else VMW(4); } else VMW(0);
        lds_inc(L_CNT + (C_LAND + qq + 1) * 4, lane);
      }
      if (k8 < 6) rd(k8 + 2, f[(k8 + 2) % 3]);
      const v4u xf = xr[8 * qq + k8];
#pragma unroll
      for (int i = 0; i < NT; ++i) acc[i] = mfma(f[k8 % 3][i], xf, acc[i]);
      csa = __builtin_amdgcn_mfma_f32_4x4x4bf16_1k(one, __builtin_bit_cast(s16x4, v2u{f[k8 % 3][0].x, f[k8 % 3][0].y}), csa, 0, 0, 0);
      csb = __builtin_amdgcn_mfma_f32_4x4x4bf16_1k(one, __builtin_bit_cast(s16x4, v2u{f[k8 % 3][0].z, f[k8 % 3][0].w}), csb, 0, 0, 0);
      if (k8 < 6) {
#pragma unroll
        for (int i = 0; i < NT; ++i) {
          __builtin_amdgcn_sched_group_barrier(0x008, 1, 0);
          __builtin_amdgcn_sched_group_barrier(0x100, 2, 0);
        }
        __builtin_amdgcn_sched_group_barrier(0x008, 2, 0);
      }
      __builtin_amdgcn_sched_barrier(0);
    }
    asm volatile("" : "+v"(acc[0]), "+v"(acc[1]), "+v"(acc[NT - 1]), "+v"(csa), "+v"(csb) :: "memory");
    lds_inc(L_CNT + (C_FREE + qq) * 4, lane);
  }
  cs[0] = csa[0] + csb[0];
}

template <bool ffn> DI u64 role_b_head(const Params& P, const Ctx& c, int wid) {
  u64 amp_a = 0, amp_b = 0; (void)amp_a; (void)amp_b;
  const int lane = c.lane, tq = c.tq, hh = c.hh, t = lane & 31;
  const int g = c.gi;
  const unsigned tok = (unsigned)(c.p * 128 + 8 * c.G + 2 * tq);
  v4u r0, r1;
  {
    const u16* rp = P.x + (size_t)tok * 512 + lane * 8;
    asm volatile("global_load_dwordx4 %0, %2, off\n\tglobal_load_dwordx4 %1, %2, off offset:1024" : "=&v"(r0), "=&v"(r1) : "v"(rp) : "memory");
  }
  const __amdgpu_buffer_rsrc_t rsX = __builtin_amdgcn_make_buffer_rsrc((void*)P.x, 0, 2048 * 1024, 0x00020000);
  const __amdgpu_buffer_rsrc_t rsW = __builtin_amdgcn_make_buffer_rsrc((void*)(ffn ? P.w1 : P.wqkv), 0, ffn ? 512 * 3584 : 512 * 3072, 0x00020000);
  constexpr unsigned RS = ffn ? 3584u : 3072u;
  const unsigned cA = (unsigned)(224 * g + 32 * (g & 1));
  const unsigned u0 = ffn ? cA * 2u : (unsigned)(512 + 64 * g) * 2u, u1 = ffn ? cA * 2u + 128u : (unsigned)(1024 + 64 * g) * 2u;
  const unsigned ua = ffn ? (cA + 128u) * 2u : (unsigned)(64 * g) * 2u;
  const unsigned ub = (unsigned)((g & 1) ? 224 * g - 32 : 224 * g + 192) * 2u;
  const unsigned voffX = (unsigned)(lane >> 3) * 1024u + (unsigned)(((lane & 7) ^ ((4 * (tq & 1) + (lane >> 4)) & 7)) * 16);
  const unsigned voffW = (unsigned)(lane >> 3) * RS + (unsigned)(((lane & 7) ^ (((lane >> 4) & 1) << 2)) * 16);
  const unsigned soffX = (unsigned)(c.p * 128 + 8 * tq) * 1024u;
  const unsigned wrow = 32u * (unsigned)(tq & 1) * RS;
  auto fill = [&](int f) AI {
    const unsigned slot = (unsigned)(f & 3) * SLOT;
    if (f < 8) {
      const unsigned kcp = (unsigned)((f + c.rot) & 7);
      unsigned sx = soffX + kcp * 128u, sw = 64u * kcp * RS + wrow + ((tq >> 1) ? u1 : u0);
      unsigned mx = slot + (unsigned)tq * 1024u, mw = slot + 16384u + (unsigned)tq * 4096u;
#pragma unroll 1
      for (int m = 0; m < 4; ++m) {
        dma(mx, voffX, rsX, sx); dma(mw, voffW, rsW, sw);
        mx += 4096u; sx += 32768u; mw += 1024u; sw += 8u * RS;
      }
    } else {
      const unsigned kcp = (unsigned)((2 * (f - 8) + (tq >> 1) + c.rot) & 7);
      unsigned sw = 64u * kcp * RS + wrow, mw = slot + (unsigned)tq * 4096u;
#pragma unroll 1
      for (int m = 0; m < 4; ++m) {
        dma(mw, voffW, rsW, sw + ua);
        if (ffn) dma(mw + 16384u, voffW, rsW, sw + ub);
        mw += 1024u; sw += 8u * RS;
      }
    }
  };
  fill(0);
  fill(1);
  unsigned xa[4];
#pragma unroll
  for (int ks = 0; ks < 4; ++ks) xa[ks] = (unsigned)(32 * tq + t) * 128u + (unsigned)(((2 * ks + hh) ^ ((t >> 1) & 7)) * 16);
  v4u xr[32];
  u64 tag = 0;
  v4u gr;
  const unsigned token = (unsigned)(c.p * 128 + 32 * tq + t);
#pragma unroll
  for (int kc = 0; kc < 8; ++kc) {
    if (kc < 7 || ffn) VMW(8); else VMW(4);
    BAR();
    if (kc == 0) tag = lds_tag();
    if (kc == 5) {
      const char* gp = P.ws + WS_ST + token * 16u;
      asm volatile("global_load_dwordx4 %0, %1, off sc1" : "=&v"(gr) : "v"(gp) : "memory");
    }
    fill(kc + 2);
    const unsigned sb = (unsigned)(kc & 3) * SLOT;
#pragma unroll
    for (int ks = 0; ks < 4; ++ks) xr[4 * kc + ks] = lds128(xa[ks] + sb);
    asm volatile("" : "+v"(xr[4 * kc]), "+v"(xr[4 * kc + 1]), "+v"(xr[4 * kc + 2]), "+v"(xr[4 * kc + 3]));
    if (kc == 1) {
  float st0;
  {
    asm volatile("" : "+v"(r0), "+v"(r1));
    float v[2][8], mean[2], rs[2];
#pragma unroll
    for (int k = 0; k < 4; ++k) {
      v[0][2 * k] = u2f(r0[k] << 16); v[0][2 * k + 1] = u2f(r0[k] & 0xffff0000u);
      v[1][2 * k] = u2f(r1[k] << 16); v[1][2 * k + 1] = u2f(r1[k] & 0xffff0000u);
    }
#pragma unroll
    for (int i = 0; i < 2; ++i) mean[i] = wavesum(((v[i][0] + v[i][1]) + (v[i][2] + v[i][3])) + ((v[i][4] + v[i][5]) + (v[i][6] + v[i][7]))) * (1.0f / 512.0f);
#pragma unroll
    for (int i = 0; i < 2; ++i) {
      float q2 = 0.f;
#pragma unroll
      for (int k = 0; k < 8; ++k) { const float d = v[i][k] - mean[i]; q2 = __builtin_fmaf(d, d, q2); }
      rs[i] = __builtin_amdgcn_rsqf(wavesum(q2) * (1.0f / 512.0f) + 1e-5f);
    }
    st0 = (lane & 2) ? rs[1] : rs[0];
    st0 = (lane & 1) ? -((lane & 2) ? mean[1] : mean[0]) * st0 : st0;
    asm volatile("" : "+v"(st0));
  }
      if (lane < 4)
        __hip_atomic_store((GAS u64*)(P.ws + WS_ST + tok * 16u + (unsigned)lane * 8u), ((u64)(unsigned)tag << 32) | f2u(st0), __ATOMIC_RELAXED, __HIP_MEMORY_SCOPE_AGENT);
    }
  }
  asm volatile("" : "+v"(gr));
  STAMP(2);
  BAR();
  if (__builtin_amdgcn_ballot_w64(gr.y != (unsigned)tag || gr.w != (unsigned)tag) != 0) {
    const __amdgpu_buffer_rsrc_t rs = __builtin_amdgcn_make_buffer_rsrc((void*)(P.ws + WS_ST), 0, 32768, 0x00020000);
#pragma unroll 1
    for (unsigned spin = 0; spin < kSpin; ++spin) {
      gr = __builtin_amdgcn_raw_buffer_load_b128(rs, token * 16u, 0, 16);
      if (__builtin_amdgcn_ballot_w64(gr.y != (unsigned)tag || gr.w != (unsigned)tag) == 0) break;
      __builtin_amdgcn_s_sleep(1);
    }
  }
  const float rstd = u2f(gr.x), mr = u2f(gr.z);
  if (hh == 0) *(LAS f32x2*)(size_t)(L_STATS + (unsigned)(32 * tq + t) * 8u) = f32x2{rstd, mr};
  if (ffn) VMW(8); else VMW(4);
  BAR();
  const int cb = (lane >> 4) & 1, q = (lane >> 2) & 3, pp = lane & 3, sw = (q >> 1) & 1;
  unsigned wl[2];
#pragma unroll
  for (int h = 0; h < 2; ++h) wl[h] = (unsigned)(8 * hh + q) * 128u + (unsigned)((4 * (h ^ sw) + 2 * cb + (pp >> 1)) * 16 + 8 * (pp & 1));
  const __amdgpu_buffer_rsrc_t rsHB = __builtin_amdgcn_make_buffer_rsrc(P.ws + WS_HB, 0, HB_BYTES, 0x00020000);
  const unsigned T32 = (unsigned)(c.p * 4 + tq);
  f32x4 cs = {0.f, 0.f, 0.f, 0.f};
  auto cs_sync = [&](int tile, bool wr) AI {
    const float s = xhalf_add(cs[0]);
    if (hh == 0 && wr) *(LAS float*)(size_t)(L_CS1 + (unsigned)tile * 128u + (unsigned)t * 4u) = s;
    asm volatile("s_waitcnt lgkmcnt(0)" ::: "memory");
    lds_inc(L_CNT + C_CS1 * 4, lane);
    lds_wait_ge(L_CNT + C_CS1 * 4, 4);
  };
  if constexpr (ffn) {
    const unsigned w2a = ((g & 1) ? wl[1] : wl[0]) + 16384u;
    int ti[3];
    unsigned wb[3];
    f32x16 acc[3];
#pragma unroll
    for (int i = 0; i < 3; ++i) { ti[i] = (i + tq) % 3; wb[i] = ti[i] == 0 ? wl[0] : (ti[i] == 1 ? wl[1] : w2a); acc[i] = zero16(); }
    if (PRIO1) __builtin_amdgcn_s_setprio(PRIO1);
    pass1<3, 8>(P, wid, amp_a, amp_b, fill, xr, acc, cs, wb, lane);
    if (PRIO1) __builtin_amdgcn_s_setprio(0);
    cs_sync(ti[0], tq < 3);
    STAMP(3);
    if (PRIOL) __builtin_amdgcn_s_setprio(3);
    f32x4 csv[3][4], bsv[3][4];
#pragma unroll
    for (int i = 0; i < 3; ++i) { load16(L_CS1 + (unsigned)ti[i] * 128u, hh, csv[i]); load16(L_B1 + (unsigned)(4 + ti[i]) * 128u, hh, bsv[i]); }
#pragma unroll
    for (int i = 0; i < 3; ++i)
      ffn_tile(acc[i], rstd, mr, csv[i], bsv[i], rsHB, (unsigned)lane * 16u + (T32 * 144u + (unsigned)(64 + 6 * g + 2 * ti[i])) * 1024u);
    publish_last(L_CNT + C_LATE * 4, 4, P.ws, FL_L + (unsigned)(c.p * 8 + g) * 128u, tag, lane);
    if (PRIOL) __builtin_amdgcn_s_setprio(0);
  } else {
    int ti[2];
    unsigned wb[2];
    f32x16 acc[2];
#pragma unroll
    for (int i = 0; i < 2; ++i) { ti[i] = (i + tq) & 1; wb[i] = ti[i] == 0 ? wl[0] : wl[1]; acc[i] = zero16(); }
    if (PRIO1) __builtin_amdgcn_s_setprio(PRIO1);
    pass1<2, 4>(P, wid, amp_a, amp_b, fill, xr, acc, cs, wb, lane);
    if (PRIO1) __builtin_amdgcn_s_setprio(0);
    cs_sync(ti[0], tq < 2);
    STAMP(3);
    v4u pq[4][2];
#pragma unroll
    for (int hd = 0; hd < 4; ++hd) {
      float y[8];
      fold8(acc[hd >> 1], 8 * (hd & 1), rstd, mr, L_CS1 + (unsigned)ti[hd >> 1] * 128u, hh, y);
      hedge(mfma(wphi_frag(2 * ti[hd >> 1] + (hd & 1), lane), pack8(y), zero16()), pq[hd][0], pq[hd][1]);
    }
    asm volatile("" : "+v"(pq[0][0]), "+v"(pq[0][1]), "+v"(pq[1][0]), "+v"(pq[1][1]), "+v"(pq[2][0]), "+v"(pq[2][1]), "+v"(pq[3][0]), "+v"(pq[3][1]));
    STAMP(4);
    if (PRIO) __builtin_amdgcn_s_setprio(3);
    if (tq == 0) { poll_flags(P.ws, FL_K + (unsigned)g * 16u * 128u, 16, tag, lane); lds_set(L_CNT + C_KVRDY * 4, 1); }
    else lds_wait_ge(L_CNT + C_KVRDY * 4, 1);
    STAMP(5);
    {
      const __amdgpu_buffer_rsrc_t rsKV = __builtin_amdgcn_make_buffer_rsrc(P.ws + WS_KVP, 0, 524288 + 65536, 0x00020000);
      const unsigned hbase = (unsigned)(g * 4 + tq) * 16u;
      v4u pv[16];
#pragma unroll
      for (int i = 0; i < 16; ++i) pv[i] = __builtin_amdgcn_raw_buffer_load_b128(rsKV, (unsigned)lane * 16u, (hbase + (unsigned)i) * 1024u, 16);
      const v4u z0 = __builtin_amdgcn_raw_buffer_load_b128(rsKV, (unsigned)lane * 16u, 524288u + hbase * 128u, 16);
      const v4u z1 = __builtin_amdgcn_raw_buffer_load_b128(rsKV, (unsigned)lane * 16u, 524288u + hbase * 128u + 1024u, 16);
      const u64 lf = __hip_atomic_load((const GAS u64*)(P.ws + FL_L + (unsigned)(c.tt >> 1) * 8u * 128u + (unsigned)(lane & 7) * 128u), __ATOMIC_RELAXED, __HIP_MEMORY_SCOPE_AGENT);
      __builtin_amdgcn_sched_barrier(0);
      v4u idf;
      {
        const int m = lane & 31, h2 = lane >> 5;
        const int j = (m < 16 && ((m >> 2) & 1) == h2) ? (((m >> 3) << 2) | (m & 3)) : -1;
#pragma unroll
        for (int k = 0; k < 4; ++k) idf[k] = (j == 2 * k ? 0x3F80u : 0u) | (j == 2 * k + 1 ? 0x3F800000u : 0u);
      }
      f32x16 sum = zero16();
#pragma unroll
      for (int i = 0; i < 16; ++i) sum = mfma(idf, pv[i], sum);
      asm volatile("" : "+v"(sum));
      lds_inc(L_CNT + C_KVIN * 4, lane);
      if (!ORD && tq == 0 && __builtin_amdgcn_ballot_w64(lf != tag) == 0) lds_set(L_CNT + C_LATERDY * 4, 1);
      const float s[8] = {sum[0], sum[1], sum[2], sum[3], sum[4], sum[5], sum[6], sum[7]};
      const unsigned afr = L_AFR + (unsigned)tq * 2048u;
      *(LAS v4u*)(size_t)(afr + (unsigned)((lane >> 4) & 1) * 1024u + (unsigned)((lane & 15) + 32 * (lane >> 5)) * 16u) = pack8(s);
      const unsigned hh2 = (unsigned)(lane >> 4) & 1u, c16 = (unsigned)lane & 15u;
      float zz[4];
#pragma unroll
      for (int k = 0; k < 4; ++k) {
        float v = dppadd<0x128>(u2f(z0[k]) + u2f(z1[k]));
        auto r = __builtin_amdgcn_permlane16_swap(f2u(v), f2u(v), false, false);
        zz[k] = xhalf_add(u2f(r[0]) + u2f(r[1]));
      }
      if (lane < 8) *(LAS f32x4*)(size_t)(L_ZT + (unsigned)tq * 128u + (unsigned)lane * 16u) = f32x4{zz[0], zz[1], zz[2], zz[3]};
      const unsigned f0 = 16u * (unsigned)(lane >> 5) + 4u * hh2;
      const f32x4 za = lds128f(L_ZT + (unsigned)tq * 128u + f0 * 4u), zb = lds128f(L_ZT + (unsigned)tq * 128u + (f0 + 8u) * 4u);
      *(LAS v4u*)(size_t)(afr + (unsigned)(lane >> 5) * 1024u + (16u + c16 + 32u * hh2) * 16u) = v4u{pk2(za[0], za[1]), pk2(za[2], za[3]), pk2(zb[0], zb[1]), pk2(zb[2], zb[3])};
      asm volatile("s_waitcnt lgkmcnt(0)" ::: "memory");
      lds_inc(L_CNT + C_AFR * 4, lane);
      lds_wait_ge(L_CNT + C_AFR * 4, 4);
    }
#pragma unroll
    for (int hd = 0; hd < 4; ++hd) {
      const int head = 2 * ti[hd >> 1] + (hd & 1);
      f32x16 d = mfma(lds128(L_AFR + (unsigned)head * 2048u + (unsigned)lane * 16u), pq[hd][0], zero16());
      d = mfma(lds128(L_AFR + (unsigned)head * 2048u + 1024u + (unsigned)lane * 16u), pq[hd][1], d);
      const float r = __builtin_amdgcn_rcpf(d[8] + 1e-6f);
      const v4u o = v4u{pk2(d[0] * r, d[1] * r), pk2(d[2] * r, d[3] * r), pk2(d[4] * r, d[5] * r), pk2(d[6] * r, d[7] * r)};
      st128(o, rsHB, (unsigned)lane * 16u + (T32 * 144u + (unsigned)(112 + 4 * g + head)) * 1024u);
    }
    publish_last(L_CNT + C_O * 4, 4, P.ws, FL_O + (unsigned)(c.p * 8 + g) * 128u, tag, lane);
    if (PRIO) __builtin_amdgcn_s_setprio(0);
  }
  STAMP(6);
  return tag;
}

DI void role_b(const Params& P, const Ctx& c, int wid, u64 amp_a0) {
  u64 amp_a = amp_a0, amp_b = 0; (void)amp_a; (void)amp_b;
  const int lane = c.lane, tq = c.tq;
  const u64 tag = c.ffn ? role_b_head<true>(P, c, wid) : role_b_head<false>(P, c, wid);
  const int pan = c.tt >> 1;
  const int tb = tq * 64 + lane;
  v4u xres[2], bres;
#pragma unroll
  for (int m = 0; m < 2; ++m) {
    const int idx = tb + 256 * m;
    xres[m] = *(const GAS v4u*)(P.x + (size_t)(64 * c.tt + (idx >> 3)) * 512 + 64 * c.ct + 8 * (idx & 7));
  }
  bres = *(const GAS v4u*)(P.b2 + 64 * c.ct + 8 * (tb & 7));
  if (tq == 0) {
    poll_flags(P.ws, (ORD ? FL_O : FL_L) + (unsigned)pan * 8u * 128u, 8, tag, lane);
    lds_set(L_CNT + (ORD ? C_ORDY : C_LATERDY) * 4, 1);
    poll_flags(P.ws, (ORD ? FL_L : FL_O) + (unsigned)pan * 8u * 128u, 8, tag, lane);
    lds_set(L_CNT + (ORD ? C_LATERDY : C_ORDY) * 4, 1);
  } else if (tq == 1 && __builtin_amdgcn_workgroup_id_x() == 0) {
    poll_flags(P.ws, FL_E, 64, tag, lane); poll_flags(P.ws, FL_E + 64 * 128, 64, tag, lane);
    poll_flags(P.ws, FL_K, 64, tag, lane); poll_flags(P.ws, FL_K + 64 * 128, 64, tag, lane);
    if (lane == 0) __hip_atomic_store((GAS u64*)(P.ws + WS_EPOCH), tag, __ATOMIC_RELAXED, __HIP_MEMORY_SCOPE_AGENT);
  }
  BAR();
#pragma unroll
  for (int m = 0; m < 2; ++m) {
    const int idx = tb + 256 * m;
    const unsigned tok = (unsigned)(idx >> 3), cg = (unsigned)(idx & 7);
    const unsigned a = (tok >> 5) * SLOT + (tok & 31u) * 256u, c0 = ((2u * cg) ^ (tok & 15u)) * 16u, c1 = ((2u * cg + 1u) ^ (tok & 15u)) * 16u;
    f32x4 lo = lds128f(a + c0), hi = lds128f(a + c1);
#pragma unroll
    for (unsigned w = 1; w < 4; ++w) { lo += lds128f(w * 8192u + a + c0); hi += lds128f(w * 8192u + a + c1); }
    v4u o;
#pragma unroll
    for (int k = 0; k < 4; ++k) {
      const float a0 = k < 2 ? lo[2 * k] : hi[2 * k - 4], a1 = k < 2 ? lo[2 * k + 1] : hi[2 * k - 3];
      const float x0 = u2f(xres[m][k] << 16), x1 = u2f(xres[m][k] & 0xffff0000u);
      const float b0 = u2f(bres[k] << 16), b1 = u2f(bres[k] & 0xffff0000u);
      o[k] = pk2(a0 + x0 + b0, a1 + x1 + b1);
    }
    *(GAS v4u*)(P.out + (size_t)(64 * c.tt + (idx >> 3)) * 512 + 64 * c.ct + 8 * (idx & 7)) = o;
  }
  STAMP(7);

}

__global__ __launch_bounds__(512, 2) NOPK void fused_kernel(Params P) {
  __shared__ __attribute__((aligned(1024))) char smem[LDS_BYTES];
  asm volatile("" ::"v"((unsigned)(size_t)(LAS char*)smem) : "memory");
  u64 pc;
  asm volatile("s_getpc_b64 %0" : "=s"(pc));
  const int tid = __builtin_amdgcn_workitem_id_x(), lane = tid & 63;
  const int wid = __builtin_amdgcn_readfirstlane(tid >> 6);
  u64 amp_a = 0, amp_b = 0;
  STAMP(0);
  Ctx c;
  c.lane = lane; c.tq = wid & 3; c.hh = lane >> 5;
  const int blk = __builtin_amdgcn_workgroup_id_x(), xcd = blk & 7, j = blk >> 3, ja = j & 7, jb = j >> 3;
  c.p = (xcd & 1) * 8 + ja;
  c.G = (xcd >> 1) * 4 + jb;
  c.ffn = !(c.G & 1);
  c.gi = c.G >> 1;
  c.rot = ROT1 == 0 ? 0 : (ROT1 == 1 ? (((ja + jb) % 3) * 3) & 7 : (ROT1 == 2 ? (ja + 2 * jb) & 7 : (ROT1 == 3 ? ((ja + jb) & 1) * 4 : (ROT1 == 4 ? ((ja + jb) & 3) * 2 : ((ja + jb) & 3)))));
  if (MAP2) { c.tt = xcd * 4 + (j & 3); c.ct = j >> 2; c.ja = j & 3; c.jb = j >> 2; }
  else { c.tt = (xcd >> 1) * 8 + ja; c.ct = (xcd & 1) * 4 + jb; c.ja = ja; c.jb = jb; }
  c.rotE = ROT2 ? (2 * ja + 4 * jb) & 15 : 0; c.rotL = ROT2 ? (ja + 3 * jb) % 12 : 0; c.rotO = ROT2 ? (ja + 2 * jb) & 7 : 0;
  if (wid < 4) {
    const unsigned kb0 = (unsigned)(j + 32 * (wid & 1)), kb = kb0 < CODE_KB ? kb0 : 0u;
    const unsigned pf = CODEPF ? *(const GAS unsigned*)((pc & ~255ull) + kb * 1024u + (unsigned)lane * 16u) : 0u;
    const u64 e = __hip_atomic_load((const GAS u64*)(P.ws + WS_EPOCH), __ATOMIC_RELAXED, __HIP_MEMORY_SCOPE_AGENT);
    if (c.ffn) {
      const int tc = tid < 224 ? tid : 0;
      const int t7 = tc >> 5, n = tc & 31, g = c.gi;
      const int cA = 224 * g + 32 * (g & 1);
      const int col = t7 < 4 ? cA + 32 * t7 : (t7 < 6 ? cA + 128 + 32 * (t7 - 4) : ((g & 1) ? 224 * g : 224 * g + 192));
      const unsigned bv = P.b1[col + n];
      *(LAS float*)(size_t)(L_B1 + (unsigned)tid * 4u) = u2f(bv << 16);
    } else {
      const v4u wv = *(const GAS v4u*)(P.wphi + (size_t)c.gi * 1024 + (tid & 127) * 8);
      *(LAS v4u*)(size_t)(L_WPHI + (unsigned)(tid & 127) * 16u) = wv;
    }
    if (tid < 32) *(LAS unsigned*)(size_t)(L_CNT + (unsigned)tid * 4u) = 0u;
    const u64 tag = e + 1u;
    if (tid == 0) *(LAS v2u*)(size_t)L_TAG = v2u{(unsigned)tag, (unsigned)(tag >> 32)};
    asm volatile("" ::"v"(pf));
    STAMP(1);
    role_a(P, c, wid, amp_a, amp_b);
  } else {
    STAMP(1);
    role_b(P, c, wid, amp_a);
  }
}

}

extern "C" void kernel_launch(void* const* d_in, const int* in_sizes, int n_in, void* d_out, int out_size, void* d_ws, size_t ws_size,
                              hipStream_t stream) {
  Params P;
  P.x = (const u16*)d_in[0]; P.wqkv = (const u16*)d_in[1]; P.wphi = (const u16*)d_in[2]; P.wo = (const u16*)d_in[3];
  P.w1 = (const u16*)d_in[5]; P.b1 = (const u16*)d_in[6]; P.w2 = (const u16*)d_in[7]; P.b2 = (const u16*)d_in[8];
  P.out = (u16*)d_out; P.ws = (char*)d_ws;
  fused_kernel<<<256, 512, 0, stream>>>(P);
}
```

```cpp
#include <hip/hip_runtime.h>
#include <stdint.h>
#include <type_traits>

namespace {

typedef unsigned short u16;
typedef unsigned long long u64;
typedef unsigned v4u __attribute__((ext_vector_type(4)));
typedef unsigned v2u __attribute__((ext_vector_type(2)));
typedef float f32x16 __attribute__((ext_vector_type(16)));
typedef float f32x4 __attribute__((ext_vector_type(4)));
typedef float f32x2 __attribute__((ext_vector_type(2)));
typedef __bf16 bf16x8 __attribute__((ext_vector_type(8)));
typedef __bf16 bf16x2 __attribute__((ext_vector_type(2)));
typedef short s16x4 __attribute__((ext_vector_type(4)));
#define LAS __attribute__((address_space(3)))
#define GAS __attribute__((address_space(1)))
#if defined(__HIP_DEVICE_COMPILE__) && !defined(USEPK)
#define NOPK __attribute__((target("no-packed-fp32-ops")))
#else
#define NOPK
#endif
#define DI __device__ __forceinline__ NOPK
#define AI __attribute__((always_inline)) NOPK

#define STAMPS 0
#define ROT1 1
#define ROT2 0
#define STR2(x) #x
#define STR(x) STR2(x)
#define W1 8
#define W2 4
#define PRIO 1
#define SLP 1
#define KVFIRST 0
#define PRIOL 1
#define MAP2 0
#define NBK 4
#define PRIO1 0
#define PRIOA 0
#define ORD 0
#define CODEPF 1
#define WARM2 1
#define CODE_KB 41

constexpr unsigned WS_EPOCH = 0;
constexpr unsigned FL_E = 4096;
constexpr unsigned FL_L = FL_E + 128 * 128;
constexpr unsigned FL_K = FL_L + 128 * 128;
constexpr unsigned FL_O = FL_K + 128 * 128;
constexpr unsigned WS_ST = 73728;
constexpr unsigned WS_KVP = 131072;
constexpr unsigned WS_ZP = WS_KVP + 524288;
constexpr unsigned WS_HB = 1048576;
constexpr unsigned HB_BYTES = 64u * 144u * 1024u;
constexpr unsigned WS_STAMP = 16u << 20;

constexpr unsigned SLOT = 32768;
constexpr unsigned L_MISC = 131072;
constexpr unsigned L_STATS = L_MISC;
constexpr unsigned L_CS0 = L_MISC + 1024;
constexpr unsigned L_CS1 = L_MISC + 1536;
constexpr unsigned L_B1 = L_MISC + 2048;
constexpr unsigned L_WPHI = L_MISC + 3072;
constexpr unsigned L_CNT = L_MISC + 5120;
constexpr unsigned L_TAG = L_MISC + 5248;
constexpr unsigned L_ZT = L_MISC + 5376;
constexpr unsigned L_AFR = L_MISC + 8192;
constexpr unsigned LDS_BYTES = 163840;
enum { C_CS1 = 0, C_LATE = 1, C_KV = 2, C_O = 3, C_AFR = 4, C_KVRDY = 5, C_LATERDY = 6, C_ORDY = 7, C_FREE = 8  , C_LAND = 12  , C_EARLY = 16, C_IMG = 17, C_KVIN = 18, C_IMGDONE = 19 };

constexpr unsigned kSpin = 1u << 16;

struct Params {
  const u16* x; const u16* wqkv; const u16* wphi; const u16* wo;
  const u16* w1; const u16* b1; const u16* w2; const u16* b2;
  u16* out; char* ws;
};

DI unsigned f2u(float f) { return __builtin_bit_cast(unsigned, f); }
DI float u2f(unsigned u) { return __builtin_bit_cast(float, u); }
DI float fmx(float a, float b) { return __builtin_fmaxf(a, b); }
DI f32x16 mfma(v4u a, v4u b, f32x16 c) {
  return __builtin_amdgcn_mfma_f32_32x32x16_bf16(__builtin_bit_cast(bf16x8, a), __builtin_bit_cast(bf16x8, b), c, 0, 0, 0);
}
DI f32x16 zero16() { f32x16 z; for (int r = 0; r < 16; ++r) z[r] = 0.f; return z; }
DI v4u lds128(unsigned a) { return *(const LAS v4u*)(size_t)a; }
DI f32x4 lds128f(unsigned a) { return *(const LAS f32x4*)(size_t)a; }
DI v2u tr64(unsigned a) { return __builtin_bit_cast(v2u, __builtin_amdgcn_ds_read_tr16_b64_v4i16((LAS s16x4*)(size_t)a)); }
DI v4u tr2(unsigned a0, unsigned a1) { v2u l = tr64(a0), h = tr64(a1); return v4u{l.x, l.y, h.x, h.y}; }
DI unsigned pk2(float a, float b) { return __builtin_bit_cast(unsigned, __builtin_convertvector(f32x2{a, b}, bf16x2)); }
DI float xhalf_add(float v) { auto r = __builtin_amdgcn_permlane32_swap(f2u(v), f2u(v), false, false); return u2f(r[0]) + u2f(r[1]); }
DI float xhalf_max(float v) { auto r = __builtin_amdgcn_permlane32_swap(f2u(v), f2u(v), false, false); return fmx(u2f(r[0]), u2f(r[1])); }
DI float shx(float v, int m, int lane) { return u2f((unsigned)__builtin_amdgcn_ds_bpermute((lane ^ m) << 2, (int)f2u(v))); }
DI f32x4 lanesum(v4u f, f32x4 c) {
  const s16x4 one = {0x3F80, 0x3F80, 0x3F80, 0x3F80};
  c = __builtin_amdgcn_mfma_f32_4x4x4bf16_1k(one, __builtin_bit_cast(s16x4, v2u{f.x, f.y}), c, 0, 0, 0);
  return __builtin_amdgcn_mfma_f32_4x4x4bf16_1k(one, __builtin_bit_cast(s16x4, v2u{f.z, f.w}), c, 0, 0, 0);
}
template <int CTRL> DI float dppadd(float v) { return v + u2f((unsigned)__builtin_amdgcn_update_dpp(0, (int)f2u(v), CTRL, 0xf, 0xf, true)); }
DI float wavesum(float v) {
  v = dppadd<0xB1>(v); v = dppadd<0x4E>(v); v = dppadd<0x141>(v); v = dppadd<0x140>(v);
  auto r = __builtin_amdgcn_permlane16_swap(f2u(v), f2u(v), false, false);
  return xhalf_add(u2f(r[0]) + u2f(r[1]));
}
DI void dma(unsigned m0v, unsigned voff, __amdgpu_buffer_rsrc_t rs, unsigned soff) {
  asm volatile("s_mov_b32 m0, %0\n\ts_nop 0\n\tbuffer_load_dwordx4 %1, %2, %3 offen lds" ::"s"(m0v), "v"(voff), "s"(rs), "s"(soff) : "memory");
}
DI void st128(v4u v, __amdgpu_buffer_rsrc_t rs, unsigned voff) { __builtin_amdgcn_raw_buffer_store_b128(v, rs, voff, 0, 16); }
#define BAR() do { __builtin_amdgcn_sched_barrier(0); asm volatile("s_waitcnt lgkmcnt(0)\n\ts_barrier" ::: "memory"); __builtin_amdgcn_sched_barrier(0); } while (0)
#define VMW(n) asm volatile("s_waitcnt vmcnt(" #n ")" ::: "memory")
constexpr unsigned ONES = 0x3F803F80u;

#define STAMP(i) do { } while (0)

DI void poll_flags(const char* ws, unsigned base, int n, u64 tag, int lane) {
  const GAS u64* f = (const GAS u64*)(ws + base + (unsigned)(lane < n ? lane : 0) * 128u);
#pragma unroll 1
  for (unsigned spin = 0; spin < kSpin; ++spin) {
    const u64 v = __hip_atomic_load(f, __ATOMIC_RELAXED, __HIP_MEMORY_SCOPE_AGENT);
    if (__builtin_amdgcn_ballot_w64(v != tag) == 0) break;
    __builtin_amdgcn_s_sleep(1);
  }
  __builtin_amdgcn_fence(__ATOMIC_ACQUIRE, "wavefront");
  asm volatile("" ::: "memory");
}
DI void lds_wait_ge(unsigned addr, unsigned want) {
#pragma unroll 1
  for (unsigned spin = 0; spin < (kSpin << 4); ++spin) {
    const unsigned v = *(volatile LAS unsigned*)(size_t)addr;
    if ((unsigned)__builtin_amdgcn_readfirstlane(v) >= want) break;
    if (SLP) __builtin_amdgcn_s_sleep(SLP);
  }
  asm volatile("" ::: "memory");
}
DI void lds_set(unsigned addr, unsigned v) { *(volatile LAS unsigned*)(size_t)addr = v; }
DI void lds_inc(unsigned addr, int lane) { if (lane == 0) __hip_atomic_fetch_add((LAS unsigned*)(size_t)addr, 1u, __ATOMIC_RELAXED, __HIP_MEMORY_SCOPE_WORKGROUP); }
DI void publish_last(unsigned cnt_addr, unsigned n, char* ws, unsigned flag_off, u64 tag, int lane) {
  VMW(0);
  if (lane == 0) {
    const unsigned old = __hip_atomic_fetch_add((LAS unsigned*)(size_t)cnt_addr, 1u, __ATOMIC_RELAXED, __HIP_MEMORY_SCOPE_WORKGROUP);
    if (old == n - 1) __hip_atomic_store((GAS u64*)(ws + flag_off), tag, __ATOMIC_RELAXED, __HIP_MEMORY_SCOPE_AGENT);
  }
}
DI u64 lds_tag() { const v2u t = *(const LAS v2u*)(size_t)L_TAG; return ((u64)(unsigned)__builtin_amdgcn_readfirstlane(t.y) << 32) | (unsigned)__builtin_amdgcn_readfirstlane(t.x); }

DI void load16(unsigned a, int hh, f32x4* v) {
#pragma unroll
  for (int g = 0; g < 4; ++g) v[g] = lds128f(a + (unsigned)g * 32u + (unsigned)hh * 16u);
}
DI void ffn_tile(const f32x16& acc, float rstd, float mr, const f32x4* cs, const f32x4* bs, __amdgpu_buffer_rsrc_t rsHB, unsigned voff) {
  f32x2 y[8], d[8];
#pragma unroll
  for (int g = 0; g < 4; ++g) {
    const f32x4 t = cs[g] * mr + bs[g];
    y[2 * g] = f32x2{acc[4 * g], acc[4 * g + 1]} * rstd + f32x2{t[0], t[1]};
    y[2 * g + 1] = f32x2{acc[4 * g + 2], acc[4 * g + 3]} * rstd + f32x2{t[2], t[3]};
  }
#pragma unroll
  for (int k = 0; k < 8; ++k) d[k] = y[k] * ((y[k] * y[k]) * (-0.10294324f) + (-2.30220819f));
#pragma unroll
  for (int k = 0; k < 8; ++k) { d[k].x = __builtin_amdgcn_exp2f(d[k].x); d[k].y = __builtin_amdgcn_exp2f(d[k].y); }
#pragma unroll
  for (int k = 0; k < 8; ++k) d[k] += 1.0f;
#pragma unroll
  for (int k = 0; k < 8; ++k) { d[k].x = __builtin_amdgcn_rcpf(d[k].x); d[k].y = __builtin_amdgcn_rcpf(d[k].y); }
  v4u o0, o1;
#pragma unroll
  for (int k = 0; k < 4; ++k) {
    const f32x2 g0 = y[k] * d[k], g1 = y[4 + k] * d[4 + k];
    o0[k] = pk2(g0.x, g0.y); o1[k] = pk2(g1.x, g1.y);
  }
  st128(o0, rsHB, voff); st128(o1, rsHB, voff + 1024u);
}
DI float max3(float a, float b, float c) { return __builtin_elementwise_maximum(__builtin_elementwise_maximum(a, b), c); }
DI void hedge4(const f32x16 (&d)[4], v4u (&o)[4][2]) {
  constexpr float L = 1.4426950408889634f;
  float m[4][2], e[4][16], s[4][2];
#pragma unroll
  for (int u = 0; u < 4; ++u)
#pragma unroll
    for (int h = 0; h < 2; ++h) {
      const float a = max3(d[u][8 * h], d[u][8 * h + 1], d[u][8 * h + 2]), b = max3(d[u][8 * h + 3], d[u][8 * h + 4], d[u][8 * h + 5]);
      m[u][h] = max3(max3(d[u][8 * h + 6], d[u][8 * h + 7], a), b, b);
    }
#pragma unroll
  for (int u = 0; u < 4; ++u)
#pragma unroll
    for (int h = 0; h < 2; ++h) {
      auto r = __builtin_amdgcn_permlane32_swap(f2u(m[u][h]), f2u(m[u][h]), false, false);
      m[u][h] = __builtin_elementwise_maximum(u2f(r[0]), u2f(r[1])) * -L;
    }
#pragma unroll
  for (int u = 0; u < 4; ++u)
#pragma unroll
    for (int i = 0; i < 16; ++i) e[u][i] = __builtin_amdgcn_exp2f(__builtin_fmaf(d[u][i], L, m[u][i >> 3]));
#pragma unroll
  for (int u = 0; u < 4; ++u)
#pragma unroll
    for (int h = 0; h < 2; ++h) {
      const float* x = &e[u][8 * h];
      s[u][h] = ((x[0] + x[1]) + (x[2] + x[3])) + ((x[4] + x[5]) + (x[6] + x[7]));
    }
#pragma unroll
  for (int u = 0; u < 4; ++u)
#pragma unroll
    for (int h = 0; h < 2; ++h) s[u][h] = __builtin_amdgcn_rcpf(xhalf_add(s[u][h]));
#pragma unroll
  for (int u = 0; u < 4; ++u)
#pragma unroll
    for (int h = 0; h < 2; ++h)
#pragma unroll
      for (int k = 0; k < 4; ++k) o[u][h][k] = pk2(e[u][8 * h + 2 * k] * s[u][h], e[u][8 * h + 2 * k + 1] * s[u][h]);
}
DI v4u fold_pack(const f32x16& acc, int hf, float rstd, float mr, const f32x4* cs) {
  float y[8];
#pragma unroll
  for (int k = 0; k < 8; ++k) y[k] = __builtin_fmaf(acc[8 * hf + k], rstd, cs[2 * hf + (k >> 2)][k & 3] * mr);
  return v4u{pk2(y[0], y[1]), pk2(y[2], y[3]), pk2(y[4], y[5]), pk2(y[6], y[7])};
}
DI v4u wphi_frag(int hd, int lane) {
  const int hh = lane >> 5, q = (lane >> 2) & 3, pp = lane & 3;
  const unsigned a = L_WPHI + (unsigned)hd * 512u + (unsigned)(4 * hh + q) * 32u + (unsigned)pp * 8u;
  v4u f = tr2(a, a + 256u);
  const unsigned sg = (lane & 16) ? 0x80008000u : 0u;
  f.x ^= sg; f.y ^= sg; f.z ^= sg; f.w ^= sg;
  return f;
}
DI v4u selv(bool s, v4u a, v4u b) { const unsigned m = s ? 0xffffffffu : 0u; return v4u{(a.x & m) | (b.x & ~m), (a.y & m) | (b.y & ~m), (a.z & m) | (b.z & ~m), (a.w & m) | (b.w & ~m)}; }
DI v4u pack8(const float* y) { return v4u{pk2(y[0], y[1]), pk2(y[2], y[3]), pk2(y[4], y[5]), pk2(y[6], y[7])}; }

constexpr int NWI = MAP2 ? 18 : 9;
template <int NI> DI void warm_w2(const Params& P, int ct, int ja, int tq, int i0, int lane, v4u* d) {
#pragma unroll
  for (int i = 0; i < NI; ++i) {
    const int row = 32 * NWI * ja + 8 * NWI * tq + 8 * (i0 + i);
    const u64 a = row < 1792 ? (u64)(size_t)P.w2 + (u64)(unsigned)row * 1024u : (u64)(size_t)P.wo + (u64)(unsigned)(row - 1792) * 1024u;
    const u64 base = ((u64)(unsigned)__builtin_amdgcn_readfirstlane((unsigned)(a >> 32)) << 32) | (unsigned)__builtin_amdgcn_readfirstlane((unsigned)a);
    const unsigned voff = (unsigned)(lane >> 3) * 1024u + (unsigned)(lane & 7) * 16u + (unsigned)ct * 128u;
    asm volatile("s_nop 4\n\tglobal_load_dwordx4 %0, %1, %2" : "+v"(d[i % 3]) : "v"(voff), "s"(base) : "memory");
  }
}
struct Ctx {
  int lane, tq, hh;
  int p, gi, G;
  int rot;
  int tt, ct, ja, jb;
  int rotE, rotL, rotO;
  bool ffn;
};

DI void role_a(const Params& P, const Ctx& c, int wid, u64 amp_a, u64 amp_b) {
  const int lane = c.lane, tq = c.tq, hh = c.hh, t = lane & 31;
  const int cb = (lane >> 4) & 1, q = (lane >> 2) & 3, pp = lane & 3, sw = (q >> 1) & 1;
  unsigned tabB, tabWlo, tabWhi;
  {
    const int jj = lane < 36 ? lane : 35;
    constexpr int J2 = ORD ? 24 : 28;
    int sg;
    auto perm = [&](int i, int n4) AI {
      if (!ROT2) return i;
      constexpr int NO = MAP2 ? 4 : 2;
      const int b = (c.jb >> 1) & (NO - 1), ng = 4 * n4 / NO;
      if (i < 2) return NO * i + b;
      const int m = (i - 2) / NO, r = (i - 2) % NO;
      if (m + 2 < ng) return r < NO - 1 ? NO * m + ((b + 1 + r) & (NO - 1)) : NO * (m + 2) + b;
      const int e = i - 2 - NO * (ng - 2);
      return NO * (ng - 2 + e / (NO - 1)) + ((b + 1 + e % (NO - 1)) & (NO - 1));
    };
    if (jj < 16) sg = 4 * perm(jj, 4) + tq;
    else if ((jj < J2) != (ORD != 0)) sg = 64 + 4 * perm(jj - (ORD ? 24 : 16), 3) + tq;
    else sg = 112 + 4 * perm(jj - (ORD ? 16 : 28), 2) + tq;
    int row;
    if (sg < 64) { const int g = sg >> 3; row = 224 * g + 32 * (g & 1) + 16 * (sg & 7); }
    else if (sg < 112) {
      const int e = sg - 64, g = e / 6, i = e - 6 * g;
      row = i < 4 ? 224 * g + 32 * (g & 1) + 128 + 16 * i : ((g & 1) ? 224 * g : 224 * g + 192) + 16 * (i - 4);
    } else row = 16 * (sg - 112);
    const u64 a = (u64)(size_t)(sg < 112 ? P.w2 : P.wo) + (u64)row * 1024u + (u64)c.ct * 128u;
    tabB = (unsigned)sg * 1024u; tabWlo = (unsigned)a; tabWhi = (unsigned)(a >> 32);
  }
  asm volatile("" : "+v"(tabB), "+v"(tabWlo), "+v"(tabWhi));
  const int th = tq & 1, up = tq >> 1;
  unsigned xa[4];
#pragma unroll
  for (int ks = 0; ks < 4; ++ks) xa[ks] = (unsigned)(64 * th + t) * 128u + (unsigned)(((2 * ks + hh) ^ ((t >> 1) & 7)) * 16);
  const unsigned wl = 16384u + (unsigned)(8 * hh + q) * 128u + (unsigned)((4 * (up ^ sw) + 2 * cb + (pp >> 1)) * 16 + 8 * (pp & 1));
  f32x16 acc[2][2];
#pragma unroll
  for (int i = 0; i < 2; ++i) { acc[i][0] = zero16(); acc[i][1] = zero16(); }
  f32x4 cs = {0.f, 0.f, 0.f, 0.f};
  unsigned x0 = xa[0], x1 = xa[1], x2 = xa[2], x3 = xa[3], w0 = wl;
  v4u xf[4][2], wf[4][2];
  auto rd = [&](int ks, v4u* X, v4u* W) AI {
    const unsigned xb = ks == 0 ? x0 : (ks == 1 ? x1 : (ks == 2 ? x2 : x3));
    X[0] = lds128(xb); X[1] = lds128(xb + 4096u);
    const unsigned a = w0 + (unsigned)(ks * 2048);
    W[0] = tr2(a, a + 512u); W[1] = tr2(a + 8192u, a + 8704u);
  };
  f32x4 cs2 = {0.f, 0.f, 0.f, 0.f};
  BAR();
  rd(0, xf[0], wf[0]); rd(1, xf[1], wf[1]);
  auto pass0 = [&](auto TH) AI {
    constexpr int CT = decltype(TH)::value;
    const s16x4 one = {0x3F80, 0x3F80, 0x3F80, 0x3F80};
#pragma unroll 1
    for (int kc = 0; kc < 8; ++kc) {
#pragma unroll
      for (int ks = 0; ks < 4; ++ks) {
        if (ks == 2) {
          __builtin_amdgcn_sched_barrier(0);
          asm volatile("s_barrier" ::: "memory");
          __builtin_amdgcn_sched_barrier(0);
          const unsigned sb = (unsigned)((kc + 1) & 3) * SLOT;
          x0 = xa[0] + sb; x1 = xa[1] + sb; w0 = wl + sb;
          asm volatile("" : "+v"(x0), "+v"(x1), "+v"(w0));
        }
        if (ks == 0) {
          const unsigned sb = (unsigned)(kc & 3) * SLOT;
          x2 = xa[2] + sb; x3 = xa[3] + sb;
          asm volatile("" : "+v"(x2), "+v"(x3));
        }
        rd((ks + 2) & 3, xf[(ks + 2) & 3], wf[(ks + 2) & 3]);
        acc[0][0] = mfma(wf[ks][0], xf[ks][0], acc[0][0]); acc[0][1] = mfma(wf[ks][0], xf[ks][1], acc[0][1]);
        acc[1][0] = mfma(wf[ks][1], xf[ks][0], acc[1][0]); acc[1][1] = mfma(wf[ks][1], xf[ks][1], acc[1][1]);
        cs = __builtin_amdgcn_mfma_f32_4x4x4bf16_1k(one, __builtin_bit_cast(s16x4, v2u{wf[ks][CT].x, wf[ks][CT].y}), cs, 0, 0, 0);
        cs2 = __builtin_amdgcn_mfma_f32_4x4x4bf16_1k(one, __builtin_bit_cast(s16x4, v2u{wf[ks][CT].z, wf[ks][CT].w}), cs2, 0, 0, 0);
        __builtin_amdgcn_sched_group_barrier(0x008, 1, 0);
        __builtin_amdgcn_sched_group_barrier(0x100, 2, 0);
        __builtin_amdgcn_sched_group_barrier(0x008, 1, 0);
        __builtin_amdgcn_sched_group_barrier(0x100, 2, 0);
        __builtin_amdgcn_sched_group_barrier(0x008, 1, 0);
        __builtin_amdgcn_sched_group_barrier(0x100, 2, 0);
        __builtin_amdgcn_sched_group_barrier(0x008, 3, 0);
        __builtin_amdgcn_sched_barrier(0);
      }
      asm volatile("" : "+v"(acc[0][0]), "+v"(acc[0][1]), "+v"(acc[1][0]), "+v"(acc[1][1]), "+v"(cs), "+v"(cs2));
    }
  };
  if (th) pass0(std::integral_constant<int, 1>{}); else pass0(std::integral_constant<int, 0>{});
  cs[0] += cs2[0];
  STAMP(2);
  {
    const float s = xhalf_add(cs[0]);
    if (hh == 0) *(LAS float*)(size_t)(L_CS0 + (unsigned)(up + 2 * th) * 128u + (unsigned)t * 4u) = s;
  }
  BAR();
  const u64 tag = lds_tag();
  float rstd[2], mr[2];
#pragma unroll
  for (int b = 0; b < 2; ++b) {
    const f32x2 st = *(const LAS f32x2*)(size_t)(L_STATS + (unsigned)(64 * th + 32 * b + t) * 8u);
    rstd[b] = st.x; mr[b] = st.y;
  }
  const __amdgpu_buffer_rsrc_t rsHB = __builtin_amdgcn_make_buffer_rsrc(P.ws + WS_HB, 0, HB_BYTES, 0x00020000);
  v4u wm[3] = {v4u{0u, 0u, 0u, 0u}, v4u{0u, 0u, 0u, 0u}, v4u{0u, 0u, 0u, 0u}};
  if (c.ffn) {
    f32x4 csv[2][4], bsv[2][4];
#pragma unroll
    for (int i = 0; i < 2; ++i) { load16(L_CS0 + (unsigned)(up + 2 * i) * 128u, hh, csv[i]); load16(L_B1 + (unsigned)(up + 2 * i) * 128u, hh, bsv[i]); }
#pragma unroll
    for (int i = 0; i < 2; ++i)
#pragma unroll
      for (int b = 0; b < 2; ++b) {
        const unsigned tile = (unsigned)(up + 2 * i), T32 = (unsigned)(c.p * 4 + 2 * th + b);
        ffn_tile(acc[i][b], rstd[b], mr[b], csv[i], bsv[i], rsHB, (unsigned)lane * 16u + (T32 * 144u + (unsigned)(8 * c.gi) + 2u * tile) * 1024u);
        if (WARM2 && 2 * i + b < 3) warm_w2<NWI / 3>(P, c.ct, c.ja, tq, (NWI / 3) * (2 * i + b), lane, wm);
      }
    STAMP(8);
    publish_last(L_CNT + C_EARLY * 4, 4, P.ws, FL_E + (unsigned)(c.p * 8 + c.gi) * 128u, tag, lane);
    if (WARM2) asm volatile("" :: "v"(wm[0]), "v"(wm[1]), "v"(wm[2]));
  } else {
    f32x4 csk[4], csw[4];
    load16(L_CS0 + (unsigned)up * 128u, hh, csk); load16(L_CS0 + (unsigned)(2 + up) * 128u, hh, csw);
    const v4u wp[2] = {wphi_frag(2 * up, lane), wphi_frag(2 * up + 1, lane)};
    if (WARM2) warm_w2<NWI>(P, c.ct, c.ja, tq, 0, lane, wm);
    f32x16 d[4];
#pragma unroll
    for (int u = 0; u < 4; ++u) d[u] = mfma(wp[u & 1], fold_pack(acc[0][u >> 1], u & 1, rstd[u >> 1], mr[u >> 1], csk), zero16());
    v4u vf[4];
#pragma unroll
    for (int u = 0; u < 4; ++u) vf[u] = fold_pack(acc[1][u >> 1], u & 1, rstd[u >> 1], mr[u >> 1], csw);
    v4u ph[4][2];
    hedge4(d, ph);
#pragma unroll
    for (int u = 0; u < 4; ++u) {
      const unsigned img = (unsigned)(2 * up + (u & 1)) * SLOT + 16384u;
      const unsigned row = (unsigned)(64 * th + 32 * (u >> 1) + t);
      const unsigned g = (row >> 1) & 7u, gv = (row >> 2) & 3u;
      *(LAS v2u*)(size_t)(img + row * 64u + (((unsigned)(0 + hh)) ^ g) * 8u) = v2u{ph[u][0].x, ph[u][0].y};
      *(LAS v2u*)(size_t)(img + row * 64u + (((unsigned)(2 + hh)) ^ g) * 8u) = v2u{ph[u][0].z, ph[u][0].w};
      *(LAS v2u*)(size_t)(img + row * 64u + (((unsigned)(4 + hh)) ^ g) * 8u) = v2u{ph[u][1].x, ph[u][1].y};
      *(LAS v2u*)(size_t)(img + row * 64u + (((unsigned)(6 + hh)) ^ g) * 8u) = v2u{ph[u][1].z, ph[u][1].w};
      *(LAS v2u*)(size_t)(img + 8192u + row * 32u + (((unsigned)(0 + hh)) ^ gv) * 8u) = v2u{vf[u].x, vf[u].y};
      *(LAS v2u*)(size_t)(img + 8192u + row * 32u + (((unsigned)(2 + hh)) ^ gv) * 8u) = v2u{vf[u].z, vf[u].w};
    }
    STAMP(8);
    asm volatile("s_waitcnt lgkmcnt(0)" ::: "memory");
    lds_inc(L_CNT + C_IMG * 4, lane);
    lds_wait_ge(L_CNT + C_IMG * 4, 4);
    const unsigned img = (unsigned)tq * SLOT + 16384u;
    f32x16 kv = zero16();
    unsigned pa[2], va[2];
#pragma unroll
    for (int i2 = 0; i2 < 2; ++i2) {
      const unsigned r = (unsigned)(8 * hh + 4 * i2 + q);
      pa[i2] = img + r * 64u + (((unsigned)(4 * cb + pp)) ^ ((r >> 1) & 7u)) * 8u;
      va[i2] = img + 8192u + r * 32u + (((unsigned)pp) ^ ((r >> 2) & 3u)) * 8u;
    }
#pragma unroll
    for (int ks = 0; ks < 8; ++ks) {
      const v4u af = tr2(pa[0] + (unsigned)ks * 1024u, pa[1] + (unsigned)ks * 1024u);
      v4u bf = tr2(va[0] + (unsigned)ks * 512u, va[1] + (unsigned)ks * 512u);
      if (cb) bf = v4u{ONES, ONES, ONES, ONES};
      kv = mfma(af, bf, kv);
    }
    asm volatile("" : "+v"(kv) :: "memory");
    lds_inc(L_CNT + C_IMGDONE * 4, lane);
    const unsigned hidx = (unsigned)(c.gi * 4 + tq) * 16u + (unsigned)c.p;
    const __amdgpu_buffer_rsrc_t rsKV = __builtin_amdgcn_make_buffer_rsrc(P.ws + WS_KVP, 0, 524288 + 65536, 0x00020000);
    if (cb == 0) {
#pragma unroll
      for (int s = 0; s < 2; ++s) {
        const v4u o = v4u{pk2(kv[8 * s], kv[8 * s + 1]), pk2(kv[8 * s + 2], kv[8 * s + 3]), pk2(kv[8 * s + 4], kv[8 * s + 5]), pk2(kv[8 * s + 6], kv[8 * s + 7])};
        st128(o, rsKV, (unsigned)((16 * s + (lane & 15) + 32 * hh) * 16) + hidx * 1024u);
      }
    } else if ((lane & 15) == 0) {
#pragma unroll
      for (int g4 = 0; g4 < 4; ++g4) {
        const v4u o = v4u{f2u(kv[4 * g4]), f2u(kv[4 * g4 + 1]), f2u(kv[4 * g4 + 2]), f2u(kv[4 * g4 + 3])};
        st128(o, rsKV, (unsigned)((8 * g4 + 4 * hh) * 4) + 524288u + hidx * 128u);
      }
    }
    publish_last(L_CNT + C_KV * 4, 4, P.ws, FL_K + (unsigned)(c.gi * 16 + c.p) * 128u, tag, lane);
    if (WARM2) asm volatile("" :: "v"(wm[0]), "v"(wm[1]), "v"(wm[2]));
  }
  STAMP(3);
  const unsigned vw0 = (unsigned)(lane >> 3) * 1024u + (unsigned)(((lane & 7) ^ (((lane >> 4) & 1) << 2)) * 16), vw1 = vw0 + 8192u;
  const unsigned ring = (unsigned)tq * 8192u;
  auto pofs = [&](int r) AI { return (unsigned)((r >> 2) & 3) * SLOT + (unsigned)(r & 3) * 2048u; };
  const unsigned vb0 = (unsigned)lane * 16u + (unsigned)(2 * c.tt) * 144u * 1024u, vb1 = vb0 + 144u * 1024u;
  auto wbase = [&](int j) AI { const unsigned lo = __builtin_amdgcn_readlane(tabWlo, j), hi = __builtin_amdgcn_readlane(tabWhi, j); return ((u64)hi << 32) | lo; };
  auto wdma1 = [&](u64 b, unsigned m, unsigned v) AI {
    asm volatile("s_mov_b32 m0, %0\n\ts_nop 0\n\tglobal_load_lds_dwordx4 %1, %2" ::"s"(m), "v"(v), "s"(b) : "memory");
  };
  auto wdma = [&](int j) AI {
    const u64 b = wbase(j);
    const unsigned m = ring + pofs(j);
    asm volatile("s_nop 4" ::: "memory");
    wdma1(b, m, vw0); wdma1(b, m + 1024u, vw1);
  };
  constexpr int NS = 6, LB = NS - 1;
  v4u br[NS][2];
  auto bload1 = [&](unsigned so, v4u& d, unsigned v) AI {
    asm volatile("buffer_load_dwordx4 %0, %1, %2, %3 offen sc1" : "+v"(d) : "v"(v), "s"(rsHB), "s"(so) : "memory");
  };
  auto bload0 = [&](unsigned so, v4u& d0, v4u& d1) AI {
    asm volatile("s_nop 4\n\tbuffer_load_dwordx4 %0, %2, %4, %5 offen sc1\n\tbuffer_load_dwordx4 %1, %3, %4, %5 offen sc1"
                 : "=&v"(d0), "=&v"(d1) : "v"(vb0), "v"(vb1), "s"(rsHB), "s"(so) : "memory");
  };
  if (!c.ffn && tq >= 2) lds_wait_ge(L_CNT + C_IMGDONE * 4, 4);
  const int pan = c.tt >> 1;
  v2u ef;
  const char* efp = P.ws + FL_E + (unsigned)pan * 8u * 128u + (unsigned)(lane & 7) * 128u;
#pragma unroll 1
  for (int s = 0; s < 2; ++s) {
    lds_wait_ge(L_CNT + (C_FREE + s) * 4, 4);
#pragma unroll 1
    for (int j = 4 * s; j < 4 * s + 4; ++j) wdma(j);
  }
  lds_wait_ge(L_CNT + (C_FREE + 2) * 4, 4);
  asm volatile("global_load_dwordx2 %0, %1, off sc1" : "=&v"(ef) : "v"(efp) : "memory");
  wdma(8);
  STAMP(4);
  asm volatile("s_waitcnt vmcnt(2)" : "+v"(ef) :: "memory");
  if (__builtin_amdgcn_ballot_w64(ef.x != (unsigned)tag || ef.y != (unsigned)(tag >> 32)) != 0) poll_flags(P.ws, FL_E + (unsigned)pan * 8u * 128u, 8, tag, lane);
  STAMP(5);
  bload0(__builtin_amdgcn_readlane(tabB, 0), br[0][0], br[0][1]);
  bload0(__builtin_amdgcn_readlane(tabB, 1), br[1][0], br[1][1]);
#pragma unroll
  for (int u = 2; u < LB; ++u) {
    wdma(7 + u);
    bload0(__builtin_amdgcn_readlane(tabB, u), br[u][0], br[u][1]);
  }
  br[LB][0] = v4u{0u, 0u, 0u, 0u}; br[LB][1] = br[LB][0];
  unsigned w2l[2];
#pragma unroll
  for (int h = 0; h < 2; ++h) w2l[h] = ring + (unsigned)(4 * hh + q) * 128u + (unsigned)((4 * (h ^ sw) + 2 * cb + (pp >> 1)) * 16 + 8 * (pp & 1));
  f32x16 o[2][2];
#pragma unroll
  for (int a = 0; a < 2; ++a)
#pragma unroll
    for (int b = 0; b < 2; ++b) o[a][b] = zero16();
  v4u af[2];
  asm volatile("s_waitcnt vmcnt(14)" : "+v"(br[0][0]), "+v"(br[0][1]) :: "memory");
  af[0] = tr2(w2l[0], w2l[0] + 1024u); af[1] = tr2(w2l[1], w2l[1] + 1024u);
  auto step = [&](int j, int u, bool refill) AI {
    v4u an[2];
    if (refill) asm volatile("s_waitcnt vmcnt(12)" : "+v"(br[(u + 1) % NS][0]), "+v"(br[(u + 1) % NS][1]) :: "memory");
    else asm volatile("s_waitcnt vmcnt(6)" : "+v"(br[(u + 1) % NS][0]), "+v"(br[(u + 1) % NS][1]) :: "memory");
    const int jn = j + LB;
    constexpr int J3 = ORD ? 24 : 28;
    if (u == (16 - LB) % NS && jn == 16) lds_wait_ge(L_CNT + (ORD ? C_ORDY : C_LATERDY) * 4, 1);
    if (u == (J3 - LB) % NS && jn == J3) lds_wait_ge(L_CNT + (ORD ? C_LATERDY : C_ORDY) * 4, 1);
    unsigned p0 = w2l[0] + pofs(j + 1), p1 = w2l[1] + pofs(j + 1);
    asm volatile("" : "+v"(p0), "+v"(p1));
    const unsigned so = jn < 36 ? (unsigned)__builtin_amdgcn_readlane(tabB, jn) : 0x7F000000u;
    u64 wb2 = 0;
    if (refill) wb2 = wbase(j + 12);
    const unsigned m = ring + pofs(j + 12);
    an[0] = tr2(p0, p0 + 1024u); an[1] = tr2(p1, p1 + 1024u);
    asm volatile("s_nop 2" ::"s"(so), "s"(wb2) : "memory");
    __builtin_amdgcn_sched_barrier(0);
    o[0][0] = mfma(af[0], br[u][0], o[0][0]);
    __builtin_amdgcn_sched_barrier(0);
    if (refill) wdma1(wb2, m, vw0);
    __builtin_amdgcn_sched_barrier(0);
    o[0][1] = mfma(af[0], br[u][1], o[0][1]);
    __builtin_amdgcn_sched_barrier(0);
    if (refill) wdma1(wb2, m + 1024u, vw1);
    __builtin_amdgcn_sched_barrier(0);
    o[1][0] = mfma(af[1], br[u][0], o[1][0]);
    __builtin_amdgcn_sched_barrier(0);
    bload1(so, br[(u + LB) % NS][0], vb0);
    __builtin_amdgcn_sched_barrier(0);
    o[1][1] = mfma(af[1], br[u][1], o[1][1]);
    __builtin_amdgcn_sched_barrier(0);
    bload1(so, br[(u + LB) % NS][1], vb1);
    __builtin_amdgcn_sched_barrier(0);
    af[0] = an[0]; af[1] = an[1];
  };
  lds_wait_ge(L_CNT + (C_FREE + 3) * 4, 4);
#pragma unroll 1
  for (int it = 0; it < 4; ++it) {
    if (it == 2) STAMP(10);
#pragma unroll
    for (int u = 0; u < NS; ++u) step(NS * it + u, u, true);
  }
  STAMP(11);
#pragma unroll 1
  for (int it = 4; it < 6; ++it) {
#pragma unroll
    for (int u = 0; u < NS; ++u) step(NS * it + u, u, false);
  }
  asm volatile("s_waitcnt vmcnt(0)" : "+v"(br[0][0]), "+v"(br[0][1]), "+v"(br[1][0]), "+v"(br[1][1]), "+v"(br[2][0]), "+v"(br[2][1]),
               "+v"(br[3][0]), "+v"(br[3][1]), "+v"(br[4][0]), "+v"(br[4][1]), "+v"(br[5][0]), "+v"(br[5][1]) :: "memory");
  STAMP(6);
#pragma unroll
  for (int a = 0; a < 2; ++a)
#pragma unroll
    for (int b = 0; b < 2; ++b)
#pragma unroll
      for (int g4 = 0; g4 < 4; ++g4)
        *(LAS f32x4*)(size_t)(ring + (unsigned)b * SLOT + (unsigned)t * 256u + (unsigned)((8 * a + 2 * g4 + hh) ^ (t & 15)) * 16u) =
            f32x4{o[a][b][4 * g4], o[a][b][4 * g4 + 1], o[a][b][4 * g4 + 2], o[a][b][4 * g4 + 3]};
  VMW(0);
  BAR();
  STAMP(7);
}

template <int NT, int NF, class Fill> DI void pass1(const Params& P, int wid, u64& amp_a, u64& amp_b, Fill& fill, const v4u (&xr)[32], f32x16 (&acc)[NT], f32x4& cs, const unsigned (&wb)[NT], int lane) {
  const s16x4 one = {0x3F80, 0x3F80, 0x3F80, 0x3F80};
  f32x4 csa = {0.f, 0.f, 0.f, 0.f}, csb = csa;
#pragma unroll
  for (int qq = 0; qq < 4; ++qq) {
    if (qq > 0) lds_wait_ge(L_CNT + (C_LAND + qq) * 4, 4);
    if (qq < 2) fill(10 + qq);
    unsigned a[NT];
#pragma unroll
    for (int i = 0; i < NT; ++i) { a[i] = wb[i] + (unsigned)qq * SLOT; asm volatile("" : "+v"(a[i])); }
    v4u f[3][NT];
    auto rd = [&](int k8, v4u* W) AI {
      const unsigned o = (unsigned)((k8 >> 2) * 8192 + (k8 & 3) * 2048);
#pragma unroll
      for (int i = 0; i < NT; ++i) W[i] = tr2(a[i] + o, a[i] + o + 512u);
    };
    rd(0, f[0]); rd(1, f[1]);
    __builtin_amdgcn_sched_barrier(0);
#pragma unroll
    for (int k8 = 0; k8 < 8; ++k8) {
      if (k8 == 4 && qq < 3) {
        if (qq < 2) { if (NF == 8) VMW(8); else VMW(4); } else VMW(0);
        lds_inc(L_CNT + (C_LAND + qq + 1) * 4, lane);
      }
      if (k8 < 6) rd(k8 + 2, f[(k8 + 2) % 3]);
      const v4u xf = xr[8 * qq + k8];
#pragma unroll
      for (int i = 0; i < NT; ++i) acc[i] = mfma(f[k8 % 3][i], xf, acc[i]);
      csa = __builtin_amdgcn_mfma_f32_4x4x4bf16_1k(one, __builtin_bit_cast(s16x4, v2u{f[k8 % 3][0].x, f[k8 % 3][0].y}), csa, 0, 0, 0);
      csb = __builtin_amdgcn_mfma_f32_4x4x4bf16_1k(one, __builtin_bit_cast(s16x4, v2u{f[k8 % 3][0].z, f[k8 % 3][0].w}), csb, 0, 0, 0);
      if (k8 < 6) {
#pragma unroll
        for (int i = 0; i < NT; ++i) {
          __builtin_amdgcn_sched_group_barrier(0x008, 1, 0);
          __builtin_amdgcn_sched_group_barrier(0x100, 2, 0);
        }
        __builtin_amdgcn_sched_group_barrier(0x008, 2, 0);
      }
      __builtin_amdgcn_sched_barrier(0);
    }
    asm volatile("" : "+v"(acc[0]), "+v"(acc[1]), "+v"(acc[NT - 1]), "+v"(csa), "+v"(csb) :: "memory");
    lds_inc(L_CNT + (C_FREE + qq) * 4, lane);
  }
  cs[0] = csa[0] + csb[0];
}

template <bool ffn> DI u64 role_b_head(const Params& P, const Ctx& c, int wid) {
  u64 amp_a = 0, amp_b = 0; (void)amp_a; (void)amp_b;
  const int lane = c.lane, tq = c.tq, hh = c.hh, t = lane & 31;
  const int g = c.gi;
  const unsigned tok = (unsigned)(c.p * 128 + 8 * c.G + 2 * tq);
  v4u r0, r1;
  {
    const u16* rp = P.x + (size_t)tok * 512 + lane * 8;
    asm volatile("global_load_dwordx4 %0, %2, off\n\tglobal_load_dwordx4 %1, %2, off offset:1024" : "=&v"(r0), "=&v"(r1) : "v"(rp) : "memory");
  }
  const __amdgpu_buffer_rsrc_t rsX = __builtin_amdgcn_make_buffer_rsrc((void*)P.x, 0, 2048 * 1024, 0x00020000);
  const __amdgpu_buffer_rsrc_t rsW = __builtin_amdgcn_make_buffer_rsrc((void*)(ffn ? P.w1 : P.wqkv), 0, ffn ? 512 * 3584 : 512 * 3072, 0x00020000);
  constexpr unsigned RS = ffn ? 3584u : 3072u;
  const unsigned cA = (unsigned)(224 * g + 32 * (g & 1));
  const unsigned u0 = ffn ? cA * 2u : (unsigned)(512 + 64 * g) * 2u, u1 = ffn ? cA * 2u + 128u : (unsigned)(1024 + 64 * g) * 2u;
  const unsigned ua = ffn ? (cA + 128u) * 2u : (unsigned)(64 * g) * 2u;
  const unsigned ub = (unsigned)((g & 1) ? 224 * g - 32 : 224 * g + 192) * 2u;
  const unsigned voffX = (unsigned)(lane >> 3) * 1024u + (unsigned)(((lane & 7) ^ ((4 * (tq & 1) + (lane >> 4)) & 7)) * 16);
  const unsigned voffW = (unsigned)(lane >> 3) * RS + (unsigned)(((lane & 7) ^ (((lane >> 4) & 1) << 2)) * 16);
  const unsigned soffX = (unsigned)(c.p * 128 + 8 * tq) * 1024u;
  const unsigned wrow = 32u * (unsigned)(tq & 1) * RS;
  auto fill = [&](int f) AI {
    const unsigned slot = (unsigned)(f & 3) * SLOT;
    if (f < 8) {
      const unsigned kcp = (unsigned)((f + c.rot) & 7);
      unsigned sx = soffX + kcp * 128u, sw = 64u * kcp * RS + wrow + ((tq >> 1) ? u1 : u0);
      unsigned mx = slot + (unsigned)tq * 1024u, mw = slot + 16384u + (unsigned)tq * 4096u;
#pragma unroll 1
      for (int m = 0; m < 4; ++m) {
        dma(mx, voffX, rsX, sx); dma(mw, voffW, rsW, sw);
        mx += 4096u; sx += 32768u; mw += 1024u; sw += 8u * RS;
      }
    } else {
      const unsigned kcp = (unsigned)((2 * (f - 8) + (tq >> 1) + c.rot) & 7);
      unsigned sw = 64u * kcp * RS + wrow, mw = slot + (unsigned)tq * 4096u;
#pragma unroll 1
      for (int m = 0; m < 4; ++m) {
        dma(mw, voffW, rsW, sw + ua);
        if (ffn) dma(mw + 16384u, voffW, rsW, sw + ub);
        mw += 1024u; sw += 8u * RS;
      }
    }
  };
  fill(0);
  fill(1);
  unsigned xa[4];
#pragma unroll
  for (int ks = 0; ks < 4; ++ks) xa[ks] = (unsigned)(32 * tq + t) * 128u + (unsigned)(((2 * ks + hh) ^ ((t >> 1) & 7)) * 16);
  v4u xr[32];
  u64 tag = 0;
  v4u gr;
  const unsigned token = (unsigned)(c.p * 128 + 32 * tq + t);
#pragma unroll
  for (int kc = 0; kc < 8; ++kc) {
    if (kc < 7 || ffn) VMW(8); else VMW(4);
    BAR();
    if (kc == 0) tag = lds_tag();
    if (kc == 5) {
      const char* gp = P.ws + WS_ST + token * 16u;
      asm volatile("global_load_dwordx4 %0, %1, off sc1" : "=&v"(gr) : "v"(gp) : "memory");
    }
    fill(kc + 2);
    const unsigned sb = (unsigned)(kc & 3) * SLOT;
#pragma unroll
    for (int ks = 0; ks < 4; ++ks) xr[4 * kc + ks] = lds128(xa[ks] + sb);
    asm volatile("" : "+v"(xr[4 * kc]), "+v"(xr[4 * kc + 1]), "+v"(xr[4 * kc + 2]), "+v"(xr[4 * kc + 3]));
    if (kc == 1) {
  float st0;
  {
    asm volatile("" : "+v"(r0), "+v"(r1));
    float v[2][8], mean[2], rs[2];
#pragma unroll
    for (int k = 0; k < 4; ++k) {
      v[0][2 * k] = u2f(r0[k] << 16); v[0][2 * k + 1] = u2f(r0[k] & 0xffff0000u);
      v[1][2 * k] = u2f(r1[k] << 16); v[1][2 * k + 1] = u2f(r1[k] & 0xffff0000u);
    }
#pragma unroll
    for (int i = 0; i < 2; ++i) mean[i] = wavesum(((v[i][0] + v[i][1]) + (v[i][2] + v[i][3])) + ((v[i][4] + v[i][5]) + (v[i][6] + v[i][7]))) * (1.0f / 512.0f);
#pragma unroll
    for (int i = 0; i < 2; ++i) {
      float q2 = 0.f;
#pragma unroll
      for (int k = 0; k < 8; ++k) { const float d = v[i][k] - mean[i]; q2 = __builtin_fmaf(d, d, q2); }
      rs[i] = __builtin_amdgcn_rsqf(wavesum(q2) * (1.0f / 512.0f) + 1e-5f);
    }
    st0 = (lane & 2) ? rs[1] : rs[0];
    st0 = (lane & 1) ? -((lane & 2) ? mean[1] : mean[0]) * st0 : st0;
    asm volatile("" : "+v"(st0));
  }
      if (lane < 4)
        __hip_atomic_store((GAS u64*)(P.ws + WS_ST + tok * 16u + (unsigned)lane * 8u), ((u64)(unsigned)tag << 32) | f2u(st0), __ATOMIC_RELAXED, __HIP_MEMORY_SCOPE_AGENT);
    }
  }
  asm volatile("" : "+v"(gr));
  STAMP(2);
  BAR();
  if (__builtin_amdgcn_ballot_w64(gr.y != (unsigned)tag || gr.w != (unsigned)tag) != 0) {
    const __amdgpu_buffer_rsrc_t rs = __builtin_amdgcn_make_buffer_rsrc((void*)(P.ws + WS_ST), 0, 32768, 0x00020000);
#pragma unroll 1
    for (unsigned spin = 0; spin < kSpin; ++spin) {
      gr = __builtin_amdgcn_raw_buffer_load_b128(rs, token * 16u, 0, 16);
      if (__builtin_amdgcn_ballot_w64(gr.y != (unsigned)tag || gr.w != (unsigned)tag) == 0) break;
      __builtin_amdgcn_s_sleep(1);
    }
  }
  const float rstd = u2f(gr.x), mr = u2f(gr.z);
  if (hh == 0) *(LAS f32x2*)(size_t)(L_STATS + (unsigned)(32 * tq + t) * 8u) = f32x2{rstd, mr};
  if (ffn) VMW(8); else VMW(4);
  BAR();
  const int cb = (lane >> 4) & 1, q = (lane >> 2) & 3, pp = lane & 3, sw = (q >> 1) & 1;
  unsigned wl[2];
#pragma unroll
  for (int h = 0; h < 2; ++h) wl[h] = (unsigned)(8 * hh + q) * 128u + (unsigned)((4 * (h ^ sw) + 2 * cb + (pp >> 1)) * 16 + 8 * (pp & 1));
  const __amdgpu_buffer_rsrc_t rsHB = __builtin_amdgcn_make_buffer_rsrc(P.ws + WS_HB, 0, HB_BYTES, 0x00020000);
  const unsigned T32 = (unsigned)(c.p * 4 + tq);
  f32x4 cs = {0.f, 0.f, 0.f, 0.f};
  auto cs_sync = [&](int tile, bool wr) AI {
    const float s = xhalf_add(cs[0]);
    if (hh == 0 && wr) *(LAS float*)(size_t)(L_CS1 + (unsigned)tile * 128u + (unsigned)t * 4u) = s;
    asm volatile("s_waitcnt lgkmcnt(0)" ::: "memory");
    lds_inc(L_CNT + C_CS1 * 4, lane);
    lds_wait_ge(L_CNT + C_CS1 * 4, 4);
  };
  if constexpr (ffn) {
    const unsigned w2a = ((g & 1) ? wl[1] : wl[0]) + 16384u;
    int ti[3];
    unsigned wb[3];
    f32x16 acc[3];
#pragma unroll
    for (int i = 0; i < 3; ++i) { ti[i] = (i + tq) % 3; wb[i] = ti[i] == 0 ? wl[0] : (ti[i] == 1 ? wl[1] : w2a); acc[i] = zero16(); }
    if (PRIO1) __builtin_amdgcn_s_setprio(PRIO1);
    pass1<3, 8>(P, wid, amp_a, amp_b, fill, xr, acc, cs, wb, lane);
    if (PRIO1) __builtin_amdgcn_s_setprio(0);
    cs_sync(ti[0], tq < 3);
    STAMP(3);
    if (PRIOL) __builtin_amdgcn_s_setprio(3);
    f32x4 csv[3][4], bsv[3][4];
#pragma unroll
    for (int i = 0; i < 3; ++i) { load16(L_CS1 + (unsigned)ti[i] * 128u, hh, csv[i]); load16(L_B1 + (unsigned)(4 + ti[i]) * 128u, hh, bsv[i]); }
#pragma unroll
    for (int i = 0; i < 3; ++i)
      ffn_tile(acc[i], rstd, mr, csv[i], bsv[i], rsHB, (unsigned)lane * 16u + (T32 * 144u + (unsigned)(64 + 6 * g + 2 * ti[i])) * 1024u);
    publish_last(L_CNT + C_LATE * 4, 4, P.ws, FL_L + (unsigned)(c.p * 8 + g) * 128u, tag, lane);
    if (PRIOL) __builtin_amdgcn_s_setprio(0);
  } else {
    int ti[2];
    unsigned wb[2];
    f32x16 acc[2];
#pragma unroll
    for (int i = 0; i < 2; ++i) { ti[i] = (i + tq) & 1; wb[i] = ti[i] == 0 ? wl[0] : wl[1]; acc[i] = zero16(); }
    if (PRIO1) __builtin_amdgcn_s_setprio(PRIO1);
    pass1<2, 4>(P, wid, amp_a, amp_b, fill, xr, acc, cs, wb, lane);
    if (PRIO1) __builtin_amdgcn_s_setprio(0);
    cs_sync(ti[0], tq < 2);
    STAMP(3);
    v4u pq[4][2];
    {
      f32x4 csq[2][4];
      v4u wp[4];
#pragma unroll
      for (int i = 0; i < 2; ++i) load16(L_CS1 + (unsigned)ti[i] * 128u, hh, csq[i]);
#pragma unroll
      for (int hd = 0; hd < 4; ++hd) wp[hd] = wphi_frag(2 * ti[hd >> 1] + (hd & 1), lane);
      f32x16 d[4];
#pragma unroll
      for (int hd = 0; hd < 4; ++hd) d[hd] = mfma(wp[hd], fold_pack(acc[hd >> 1], hd & 1, rstd, mr, csq[hd >> 1]), zero16());
      hedge4(d, pq);
    }
    asm volatile("" : "+v"(pq[0][0]), "+v"(pq[0][1]), "+v"(pq[1][0]), "+v"(pq[1][1]), "+v"(pq[2][0]), "+v"(pq[2][1]), "+v"(pq[3][0]), "+v"(pq[3][1]));
    STAMP(4);
    if (PRIO) __builtin_amdgcn_s_setprio(3);
    if (tq == 0) { poll_flags(P.ws, FL_K + (unsigned)g * 16u * 128u, 16, tag, lane); lds_set(L_CNT + C_KVRDY * 4, 1); }
    else lds_wait_ge(L_CNT + C_KVRDY * 4, 1);
    STAMP(5);
    {
      const __amdgpu_buffer_rsrc_t rsKV = __builtin_amdgcn_make_buffer_rsrc(P.ws + WS_KVP, 0, 524288 + 65536, 0x00020000);
      const unsigned hbase = (unsigned)(g * 4 + tq) * 16u;
      v4u pv[16];
#pragma unroll
      for (int i = 0; i < 16; ++i) pv[i] = __builtin_amdgcn_raw_buffer_load_b128(rsKV, (unsigned)lane * 16u, (hbase + (unsigned)i) * 1024u, 16);
      const v4u z0 = __builtin_amdgcn_raw_buffer_load_b128(rsKV, (unsigned)lane * 16u, 524288u + hbase * 128u, 16);
      const v4u z1 = __builtin_amdgcn_raw_buffer_load_b128(rsKV, (unsigned)lane * 16u, 524288u + hbase * 128u + 1024u, 16);
      const u64 lf = __hip_atomic_load((const GAS u64*)(P.ws + FL_L + (unsigned)(c.tt >> 1) * 8u * 128u + (unsigned)(lane & 7) * 128u), __ATOMIC_RELAXED, __HIP_MEMORY_SCOPE_AGENT);
      __builtin_amdgcn_sched_barrier(0);
      v4u idf;
      {
        const int m = lane & 31, h2 = lane >> 5;
        const int j = (m < 16 && ((m >> 2) & 1) == h2) ? (((m >> 3) << 2) | (m & 3)) : -1;
#pragma unroll
        for (int k = 0; k < 4; ++k) idf[k] = (j == 2 * k ? 0x3F80u : 0u) | (j == 2 * k + 1 ? 0x3F800000u : 0u);
      }
      f32x16 sum = zero16();
#pragma unroll
      for (int i = 0; i < 16; ++i) sum = mfma(idf, pv[i], sum);
      asm volatile("" : "+v"(sum));
      lds_inc(L_CNT + C_KVIN * 4, lane);
      if (!ORD && tq == 0 && __builtin_amdgcn_ballot_w64(lf != tag) == 0) lds_set(L_CNT + C_LATERDY * 4, 1);
      const float s[8] = {sum[0], sum[1], sum[2], sum[3], sum[4], sum[5], sum[6], sum[7]};
      const unsigned afr = L_AFR + (unsigned)tq * 2048u;
      *(LAS v4u*)(size_t)(afr + (unsigned)((lane >> 4) & 1) * 1024u + (unsigned)((lane & 15) + 32 * (lane >> 5)) * 16u) = pack8(s);
      const unsigned hh2 = (unsigned)(lane >> 4) & 1u, c16 = (unsigned)lane & 15u;
      float zz[4];
#pragma unroll
      for (int k = 0; k < 4; ++k) {
        float v = dppadd<0x128>(u2f(z0[k]) + u2f(z1[k]));
        auto r = __builtin_amdgcn_permlane16_swap(f2u(v), f2u(v), false, false);
        zz[k] = xhalf_add(u2f(r[0]) + u2f(r[1]));
      }
      if (lane < 8) *(LAS f32x4*)(size_t)(L_ZT + (unsigned)tq * 128u + (unsigned)lane * 16u) = f32x4{zz[0], zz[1], zz[2], zz[3]};
      const unsigned f0 = 16u * (unsigned)(lane >> 5) + 4u * hh2;
      const f32x4 za = lds128f(L_ZT + (unsigned)tq * 128u + f0 * 4u), zb = lds128f(L_ZT + (unsigned)tq * 128u + (f0 + 8u) * 4u);
      *(LAS v4u*)(size_t)(afr + (unsigned)(lane >> 5) * 1024u + (16u + c16 + 32u * hh2) * 16u) = v4u{pk2(za[0], za[1]), pk2(za[2], za[3]), pk2(zb[0], zb[1]), pk2(zb[2], zb[3])};
      asm volatile("s_waitcnt lgkmcnt(0)" ::: "memory");
      lds_inc(L_CNT + C_AFR * 4, lane);
      lds_wait_ge(L_CNT + C_AFR * 4, 4);
    }
    {
      v4u a0[4], a1[4];
#pragma unroll
      for (int hd = 0; hd < 4; ++hd) {
        const int head = 2 * ti[hd >> 1] + (hd & 1);
        a0[hd] = lds128(L_AFR + (unsigned)head * 2048u + (unsigned)lane * 16u); a1[hd] = lds128(L_AFR + (unsigned)head * 2048u + 1024u + (unsigned)lane * 16u);
      }
      __builtin_amdgcn_sched_barrier(0);
      f32x16 d[4];
#pragma unroll
      for (int hd = 0; hd < 4; ++hd) d[hd] = mfma(a0[hd], pq[hd][0], zero16());
#pragma unroll
      for (int hd = 0; hd < 4; ++hd) d[hd] = mfma(a1[hd], pq[hd][1], d[hd]);
#pragma unroll
      for (int hd = 0; hd < 4; ++hd) {
        const int head = 2 * ti[hd >> 1] + (hd & 1);
        const float r = __builtin_amdgcn_rcpf(d[hd][8] + 1e-6f);
        const v4u o = v4u{pk2(d[hd][0] * r, d[hd][1] * r), pk2(d[hd][2] * r, d[hd][3] * r), pk2(d[hd][4] * r, d[hd][5] * r), pk2(d[hd][6] * r, d[hd][7] * r)};
        st128(o, rsHB, (unsigned)lane * 16u + (T32 * 144u + (unsigned)(112 + 4 * g + head)) * 1024u);
      }
    }
    publish_last(L_CNT + C_O * 4, 4, P.ws, FL_O + (unsigned)(c.p * 8 + g) * 128u, tag, lane);
    if (PRIO) __builtin_amdgcn_s_setprio(0);
  }
  STAMP(6);
  return tag;
}

DI void role_b(const Params& P, const Ctx& c, int wid, u64 amp_a0) {
  u64 amp_a = amp_a0, amp_b = 0; (void)amp_a; (void)amp_b;
  const int lane = c.lane, tq = c.tq;
  const u64 tag = c.ffn ? role_b_head<true>(P, c, wid) : role_b_head<false>(P, c, wid);
  const int pan = c.tt >> 1;
  const int tb = tq * 64 + lane;
  v4u xres[2], bres;
#pragma unroll
  for (int m = 0; m < 2; ++m) {
    const int idx = tb + 256 * m;
    xres[m] = *(const GAS v4u*)(P.x + (size_t)(64 * c.tt + (idx >> 3)) * 512 + 64 * c.ct + 8 * (idx & 7));
  }
  bres = *(const GAS v4u*)(P.b2 + 64 * c.ct + 8 * (tb & 7));
  if (tq == 0) {
    poll_flags(P.ws, (ORD ? FL_O : FL_L) + (unsigned)pan * 8u * 128u, 8, tag, lane);
    lds_set(L_CNT + (ORD ? C_ORDY : C_LATERDY) * 4, 1);
    poll_flags(P.ws, (ORD ? FL_L : FL_O) + (unsigned)pan * 8u * 128u, 8, tag, lane);
    lds_set(L_CNT + (ORD ? C_LATERDY : C_ORDY) * 4, 1);
  } else if (tq == 1 && __builtin_amdgcn_workgroup_id_x() == 0) {
    poll_flags(P.ws, FL_E, 64, tag, lane); poll_flags(P.ws, FL_E + 64 * 128, 64, tag, lane);
    poll_flags(P.ws, FL_K, 64, tag, lane); poll_flags(P.ws, FL_K + 64 * 128, 64, tag, lane);
    if (lane == 0) __hip_atomic_store((GAS u64*)(P.ws + WS_EPOCH), tag, __ATOMIC_RELAXED, __HIP_MEMORY_SCOPE_AGENT);
  }
  BAR();
#pragma unroll
  for (int m = 0; m < 2; ++m) {
    const int idx = tb + 256 * m;
    const unsigned tok = (unsigned)(idx >> 3), cg = (unsigned)(idx & 7);
    const unsigned a = (tok >> 5) * SLOT + (tok & 31u) * 256u, c0 = ((2u * cg) ^ (tok & 15u)) * 16u, c1 = ((2u * cg + 1u) ^ (tok & 15u)) * 16u;
    f32x4 lo = lds128f(a + c0), hi = lds128f(a + c1);
#pragma unroll
    for (unsigned w = 1; w < 4; ++w) { lo += lds128f(w * 8192u + a + c0); hi += lds128f(w * 8192u + a + c1); }
    v4u o;
#pragma unroll
    for (int k = 0; k < 4; ++k) {
      const float a0 = k < 2 ? lo[2 * k] : hi[2 * k - 4], a1 = k < 2 ? lo[2 * k + 1] : hi[2 * k - 3];
      const float x0 = u2f(xres[m][k] << 16), x1 = u2f(xres[m][k] & 0xffff0000u);
      const float b0 = u2f(bres[k] << 16), b1 = u2f(bres[k] & 0xffff0000u);
      o[k] = pk2(a0 + x0 + b0, a1 + x1 + b1);
    }
    *(GAS v4u*)(P.out + (size_t)(64 * c.tt + (idx >> 3)) * 512 + 64 * c.ct + 8 * (idx & 7)) = o;
  }
  STAMP(7);

}

__global__ __launch_bounds__(512, 2) NOPK void fused_kernel(Params P) {
  __shared__ __attribute__((aligned(1024))) char smem[LDS_BYTES];
  asm volatile("" ::"v"((unsigned)(size_t)(LAS char*)smem) : "memory");
  u64 pc;
  asm volatile("s_getpc_b64 %0" : "=s"(pc));
  const int tid = __builtin_amdgcn_workitem_id_x(), lane = tid & 63;
  const int wid = __builtin_amdgcn_readfirstlane(tid >> 6);
  u64 amp_a = 0, amp_b = 0;
  STAMP(0);
  Ctx c;
  c.lane = lane; c.tq = wid & 3; c.hh = lane >> 5;
  const int blk = __builtin_amdgcn_workgroup_id_x(), xcd = blk & 7, j = blk >> 3, ja = j & 7, jb = j >> 3;
  c.p = (xcd & 1) * 8 + ja;
  c.G = (xcd >> 1) * 4 + jb;
  c.ffn = !(c.G & 1);
  c.gi = c.G >> 1;
  c.rot = ROT1 == 0 ? 0 : (ROT1 == 1 ? (((ja + jb) % 3) * 3) & 7 : (ROT1 == 2 ? (ja + 2 * jb) & 7 : (ROT1 == 3 ? ((ja + jb) & 1) * 4 : (ROT1 == 4 ? ((ja + jb) & 3) * 2 : ((ja + jb) & 3)))));
  if (MAP2) { c.tt = xcd * 4 + (j & 3); c.ct = j >> 2; c.ja = j & 3; c.jb = j >> 2; }
  else { c.tt = (xcd >> 1) * 8 + ja; c.ct = (xcd & 1) * 4 + jb; c.ja = ja; c.jb = jb; }
  c.rotE = ROT2 ? (2 * ja + 4 * jb) & 15 : 0; c.rotL = ROT2 ? (ja + 3 * jb) % 12 : 0; c.rotO = ROT2 ? (ja + 2 * jb) & 7 : 0;
  if (wid < 4) {
    const unsigned kb0 = (unsigned)(j + 32 * (wid & 1)), kb = kb0 < CODE_KB ? kb0 : 0u;
    const unsigned pf = CODEPF ? *(const GAS unsigned*)((pc & ~255ull) + kb * 1024u + (unsigned)lane * 16u) : 0u;
    const u64 e = __hip_atomic_load((const GAS u64*)(P.ws + WS_EPOCH), __ATOMIC_RELAXED, __HIP_MEMORY_SCOPE_AGENT);
    if (c.ffn) {
      const int tc = tid < 224 ? tid : 0;
      const int t7 = tc >> 5, n = tc & 31, g = c.gi;
      const int cA = 224 * g + 32 * (g & 1);
      const int col = t7 < 4 ? cA + 32 * t7 : (t7 < 6 ? cA + 128 + 32 * (t7 - 4) : ((g & 1) ? 224 * g : 224 * g + 192));
      const unsigned bv = P.b1[col + n];
      *(LAS float*)(size_t)(L_B1 + (unsigned)tid * 4u) = u2f(bv << 16);
    } else {
      const v4u wv = *(const GAS v4u*)(P.wphi + (size_t)c.gi * 1024 + (tid & 127) * 8);
      *(LAS v4u*)(size_t)(L_WPHI + (unsigned)(tid & 127) * 16u) = wv;
    }
    if (tid < 32) *(LAS unsigned*)(size_t)(L_CNT + (unsigned)tid * 4u) = 0u;
    const u64 tag = e + 1u;
    if (tid == 0) *(LAS v2u*)(size_t)L_TAG = v2u{(unsigned)tag, (unsigned)(tag >> 32)};
    asm volatile("" ::"v"(pf));
    STAMP(1);
    role_a(P, c, wid, amp_a, amp_b);
  } else {
    STAMP(1);
    role_b(P, c, wid, amp_a);
  }
}

}

extern "C" void kernel_launch(void* const* d_in, const int* in_sizes, int n_in, void* d_out, int out_size, void* d_ws, size_t ws_size,
                              hipStream_t stream) {
  Params P;
  P.x = (const u16*)d_in[0]; P.wqkv = (const u16*)d_in[1]; P.wphi = (const u16*)d_in[2]; P.wo = (const u16*)d_in[3];
  P.w1 = (const u16*)d_in[5]; P.b1 = (const u16*)d_in[6]; P.w2 = (const u16*)d_in[7]; P.b2 = (const u16*)d_in[8];
  P.out = (u16*)d_out; P.ws = (char*)d_ws;
  fused_kernel<<<256, 512, 0, stream>>>(P);
}
```

```cpp
#include <hip/hip_runtime.h>
#include <stdint.h>
#include <type_traits>

namespace {

typedef unsigned short u16;
typedef unsigned long long u64;
typedef unsigned v4u __attribute__((ext_vector_type(4)));
typedef unsigned v2u __attribute__((ext_vector_type(2)));
typedef float f32x16 __attribute__((ext_vector_type(16)));
typedef float f32x4 __attribute__((ext_vector_type(4)));
typedef float f32x2 __attribute__((ext_vector_type(2)));
typedef __bf16 bf16x8 __attribute__((ext_vector_type(8)));
typedef __bf16 bf16x2 __attribute__((ext_vector_type(2)));
typedef short s16x4 __attribute__((ext_vector_type(4)));
#define LAS __attribute__((address_space(3)))
#define GAS __attribute__((address_space(1)))
#if defined(__HIP_DEVICE_COMPILE__) && !defined(USEPK)
#define NOPK __attribute__((target("no-packed-fp32-ops")))
#else
#define NOPK
#endif
#define DI __device__ __forceinline__ NOPK
#define AI __attribute__((always_inline)) NOPK

#define STAMPS 0
#define ROT1 1
#define ROT2 0
#define STR2(x) #x
#define STR(x) STR2(x)
#define W1 8
#define W2 4
#define PRIO 1
#define SLP 1
#define KVFIRST 0
#define PRIOL 0
#define MAP2 0
#define NBK 4
#define PRIO1 0
#define PRIOA 0
#define ORD 0
#define CODEPF 1
#define WARM2 1
#define CODE_KB 41

constexpr unsigned WS_EPOCH = 0;
constexpr unsigned FL_E = 4096;
constexpr unsigned FL_L = FL_E + 128 * 128;
constexpr unsigned FL_K = FL_L + 128 * 128;
constexpr unsigned FL_O = FL_K + 128 * 128;
constexpr unsigned WS_ST = 73728;
constexpr unsigned WS_KVP = 131072;
constexpr unsigned WS_ZP = WS_KVP + 524288;
constexpr unsigned WS_HB = 1048576;
constexpr unsigned HB_BYTES = 64u * 144u * 1024u;
constexpr unsigned WS_STAMP = 16u << 20;

constexpr unsigned SLOT = 32768;
constexpr unsigned L_MISC = 131072;
constexpr unsigned L_STATS = L_MISC;
constexpr unsigned L_CS0 = L_MISC + 1024;
constexpr unsigned L_CS1 = L_MISC + 1536;
constexpr unsigned L_B1 = L_MISC + 2048;
constexpr unsigned L_WPHI = L_MISC + 3072;
constexpr unsigned L_CNT = L_MISC + 5120;
constexpr unsigned L_TAG = L_MISC + 5248;
constexpr unsigned L_ZT = L_MISC + 5376;
constexpr unsigned L_AFR = L_MISC + 8192;
constexpr unsigned LDS_BYTES = 163840;
enum { C_CS1 = 0, C_LATE = 1, C_KV = 2, C_O = 3, C_AFR = 4, C_KVRDY = 5, C_LATERDY = 6, C_ORDY = 7, C_FREE = 8  , C_LAND = 12  , C_EARLY = 16, C_IMG = 17, C_KVIN = 18, C_IMGDONE = 19 };

constexpr unsigned kSpin = 1u << 16;

struct Params {
  const u16* x; const u16* wqkv; const u16* wphi; const u16* wo;
  const u16* w1; const u16* b1; const u16* w2; const u16* b2;
  u16* out; char* ws;
};

DI unsigned f2u(float f) { return __builtin_bit_cast(unsigned, f); }
DI float u2f(unsigned u) { return __builtin_bit_cast(float, u); }
DI float fmx(float a, float b) { return __builtin_fmaxf(a, b); }
DI f32x16 mfma(v4u a, v4u b, f32x16 c) {
  return __builtin_amdgcn_mfma_f32_32x32x16_bf16(__builtin_bit_cast(bf16x8, a), __builtin_bit_cast(bf16x8, b), c, 0, 0, 0);
}
DI f32x16 zero16() { f32x16 z; for (int r = 0; r < 16; ++r) z[r] = 0.f; return z; }
DI v4u lds128(unsigned a) { return *(const LAS v4u*)(size_t)a; }
DI f32x4 lds128f(unsigned a) { return *(const LAS f32x4*)(size_t)a; }
DI v2u tr64(unsigned a) { return __builtin_bit_cast(v2u, __builtin_amdgcn_ds_read_tr16_b64_v4i16((LAS s16x4*)(size_t)a)); }
DI v4u tr2(unsigned a0, unsigned a1) { v2u l = tr64(a0), h = tr64(a1); return v4u{l.x, l.y, h.x, h.y}; }
DI unsigned pk2(float a, float b) { return __builtin_bit_cast(unsigned, __builtin_convertvector(f32x2{a, b}, bf16x2)); }
DI float xhalf_add(float v) { auto r = __builtin_amdgcn_permlane32_swap(f2u(v), f2u(v), false, false); return u2f(r[0]) + u2f(r[1]); }
DI float xhalf_max(float v) { auto r = __builtin_amdgcn_permlane32_swap(f2u(v), f2u(v), false, false); return fmx(u2f(r[0]), u2f(r[1])); }
DI float shx(float v, int m, int lane) { return u2f((unsigned)__builtin_amdgcn_ds_bpermute((lane ^ m) << 2, (int)f2u(v))); }
DI f32x4 lanesum(v4u f, f32x4 c) {
  const s16x4 one = {0x3F80, 0x3F80, 0x3F80, 0x3F80};
  c = __builtin_amdgcn_mfma_f32_4x4x4bf16_1k(one, __builtin_bit_cast(s16x4, v2u{f.x, f.y}), c, 0, 0, 0);
  return __builtin_amdgcn_mfma_f32_4x4x4bf16_1k(one, __builtin_bit_cast(s16x4, v2u{f.z, f.w}), c, 0, 0, 0);
}
template <int CTRL> DI float dppadd(float v) { return v + u2f((unsigned)__builtin_amdgcn_update_dpp(0, (int)f2u(v), CTRL, 0xf, 0xf, true)); }
DI float wavesum(float v) {
  v = dppadd<0xB1>(v); v = dppadd<0x4E>(v); v = dppadd<0x141>(v); v = dppadd<0x140>(v);
  auto r = __builtin_amdgcn_permlane16_swap(f2u(v), f2u(v), false, false);
  return xhalf_add(u2f(r[0]) + u2f(r[1]));
}
DI void dma(unsigned m0v, unsigned voff, __amdgpu_buffer_rsrc_t rs, unsigned soff) {
  asm volatile("s_mov_b32 m0, %0\n\ts_nop 0\n\tbuffer_load_dwordx4 %1, %2, %3 offen lds" ::"s"(m0v), "v"(voff), "s"(rs), "s"(soff) : "memory");
}
DI void st128(v4u v, __amdgpu_buffer_rsrc_t rs, unsigned voff) { __builtin_amdgcn_raw_buffer_store_b128(v, rs, voff, 0, 16); }
#define BAR() do { __builtin_amdgcn_sched_barrier(0); asm volatile("s_waitcnt lgkmcnt(0)\n\ts_barrier" ::: "memory"); __builtin_amdgcn_sched_barrier(0); } while (0)
#define VMW(n) asm volatile("s_waitcnt vmcnt(" #n ")" ::: "memory")
constexpr unsigned ONES = 0x3F803F80u;

#define STAMP(i) do { } while (0)

DI void poll_flags(const char* ws, unsigned base, int n, u64 tag, int lane) {
  const GAS u64* f = (const GAS u64*)(ws + base + (unsigned)(lane < n ? lane : 0) * 128u);
#pragma unroll 1
  for (unsigned spin = 0; spin < kSpin; ++spin) {
    const u64 v = __hip_atomic_load(f, __ATOMIC_RELAXED, __HIP_MEMORY_SCOPE_AGENT);
    if (__builtin_amdgcn_ballot_w64(v != tag) == 0) break;
    __builtin_amdgcn_s_sleep(1);
  }
  __builtin_amdgcn_fence(__ATOMIC_ACQUIRE, "wavefront");
  asm volatile("" ::: "memory");
}
DI void lds_wait_ge(unsigned addr, unsigned want) {
#pragma unroll 1
  for (unsigned spin = 0; spin < (kSpin << 4); ++spin) {
    const unsigned v = *(volatile LAS unsigned*)(size_t)addr;
    if ((unsigned)__builtin_amdgcn_readfirstlane(v) >= want) break;
    if (SLP) __builtin_amdgcn_s_sleep(SLP);
  }
  asm volatile("" ::: "memory");
}
DI void lds_set(unsigned addr, unsigned v) { *(volatile LAS unsigned*)(size_t)addr = v; }
DI void lds_inc(unsigned addr, int lane) { if (lane == 0) __hip_atomic_fetch_add((LAS unsigned*)(size_t)addr, 1u, __ATOMIC_RELAXED, __HIP_MEMORY_SCOPE_WORKGROUP); }
DI void publish_last(unsigned cnt_addr, unsigned n, char* ws, unsigned flag_off, u64 tag, int lane) {
  VMW(0);
  if (lane == 0) {
    const unsigned old = __hip_atomic_fetch_add((LAS unsigned*)(size_t)cnt_addr, 1u, __ATOMIC_RELAXED, __HIP_MEMORY_SCOPE_WORKGROUP);
    if (old == n - 1) __hip_atomic_store((GAS u64*)(ws + flag_off), tag, __ATOMIC_RELAXED, __HIP_MEMORY_SCOPE_AGENT);
  }
}
DI u64 lds_tag() { const v2u t = *(const LAS v2u*)(size_t)L_TAG; return ((u64)(unsigned)__builtin_amdgcn_readfirstlane(t.y) << 32) | (unsigned)__builtin_amdgcn_readfirstlane(t.x); }

DI void load16(unsigned a, int hh, f32x4* v) {
#pragma unroll
  for (int g = 0; g < 4; ++g) v[g] = lds128f(a + (unsigned)g * 32u + (unsigned)hh * 16u);
}
DI void ffn_tile(const f32x16& acc, float rstd, float mr, const f32x4* cs, const f32x4* bs, __amdgpu_buffer_rsrc_t rsHB, unsigned voff) {
  f32x2 y[8], d[8];
#pragma unroll
  for (int g = 0; g < 4; ++g) {
    const f32x4 t = cs[g] * mr + bs[g];
    y[2 * g] = f32x2{acc[4 * g], acc[4 * g + 1]} * rstd + f32x2{t[0], t[1]};
    y[2 * g + 1] = f32x2{acc[4 * g + 2], acc[4 * g + 3]} * rstd + f32x2{t[2], t[3]};
  }
#pragma unroll
  for (int k = 0; k < 8; ++k) d[k] = y[k] * ((y[k] * y[k]) * (-0.10294324f) + (-2.30220819f));
#pragma unroll
  for (int k = 0; k < 8; ++k) { d[k].x = __builtin_amdgcn_exp2f(d[k].x); d[k].y = __builtin_amdgcn_exp2f(d[k].y); }
#pragma unroll
  for (int k = 0; k < 8; ++k) d[k] += 1.0f;
#pragma unroll
  for (int k = 0; k < 8; ++k) { d[k].x = __builtin_amdgcn_rcpf(d[k].x); d[k].y = __builtin_amdgcn_rcpf(d[k].y); }
  v4u o0, o1;
#pragma unroll
  for (int k = 0; k < 4; ++k) {
    const f32x2 g0 = y[k] * d[k], g1 = y[4 + k] * d[4 + k];
    o0[k] = pk2(g0.x, g0.y); o1[k] = pk2(g1.x, g1.y);
  }
  st128(o0, rsHB, voff); st128(o1, rsHB, voff + 1024u);
}
DI float max3(float a, float b, float c) { return __builtin_elementwise_maximum(__builtin_elementwise_maximum(a, b), c); }
DI void hedge4(const f32x16 (&d)[4], v4u (&o)[4][2]) {
  constexpr float L = 1.4426950408889634f;
  float m[4][2], e[4][16], s[4][2];
#pragma unroll
  for (int u = 0; u < 4; ++u)
#pragma unroll
    for (int h = 0; h < 2; ++h) {
      const float a = max3(d[u][8 * h], d[u][8 * h + 1], d[u][8 * h + 2]), b = max3(d[u][8 * h + 3], d[u][8 * h + 4], d[u][8 * h + 5]);
      m[u][h] = max3(max3(d[u][8 * h + 6], d[u][8 * h + 7], a), b, b);
    }
#pragma unroll
  for (int u = 0; u < 4; ++u)
#pragma unroll
    for (int h = 0; h < 2; ++h) {
      auto r = __builtin_amdgcn_permlane32_swap(f2u(m[u][h]), f2u(m[u][h]), false, false);
      m[u][h] = __builtin_elementwise_maximum(u2f(r[0]), u2f(r[1])) * -L;
    }
#pragma unroll
  for (int u = 0; u < 4; ++u)
#pragma unroll
    for (int i = 0; i < 16; ++i) e[u][i] = __builtin_amdgcn_exp2f(__builtin_fmaf(d[u][i], L, m[u][i >> 3]));
#pragma unroll
  for (int u = 0; u < 4; ++u)
#pragma unroll
    for (int h = 0; h < 2; ++h) {
      const float* x = &e[u][8 * h];
      s[u][h] = ((x[0] + x[1]) + (x[2] + x[3])) + ((x[4] + x[5]) + (x[6] + x[7]));
    }
#pragma unroll
  for (int u = 0; u < 4; ++u)
#pragma unroll
    for (int h = 0; h < 2; ++h) s[u][h] = __builtin_amdgcn_rcpf(xhalf_add(s[u][h]));
#pragma unroll
  for (int u = 0; u < 4; ++u)
#pragma unroll
    for (int h = 0; h < 2; ++h)
#pragma unroll
      for (int k = 0; k < 4; ++k) o[u][h][k] = pk2(e[u][8 * h + 2 * k] * s[u][h], e[u][8 * h + 2 * k + 1] * s[u][h]);
}
DI v4u fold_pack(const f32x16& acc, int hf, float rstd, float mr, const f32x4* cs) {
  float y[8];
#pragma unroll
  for (int k = 0; k < 8; ++k) y[k] = __builtin_fmaf(acc[8 * hf + k], rstd, cs[2 * hf + (k >> 2)][k & 3] * mr);
  return v4u{pk2(y[0], y[1]), pk2(y[2], y[3]), pk2(y[4], y[5]), pk2(y[6], y[7])};
}
DI v4u wphi_frag(int hd, int lane) {
  const int hh = lane >> 5, q = (lane >> 2) & 3, pp = lane & 3;
  const unsigned a = L_WPHI + (unsigned)hd * 512u + (unsigned)(4 * hh + q) * 32u + (unsigned)pp * 8u;
  v4u f = tr2(a, a + 256u);
  const unsigned sg = (lane & 16) ? 0x80008000u : 0u;
  f.x ^= sg; f.y ^= sg; f.z ^= sg; f.w ^= sg;
  return f;
}
DI v4u selv(bool s, v4u a, v4u b) { const unsigned m = s ? 0xffffffffu : 0u; return v4u{(a.x & m) | (b.x & ~m), (a.y & m) | (b.y & ~m), (a.z & m) | (b.z & ~m), (a.w & m) | (b.w & ~m)}; }
DI v4u pack8(const float* y) { return v4u{pk2(y[0], y[1]), pk2(y[2], y[3]), pk2(y[4], y[5]), pk2(y[6], y[7])}; }

constexpr int NWI = MAP2 ? 18 : 9;
template <int NI> DI void warm_w2(const Params& P, int ct, int ja, int tq, int i0, int lane, v4u* d) {
#pragma unroll
  for (int i = 0; i < NI; ++i) {
    const int row = 32 * NWI * ja + 8 * NWI * tq + 8 * (i0 + i);
    const u64 a = row < 1792 ? (u64)(size_t)P.w2 + (u64)(unsigned)row * 1024u : (u64)(size_t)P.wo + (u64)(unsigned)(row - 1792) * 1024u;
    const u64 base = ((u64)(unsigned)__builtin_amdgcn_readfirstlane((unsigned)(a >> 32)) << 32) | (unsigned)__builtin_amdgcn_readfirstlane((unsigned)a);
    const unsigned voff = (unsigned)(lane >> 3) * 1024u + (unsigned)(lane & 7) * 16u + (unsigned)ct * 128u;
    asm volatile("s_nop 4\n\tglobal_load_dwordx4 %0, %1, %2" : "+v"(d[i % 3]) : "v"(voff), "s"(base) : "memory");
  }
}
struct Ctx {
  int lane, tq, hh;
  int p, gi, G;
  int rot;
  int tt, ct, ja, jb;
  int rotE, rotL, rotO;
  bool ffn;
};

DI void role_a(const Params& P, const Ctx& c, int wid, u64 amp_a, u64 amp_b) {
  const int lane = c.lane, tq = c.tq, hh = c.hh, t = lane & 31;
  const int cb = (lane >> 4) & 1, q = (lane >> 2) & 3, pp = lane & 3, sw = (q >> 1) & 1;
  unsigned tabB, tabWlo, tabWhi;
  {
    const int jj = lane < 36 ? lane : 35;
    constexpr int J2 = ORD ? 24 : 28;
    int sg;
    auto perm = [&](int i, int n4) AI {
      if (!ROT2) return i;
      constexpr int NO = MAP2 ? 4 : 2;
      const int b = (c.jb >> 1) & (NO - 1), ng = 4 * n4 / NO;
      if (i < 2) return NO * i + b;
      const int m = (i - 2) / NO, r = (i - 2) % NO;
      if (m + 2 < ng) return r < NO - 1 ? NO * m + ((b + 1 + r) & (NO - 1)) : NO * (m + 2) + b;
      const int e = i - 2 - NO * (ng - 2);
      return NO * (ng - 2 + e / (NO - 1)) + ((b + 1 + e % (NO - 1)) & (NO - 1));
    };
    if (jj < 16) sg = 4 * perm(jj, 4) + tq;
    else if ((jj < J2) != (ORD != 0)) sg = 64 + 4 * perm(jj - (ORD ? 24 : 16), 3) + tq;
    else sg = 112 + 4 * perm(jj - (ORD ? 16 : 28), 2) + tq;
    int row;
    if (sg < 64) { const int g = sg >> 3; row = 224 * g + 32 * (g & 1) + 16 * (sg & 7); }
    else if (sg < 112) {
      const int e = sg - 64, g = e / 6, i = e - 6 * g;
      row = i < 4 ? 224 * g + 32 * (g & 1) + 128 + 16 * i : ((g & 1) ? 224 * g : 224 * g + 192) + 16 * (i - 4);
    } else row = 16 * (sg - 112);
    const u64 a = (u64)(size_t)(sg < 112 ? P.w2 : P.wo) + (u64)row * 1024u + (u64)c.ct * 128u;
    tabB = (unsigned)sg * 1024u; tabWlo = (unsigned)a; tabWhi = (unsigned)(a >> 32);
  }
  asm volatile("" : "+v"(tabB), "+v"(tabWlo), "+v"(tabWhi));
  const int th = tq & 1, up = tq >> 1;
  unsigned xa[4];
#pragma unroll
  for (int ks = 0; ks < 4; ++ks) xa[ks] = (unsigned)(64 * th + t) * 128u + (unsigned)(((2 * ks + hh) ^ ((t >> 1) & 7)) * 16);
  const unsigned wl = 16384u + (unsigned)(8 * hh + q) * 128u + (unsigned)((4 * (up ^ sw) + 2 * cb + (pp >> 1)) * 16 + 8 * (pp & 1));
  f32x16 acc[2][2];
#pragma unroll
  for (int i = 0; i < 2; ++i) { acc[i][0] = zero16(); acc[i][1] = zero16(); }
  f32x4 cs = {0.f, 0.f, 0.f, 0.f};
  unsigned x0 = xa[0], x1 = xa[1], x2 = xa[2], x3 = xa[3], w0 = wl;
  v4u xf[4][2], wf[4][2];
  auto rd = [&](int ks, v4u* X, v4u* W) AI {
    const unsigned xb = ks == 0 ? x0 : (ks == 1 ? x1 : (ks == 2 ? x2 : x3));
    X[0] = lds128(xb); X[1] = lds128(xb + 4096u);
    const unsigned a = w0 + (unsigned)(ks * 2048);
    W[0] = tr2(a, a + 512u); W[1] = tr2(a + 8192u, a + 8704u);
  };
  f32x4 cs2 = {0.f, 0.f, 0.f, 0.f};
  BAR();
  rd(0, xf[0], wf[0]); rd(1, xf[1], wf[1]);
  auto pass0 = [&](auto TH) AI {
    constexpr int CT = decltype(TH)::value;
    const s16x4 one = {0x3F80, 0x3F80, 0x3F80, 0x3F80};
#pragma unroll 1
    for (int kc = 0; kc < 8; ++kc) {
#pragma unroll
      for (int ks = 0; ks < 4; ++ks) {
        if (ks == 2) {
          __builtin_amdgcn_sched_barrier(0);
          asm volatile("s_barrier" ::: "memory");
          __builtin_amdgcn_sched_barrier(0);
          const unsigned sb = (unsigned)((kc + 1) & 3) * SLOT;
          x0 = xa[0] + sb; x1 = xa[1] + sb; w0 = wl + sb;
          asm volatile("" : "+v"(x0), "+v"(x1), "+v"(w0));
        }
        if (ks == 0) {
          const unsigned sb = (unsigned)(kc & 3) * SLOT;
          x2 = xa[2] + sb; x3 = xa[3] + sb;
          asm volatile("" : "+v"(x2), "+v"(x3));
        }
        rd((ks + 2) & 3, xf[(ks + 2) & 3], wf[(ks + 2) & 3]);
        acc[0][0] = mfma(wf[ks][0], xf[ks][0], acc[0][0]); acc[0][1] = mfma(wf[ks][0], xf[ks][1], acc[0][1]);
        acc[1][0] = mfma(wf[ks][1], xf[ks][0], acc[1][0]); acc[1][1] = mfma(wf[ks][1], xf[ks][1], acc[1][1]);
        cs = __builtin_amdgcn_mfma_f32_4x4x4bf16_1k(one, __builtin_bit_cast(s16x4, v2u{wf[ks][CT].x, wf[ks][CT].y}), cs, 0, 0, 0);
        cs2 = __builtin_amdgcn_mfma_f32_4x4x4bf16_1k(one, __builtin_bit_cast(s16x4, v2u{wf[ks][CT].z, wf[ks][CT].w}), cs2, 0, 0, 0);
        __builtin_amdgcn_sched_group_barrier(0x008, 1, 0);
        __builtin_amdgcn_sched_group_barrier(0x100, 2, 0);
        __builtin_amdgcn_sched_group_barrier(0x008, 1, 0);
        __builtin_amdgcn_sched_group_barrier(0x100, 2, 0);
        __builtin_amdgcn_sched_group_barrier(0x008, 1, 0);
        __builtin_amdgcn_sched_group_barrier(0x100, 2, 0);
        __builtin_amdgcn_sched_group_barrier(0x008, 3, 0);
        __builtin_amdgcn_sched_barrier(0);
      }
      asm volatile("" : "+v"(acc[0][0]), "+v"(acc[0][1]), "+v"(acc[1][0]), "+v"(acc[1][1]), "+v"(cs), "+v"(cs2));
    }
  };
  if (th) pass0(std::integral_constant<int, 1>{}); else pass0(std::integral_constant<int, 0>{});
  cs[0] += cs2[0];
  STAMP(2);
  {
    const float s = xhalf_add(cs[0]);
    if (hh == 0) *(LAS float*)(size_t)(L_CS0 + (unsigned)(up + 2 * th) * 128u + (unsigned)t * 4u) = s;
  }
  BAR();
  const u64 tag = lds_tag();
  float rstd[2], mr[2];
#pragma unroll
  for (int b = 0; b < 2; ++b) {
    const f32x2 st = *(const LAS f32x2*)(size_t)(L_STATS + (unsigned)(64 * th + 32 * b + t) * 8u);
    rstd[b] = st.x; mr[b] = st.y;
  }
  const __amdgpu_buffer_rsrc_t rsHB = __builtin_amdgcn_make_buffer_rsrc(P.ws + WS_HB, 0, HB_BYTES, 0x00020000);
  v4u wm[3] = {v4u{0u, 0u, 0u, 0u}, v4u{0u, 0u, 0u, 0u}, v4u{0u, 0u, 0u, 0u}};
  if (c.ffn) {
    f32x4 csv[2][4], bsv[2][4];
#pragma unroll
    for (int i = 0; i < 2; ++i) { load16(L_CS0 + (unsigned)(up + 2 * i) * 128u, hh, csv[i]); load16(L_B1 + (unsigned)(up + 2 * i) * 128u, hh, bsv[i]); }
#pragma unroll
    for (int i = 0; i < 2; ++i)
#pragma unroll
      for (int b = 0; b < 2; ++b) {
        const unsigned tile = (unsigned)(up + 2 * i), T32 = (unsigned)(c.p * 4 + 2 * th + b);
        ffn_tile(acc[i][b], rstd[b], mr[b], csv[i], bsv[i], rsHB, (unsigned)lane * 16u + (T32 * 144u + (unsigned)(8 * c.gi) + 2u * tile) * 1024u);
        if (WARM2 && 2 * i + b == 0) warm_w2<NWI>(P, c.ct, c.ja, tq, 0, lane, wm);
      }
    STAMP(8);
    publish_last(L_CNT + C_EARLY * 4, 4, P.ws, FL_E + (unsigned)(c.p * 8 + c.gi) * 128u, tag, lane);
    if (WARM2) asm volatile("" :: "v"(wm[0]), "v"(wm[1]), "v"(wm[2]));
  } else {
    f32x4 csk[4], csw[4];
    load16(L_CS0 + (unsigned)up * 128u, hh, csk); load16(L_CS0 + (unsigned)(2 + up) * 128u, hh, csw);
    const v4u wp[2] = {wphi_frag(2 * up, lane), wphi_frag(2 * up + 1, lane)};
    if (WARM2) warm_w2<NWI>(P, c.ct, c.ja, tq, 0, lane, wm);
    f32x16 d[4];
#pragma unroll
    for (int u = 0; u < 4; ++u) d[u] = mfma(wp[u & 1], fold_pack(acc[0][u >> 1], u & 1, rstd[u >> 1], mr[u >> 1], csk), zero16());
    v4u vf[4];
#pragma unroll
    for (int u = 0; u < 4; ++u) vf[u] = fold_pack(acc[1][u >> 1], u & 1, rstd[u >> 1], mr[u >> 1], csw);
    v4u ph[4][2];
    hedge4(d, ph);
#pragma unroll
    for (int u = 0; u < 4; ++u) {
      const unsigned img = (unsigned)(2 * up + (u & 1)) * SLOT + 16384u;
      const unsigned row = (unsigned)(64 * th + 32 * (u >> 1) + t);
      const unsigned g = (row >> 1) & 7u, gv = (row >> 2) & 3u;
      *(LAS v2u*)(size_t)(img + row * 64u + (((unsigned)(0 + hh)) ^ g) * 8u) = v2u{ph[u][0].x, ph[u][0].y};
      *(LAS v2u*)(size_t)(img + row * 64u + (((unsigned)(2 + hh)) ^ g) * 8u) = v2u{ph[u][0].z, ph[u][0].w};
      *(LAS v2u*)(size_t)(img + row * 64u + (((unsigned)(4 + hh)) ^ g) * 8u) = v2u{ph[u][1].x, ph[u][1].y};
      *(LAS v2u*)(size_t)(img + row * 64u + (((unsigned)(6 + hh)) ^ g) * 8u) = v2u{ph[u][1].z, ph[u][1].w};
      *(LAS v2u*)(size_t)(img + 8192u + row * 32u + (((unsigned)(0 + hh)) ^ gv) * 8u) = v2u{vf[u].x, vf[u].y};
      *(LAS v2u*)(size_t)(img + 8192u + row * 32u + (((unsigned)(2 + hh)) ^ gv) * 8u) = v2u{vf[u].z, vf[u].w};
    }
    STAMP(8);
    asm volatile("s_waitcnt lgkmcnt(0)" ::: "memory");
    lds_inc(L_CNT + C_IMG * 4, lane);
    lds_wait_ge(L_CNT + C_IMG * 4, 4);
    const unsigned img = (unsigned)tq * SLOT + 16384u;
    f32x16 kv = zero16();
    unsigned pa[2], va[2];
#pragma unroll
    for (int i2 = 0; i2 < 2; ++i2) {
      const unsigned r = (unsigned)(8 * hh + 4 * i2 + q);
      pa[i2] = img + r * 64u + (((unsigned)(4 * cb + pp)) ^ ((r >> 1) & 7u)) * 8u;
      va[i2] = img + 8192u + r * 32u + (((unsigned)pp) ^ ((r >> 2) & 3u)) * 8u;
    }
#pragma unroll
    for (int ks = 0; ks < 8; ++ks) {
      const v4u af = tr2(pa[0] + (unsigned)ks * 1024u, pa[1] + (unsigned)ks * 1024u);
      v4u bf = tr2(va[0] + (unsigned)ks * 512u, va[1] + (unsigned)ks * 512u);
      if (cb) bf = v4u{ONES, ONES, ONES, ONES};
      kv = mfma(af, bf, kv);
    }
    asm volatile("" : "+v"(kv) :: "memory");
    lds_inc(L_CNT + C_IMGDONE * 4, lane);
    const unsigned hidx = (unsigned)(c.gi * 4 + tq) * 16u + (unsigned)c.p;
    const __amdgpu_buffer_rsrc_t rsKV = __builtin_amdgcn_make_buffer_rsrc(P.ws + WS_KVP, 0, 524288 + 65536, 0x00020000);
    if (cb == 0) {
#pragma unroll
      for (int s = 0; s < 2; ++s) {
        const v4u o = v4u{pk2(kv[8 * s], kv[8 * s + 1]), pk2(kv[8 * s + 2], kv[8 * s + 3]), pk2(kv[8 * s + 4], kv[8 * s + 5]), pk2(kv[8 * s + 6], kv[8 * s + 7])};
        st128(o, rsKV, (unsigned)((16 * s + (lane & 15) + 32 * hh) * 16) + hidx * 1024u);
      }
    } else if ((lane & 15) == 0) {
#pragma unroll
      for (int g4 = 0; g4 < 4; ++g4) {
        const v4u o = v4u{f2u(kv[4 * g4]), f2u(kv[4 * g4 + 1]), f2u(kv[4 * g4 + 2]), f2u(kv[4 * g4 + 3])};
        st128(o, rsKV, (unsigned)((8 * g4 + 4 * hh) * 4) + 524288u + hidx * 128u);
      }
    }
    publish_last(L_CNT + C_KV * 4, 4, P.ws, FL_K + (unsigned)(c.gi * 16 + c.p) * 128u, tag, lane);
    if (WARM2) asm volatile("" :: "v"(wm[0]), "v"(wm[1]), "v"(wm[2]));
  }
  STAMP(3);
  const unsigned vw0 = (unsigned)(lane >> 3) * 1024u + (unsigned)(((lane & 7) ^ (((lane >> 4) & 1) << 2)) * 16), vw1 = vw0 + 8192u;
  const unsigned ring = (unsigned)tq * 8192u;
  auto pofs = [&](int r) AI { return (unsigned)((r >> 2) & 3) * SLOT + (unsigned)(r & 3) * 2048u; };
  const unsigned vb0 = (unsigned)lane * 16u + (unsigned)(2 * c.tt) * 144u * 1024u, vb1 = vb0 + 144u * 1024u;
  auto wbase = [&](int j) AI { const unsigned lo = __builtin_amdgcn_readlane(tabWlo, j), hi = __builtin_amdgcn_readlane(tabWhi, j); return ((u64)hi << 32) | lo; };
  auto wdma1 = [&](u64 b, unsigned m, unsigned v) AI {
    asm volatile("s_mov_b32 m0, %0\n\ts_nop 0\n\tglobal_load_lds_dwordx4 %1, %2" ::"s"(m), "v"(v), "s"(b) : "memory");
  };
  auto wdma = [&](int j) AI {
    const u64 b = wbase(j);
    const unsigned m = ring + pofs(j);
    asm volatile("s_nop 4" ::: "memory");
    wdma1(b, m, vw0); wdma1(b, m + 1024u, vw1);
  };
  constexpr int NS = 6, LB = NS - 1;
  v4u br[NS][2];
  auto bload1 = [&](unsigned so, v4u& d, unsigned v) AI {
    asm volatile("buffer_load_dwordx4 %0, %1, %2, %3 offen sc1" : "+v"(d) : "v"(v), "s"(rsHB), "s"(so) : "memory");
  };
  auto bload0 = [&](unsigned so, v4u& d0, v4u& d1) AI {
    asm volatile("s_nop 4\n\tbuffer_load_dwordx4 %0, %2, %4, %5 offen sc1\n\tbuffer_load_dwordx4 %1, %3, %4, %5 offen sc1"
                 : "=&v"(d0), "=&v"(d1) : "v"(vb0), "v"(vb1), "s"(rsHB), "s"(so) : "memory");
  };
  if (!c.ffn && tq >= 2) lds_wait_ge(L_CNT + C_IMGDONE * 4, 4);
  const int pan = c.tt >> 1;
  v2u ef;
  const char* efp = P.ws + FL_E + (unsigned)pan * 8u * 128u + (unsigned)(lane & 7) * 128u;
#pragma unroll 1
  for (int s = 0; s < 2; ++s) {
    lds_wait_ge(L_CNT + (C_FREE + s) * 4, 4);
#pragma unroll 1
    for (int j = 4 * s; j < 4 * s + 4; ++j) wdma(j);
  }
  lds_wait_ge(L_CNT + (C_FREE + 2) * 4, 4);
  asm volatile("global_load_dwordx2 %0, %1, off sc1" : "=&v"(ef) : "v"(efp) : "memory");
  wdma(8);
  STAMP(4);
  asm volatile("s_waitcnt vmcnt(2)" : "+v"(ef) :: "memory");
  if (__builtin_amdgcn_ballot_w64(ef.x != (unsigned)tag || ef.y != (unsigned)(tag >> 32)) != 0) poll_flags(P.ws, FL_E + (unsigned)pan * 8u * 128u, 8, tag, lane);
  STAMP(5);
  bload0(__builtin_amdgcn_readlane(tabB, 0), br[0][0], br[0][1]);
  bload0(__builtin_amdgcn_readlane(tabB, 1), br[1][0], br[1][1]);
#pragma unroll
  for (int u = 2; u < LB; ++u) {
    wdma(7 + u);
    bload0(__builtin_amdgcn_readlane(tabB, u), br[u][0], br[u][1]);
  }
  br[LB][0] = v4u{0u, 0u, 0u, 0u}; br[LB][1] = br[LB][0];
  unsigned w2l[2];
#pragma unroll
  for (int h = 0; h < 2; ++h) w2l[h] = ring + (unsigned)(4 * hh + q) * 128u + (unsigned)((4 * (h ^ sw) + 2 * cb + (pp >> 1)) * 16 + 8 * (pp & 1));
  f32x16 o[2][2];
#pragma unroll
  for (int a = 0; a < 2; ++a)
#pragma unroll
    for (int b = 0; b < 2; ++b) o[a][b] = zero16();
  v4u af[2];
  asm volatile("s_waitcnt vmcnt(14)" : "+v"(br[0][0]), "+v"(br[0][1]) :: "memory");
  af[0] = tr2(w2l[0], w2l[0] + 1024u); af[1] = tr2(w2l[1], w2l[1] + 1024u);
  auto step = [&](int j, int u, bool refill) AI {
    v4u an[2];
    if (refill) asm volatile("s_waitcnt vmcnt(12)" : "+v"(br[(u + 1) % NS][0]), "+v"(br[(u + 1) % NS][1]) :: "memory");
    else asm volatile("s_waitcnt vmcnt(6)" : "+v"(br[(u + 1) % NS][0]), "+v"(br[(u + 1) % NS][1]) :: "memory");
    const int jn = j + LB;
    constexpr int J3 = ORD ? 24 : 28;
    if (u == (16 - LB) % NS && jn == 16) lds_wait_ge(L_CNT + (ORD ? C_ORDY : C_LATERDY) * 4, 1);
    if (u == (J3 - LB) % NS && jn == J3) lds_wait_ge(L_CNT + (ORD ? C_LATERDY : C_ORDY) * 4, 1);
    unsigned p0 = w2l[0] + pofs(j + 1), p1 = w2l[1] + pofs(j + 1);
    asm volatile("" : "+v"(p0), "+v"(p1));
    const unsigned so = jn < 36 ? (unsigned)__builtin_amdgcn_readlane(tabB, jn) : 0x7F000000u;
    u64 wb2 = 0;
    if (refill) wb2 = wbase(j + 12);
    const unsigned m = ring + pofs(j + 12);
    an[0] = tr2(p0, p0 + 1024u); an[1] = tr2(p1, p1 + 1024u);
    asm volatile("s_nop 2" ::"s"(so), "s"(wb2) : "memory");
    __builtin_amdgcn_sched_barrier(0);
    o[0][0] = mfma(af[0], br[u][0], o[0][0]);
    __builtin_amdgcn_sched_barrier(0);
    if (refill) wdma1(wb2, m, vw0);
    __builtin_amdgcn_sched_barrier(0);
    o[0][1] = mfma(af[0], br[u][1], o[0][1]);
    __builtin_amdgcn_sched_barrier(0);
    if (refill) wdma1(wb2, m + 1024u, vw1);
    __builtin_amdgcn_sched_barrier(0);
    o[1][0] = mfma(af[1], br[u][0], o[1][0]);
    __builtin_amdgcn_sched_barrier(0);
    bload1(so, br[(u + LB) % NS][0], vb0);
    __builtin_amdgcn_sched_barrier(0);
    o[1][1] = mfma(af[1], br[u][1], o[1][1]);
    __builtin_amdgcn_sched_barrier(0);
    bload1(so, br[(u + LB) % NS][1], vb1);
    __builtin_amdgcn_sched_barrier(0);
    af[0] = an[0]; af[1] = an[1];
  };
  lds_wait_ge(L_CNT + (C_FREE + 3) * 4, 4);
#pragma unroll 1
  for (int it = 0; it < 4; ++it) {
    if (it == 2) STAMP(10);
#pragma unroll
    for (int u = 0; u < NS; ++u) step(NS * it + u, u, true);
  }
  STAMP(11);
#pragma unroll 1
  for (int it = 4; it < 6; ++it) {
#pragma unroll
    for (int u = 0; u < NS; ++u) step(NS * it + u, u, false);
  }
  asm volatile("s_waitcnt vmcnt(0)" : "+v"(br[0][0]), "+v"(br[0][1]), "+v"(br[1][0]), "+v"(br[1][1]), "+v"(br[2][0]), "+v"(br[2][1]),
               "+v"(br[3][0]), "+v"(br[3][1]), "+v"(br[4][0]), "+v"(br[4][1]), "+v"(br[5][0]), "+v"(br[5][1]) :: "memory");
  STAMP(6);
#pragma unroll
  for (int a = 0; a < 2; ++a)
#pragma unroll
    for (int b = 0; b < 2; ++b)
#pragma unroll
      for (int g4 = 0; g4 < 4; ++g4)
        *(LAS f32x4*)(size_t)(ring + (unsigned)b * SLOT + (unsigned)t * 256u + (unsigned)((8 * a + 2 * g4 + hh) ^ (t & 15)) * 16u) =
            f32x4{o[a][b][4 * g4], o[a][b][4 * g4 + 1], o[a][b][4 * g4 + 2], o[a][b][4 * g4 + 3]};
  VMW(0);
  BAR();
  STAMP(7);
}

template <int NT, int NF, class Fill> DI void pass1(const Params& P, int wid, u64& amp_a, u64& amp_b, Fill& fill, const v4u (&xr)[32], f32x16 (&acc)[NT], f32x4& cs, const unsigned (&wb)[NT], int lane) {
  const s16x4 one = {0x3F80, 0x3F80, 0x3F80, 0x3F80};
  f32x4 csa = {0.f, 0.f, 0.f, 0.f}, csb = csa;
#pragma unroll
  for (int qq = 0; qq < 4; ++qq) {
    if (qq > 0) lds_wait_ge(L_CNT + (C_LAND + qq) * 4, 4);
    if (qq < 2) fill(10 + qq);
    unsigned a[NT];
#pragma unroll
    for (int i = 0; i < NT; ++i) { a[i] = wb[i] + (unsigned)qq * SLOT; asm volatile("" : "+v"(a[i])); }
    v4u f[3][NT];
    auto rd = [&](int k8, v4u* W) AI {
      const unsigned o = (unsigned)((k8 >> 2) * 8192 + (k8 & 3) * 2048);
#pragma unroll
      for (int i = 0; i < NT; ++i) W[i] = tr2(a[i] + o, a[i] + o + 512u);
    };
    rd(0, f[0]); rd(1, f[1]);
    __builtin_amdgcn_sched_barrier(0);
#pragma unroll
    for (int k8 = 0; k8 < 8; ++k8) {
      if (k8 == 4 && qq < 3) {
        if (qq < 2) { if (NF == 8) VMW(8); else VMW(4); } else VMW(0);
        lds_inc(L_CNT + (C_LAND + qq + 1) * 4, lane);
      }
      if (k8 < 6) rd(k8 + 2, f[(k8 + 2) % 3]);
      const v4u xf = xr[8 * qq + k8];
#pragma unroll
      for (int i = 0; i < NT; ++i) acc[i] = mfma(f[k8 % 3][i], xf, acc[i]);
      csa = __builtin_amdgcn_mfma_f32_4x4x4bf16_1k(one, __builtin_bit_cast(s16x4, v2u{f[k8 % 3][0].x, f[k8 % 3][0].y}), csa, 0, 0, 0);
      csb = __builtin_amdgcn_mfma_f32_4x4x4bf16_1k(one, __builtin_bit_cast(s16x4, v2u{f[k8 % 3][0].z, f[k8 % 3][0].w}), csb, 0, 0, 0);
      if (k8 < 6) {
#pragma unroll
        for (int i = 0; i < NT; ++i) {
          __builtin_amdgcn_sched_group_barrier(0x008, 1, 0);
          __builtin_amdgcn_sched_group_barrier(0x100, 2, 0);
        }
        __builtin_amdgcn_sched_group_barrier(0x008, 2, 0);
      }
      __builtin_amdgcn_sched_barrier(0);
    }
    asm volatile("" : "+v"(acc[0]), "+v"(acc[1]), "+v"(acc[NT - 1]), "+v"(csa), "+v"(csb) :: "memory");
    lds_inc(L_CNT + (C_FREE + qq) * 4, lane);
  }
  cs[0] = csa[0] + csb[0];
}

DI void role_b_start(const Params& P, const Ctx& c, v4u& r0, v4u& r1) {
  const int lane = c.lane, tq = c.tq, g = c.gi;
  const u16* rp = P.x + (size_t)(c.p * 128 + 8 * c.G + 2 * tq) * 512 + lane * 8;
  asm volatile("global_load_dwordx4 %0, %2, off\n\tglobal_load_dwordx4 %1, %2, off offset:1024" : "=&v"(r0), "=&v"(r1) : "v"(rp) : "memory");
  const unsigned RS = c.ffn ? 3584u : 3072u;
  const __amdgpu_buffer_rsrc_t rsX = __builtin_amdgcn_make_buffer_rsrc((void*)P.x, 0, 2048 * 1024, 0x00020000);
  const __amdgpu_buffer_rsrc_t rsW = __builtin_amdgcn_make_buffer_rsrc((void*)(c.ffn ? P.w1 : P.wqkv), 0, 512u * RS, 0x00020000);
  const unsigned ub = c.ffn ? (unsigned)(224 * g + 32 * (g & 1)) * 2u + (unsigned)(tq >> 1) * 128u : (unsigned)(512 + 512 * (tq >> 1) + 64 * g) * 2u;
  const unsigned voffX = (unsigned)(lane >> 3) * 1024u + (unsigned)(((lane & 7) ^ ((4 * (tq & 1) + (lane >> 4)) & 7)) * 16);
  const unsigned voffW = (unsigned)(lane >> 3) * RS + (unsigned)(((lane & 7) ^ (((lane >> 4) & 1) << 2)) * 16);
#pragma unroll 1
  for (int f = 0; f < 2; ++f) {
    const unsigned kcp = (unsigned)((f + c.rot) & 7);
    unsigned sx = (unsigned)(c.p * 128 + 8 * tq) * 1024u + kcp * 128u, sw = (64u * kcp + 32u * (unsigned)(tq & 1)) * RS + ub;
    unsigned mx = (unsigned)f * SLOT + (unsigned)tq * 1024u, mw = (unsigned)f * SLOT + 16384u + (unsigned)tq * 4096u;
#pragma unroll 1
    for (int m = 0; m < 4; ++m) {
      dma(mx, voffX, rsX, sx); dma(mw, voffW, rsW, sw);
      mx += 4096u; sx += 32768u; mw += 1024u; sw += 8u * RS;
    }
  }
}

template <bool ffn> DI u64 role_b_head(const Params& P, const Ctx& c, int wid, v4u r0, v4u r1) {
  u64 amp_a = 0, amp_b = 0; (void)amp_a; (void)amp_b;
  const int lane = c.lane, tq = c.tq, hh = c.hh, t = lane & 31;
  const int g = c.gi;
  const unsigned tok = (unsigned)(c.p * 128 + 8 * c.G + 2 * tq);
  const __amdgpu_buffer_rsrc_t rsX = __builtin_amdgcn_make_buffer_rsrc((void*)P.x, 0, 2048 * 1024, 0x00020000);
  const __amdgpu_buffer_rsrc_t rsW = __builtin_amdgcn_make_buffer_rsrc((void*)(ffn ? P.w1 : P.wqkv), 0, ffn ? 512 * 3584 : 512 * 3072, 0x00020000);
  constexpr unsigned RS = ffn ? 3584u : 3072u;
  const unsigned cA = (unsigned)(224 * g + 32 * (g & 1));
  const unsigned u0 = ffn ? cA * 2u : (unsigned)(512 + 64 * g) * 2u, u1 = ffn ? cA * 2u + 128u : (unsigned)(1024 + 64 * g) * 2u;
  const unsigned ua = ffn ? (cA + 128u) * 2u : (unsigned)(64 * g) * 2u;
  const unsigned ub = (unsigned)((g & 1) ? 224 * g - 32 : 224 * g + 192) * 2u;
  const unsigned voffX = (unsigned)(lane >> 3) * 1024u + (unsigned)(((lane & 7) ^ ((4 * (tq & 1) + (lane >> 4)) & 7)) * 16);
  const unsigned voffW = (unsigned)(lane >> 3) * RS + (unsigned)(((lane & 7) ^ (((lane >> 4) & 1) << 2)) * 16);
  const unsigned soffX = (unsigned)(c.p * 128 + 8 * tq) * 1024u;
  const unsigned wrow = 32u * (unsigned)(tq & 1) * RS;
  auto fill = [&](int f) AI {
    const unsigned slot = (unsigned)(f & 3) * SLOT;
    if (f < 8) {
      const unsigned kcp = (unsigned)((f + c.rot) & 7);
      unsigned sx = soffX + kcp * 128u, sw = 64u * kcp * RS + wrow + ((tq >> 1) ? u1 : u0);
      unsigned mx = slot + (unsigned)tq * 1024u, mw = slot + 16384u + (unsigned)tq * 4096u;
#pragma unroll 1
      for (int m = 0; m < 4; ++m) {
        dma(mx, voffX, rsX, sx); dma(mw, voffW, rsW, sw);
        mx += 4096u; sx += 32768u; mw += 1024u; sw += 8u * RS;
      }
    } else {
      const unsigned kcp = (unsigned)((2 * (f - 8) + (tq >> 1) + c.rot) & 7);
      unsigned sw = 64u * kcp * RS + wrow, mw = slot + (unsigned)tq * 4096u;
#pragma unroll 1
      for (int m = 0; m < 4; ++m) {
        dma(mw, voffW, rsW, sw + ua);
        if (ffn) dma(mw + 16384u, voffW, rsW, sw + ub);
        mw += 1024u; sw += 8u * RS;
      }
    }
  };
  unsigned xa[4];
#pragma unroll
  for (int ks = 0; ks < 4; ++ks) xa[ks] = (unsigned)(32 * tq + t) * 128u + (unsigned)(((2 * ks + hh) ^ ((t >> 1) & 7)) * 16);
  v4u xr[32];
  u64 tag = 0;
  v4u gr;
  const unsigned token = (unsigned)(c.p * 128 + 32 * tq + t);
#pragma unroll
  for (int kc = 0; kc < 8; ++kc) {
    if (kc < 7 || ffn) VMW(8); else VMW(4);
    BAR();
    if (kc == 0) tag = lds_tag();
    if (kc == 5) {
      const char* gp = P.ws + WS_ST + token * 16u;
      asm volatile("global_load_dwordx4 %0, %1, off sc1" : "=&v"(gr) : "v"(gp) : "memory");
    }
    fill(kc + 2);
    const unsigned sb = (unsigned)(kc & 3) * SLOT;
#pragma unroll
    for (int ks = 0; ks < 4; ++ks) xr[4 * kc + ks] = lds128(xa[ks] + sb);
    asm volatile("" : "+v"(xr[4 * kc]), "+v"(xr[4 * kc + 1]), "+v"(xr[4 * kc + 2]), "+v"(xr[4 * kc + 3]));
    if (kc == 1) {
  float st0;
  {
    asm volatile("" : "+v"(r0), "+v"(r1));
    float v[2][8], mean[2], rs[2];
#pragma unroll
    for (int k = 0; k < 4; ++k) {
      v[0][2 * k] = u2f(r0[k] << 16); v[0][2 * k + 1] = u2f(r0[k] & 0xffff0000u);
      v[1][2 * k] = u2f(r1[k] << 16); v[1][2 * k + 1] = u2f(r1[k] & 0xffff0000u);
    }
#pragma unroll
    for (int i = 0; i < 2; ++i) mean[i] = wavesum(((v[i][0] + v[i][1]) + (v[i][2] + v[i][3])) + ((v[i][4] + v[i][5]) + (v[i][6] + v[i][7]))) * (1.0f / 512.0f);
#pragma unroll
    for (int i = 0; i < 2; ++i) {
      float q2 = 0.f;
#pragma unroll
      for (int k = 0; k < 8; ++k) { const float d = v[i][k] - mean[i]; q2 = __builtin_fmaf(d, d, q2); }
      rs[i] = __builtin_amdgcn_rsqf(wavesum(q2) * (1.0f / 512.0f) + 1e-5f);
    }
    st0 = (lane & 2) ? rs[1] : rs[0];
    st0 = (lane & 1) ? -((lane & 2) ? mean[1] : mean[0]) * st0 : st0;
    asm volatile("" : "+v"(st0));
  }
      if (lane < 4)
        __hip_atomic_store((GAS u64*)(P.ws + WS_ST + tok * 16u + (unsigned)lane * 8u), ((u64)(unsigned)tag << 32) | f2u(st0), __ATOMIC_RELAXED, __HIP_MEMORY_SCOPE_AGENT);
    }
  }
  asm volatile("" : "+v"(gr));
  STAMP(2);
  BAR();
  if (__builtin_amdgcn_ballot_w64(gr.y != (unsigned)tag || gr.w != (unsigned)tag) != 0) {
    const __amdgpu_buffer_rsrc_t rs = __builtin_amdgcn_make_buffer_rsrc((void*)(P.ws + WS_ST), 0, 32768, 0x00020000);
#pragma unroll 1
    for (unsigned spin = 0; spin < kSpin; ++spin) {
      gr = __builtin_amdgcn_raw_buffer_load_b128(rs, token * 16u, 0, 16);
      if (__builtin_amdgcn_ballot_w64(gr.y != (unsigned)tag || gr.w != (unsigned)tag) == 0) break;
      __builtin_amdgcn_s_sleep(1);
    }
  }
  const float rstd = u2f(gr.x), mr = u2f(gr.z);
  if (hh == 0) *(LAS f32x2*)(size_t)(L_STATS + (unsigned)(32 * tq + t) * 8u) = f32x2{rstd, mr};
  if (ffn) VMW(8); else VMW(4);
  BAR();
  const int cb = (lane >> 4) & 1, q = (lane >> 2) & 3, pp = lane & 3, sw = (q >> 1) & 1;
  unsigned wl[2];
#pragma unroll
  for (int h = 0; h < 2; ++h) wl[h] = (unsigned)(8 * hh + q) * 128u + (unsigned)((4 * (h ^ sw) + 2 * cb + (pp >> 1)) * 16 + 8 * (pp & 1));
  const __amdgpu_buffer_rsrc_t rsHB = __builtin_amdgcn_make_buffer_rsrc(P.ws + WS_HB, 0, HB_BYTES, 0x00020000);
  const unsigned T32 = (unsigned)(c.p * 4 + tq);
  f32x4 cs = {0.f, 0.f, 0.f, 0.f};
  auto cs_sync = [&](int tile, bool wr) AI {
    const float s = xhalf_add(cs[0]);
    if (hh == 0 && wr) *(LAS float*)(size_t)(L_CS1 + (unsigned)tile * 128u + (unsigned)t * 4u) = s;
    asm volatile("s_waitcnt lgkmcnt(0)" ::: "memory");
    lds_inc(L_CNT + C_CS1 * 4, lane);
    lds_wait_ge(L_CNT + C_CS1 * 4, 4);
  };
  if constexpr (ffn) {
    const unsigned w2a = ((g & 1) ? wl[1] : wl[0]) + 16384u;
    int ti[3];
    unsigned wb[3];
    f32x16 acc[3];
#pragma unroll
    for (int i = 0; i < 3; ++i) { ti[i] = (i + tq) % 3; wb[i] = ti[i] == 0 ? wl[0] : (ti[i] == 1 ? wl[1] : w2a); acc[i] = zero16(); }
    if (PRIO1) __builtin_amdgcn_s_setprio(PRIO1);
    pass1<3, 8>(P, wid, amp_a, amp_b, fill, xr, acc, cs, wb, lane);
    if (PRIO1) __builtin_amdgcn_s_setprio(0);
    cs_sync(ti[0], tq < 3);
    STAMP(3);
    if (PRIOL) __builtin_amdgcn_s_setprio(3);
    f32x4 csv[3][4], bsv[3][4];
#pragma unroll
    for (int i = 0; i < 3; ++i) { load16(L_CS1 + (unsigned)ti[i] * 128u, hh, csv[i]); load16(L_B1 + (unsigned)(4 + ti[i]) * 128u, hh, bsv[i]); }
#pragma unroll
    for (int i = 0; i < 3; ++i)
      ffn_tile(acc[i], rstd, mr, csv[i], bsv[i], rsHB, (unsigned)lane * 16u + (T32 * 144u + (unsigned)(64 + 6 * g + 2 * ti[i])) * 1024u);
    publish_last(L_CNT + C_LATE * 4, 4, P.ws, FL_L + (unsigned)(c.p * 8 + g) * 128u, tag, lane);
    if (PRIOL) __builtin_amdgcn_s_setprio(0);
  } else {
    int ti[2];
    unsigned wb[2];
    f32x16 acc[2];
#pragma unroll
    for (int i = 0; i < 2; ++i) { ti[i] = (i + tq) & 1; wb[i] = ti[i] == 0 ? wl[0] : wl[1]; acc[i] = zero16(); }
    if (PRIO1) __builtin_amdgcn_s_setprio(PRIO1);
    pass1<2, 4>(P, wid, amp_a, amp_b, fill, xr, acc, cs, wb, lane);
    if (PRIO1) __builtin_amdgcn_s_setprio(0);
    cs_sync(ti[0], tq < 2);
    STAMP(3);
    v4u pq[4][2];
    {
      f32x4 csq[2][4];
      v4u wp[4];
#pragma unroll
      for (int i = 0; i < 2; ++i) load16(L_CS1 + (unsigned)ti[i] * 128u, hh, csq[i]);
#pragma unroll
      for (int hd = 0; hd < 4; ++hd) wp[hd] = wphi_frag(2 * ti[hd >> 1] + (hd & 1), lane);
      f32x16 d[4];
#pragma unroll
      for (int hd = 0; hd < 4; ++hd) d[hd] = mfma(wp[hd], fold_pack(acc[hd >> 1], hd & 1, rstd, mr, csq[hd >> 1]), zero16());
      hedge4(d, pq);
    }
    asm volatile("" : "+v"(pq[0][0]), "+v"(pq[0][1]), "+v"(pq[1][0]), "+v"(pq[1][1]), "+v"(pq[2][0]), "+v"(pq[2][1]), "+v"(pq[3][0]), "+v"(pq[3][1]));
    STAMP(4);
    if (PRIO) __builtin_amdgcn_s_setprio(3);
    if (tq == 0) { poll_flags(P.ws, FL_K + (unsigned)g * 16u * 128u, 16, tag, lane); lds_set(L_CNT + C_KVRDY * 4, 1); }
    else lds_wait_ge(L_CNT + C_KVRDY * 4, 1);
    STAMP(5);
    {
      const __amdgpu_buffer_rsrc_t rsKV = __builtin_amdgcn_make_buffer_rsrc(P.ws + WS_KVP, 0, 524288 + 65536, 0x00020000);
      const unsigned hbase = (unsigned)(g * 4 + tq) * 16u;
      v4u pv[16];
#pragma unroll
      for (int i = 0; i < 16; ++i) pv[i] = __builtin_amdgcn_raw_buffer_load_b128(rsKV, (unsigned)lane * 16u, (hbase + (unsigned)i) * 1024u, 16);
      const v4u z0 = __builtin_amdgcn_raw_buffer_load_b128(rsKV, (unsigned)lane * 16u, 524288u + hbase * 128u, 16);
      const v4u z1 = __builtin_amdgcn_raw_buffer_load_b128(rsKV, (unsigned)lane * 16u, 524288u + hbase * 128u + 1024u, 16);
      const u64 lf = __hip_atomic_load((const GAS u64*)(P.ws + FL_L + (unsigned)(c.tt >> 1) * 8u * 128u + (unsigned)(lane & 7) * 128u), __ATOMIC_RELAXED, __HIP_MEMORY_SCOPE_AGENT);
      __builtin_amdgcn_sched_barrier(0);
      v4u idf;
      {
        const int m = lane & 31, h2 = lane >> 5;
        const int j = (m < 16 && ((m >> 2) & 1) == h2) ? (((m >> 3) << 2) | (m & 3)) : -1;
#pragma unroll
        for (int k = 0; k < 4; ++k) idf[k] = (j == 2 * k ? 0x3F80u : 0u) | (j == 2 * k + 1 ? 0x3F800000u : 0u);
      }
      f32x16 sum = zero16();
#pragma unroll
      for (int i = 0; i < 16; ++i) sum = mfma(idf, pv[i], sum);
      asm volatile("" : "+v"(sum));
      lds_inc(L_CNT + C_KVIN * 4, lane);
      if (!ORD && tq == 0 && __builtin_amdgcn_ballot_w64(lf != tag) == 0) lds_set(L_CNT + C_LATERDY * 4, 1);
      const float s[8] = {sum[0], sum[1], sum[2], sum[3], sum[4], sum[5], sum[6], sum[7]};
      const unsigned afr = L_AFR + (unsigned)tq * 2048u;
      *(LAS v4u*)(size_t)(afr + (unsigned)((lane >> 4) & 1) * 1024u + (unsigned)((lane & 15) + 32 * (lane >> 5)) * 16u) = pack8(s);
      const unsigned hh2 = (unsigned)(lane >> 4) & 1u, c16 = (unsigned)lane & 15u;
      float zz[4];
#pragma unroll
      for (int k = 0; k < 4; ++k) {
        float v = dppadd<0x128>(u2f(z0[k]) + u2f(z1[k]));
        auto r = __builtin_amdgcn_permlane16_swap(f2u(v), f2u(v), false, false);
        zz[k] = xhalf_add(u2f(r[0]) + u2f(r[1]));
      }
      if (lane < 8) *(LAS f32x4*)(size_t)(L_ZT + (unsigned)tq * 128u + (unsigned)lane * 16u) = f32x4{zz[0], zz[1], zz[2], zz[3]};
      const unsigned f0 = 16u * (unsigned)(lane >> 5) + 4u * hh2;
      const f32x4 za = lds128f(L_ZT + (unsigned)tq * 128u + f0 * 4u), zb = lds128f(L_ZT + (unsigned)tq * 128u + (f0 + 8u) * 4u);
      *(LAS v4u*)(size_t)(afr + (unsigned)(lane >> 5) * 1024u + (16u + c16 + 32u * hh2) * 16u) = v4u{pk2(za[0], za[1]), pk2(za[2], za[3]), pk2(zb[0], zb[1]), pk2(zb[2], zb[3])};
      asm volatile("s_waitcnt lgkmcnt(0)" ::: "memory");
      lds_inc(L_CNT + C_AFR * 4, lane);
      lds_wait_ge(L_CNT + C_AFR * 4, 4);
    }
    {
      v4u a0[4], a1[4];
#pragma unroll
      for (int hd = 0; hd < 4; ++hd) {
        const int head = 2 * ti[hd >> 1] + (hd & 1);
        a0[hd] = lds128(L_AFR + (unsigned)head * 2048u + (unsigned)lane * 16u); a1[hd] = lds128(L_AFR + (unsigned)head * 2048u + 1024u + (unsigned)lane * 16u);
      }
      __builtin_amdgcn_sched_barrier(0);
      f32x16 d[4];
#pragma unroll
      for (int hd = 0; hd < 4; ++hd) d[hd] = mfma(a0[hd], pq[hd][0], zero16());
#pragma unroll
      for (int hd = 0; hd < 4; ++hd) d[hd] = mfma(a1[hd], pq[hd][1], d[hd]);
#pragma unroll
      for (int hd = 0; hd < 4; ++hd) {
        const int head = 2 * ti[hd >> 1] + (hd & 1);
        const float r = __builtin_amdgcn_rcpf(d[hd][8] + 1e-6f);
        const v4u o = v4u{pk2(d[hd][0] * r, d[hd][1] * r), pk2(d[hd][2] * r, d[hd][3] * r), pk2(d[hd][4] * r, d[hd][5] * r), pk2(d[hd][6] * r, d[hd][7] * r)};
        st128(o, rsHB, (unsigned)lane * 16u + (T32 * 144u + (unsigned)(112 + 4 * g + head)) * 1024u);
      }
    }
    publish_last(L_CNT + C_O * 4, 4, P.ws, FL_O + (unsigned)(c.p * 8 + g) * 128u, tag, lane);
    if (PRIO) __builtin_amdgcn_s_setprio(0);
  }
  STAMP(6);
  return tag;
}

DI void role_b(const Params& P, const Ctx& c, int wid, u64 amp_a0) {
  u64 amp_a = amp_a0, amp_b = 0; (void)amp_a; (void)amp_b;
  const int lane = c.lane, tq = c.tq;
  v4u r0, r1;
  role_b_start(P, c, r0, r1);
  const u64 tag = c.ffn ? role_b_head<true>(P, c, wid, r0, r1) : role_b_head<false>(P, c, wid, r0, r1);
  const int pan = c.tt >> 1;
  const int tb = tq * 64 + lane;
  v4u xres[2], bres;
#pragma unroll
  for (int m = 0; m < 2; ++m) {
    const int idx = tb + 256 * m;
    xres[m] = *(const GAS v4u*)(P.x + (size_t)(64 * c.tt + (idx >> 3)) * 512 + 64 * c.ct + 8 * (idx & 7));
  }
  bres = *(const GAS v4u*)(P.b2 + 64 * c.ct + 8 * (tb & 7));
  if (tq == 0) {
    poll_flags(P.ws, (ORD ? FL_O : FL_L) + (unsigned)pan * 8u * 128u, 8, tag, lane);
    lds_set(L_CNT + (ORD ? C_ORDY : C_LATERDY) * 4, 1);
    poll_flags(P.ws, (ORD ? FL_L : FL_O) + (unsigned)pan * 8u * 128u, 8, tag, lane);
    lds_set(L_CNT + (ORD ? C_LATERDY : C_ORDY) * 4, 1);
  } else if (tq == 1 && __builtin_amdgcn_workgroup_id_x() == 0) {
    poll_flags(P.ws, FL_E, 64, tag, lane); poll_flags(P.ws, FL_E + 64 * 128, 64, tag, lane);
    poll_flags(P.ws, FL_K, 64, tag, lane); poll_flags(P.ws, FL_K + 64 * 128, 64, tag, lane);
    if (lane == 0) __hip_atomic_store((GAS u64*)(P.ws + WS_EPOCH), tag, __ATOMIC_RELAXED, __HIP_MEMORY_SCOPE_AGENT);
  }
  BAR();
#pragma unroll
  for (int m = 0; m < 2; ++m) {
    const int idx = tb + 256 * m;
    const unsigned tok = (unsigned)(idx >> 3), cg = (unsigned)(idx & 7);
    const unsigned a = (tok >> 5) * SLOT + (tok & 31u) * 256u, c0 = ((2u * cg) ^ (tok & 15u)) * 16u, c1 = ((2u * cg + 1u) ^ (tok & 15u)) * 16u;
    f32x4 lo = lds128f(a + c0), hi = lds128f(a + c1);
#pragma unroll
    for (unsigned w = 1; w < 4; ++w) { lo += lds128f(w * 8192u + a + c0); hi += lds128f(w * 8192u + a + c1); }
    v4u o;
#pragma unroll
    for (int k = 0; k < 4; ++k) {
      const float a0 = k < 2 ? lo[2 * k] : hi[2 * k - 4], a1 = k < 2 ? lo[2 * k + 1] : hi[2 * k - 3];
      const float x0 = u2f(xres[m][k] << 16), x1 = u2f(xres[m][k] & 0xffff0000u);
      const float b0 = u2f(bres[k] << 16), b1 = u2f(bres[k] & 0xffff0000u);
      o[k] = pk2(a0 + x0 + b0, a1 + x1 + b1);
    }
    *(GAS v4u*)(P.out + (size_t)(64 * c.tt + (idx >> 3)) * 512 + 64 * c.ct + 8 * (idx & 7)) = o;
  }
  STAMP(7);

}

__global__ __launch_bounds__(512, 2) NOPK void fused_kernel(Params P) {
  __shared__ __attribute__((aligned(1024))) char smem[LDS_BYTES];
  asm volatile("" ::"v"((unsigned)(size_t)(LAS char*)smem) : "memory");
  u64 pc;
  asm volatile("s_getpc_b64 %0" : "=s"(pc));
  const int tid = __builtin_amdgcn_workitem_id_x(), lane = tid & 63;
  const int wid = __builtin_amdgcn_readfirstlane(tid >> 6);
  u64 amp_a = 0, amp_b = 0;
  STAMP(0);
  Ctx c;
  c.lane = lane; c.tq = wid & 3; c.hh = lane >> 5;
  const int blk = __builtin_amdgcn_workgroup_id_x(), xcd = blk & 7, j = blk >> 3, ja = j & 7, jb = j >> 3;
  c.p = (xcd & 1) * 8 + ja;
  c.G = (xcd >> 1) * 4 + jb;
  c.ffn = !(c.G & 1);
  c.gi = c.G >> 1;
  c.rot = ROT1 == 0 ? 0 : (ROT1 == 1 ? (((ja + jb) % 3) * 3) & 7 : (ROT1 == 2 ? (ja + 2 * jb) & 7 : (ROT1 == 3 ? ((ja + jb) & 1) * 4 : (ROT1 == 4 ? ((ja + jb) & 3) * 2 : ((ja + jb) & 3)))));
  if (MAP2) { c.tt = xcd * 4 + (j & 3); c.ct = j >> 2; c.ja = j & 3; c.jb = j >> 2; }
  else { c.tt = (xcd >> 1) * 8 + ja; c.ct = (xcd & 1) * 4 + jb; c.ja = ja; c.jb = jb; }
  c.rotE = ROT2 ? (2 * ja + 4 * jb) & 15 : 0; c.rotL = ROT2 ? (ja + 3 * jb) % 12 : 0; c.rotO = ROT2 ? (ja + 2 * jb) & 7 : 0;
  if (wid < 4) {
    const unsigned kb0 = (unsigned)(j + 32 * (wid & 1)), kb = kb0 < CODE_KB ? kb0 : 0u;
    const unsigned pf = CODEPF ? *(const GAS unsigned*)((pc & ~255ull) + kb * 1024u + (unsigned)lane * 16u) : 0u;
    const u64 e = __hip_atomic_load((const GAS u64*)(P.ws + WS_EPOCH), __ATOMIC_RELAXED, __HIP_MEMORY_SCOPE_AGENT);
    if (c.ffn) {
      const int tc = tid < 224 ? tid : 0;
      const int t7 = tc >> 5, n = tc & 31, g = c.gi;
      const int cA = 224 * g + 32 * (g & 1);
      const int col = t7 < 4 ? cA + 32 * t7 : (t7 < 6 ? cA + 128 + 32 * (t7 - 4) : ((g & 1) ? 224 * g : 224 * g + 192));
      const unsigned bv = P.b1[col + n];
      *(LAS float*)(size_t)(L_B1 + (unsigned)tid * 4u) = u2f(bv << 16);
    } else {
      const v4u wv = *(const GAS v4u*)(P.wphi + (size_t)c.gi * 1024 + (tid & 127) * 8);
      *(LAS v4u*)(size_t)(L_WPHI + (unsigned)(tid & 127) * 16u) = wv;
    }
    if (tid < 32) *(LAS unsigned*)(size_t)(L_CNT + (unsigned)tid * 4u) = 0u;
    const u64 tag = e + 1u;
    if (tid == 0) *(LAS v2u*)(size_t)L_TAG = v2u{(unsigned)tag, (unsigned)(tag >> 32)};
    asm volatile("" ::"v"(pf));
    STAMP(1);
    role_a(P, c, wid, amp_a, amp_b);
  } else {
    STAMP(1);
    role_b(P, c, wid, amp_a);
  }
}

}

extern "C" void kernel_launch(void* const* d_in, const int* in_sizes, int n_in, void* d_out, int out_size, void* d_ws, size_t ws_size,
                              hipStream_t stream) {
  Params P;
  P.x = (const u16*)d_in[0]; P.wqkv = (const u16*)d_in[1]; P.wphi = (const u16*)d_in[2]; P.wo = (const u16*)d_in[3];
  P.w1 = (const u16*)d_in[5]; P.b1 = (const u16*)d_in[6]; P.w2 = (const u16*)d_in[7]; P.b2 = (const u16*)d_in[8];
  P.out = (u16*)d_out; P.ws = (char*)d_ws;
  fused_kernel<<<256, 512, 0, stream>>>(P);
}
```

```cpp
#include <hip/hip_runtime.h>
#include <stdint.h>
#include <type_traits>

namespace {

typedef unsigned short u16;
typedef unsigned long long u64;
typedef unsigned v4u __attribute__((ext_vector_type(4)));
typedef unsigned v2u __attribute__((ext_vector_type(2)));
typedef float f32x16 __attribute__((ext_vector_type(16)));
typedef float f32x4 __attribute__((ext_vector_type(4)));
typedef float f32x2 __attribute__((ext_vector_type(2)));
typedef __bf16 bf16x8 __attribute__((ext_vector_type(8)));
typedef __bf16 bf16x2 __attribute__((ext_vector_type(2)));
typedef short s16x4 __attribute__((ext_vector_type(4)));
#define LAS __attribute__((address_space(3)))
#define GAS __attribute__((address_space(1)))
#if defined(__HIP_DEVICE_COMPILE__) && !defined(USEPK)
#define NOPK __attribute__((target("no-packed-fp32-ops")))
#else
#define NOPK
#endif
#define DI __device__ __forceinline__ NOPK
#define AI __attribute__((always_inline)) NOPK

#define STAMPS 0
#define ROT1 1
#define ROT2 0
#define STR2(x) #x
#define STR(x) STR2(x)
#define W1 8
#define W2 4
#define PRIO 1
#define SLP 1
#define KVFIRST 0
#define PRIOL 0
#define MAP2 0
#define NBK 4
#define PRIO1 0
#define PRIOA 0
#define ORD 0
#define CODEPF 1
#define WARM2 1
#define CODE_KB 41

constexpr unsigned WS_EPOCH = 0;
constexpr unsigned FL_E = 4096;
constexpr unsigned FL_L = FL_E + 128 * 128;
constexpr unsigned FL_K = FL_L + 128 * 128;
constexpr unsigned FL_O = FL_K + 128 * 128;
constexpr unsigned WS_ST = 73728;
constexpr unsigned WS_KVP = 131072;
constexpr unsigned WS_ZP = WS_KVP + 524288;
constexpr unsigned WS_HB = 1048576;
constexpr unsigned HB_BYTES = 64u * 144u * 1024u;
constexpr unsigned WS_STAMP = 16u << 20;

constexpr unsigned SLOT = 32768;
constexpr unsigned L_MISC = 131072;
constexpr unsigned L_STATS = L_MISC;
constexpr unsigned L_CS0 = L_MISC + 1024;
constexpr unsigned L_CS1 = L_MISC + 1536;
constexpr unsigned L_B1 = L_MISC + 2048;
constexpr unsigned L_WPHI = L_MISC + 3072;
constexpr unsigned L_CNT = L_MISC + 5120;
constexpr unsigned L_TAG = L_MISC + 5248;
constexpr unsigned L_ZT = L_MISC + 5376;
constexpr unsigned L_AFR = L_MISC + 8192;
constexpr unsigned LDS_BYTES = 163840;
enum { C_CS1 = 0, C_LATE = 1, C_KV = 2, C_O = 3, C_AFR = 4, C_KVRDY = 5, C_LATERDY = 6, C_ORDY = 7, C_FREE = 8  , C_LAND = 12  , C_EARLY = 16, C_IMG = 17, C_KVIN = 18, C_IMGDONE = 19 };

constexpr unsigned kSpin = 1u << 16;

struct Params {
  const u16* x; const u16* wqkv; const u16* wphi; const u16* wo;
  const u16* w1; const u16* b1; const u16* w2; const u16* b2;
  u16* out; char* ws;
};

DI unsigned f2u(float f) { return __builtin_bit_cast(unsigned, f); }
DI float u2f(unsigned u) { return __builtin_bit_cast(float, u); }
DI float fmx(float a, float b) { return __builtin_fmaxf(a, b); }
DI f32x16 mfma(v4u a, v4u b, f32x16 c) {
  return __builtin_amdgcn_mfma_f32_32x32x16_bf16(__builtin_bit_cast(bf16x8, a), __builtin_bit_cast(bf16x8, b), c, 0, 0, 0);
}
DI f32x16 zero16() { f32x16 z; for (int r = 0; r < 16; ++r) z[r] = 0.f; return z; }
DI v4u lds128(unsigned a) { return *(const LAS v4u*)(size_t)a; }
DI f32x4 lds128f(unsigned a) { return *(const LAS f32x4*)(size_t)a; }
DI v2u tr64(unsigned a) { return __builtin_bit_cast(v2u, __builtin_amdgcn_ds_read_tr16_b64_v4i16((LAS s16x4*)(size_t)a)); }
DI v4u tr2(unsigned a0, unsigned a1) { v2u l = tr64(a0), h = tr64(a1); return v4u{l.x, l.y, h.x, h.y}; }
DI unsigned pk2(float a, float b) { return __builtin_bit_cast(unsigned, __builtin_convertvector(f32x2{a, b}, bf16x2)); }
DI float xhalf_add(float v) { auto r = __builtin_amdgcn_permlane32_swap(f2u(v), f2u(v), false, false); return u2f(r[0]) + u2f(r[1]); }
DI float xhalf_max(float v) { auto r = __builtin_amdgcn_permlane32_swap(f2u(v), f2u(v), false, false); return fmx(u2f(r[0]), u2f(r[1])); }
DI float shx(float v, int m, int lane) { return u2f((unsigned)__builtin_amdgcn_ds_bpermute((lane ^ m) << 2, (int)f2u(v))); }
DI f32x4 lanesum(v4u f, f32x4 c) {
  const s16x4 one = {0x3F80, 0x3F80, 0x3F80, 0x3F80};
  c = __builtin_amdgcn_mfma_f32_4x4x4bf16_1k(one, __builtin_bit_cast(s16x4, v2u{f.x, f.y}), c, 0, 0, 0);
  return __builtin_amdgcn_mfma_f32_4x4x4bf16_1k(one, __builtin_bit_cast(s16x4, v2u{f.z, f.w}), c, 0, 0, 0);
}
template <int CTRL> DI float dppadd(float v) { return v + u2f((unsigned)__builtin_amdgcn_update_dpp(0, (int)f2u(v), CTRL, 0xf, 0xf, true)); }
DI float wavesum(float v) {
  v = dppadd<0xB1>(v); v = dppadd<0x4E>(v); v = dppadd<0x141>(v); v = dppadd<0x140>(v);
  auto r = __builtin_amdgcn_permlane16_swap(f2u(v), f2u(v), false, false);
  return xhalf_add(u2f(r[0]) + u2f(r[1]));
}
DI void dma(unsigned m0v, unsigned voff, __amdgpu_buffer_rsrc_t rs, unsigned soff) {
  asm volatile("s_mov_b32 m0, %0\n\ts_nop 0\n\tbuffer_load_dwordx4 %1, %2, %3 offen lds" ::"s"(m0v), "v"(voff), "s"(rs), "s"(soff) : "memory");
}
DI void st128(v4u v, __amdgpu_buffer_rsrc_t rs, unsigned voff) { __builtin_amdgcn_raw_buffer_store_b128(v, rs, voff, 0, 16); }
#define BAR() do { __builtin_amdgcn_sched_barrier(0); asm volatile("s_waitcnt lgkmcnt(0)\n\ts_barrier" ::: "memory"); __builtin_amdgcn_sched_barrier(0); } while (0)
#define VMW(n) asm volatile("s_waitcnt vmcnt(" #n ")" ::: "memory")
constexpr unsigned ONES = 0x3F803F80u;

#define STAMP(i) do { } while (0)

DI void poll_flags(const char* ws, unsigned base, int n, u64 tag, int lane) {
  const GAS u64* f = (const GAS u64*)(ws + base + (unsigned)(lane < n ? lane : 0) * 128u);
#pragma unroll 1
  for (unsigned spin = 0; spin < kSpin; ++spin) {
    const u64 v = __hip_atomic_load(f, __ATOMIC_RELAXED, __HIP_MEMORY_SCOPE_AGENT);
    if (__builtin_amdgcn_ballot_w64(v != tag) == 0) break;
    __builtin_amdgcn_s_sleep(1);
  }
  __builtin_amdgcn_fence(__ATOMIC_ACQUIRE, "wavefront");
  asm volatile("" ::: "memory");
}
DI void lds_wait_ge(unsigned addr, unsigned want) {
#pragma unroll 1
  for (unsigned spin = 0; spin < (kSpin << 4); ++spin) {
    const unsigned v = *(volatile LAS unsigned*)(size_t)addr;
    if ((unsigned)__builtin_amdgcn_readfirstlane(v) >= want) break;
    if (SLP) __builtin_amdgcn_s_sleep(SLP);
  }
  asm volatile("" ::: "memory");
}
DI void lds_set(unsigned addr, unsigned v) { *(volatile LAS unsigned*)(size_t)addr = v; }
DI void lds_inc(unsigned addr, int lane) { if (lane == 0) __hip_atomic_fetch_add((LAS unsigned*)(size_t)addr, 1u, __ATOMIC_RELAXED, __HIP_MEMORY_SCOPE_WORKGROUP); }
DI void publish_last(unsigned cnt_addr, unsigned n, char* ws, unsigned flag_off, u64 tag, int lane) {
  VMW(0);
  if (lane == 0) {
    const unsigned old = __hip_atomic_fetch_add((LAS unsigned*)(size_t)cnt_addr, 1u, __ATOMIC_RELAXED, __HIP_MEMORY_SCOPE_WORKGROUP);
    if (old == n - 1) __hip_atomic_store((GAS u64*)(ws + flag_off), tag, __ATOMIC_RELAXED, __HIP_MEMORY_SCOPE_AGENT);
  }
}
DI u64 lds_tag() { const v2u t = *(const LAS v2u*)(size_t)L_TAG; return ((u64)(unsigned)__builtin_amdgcn_readfirstlane(t.y) << 32) | (unsigned)__builtin_amdgcn_readfirstlane(t.x); }

DI void load16(unsigned a, int hh, f32x4* v) {
#pragma unroll
  for (int g = 0; g < 4; ++g) v[g] = lds128f(a + (unsigned)g * 32u + (unsigned)hh * 16u);
}
DI void ffn_tile(const f32x16& acc, float rstd, float mr, const f32x4* cs, const f32x4* bs, __amdgpu_buffer_rsrc_t rsHB, unsigned voff) {
  f32x2 y[8], d[8];
#pragma unroll
  for (int g = 0; g < 4; ++g) {
    const f32x4 t = cs[g] * mr + bs[g];
    y[2 * g] = f32x2{acc[4 * g], acc[4 * g + 1]} * rstd + f32x2{t[0], t[1]};
    y[2 * g + 1] = f32x2{acc[4 * g + 2], acc[4 * g + 3]} * rstd + f32x2{t[2], t[3]};
  }
#pragma unroll
  for (int k = 0; k < 8; ++k) d[k] = y[k] * ((y[k] * y[k]) * (-0.10294324f) + (-2.30220819f));
#pragma unroll
  for (int k = 0; k < 8; ++k) { d[k].x = __builtin_amdgcn_exp2f(d[k].x); d[k].y = __builtin_amdgcn_exp2f(d[k].y); }
#pragma unroll
  for (int k = 0; k < 8; ++k) d[k] += 1.0f;
#pragma unroll
  for (int k = 0; k < 8; ++k) { d[k].x = __builtin_amdgcn_rcpf(d[k].x); d[k].y = __builtin_amdgcn_rcpf(d[k].y); }
  v4u o0, o1;
#pragma unroll
  for (int k = 0; k < 4; ++k) {
    const f32x2 g0 = y[k] * d[k], g1 = y[4 + k] * d[4 + k];
    o0[k] = pk2(g0.x, g0.y); o1[k] = pk2(g1.x, g1.y);
  }
  st128(o0, rsHB, voff); st128(o1, rsHB, voff + 1024u);
}
DI float max3(float a, float b, float c) { return __builtin_elementwise_maximum(__builtin_elementwise_maximum(a, b), c); }
DI void hedge4(const f32x16 (&d)[4], v4u (&o)[4][2]) {
  constexpr float L = 1.4426950408889634f;
  float m[4][2], e[4][16], s[4][2];
#pragma unroll
  for (int u = 0; u < 4; ++u)
#pragma unroll
    for (int h = 0; h < 2; ++h) {
      const float a = max3(d[u][8 * h], d[u][8 * h + 1], d[u][8 * h + 2]), b = max3(d[u][8 * h + 3], d[u][8 * h + 4], d[u][8 * h + 5]);
      m[u][h] = max3(max3(d[u][8 * h + 6], d[u][8 * h + 7], a), b, b);
    }
#pragma unroll
  for (int u = 0; u < 4; ++u)
#pragma unroll
    for (int h = 0; h < 2; ++h) {
      auto r = __builtin_amdgcn_permlane32_swap(f2u(m[u][h]), f2u(m[u][h]), false, false);
      m[u][h] = __builtin_elementwise_maximum(u2f(r[0]), u2f(r[1])) * -L;
    }
#pragma unroll
  for (int u = 0; u < 4; ++u)
#pragma unroll
    for (int i = 0; i < 16; ++i) e[u][i] = __builtin_amdgcn_exp2f(__builtin_fmaf(d[u][i], L, m[u][i >> 3]));
#pragma unroll
  for (int u = 0; u < 4; ++u)
#pragma unroll
    for (int h = 0; h < 2; ++h) {
      const float* x = &e[u][8 * h];
      s[u][h] = ((x[0] + x[1]) + (x[2] + x[3])) + ((x[4] + x[5]) + (x[6] + x[7]));
    }
#pragma unroll
  for (int u = 0; u < 4; ++u)
#pragma unroll
    for (int h = 0; h < 2; ++h) s[u][h] = __builtin_amdgcn_rcpf(xhalf_add(s[u][h]));
#pragma unroll
  for (int u = 0; u < 4; ++u)
#pragma unroll
    for (int h = 0; h < 2; ++h)
#pragma unroll
      for (int k = 0; k < 4; ++k) o[u][h][k] = pk2(e[u][8 * h + 2 * k] * s[u][h], e[u][8 * h + 2 * k + 1] * s[u][h]);
}
DI v4u fold_pack(const f32x16& acc, int hf, float rstd, float mr, const f32x4* cs) {
  float y[8];
#pragma unroll
  for (int k = 0; k < 8; ++k) y[k] = __builtin_fmaf(acc[8 * hf + k], rstd, cs[2 * hf + (k >> 2)][k & 3] * mr);
  return v4u{pk2(y[0], y[1]), pk2(y[2], y[3]), pk2(y[4], y[5]), pk2(y[6], y[7])};
}
DI v4u wphi_frag(int hd, int lane) {
  const int hh = lane >> 5, q = (lane >> 2) & 3, pp = lane & 3;
  const unsigned a = L_WPHI + (unsigned)hd * 512u + (unsigned)(4 * hh + q) * 32u + (unsigned)pp * 8u;
  v4u f = tr2(a, a + 256u);
  const unsigned sg = (lane & 16) ? 0x80008000u : 0u;
  f.x ^= sg; f.y ^= sg; f.z ^= sg; f.w ^= sg;
  return f;
}
DI v4u selv(bool s, v4u a, v4u b) { const unsigned m = s ? 0xffffffffu : 0u; return v4u{(a.x & m) | (b.x & ~m), (a.y & m) | (b.y & ~m), (a.z & m) | (b.z & ~m), (a.w & m) | (b.w & ~m)}; }
DI v4u pack8(const float* y) { return v4u{pk2(y[0], y[1]), pk2(y[2], y[3]), pk2(y[4], y[5]), pk2(y[6], y[7])}; }

constexpr int NWI = MAP2 ? 18 : 9;
template <int NI> DI void warm_w2(const Params& P, int ct, int ja, int tq, int i0, int lane, v4u* d) {
#pragma unroll
  for (int i = 0; i < NI; ++i) {
    const int row = 32 * NWI * ja + 8 * NWI * tq + 8 * (i0 + i);
    const u64 a = row < 1792 ? (u64)(size_t)P.w2 + (u64)(unsigned)row * 1024u : (u64)(size_t)P.wo + (u64)(unsigned)(row - 1792) * 1024u;
    const u64 base = ((u64)(unsigned)__builtin_amdgcn_readfirstlane((unsigned)(a >> 32)) << 32) | (unsigned)__builtin_amdgcn_readfirstlane((unsigned)a);
    const unsigned voff = (unsigned)(lane >> 3) * 1024u + (unsigned)(lane & 7) * 16u + (unsigned)ct * 128u;
    asm volatile("s_nop 4\n\tglobal_load_dwordx4 %0, %1, %2" : "+v"(d[i % 3]) : "v"(voff), "s"(base) : "memory");
  }
}
struct Ctx {
  int lane, tq, hh;
  int p, gi, G;
  int rot;
  int tt, ct, ja, jb;
  int rotE, rotL, rotO;
  bool ffn;
};

DI void role_a(const Params& P, const Ctx& c, int wid, u64 amp_a, u64 amp_b) {
  const int lane = c.lane, tq = c.tq, hh = c.hh, t = lane & 31;
  const int cb = (lane >> 4) & 1, q = (lane >> 2) & 3, pp = lane & 3, sw = (q >> 1) & 1;
  unsigned tabB, tabWlo, tabWhi;
  {
    const int jj = lane < 36 ? lane : 35;
    constexpr int J2 = ORD ? 24 : 28;
    int sg;
    auto perm = [&](int i, int n4) AI {
      if (!ROT2) return i;
      constexpr int NO = MAP2 ? 4 : 2;
      const int b = (c.jb >> 1) & (NO - 1), ng = 4 * n4 / NO;
      if (i < 2) return NO * i + b;
      const int m = (i - 2) / NO, r = (i - 2) % NO;
      if (m + 2 < ng) return r < NO - 1 ? NO * m + ((b + 1 + r) & (NO - 1)) : NO * (m + 2) + b;
      const int e = i - 2 - NO * (ng - 2);
      return NO * (ng - 2 + e / (NO - 1)) + ((b + 1 + e % (NO - 1)) & (NO - 1));
    };
    if (jj < 16) sg = 4 * perm(jj, 4) + tq;
    else if ((jj < J2) != (ORD != 0)) sg = 64 + 4 * perm(jj - (ORD ? 24 : 16), 3) + tq;
    else sg = 112 + 4 * perm(jj - (ORD ? 16 : 28), 2) + tq;
    int row;
    if (sg < 64) { const int g = sg >> 3; row = 224 * g + 32 * (g & 1) + 16 * (sg & 7); }
    else if (sg < 112) {
      const int e = sg - 64, g = e / 6, i = e - 6 * g;
      row = i < 4 ? 224 * g + 32 * (g & 1) + 128 + 16 * i : ((g & 1) ? 224 * g : 224 * g + 192) + 16 * (i - 4);
    } else row = 16 * (sg - 112);
    const u64 a = (u64)(size_t)(sg < 112 ? P.w2 : P.wo) + (u64)row * 1024u + (u64)c.ct * 128u;
    tabB = (unsigned)sg * 1024u; tabWlo = (unsigned)a; tabWhi = (unsigned)(a >> 32);
  }
  asm volatile("" : "+v"(tabB), "+v"(tabWlo), "+v"(tabWhi));
  const int th = tq & 1, up = tq >> 1;
  unsigned xa[4];
#pragma unroll
  for (int ks = 0; ks < 4; ++ks) xa[ks] = (unsigned)(64 * th + t) * 128u + (unsigned)(((2 * ks + hh) ^ ((t >> 1) & 7)) * 16);
  const unsigned wl = 16384u + (unsigned)(8 * hh + q) * 128u + (unsigned)((4 * (up ^ sw) + 2 * cb + (pp >> 1)) * 16 + 8 * (pp & 1));
  f32x16 acc[2][2];
#pragma unroll
  for (int i = 0; i < 2; ++i) { acc[i][0] = zero16(); acc[i][1] = zero16(); }
  f32x4 cs = {0.f, 0.f, 0.f, 0.f};
  unsigned x0 = xa[0], x1 = xa[1], x2 = xa[2], x3 = xa[3], w0 = wl;
  v4u xf[4][2], wf[4][2];
  auto rd = [&](int ks, v4u* X, v4u* W) AI {
    const unsigned xb = ks == 0 ? x0 : (ks == 1 ? x1 : (ks == 2 ? x2 : x3));
    X[0] = lds128(xb); X[1] = lds128(xb + 4096u);
    const unsigned a = w0 + (unsigned)(ks * 2048);
    W[0] = tr2(a, a + 512u); W[1] = tr2(a + 8192u, a + 8704u);
  };
  f32x4 cs2 = {0.f, 0.f, 0.f, 0.f};
  BAR();
  rd(0, xf[0], wf[0]); rd(1, xf[1], wf[1]);
  auto pass0 = [&](auto TH) AI {
    constexpr int CT = decltype(TH)::value;
    const s16x4 one = {0x3F80, 0x3F80, 0x3F80, 0x3F80};
#pragma unroll 1
    for (int kc = 0; kc < 8; ++kc) {
#pragma unroll
      for (int ks = 0; ks < 4; ++ks) {
        if (ks == 2) {
          __builtin_amdgcn_sched_barrier(0);
          asm volatile("s_barrier" ::: "memory");
          __builtin_amdgcn_sched_barrier(0);
          const unsigned sb = (unsigned)((kc + 1) & 3) * SLOT;
          x0 = xa[0] + sb; x1 = xa[1] + sb; w0 = wl + sb;
          asm volatile("" : "+v"(x0), "+v"(x1), "+v"(w0));
        }
        if (ks == 0) {
          const unsigned sb = (unsigned)(kc & 3) * SLOT;
          x2 = xa[2] + sb; x3 = xa[3] + sb;
          asm volatile("" : "+v"(x2), "+v"(x3));
        }
        rd((ks + 2) & 3, xf[(ks + 2) & 3], wf[(ks + 2) & 3]);
        acc[0][0] = mfma(wf[ks][0], xf[ks][0], acc[0][0]); acc[0][1] = mfma(wf[ks][0], xf[ks][1], acc[0][1]);
        acc[1][0] = mfma(wf[ks][1], xf[ks][0], acc[1][0]); acc[1][1] = mfma(wf[ks][1], xf[ks][1], acc[1][1]);
        cs = __builtin_amdgcn_mfma_f32_4x4x4bf16_1k(one, __builtin_bit_cast(s16x4, v2u{wf[ks][CT].x, wf[ks][CT].y}), cs, 0, 0, 0);
        cs2 = __builtin_amdgcn_mfma_f32_4x4x4bf16_1k(one, __builtin_bit_cast(s16x4, v2u{wf[ks][CT].z, wf[ks][CT].w}), cs2, 0, 0, 0);
        __builtin_amdgcn_sched_group_barrier(0x008, 1, 0);
        __builtin_amdgcn_sched_group_barrier(0x100, 2, 0);
        __builtin_amdgcn_sched_group_barrier(0x008, 1, 0);
        __builtin_amdgcn_sched_group_barrier(0x100, 2, 0);
        __builtin_amdgcn_sched_group_barrier(0x008, 1, 0);
        __builtin_amdgcn_sched_group_barrier(0x100, 2, 0);
        __builtin_amdgcn_sched_group_barrier(0x008, 3, 0);
        __builtin_amdgcn_sched_barrier(0);
      }
      asm volatile("" : "+v"(acc[0][0]), "+v"(acc[0][1]), "+v"(acc[1][0]), "+v"(acc[1][1]), "+v"(cs), "+v"(cs2));
    }
  };
  if (th) pass0(std::integral_constant<int, 1>{}); else pass0(std::integral_constant<int, 0>{});
  cs[0] += cs2[0];
  STAMP(2);
  {
    const float s = xhalf_add(cs[0]);
    if (hh == 0) *(LAS float*)(size_t)(L_CS0 + (unsigned)(up + 2 * th) * 128u + (unsigned)t * 4u) = s;
  }
  BAR();
  const u64 tag = lds_tag();
  float rstd[2], mr[2];
#pragma unroll
  for (int b = 0; b < 2; ++b) {
    const f32x2 st = *(const LAS f32x2*)(size_t)(L_STATS + (unsigned)(64 * th + 32 * b + t) * 8u);
    rstd[b] = st.x; mr[b] = st.y;
  }
  const __amdgpu_buffer_rsrc_t rsHB = __builtin_amdgcn_make_buffer_rsrc(P.ws + WS_HB, 0, HB_BYTES, 0x00020000);
  v4u wm[3] = {v4u{0u, 0u, 0u, 0u}, v4u{0u, 0u, 0u, 0u}, v4u{0u, 0u, 0u, 0u}};
  if (c.ffn) {
    f32x4 csv[2][4], bsv[2][4];
#pragma unroll
    for (int i = 0; i < 2; ++i) { load16(L_CS0 + (unsigned)(up + 2 * i) * 128u, hh, csv[i]); load16(L_B1 + (unsigned)(up + 2 * i) * 128u, hh, bsv[i]); }
#pragma unroll
    for (int i = 0; i < 2; ++i)
#pragma unroll
      for (int b = 0; b < 2; ++b) {
        const unsigned tile = (unsigned)(up + 2 * i), T32 = (unsigned)(c.p * 4 + 2 * th + b);
        ffn_tile(acc[i][b], rstd[b], mr[b], csv[i], bsv[i], rsHB, (unsigned)lane * 16u + (T32 * 144u + (unsigned)(8 * c.gi) + 2u * tile) * 1024u);
        if (WARM2 && 2 * i + b < 3) warm_w2<NWI / 3>(P, c.ct, c.ja, tq, (NWI / 3) * (2 * i + b), lane, wm);
      }
    STAMP(8);
    publish_last(L_CNT + C_EARLY * 4, 4, P.ws, FL_E + (unsigned)(c.p * 8 + c.gi) * 128u, tag, lane);
    if (WARM2) asm volatile("" :: "v"(wm[0]), "v"(wm[1]), "v"(wm[2]));
  } else {
    f32x4 csk[4], csw[4];
    load16(L_CS0 + (unsigned)up * 128u, hh, csk); load16(L_CS0 + (unsigned)(2 + up) * 128u, hh, csw);
    const v4u wp[2] = {wphi_frag(2 * up, lane), wphi_frag(2 * up + 1, lane)};
    if (WARM2) warm_w2<NWI>(P, c.ct, c.ja, tq, 0, lane, wm);
    f32x16 d[4];
#pragma unroll
    for (int u = 0; u < 4; ++u) d[u] = mfma(wp[u & 1], fold_pack(acc[0][u >> 1], u & 1, rstd[u >> 1], mr[u >> 1], csk), zero16());
    v4u vf[4];
#pragma unroll
    for (int u = 0; u < 4; ++u) vf[u] = fold_pack(acc[1][u >> 1], u & 1, rstd[u >> 1], mr[u >> 1], csw);
    v4u ph[4][2];
    hedge4(d, ph);
#pragma unroll
    for (int u = 0; u < 4; ++u) {
      const unsigned img = (unsigned)(2 * up + (u & 1)) * SLOT + 16384u;
      const unsigned row = (unsigned)(64 * th + 32 * (u >> 1) + t);
      const unsigned g = (row >> 1) & 7u, gv = (row >> 2) & 3u;
      *(LAS v2u*)(size_t)(img + row * 64u + (((unsigned)(0 + hh)) ^ g) * 8u) = v2u{ph[u][0].x, ph[u][0].y};
      *(LAS v2u*)(size_t)(img + row * 64u + (((unsigned)(2 + hh)) ^ g) * 8u) = v2u{ph[u][0].z, ph[u][0].w};
      *(LAS v2u*)(size_t)(img + row * 64u + (((unsigned)(4 + hh)) ^ g) * 8u) = v2u{ph[u][1].x, ph[u][1].y};
      *(LAS v2u*)(size_t)(img + row * 64u + (((unsigned)(6 + hh)) ^ g) * 8u) = v2u{ph[u][1].z, ph[u][1].w};
      *(LAS v2u*)(size_t)(img + 8192u + row * 32u + (((unsigned)(0 + hh)) ^ gv) * 8u) = v2u{vf[u].x, vf[u].y};
      *(LAS v2u*)(size_t)(img + 8192u + row * 32u + (((unsigned)(2 + hh)) ^ gv) * 8u) = v2u{vf[u].z, vf[u].w};
    }
    STAMP(8);
    asm volatile("s_waitcnt lgkmcnt(0)" ::: "memory");
    lds_inc(L_CNT + C_IMG * 4, lane);
    lds_wait_ge(L_CNT + C_IMG * 4, 4);
    const unsigned img = (unsigned)tq * SLOT + 16384u;
    f32x16 kv = zero16();
    unsigned pa[2], va[2];
#pragma unroll
    for (int i2 = 0; i2 < 2; ++i2) {
      const unsigned r = (unsigned)(8 * hh + 4 * i2 + q);
      pa[i2] = img + r * 64u + (((unsigned)(4 * cb + pp)) ^ ((r >> 1) & 7u)) * 8u;
      va[i2] = img + 8192u + r * 32u + (((unsigned)pp) ^ ((r >> 2) & 3u)) * 8u;
    }
#pragma unroll
    for (int ks = 0; ks < 8; ++ks) {
      const v4u af = tr2(pa[0] + (unsigned)ks * 1024u, pa[1] + (unsigned)ks * 1024u);
      v4u bf = tr2(va[0] + (unsigned)ks * 512u, va[1] + (unsigned)ks * 512u);
      if (cb) bf = v4u{ONES, ONES, ONES, ONES};
      kv = mfma(af, bf, kv);
    }
    asm volatile("" : "+v"(kv) :: "memory");
    lds_inc(L_CNT + C_IMGDONE * 4, lane);
    const unsigned hidx = (unsigned)(c.gi * 4 + tq) * 16u + (unsigned)c.p;
    const __amdgpu_buffer_rsrc_t rsKV = __builtin_amdgcn_make_buffer_rsrc(P.ws + WS_KVP, 0, 524288 + 65536, 0x00020000);
    if (cb == 0) {
#pragma unroll
      for (int s = 0; s < 2; ++s) {
        const v4u o = v4u{pk2(kv[8 * s], kv[8 * s + 1]), pk2(kv[8 * s + 2], kv[8 * s + 3]), pk2(kv[8 * s + 4], kv[8 * s + 5]), pk2(kv[8 * s + 6], kv[8 * s + 7])};
        st128(o, rsKV, (unsigned)((16 * s + (lane & 15) + 32 * hh) * 16) + hidx * 1024u);
      }
    } else if ((lane & 15) == 0) {
#pragma unroll
      for (int g4 = 0; g4 < 4; ++g4) {
        const v4u o = v4u{f2u(kv[4 * g4]), f2u(kv[4 * g4 + 1]), f2u(kv[4 * g4 + 2]), f2u(kv[4 * g4 + 3])};
        st128(o, rsKV, (unsigned)((8 * g4 + 4 * hh) * 4) + 524288u + hidx * 128u);
      }
    }
    publish_last(L_CNT + C_KV * 4, 4, P.ws, FL_K + (unsigned)(c.gi * 16 + c.p) * 128u, tag, lane);
    if (WARM2) asm volatile("" :: "v"(wm[0]), "v"(wm[1]), "v"(wm[2]));
  }
  STAMP(3);
  const unsigned vw0 = (unsigned)(lane >> 3) * 1024u + (unsigned)(((lane & 7) ^ (((lane >> 4) & 1) << 2)) * 16), vw1 = vw0 + 8192u;
  const unsigned ring = (unsigned)tq * 8192u;
  auto pofs = [&](int r) AI { return (unsigned)((r >> 2) & 3) * SLOT + (unsigned)(r & 3) * 2048u; };
  const unsigned vb0 = (unsigned)lane * 16u + (unsigned)(2 * c.tt) * 144u * 1024u, vb1 = vb0 + 144u * 1024u;
  auto wbase = [&](int j) AI { const unsigned lo = __builtin_amdgcn_readlane(tabWlo, j), hi = __builtin_amdgcn_readlane(tabWhi, j); return ((u64)hi << 32) | lo; };
  auto wdma1 = [&](u64 b, unsigned m, unsigned v) AI {
    asm volatile("s_mov_b32 m0, %0\n\ts_nop 0\n\tglobal_load_lds_dwordx4 %1, %2" ::"s"(m), "v"(v), "s"(b) : "memory");
  };
  auto wdma = [&](int j) AI {
    const u64 b = wbase(j);
    const unsigned m = ring + pofs(j);
    asm volatile("s_nop 4" ::: "memory");
    wdma1(b, m, vw0); wdma1(b, m + 1024u, vw1);
  };
  constexpr int NS = 6, LB = NS - 1;
  v4u br[NS][2];
  auto bload1 = [&](unsigned so, v4u& d, unsigned v) AI {
    asm volatile("buffer_load_dwordx4 %0, %1, %2, %3 offen sc1" : "+v"(d) : "v"(v), "s"(rsHB), "s"(so) : "memory");
  };
  auto bload0 = [&](unsigned so, v4u& d0, v4u& d1) AI {
    asm volatile("s_nop 4\n\tbuffer_load_dwordx4 %0, %2, %4, %5 offen sc1\n\tbuffer_load_dwordx4 %1, %3, %4, %5 offen sc1"
                 : "=&v"(d0), "=&v"(d1) : "v"(vb0), "v"(vb1), "s"(rsHB), "s"(so) : "memory");
  };
  if (!c.ffn && tq >= 2) lds_wait_ge(L_CNT + C_IMGDONE * 4, 4);
  const int pan = c.tt >> 1;
  v2u ef;
  const char* efp = P.ws + FL_E + (unsigned)pan * 8u * 128u + (unsigned)(lane & 7) * 128u;
#pragma unroll 1
  for (int s = 0; s < 2; ++s) {
    lds_wait_ge(L_CNT + (C_FREE + s) * 4, 4);
#pragma unroll 1
    for (int j = 4 * s; j < 4 * s + 4; ++j) wdma(j);
  }
  lds_wait_ge(L_CNT + (C_FREE + 2) * 4, 4);
  asm volatile("global_load_dwordx2 %0, %1, off sc1" : "=&v"(ef) : "v"(efp) : "memory");
  wdma(8);
  STAMP(4);
  asm volatile("s_waitcnt vmcnt(2)" : "+v"(ef) :: "memory");
  if (__builtin_amdgcn_ballot_w64(ef.x != (unsigned)tag || ef.y != (unsigned)(tag >> 32)) != 0) poll_flags(P.ws, FL_E + (unsigned)pan * 8u * 128u, 8, tag, lane);
  STAMP(5);
  bload0(__builtin_amdgcn_readlane(tabB, 0), br[0][0], br[0][1]);
  bload0(__builtin_amdgcn_readlane(tabB, 1), br[1][0], br[1][1]);
#pragma unroll
  for (int u = 2; u < LB; ++u) {
    wdma(7 + u);
    bload0(__builtin_amdgcn_readlane(tabB, u), br[u][0], br[u][1]);
  }
  br[LB][0] = v4u{0u, 0u, 0u, 0u}; br[LB][1] = br[LB][0];
  unsigned w2l[2];
#pragma unroll
  for (int h = 0; h < 2; ++h) w2l[h] = ring + (unsigned)(4 * hh + q) * 128u + (unsigned)((4 * (h ^ sw) + 2 * cb + (pp >> 1)) * 16 + 8 * (pp & 1));
  f32x16 o[2][2];
#pragma unroll
  for (int a = 0; a < 2; ++a)
#pragma unroll
    for (int b = 0; b < 2; ++b) o[a][b] = zero16();
  v4u af[2];
  asm volatile("s_waitcnt vmcnt(14)" : "+v"(br[0][0]), "+v"(br[0][1]) :: "memory");
  af[0] = tr2(w2l[0], w2l[0] + 1024u); af[1] = tr2(w2l[1], w2l[1] + 1024u);
  auto step = [&](int j, int u, bool refill) AI {
    v4u an[2];
    if (refill) asm volatile("s_waitcnt vmcnt(12)" : "+v"(br[(u + 1) % NS][0]), "+v"(br[(u + 1) % NS][1]) :: "memory");
    else asm volatile("s_waitcnt vmcnt(6)" : "+v"(br[(u + 1) % NS][0]), "+v"(br[(u + 1) % NS][1]) :: "memory");
    const int jn = j + LB;
    constexpr int J3 = ORD ? 24 : 28;
    if (u == (16 - LB) % NS && jn == 16) lds_wait_ge(L_CNT + (ORD ? C_ORDY : C_LATERDY) * 4, 1);
    if (u == (J3 - LB) % NS && jn == J3) lds_wait_ge(L_CNT + (ORD ? C_LATERDY : C_ORDY) * 4, 1);
    unsigned p0 = w2l[0] + pofs(j + 1), p1 = w2l[1] + pofs(j + 1);
    asm volatile("" : "+v"(p0), "+v"(p1));
    const unsigned so = jn < 36 ? (unsigned)__builtin_amdgcn_readlane(tabB, jn) : 0x7F000000u;
    u64 wb2 = 0;
    if (refill) wb2 = wbase(j + 12);
    const unsigned m = ring + pofs(j + 12);
    an[0] = tr2(p0, p0 + 1024u); an[1] = tr2(p1, p1 + 1024u);
    asm volatile("s_nop 2" ::"s"(so), "s"(wb2) : "memory");
    __builtin_amdgcn_sched_barrier(0);
    o[0][0] = mfma(af[0], br[u][0], o[0][0]);
    __builtin_amdgcn_sched_barrier(0);
    if (refill) wdma1(wb2, m, vw0);
    __builtin_amdgcn_sched_barrier(0);
    o[0][1] = mfma(af[0], br[u][1], o[0][1]);
    __builtin_amdgcn_sched_barrier(0);
    if (refill) wdma1(wb2, m + 1024u, vw1);
    __builtin_amdgcn_sched_barrier(0);
    o[1][0] = mfma(af[1], br[u][0], o[1][0]);
    __builtin_amdgcn_sched_barrier(0);
    bload1(so, br[(u + LB) % NS][0], vb0);
    __builtin_amdgcn_sched_barrier(0);
    o[1][1] = mfma(af[1], br[u][1], o[1][1]);
    __builtin_amdgcn_sched_barrier(0);
    bload1(so, br[(u + LB) % NS][1], vb1);
    __builtin_amdgcn_sched_barrier(0);
    af[0] = an[0]; af[1] = an[1];
  };
  lds_wait_ge(L_CNT + (C_FREE + 3) * 4, 4);
#pragma unroll 1
  for (int it = 0; it < 4; ++it) {
    if (it == 2) STAMP(10);
#pragma unroll
    for (int u = 0; u < NS; ++u) step(NS * it + u, u, true);
  }
  STAMP(11);
#pragma unroll 1
  for (int it = 4; it < 6; ++it) {
#pragma unroll
    for (int u = 0; u < NS; ++u) step(NS * it + u, u, false);
  }
  asm volatile("s_waitcnt vmcnt(0)" : "+v"(br[0][0]), "+v"(br[0][1]), "+v"(br[1][0]), "+v"(br[1][1]), "+v"(br[2][0]), "+v"(br[2][1]),
               "+v"(br[3][0]), "+v"(br[3][1]), "+v"(br[4][0]), "+v"(br[4][1]), "+v"(br[5][0]), "+v"(br[5][1]) :: "memory");
  STAMP(6);
#pragma unroll
  for (int a = 0; a < 2; ++a)
#pragma unroll
    for (int b = 0; b < 2; ++b)
#pragma unroll
      for (int g4 = 0; g4 < 4; ++g4)
        *(LAS f32x4*)(size_t)(ring + (unsigned)b * SLOT + (unsigned)t * 256u + (unsigned)((8 * a + 2 * g4 + hh) ^ (t & 15)) * 16u) =
            f32x4{o[a][b][4 * g4], o[a][b][4 * g4 + 1], o[a][b][4 * g4 + 2], o[a][b][4 * g4 + 3]};
  VMW(0);
  BAR();
  STAMP(7);
}

template <int NT, int NF, class Fill> DI void pass1(const Params& P, int wid, u64& amp_a, u64& amp_b, Fill& fill, const v4u (&xr)[32], f32x16 (&acc)[NT], f32x4& cs, const unsigned (&wb)[NT], int lane) {
  const s16x4 one = {0x3F80, 0x3F80, 0x3F80, 0x3F80};
  f32x4 csa = {0.f, 0.f, 0.f, 0.f}, csb = csa;
#pragma unroll
  for (int qq = 0; qq < 4; ++qq) {
    if (qq > 0) lds_wait_ge(L_CNT + (C_LAND + qq) * 4, 4);
    if (qq < 2) fill(10 + qq);
    unsigned a[NT];
#pragma unroll
    for (int i = 0; i < NT; ++i) { a[i] = wb[i] + (unsigned)qq * SLOT; asm volatile("" : "+v"(a[i])); }
    v4u f[3][NT];
    auto rd = [&](int k8, v4u* W) AI {
      const unsigned o = (unsigned)((k8 >> 2) * 8192 + (k8 & 3) * 2048);
#pragma unroll
      for (int i = 0; i < NT; ++i) W[i] = tr2(a[i] + o, a[i] + o + 512u);
    };
    rd(0, f[0]); rd(1, f[1]);
    __builtin_amdgcn_sched_barrier(0);
#pragma unroll
    for (int k8 = 0; k8 < 8; ++k8) {
      if (k8 == 4 && qq < 3) {
        if (qq < 2) { if (NF == 8) VMW(8); else VMW(4); } else VMW(0);
        lds_inc(L_CNT + (C_LAND + qq + 1) * 4, lane);
      }
      if (k8 < 6) rd(k8 + 2, f[(k8 + 2) % 3]);
      const v4u xf = xr[8 * qq + k8];
#pragma unroll
      for (int i = 0; i < NT; ++i) acc[i] = mfma(f[k8 % 3][i], xf, acc[i]);
      csa = __builtin_amdgcn_mfma_f32_4x4x4bf16_1k(one, __builtin_bit_cast(s16x4, v2u{f[k8 % 3][0].x, f[k8 % 3][0].y}), csa, 0, 0, 0);
      csb = __builtin_amdgcn_mfma_f32_4x4x4bf16_1k(one, __builtin_bit_cast(s16x4, v2u{f[k8 % 3][0].z, f[k8 % 3][0].w}), csb, 0, 0, 0);
      if (k8 < 6) {
#pragma unroll
        for (int i = 0; i < NT; ++i) {
          __builtin_amdgcn_sched_group_barrier(0x008, 1, 0);
          __builtin_amdgcn_sched_group_barrier(0x100, 2, 0);
        }
        __builtin_amdgcn_sched_group_barrier(0x008, 2, 0);
      }
      __builtin_amdgcn_sched_barrier(0);
    }
    asm volatile("" : "+v"(acc[0]), "+v"(acc[1]), "+v"(acc[NT - 1]), "+v"(csa), "+v"(csb) :: "memory");
    lds_inc(L_CNT + (C_FREE + qq) * 4, lane);
  }
  cs[0] = csa[0] + csb[0];
}

template <bool ffn> DI u64 role_b_head(const Params& P, const Ctx& c, int wid) {
  u64 amp_a = 0, amp_b = 0; (void)amp_a; (void)amp_b;
  const int lane = c.lane, tq = c.tq, hh = c.hh, t = lane & 31;
  const int g = c.gi;
  const unsigned tok = (unsigned)(c.p * 128 + 8 * c.G + 2 * tq);
  v4u r0, r1;
  {
    const u16* rp = P.x + (size_t)tok * 512 + lane * 8;
    asm volatile("global_load_dwordx4 %0, %2, off\n\tglobal_load_dwordx4 %1, %2, off offset:1024" : "=&v"(r0), "=&v"(r1) : "v"(rp) : "memory");
  }
  const __amdgpu_buffer_rsrc_t rsX = __builtin_amdgcn_make_buffer_rsrc((void*)P.x, 0, 2048 * 1024, 0x00020000);
  const __amdgpu_buffer_rsrc_t rsW = __builtin_amdgcn_make_buffer_rsrc((void*)(ffn ? P.w1 : P.wqkv), 0, ffn ? 512 * 3584 : 512 * 3072, 0x00020000);
  constexpr unsigned RS = ffn ? 3584u : 3072u;
  const unsigned cA = (unsigned)(224 * g + 32 * (g & 1));
  const unsigned u0 = ffn ? cA * 2u : (unsigned)(512 + 64 * g) * 2u, u1 = ffn ? cA * 2u + 128u : (unsigned)(1024 + 64 * g) * 2u;
  const unsigned ua = ffn ? (cA + 128u) * 2u : (unsigned)(64 * g) * 2u;
  const unsigned ub = (unsigned)((g & 1) ? 224 * g - 32 : 224 * g + 192) * 2u;
  const unsigned voffX = (unsigned)(lane >> 3) * 1024u + (unsigned)(((lane & 7) ^ ((4 * (tq & 1) + (lane >> 4)) & 7)) * 16);
  const unsigned voffW = (unsigned)(lane >> 3) * RS + (unsigned)(((lane & 7) ^ (((lane >> 4) & 1) << 2)) * 16);
  const unsigned soffX = (unsigned)(c.p * 128 + 8 * tq) * 1024u;
  const unsigned wrow = 32u * (unsigned)(tq & 1) * RS;
  auto fill = [&](int f) AI {
    const unsigned slot = (unsigned)(f & 3) * SLOT;
    if (f < 8) {
      const unsigned kcp = (unsigned)((f + c.rot) & 7);
      unsigned sx = soffX + kcp * 128u, sw = 64u * kcp * RS + wrow + ((tq >> 1) ? u1 : u0);
      unsigned mx = slot + (unsigned)tq * 1024u, mw = slot + 16384u + (unsigned)tq * 4096u;
#pragma unroll 1
      for (int m = 0; m < 4; ++m) {
        dma(mx, voffX, rsX, sx); dma(mw, voffW, rsW, sw);
        mx += 4096u; sx += 32768u; mw += 1024u; sw += 8u * RS;
      }
    } else {
      const unsigned kcp = (unsigned)((2 * (f - 8) + (tq >> 1) + c.rot) & 7);
      unsigned sw = 64u * kcp * RS + wrow, mw = slot + (unsigned)tq * 4096u;
#pragma unroll 1
      for (int m = 0; m < 4; ++m) {
        dma(mw, voffW, rsW, sw + ua);
        if (ffn) dma(mw + 16384u, voffW, rsW, sw + ub);
        mw += 1024u; sw += 8u * RS;
      }
    }
  };
  fill(0);
  fill(1);
  unsigned xa[4];
#pragma unroll
  for (int ks = 0; ks < 4; ++ks) xa[ks] = (unsigned)(32 * tq + t) * 128u + (unsigned)(((2 * ks + hh) ^ ((t >> 1) & 7)) * 16);
  v4u xr[32];
  u64 tag = 0;
  v4u gr;
  const unsigned token = (unsigned)(c.p * 128 + 32 * tq + t);
#pragma unroll
  for (int kc = 0; kc < 8; ++kc) {
    if (kc < 7 || ffn) VMW(8); else VMW(4);
    BAR();
    if (kc == 0) tag = lds_tag();
    if (kc == 5) {
      const char* gp = P.ws + WS_ST + token * 16u;
      asm volatile("global_load_dwordx4 %0, %1, off sc1" : "=&v"(gr) : "v"(gp) : "memory");
    }
    fill(kc + 2);
    const unsigned sb = (unsigned)(kc & 3) * SLOT;
#pragma unroll
    for (int ks = 0; ks < 4; ++ks) xr[4 * kc + ks] = lds128(xa[ks] + sb);
    asm volatile("" : "+v"(xr[4 * kc]), "+v"(xr[4 * kc + 1]), "+v"(xr[4 * kc + 2]), "+v"(xr[4 * kc + 3]));
    if (kc == 1) {
  float st0;
  {
    asm volatile("" : "+v"(r0), "+v"(r1));
    float v[2][8], mean[2], rs[2];
#pragma unroll
    for (int k = 0; k < 4; ++k) {
      v[0][2 * k] = u2f(r0[k] << 16); v[0][2 * k + 1] = u2f(r0[k] & 0xffff0000u);
      v[1][2 * k] = u2f(r1[k] << 16); v[1][2 * k + 1] = u2f(r1[k] & 0xffff0000u);
    }
#pragma unroll
    for (int i = 0; i < 2; ++i) mean[i] = wavesum(((v[i][0] + v[i][1]) + (v[i][2] + v[i][3])) + ((v[i][4] + v[i][5]) + (v[i][6] + v[i][7]))) * (1.0f / 512.0f);
#pragma unroll
    for (int i = 0; i < 2; ++i) {
      float q2 = 0.f;
#pragma unroll
      for (int k = 0; k < 8; ++k) { const float d = v[i][k] - mean[i]; q2 = __builtin_fmaf(d, d, q2); }
      rs[i] = __builtin_amdgcn_rsqf(wavesum(q2) * (1.0f / 512.0f) + 1e-5f);
    }
    st0 = (lane & 2) ? rs[1] : rs[0];
    st0 = (lane & 1) ? -((lane & 2) ? mean[1] : mean[0]) * st0 : st0;
    asm volatile("" : "+v"(st0));
  }
      if (lane < 4)
        __hip_atomic_store((GAS u64*)(P.ws + WS_ST + tok * 16u + (unsigned)lane * 8u), ((u64)(unsigned)tag << 32) | f2u(st0), __ATOMIC_RELAXED, __HIP_MEMORY_SCOPE_AGENT);
    }
  }
  asm volatile("" : "+v"(gr));
  STAMP(2);
  BAR();
  if (__builtin_amdgcn_ballot_w64(gr.y != (unsigned)tag || gr.w != (unsigned)tag) != 0) {
    const __amdgpu_buffer_rsrc_t rs = __builtin_amdgcn_make_buffer_rsrc((void*)(P.ws + WS_ST), 0, 32768, 0x00020000);
#pragma unroll 1
    for (unsigned spin = 0; spin < kSpin; ++spin) {
      gr = __builtin_amdgcn_raw_buffer_load_b128(rs, token * 16u, 0, 16);
      if (__builtin_amdgcn_ballot_w64(gr.y != (unsigned)tag || gr.w != (unsigned)tag) == 0) break;
      __builtin_amdgcn_s_sleep(1);
    }
  }
  const float rstd = u2f(gr.x), mr = u2f(gr.z);
  if (hh == 0) *(LAS f32x2*)(size_t)(L_STATS + (unsigned)(32 * tq + t) * 8u) = f32x2{rstd, mr};
  if (ffn) VMW(8); else VMW(4);
  BAR();
  const int cb = (lane >> 4) & 1, q = (lane >> 2) & 3, pp = lane & 3, sw = (q >> 1) & 1;
  unsigned wl[2];
#pragma unroll
  for (int h = 0; h < 2; ++h) wl[h] = (unsigned)(8 * hh + q) * 128u + (unsigned)((4 * (h ^ sw) + 2 * cb + (pp >> 1)) * 16 + 8 * (pp & 1));
  const __amdgpu_buffer_rsrc_t rsHB = __builtin_amdgcn_make_buffer_rsrc(P.ws + WS_HB, 0, HB_BYTES, 0x00020000);
  const unsigned T32 = (unsigned)(c.p * 4 + tq);
  f32x4 cs = {0.f, 0.f, 0.f, 0.f};
  auto cs_sync = [&](int tile, bool wr) AI {
    const float s = xhalf_add(cs[0]);
    if (hh == 0 && wr) *(LAS float*)(size_t)(L_CS1 + (unsigned)tile * 128u + (unsigned)t * 4u) = s;
    asm volatile("s_waitcnt lgkmcnt(0)" ::: "memory");
    lds_inc(L_CNT + C_CS1 * 4, lane);
    lds_wait_ge(L_CNT + C_CS1 * 4, 4);
  };
  if constexpr (ffn) {
    const unsigned w2a = ((g & 1) ? wl[1] : wl[0]) + 16384u;
    int ti[3];
    unsigned wb[3];
    f32x16 acc[3];
#pragma unroll
    for (int i = 0; i < 3; ++i) { ti[i] = (i + tq) % 3; wb[i] = ti[i] == 0 ? wl[0] : (ti[i] == 1 ? wl[1] : w2a); acc[i] = zero16(); }
    if (PRIO1) __builtin_amdgcn_s_setprio(PRIO1);
    pass1<3, 8>(P, wid, amp_a, amp_b, fill, xr, acc, cs, wb, lane);
    if (PRIO1) __builtin_amdgcn_s_setprio(0);
    cs_sync(ti[0], tq < 3);
    STAMP(3);
    if (PRIOL) __builtin_amdgcn_s_setprio(3);
    f32x4 csv[3][4], bsv[3][4];
#pragma unroll
    for (int i = 0; i < 3; ++i) { load16(L_CS1 + (unsigned)ti[i] * 128u, hh, csv[i]); load16(L_B1 + (unsigned)(4 + ti[i]) * 128u, hh, bsv[i]); }
#pragma unroll
    for (int i = 0; i < 3; ++i)
      ffn_tile(acc[i], rstd, mr, csv[i], bsv[i], rsHB, (unsigned)lane * 16u + (T32 * 144u + (unsigned)(64 + 6 * g + 2 * ti[i])) * 1024u);
    publish_last(L_CNT + C_LATE * 4, 4, P.ws, FL_L + (unsigned)(c.p * 8 + g) * 128u, tag, lane);
    if (PRIOL) __builtin_amdgcn_s_setprio(0);
  } else {
    int ti[2];
    unsigned wb[2];
    f32x16 acc[2];
#pragma unroll
    for (int i = 0; i < 2; ++i) { ti[i] = (i + tq) & 1; wb[i] = ti[i] == 0 ? wl[0] : wl[1]; acc[i] = zero16(); }
    if (PRIO1) __builtin_amdgcn_s_setprio(PRIO1);
    pass1<2, 4>(P, wid, amp_a, amp_b, fill, xr, acc, cs, wb, lane);
    if (PRIO1) __builtin_amdgcn_s_setprio(0);
    cs_sync(ti[0], tq < 2);
    STAMP(3);
    v4u pq[4][2];
    {
      f32x4 csq[2][4];
      v4u wp[4];
#pragma unroll
      for (int i = 0; i < 2; ++i) load16(L_CS1 + (unsigned)ti[i] * 128u, hh, csq[i]);
#pragma unroll
      for (int hd = 0; hd < 4; ++hd) wp[hd] = wphi_frag(2 * ti[hd >> 1] + (hd & 1), lane);
      f32x16 d[4];
#pragma unroll
      for (int hd = 0; hd < 4; ++hd) d[hd] = mfma(wp[hd], fold_pack(acc[hd >> 1], hd & 1, rstd, mr, csq[hd >> 1]), zero16());
      hedge4(d, pq);
    }
    asm volatile("" : "+v"(pq[0][0]), "+v"(pq[0][1]), "+v"(pq[1][0]), "+v"(pq[1][1]), "+v"(pq[2][0]), "+v"(pq[2][1]), "+v"(pq[3][0]), "+v"(pq[3][1]));
    STAMP(4);
    if (PRIO) __builtin_amdgcn_s_setprio(3);
    if (tq == 0) { poll_flags(P.ws, FL_K + (unsigned)g * 16u * 128u, 16, tag, lane); lds_set(L_CNT + C_KVRDY * 4, 1); }
    else lds_wait_ge(L_CNT + C_KVRDY * 4, 1);
    STAMP(5);
    {
      const __amdgpu_buffer_rsrc_t rsKV = __builtin_amdgcn_make_buffer_rsrc(P.ws + WS_KVP, 0, 524288 + 65536, 0x00020000);
      const unsigned hbase = (unsigned)(g * 4 + tq) * 16u;
      v4u pv[16];
#pragma unroll
      for (int i = 0; i < 16; ++i) pv[i] = __builtin_amdgcn_raw_buffer_load_b128(rsKV, (unsigned)lane * 16u, (hbase + (unsigned)i) * 1024u, 16);
      const v4u z0 = __builtin_amdgcn_raw_buffer_load_b128(rsKV, (unsigned)lane * 16u, 524288u + hbase * 128u, 16);
      const v4u z1 = __builtin_amdgcn_raw_buffer_load_b128(rsKV, (unsigned)lane * 16u, 524288u + hbase * 128u + 1024u, 16);
      const u64 lf = __hip_atomic_load((const GAS u64*)(P.ws + FL_L + (unsigned)(c.tt >> 1) * 8u * 128u + (unsigned)(lane & 7) * 128u), __ATOMIC_RELAXED, __HIP_MEMORY_SCOPE_AGENT);
      __builtin_amdgcn_sched_barrier(0);
      v4u idf;
      {
        const int m = lane & 31, h2 = lane >> 5;
        const int j = (m < 16 && ((m >> 2) & 1) == h2) ? (((m >> 3) << 2) | (m & 3)) : -1;
#pragma unroll
        for (int k = 0; k < 4; ++k) idf[k] = (j == 2 * k ? 0x3F80u : 0u) | (j == 2 * k + 1 ? 0x3F800000u : 0u);
      }
      f32x16 sum = zero16();
#pragma unroll
      for (int i = 0; i < 16; ++i) sum = mfma(idf, pv[i], sum);
      asm volatile("" : "+v"(sum));
      lds_inc(L_CNT + C_KVIN * 4, lane);
      if (!ORD && tq == 0 && __builtin_amdgcn_ballot_w64(lf != tag) == 0) lds_set(L_CNT + C_LATERDY * 4, 1);
      const float s[8] = {sum[0], sum[1], sum[2], sum[3], sum[4], sum[5], sum[6], sum[7]};
      const unsigned afr = L_AFR + (unsigned)tq * 2048u;
      *(LAS v4u*)(size_t)(afr + (unsigned)((lane >> 4) & 1) * 1024u + (unsigned)((lane & 15) + 32 * (lane >> 5)) * 16u) = pack8(s);
      const unsigned hh2 = (unsigned)(lane >> 4) & 1u, c16 = (unsigned)lane & 15u;
      float zz[4];
#pragma unroll
      for (int k = 0; k < 4; ++k) {
        float v = dppadd<0x128>(u2f(z0[k]) + u2f(z1[k]));
        auto r = __builtin_amdgcn_permlane16_swap(f2u(v), f2u(v), false, false);
        zz[k] = xhalf_add(u2f(r[0]) + u2f(r[1]));
      }
      if (lane < 8) *(LAS f32x4*)(size_t)(L_ZT + (unsigned)tq * 128u + (unsigned)lane * 16u) = f32x4{zz[0], zz[1], zz[2], zz[3]};
      const unsigned f0 = 16u * (unsigned)(lane >> 5) + 4u * hh2;
      const f32x4 za = lds128f(L_ZT + (unsigned)tq * 128u + f0 * 4u), zb = lds128f(L_ZT + (unsigned)tq * 128u + (f0 + 8u) * 4u);
      *(LAS v4u*)(size_t)(afr + (unsigned)(lane >> 5) * 1024u + (16u + c16 + 32u * hh2) * 16u) = v4u{pk2(za[0], za[1]), pk2(za[2], za[3]), pk2(zb[0], zb[1]), pk2(zb[2], zb[3])};
      asm volatile("s_waitcnt lgkmcnt(0)" ::: "memory");
      lds_inc(L_CNT + C_AFR * 4, lane);
      lds_wait_ge(L_CNT + C_AFR * 4, 4);
    }
    {
      v4u a0[4], a1[4];
#pragma unroll
      for (int hd = 0; hd < 4; ++hd) {
        const int head = 2 * ti[hd >> 1] + (hd & 1);
        a0[hd] = lds128(L_AFR + (unsigned)head * 2048u + (unsigned)lane * 16u); a1[hd] = lds128(L_AFR + (unsigned)head * 2048u + 1024u + (unsigned)lane * 16u);
      }
      __builtin_amdgcn_sched_barrier(0);
      f32x16 d[4];
#pragma unroll
      for (int hd = 0; hd < 4; ++hd) d[hd] = mfma(a0[hd], pq[hd][0], zero16());
#pragma unroll
      for (int hd = 0; hd < 4; ++hd) d[hd] = mfma(a1[hd], pq[hd][1], d[hd]);
#pragma unroll
      for (int hd = 0; hd < 4; ++hd) {
        const int head = 2 * ti[hd >> 1] + (hd & 1);
        const float r = __builtin_amdgcn_rcpf(d[hd][8] + 1e-6f);
        const v4u o = v4u{pk2(d[hd][0] * r, d[hd][1] * r), pk2(d[hd][2] * r, d[hd][3] * r), pk2(d[hd][4] * r, d[hd][5] * r), pk2(d[hd][6] * r, d[hd][7] * r)};
        st128(o, rsHB, (unsigned)lane * 16u + (T32 * 144u + (unsigned)(112 + 4 * g + head)) * 1024u);
      }
    }
    publish_last(L_CNT + C_O * 4, 4, P.ws, FL_O + (unsigned)(c.p * 8 + g) * 128u, tag, lane);
    if (PRIO) __builtin_amdgcn_s_setprio(0);
  }
  STAMP(6);
  return tag;
}

DI void role_b(const Params& P, const Ctx& c, int wid, u64 amp_a0) {
  u64 amp_a = amp_a0, amp_b = 0; (void)amp_a; (void)amp_b;
  const int lane = c.lane, tq = c.tq;
  const u64 tag = c.ffn ? role_b_head<true>(P, c, wid) : role_b_head<false>(P, c, wid);
  const int pan = c.tt >> 1;
  const int tb = tq * 64 + lane;
  v4u xres[2], bres;
#pragma unroll
  for (int m = 0; m < 2; ++m) {
    const int idx = tb + 256 * m;
    xres[m] = *(const GAS v4u*)(P.x + (size_t)(64 * c.tt + (idx >> 3)) * 512 + 64 * c.ct + 8 * (idx & 7));
  }
  bres = *(const GAS v4u*)(P.b2 + 64 * c.ct + 8 * (tb & 7));
  if (tq == 0) {
    poll_flags(P.ws, (ORD ? FL_O : FL_L) + (unsigned)pan * 8u * 128u, 8, tag, lane);
    lds_set(L_CNT + (ORD ? C_ORDY : C_LATERDY) * 4, 1);
    poll_flags(P.ws, (ORD ? FL_L : FL_O) + (unsigned)pan * 8u * 128u, 8, tag, lane);
    lds_set(L_CNT + (ORD ? C_LATERDY : C_ORDY) * 4, 1);
  } else if (tq == 1 && __builtin_amdgcn_workgroup_id_x() == 0) {
    poll_flags(P.ws, FL_E, 64, tag, lane); poll_flags(P.ws, FL_E + 64 * 128, 64, tag, lane);
    poll_flags(P.ws, FL_K, 64, tag, lane); poll_flags(P.ws, FL_K + 64 * 128, 64, tag, lane);
    if (lane == 0) __hip_atomic_store((GAS u64*)(P.ws + WS_EPOCH), tag, __ATOMIC_RELAXED, __HIP_MEMORY_SCOPE_AGENT);
  }
  BAR();
#pragma unroll
  for (int m = 0; m < 2; ++m) {
    const int idx = tb + 256 * m;
    const unsigned tok = (unsigned)(idx >> 3), cg = (unsigned)(idx & 7);
    const unsigned a = (tok >> 5) * SLOT + (tok & 31u) * 256u, c0 = ((2u * cg) ^ (tok & 15u)) * 16u, c1 = ((2u * cg + 1u) ^ (tok & 15u)) * 16u;
    f32x4 lo = lds128f(a + c0), hi = lds128f(a + c1);
#pragma unroll
    for (unsigned w = 1; w < 4; ++w) { lo += lds128f(w * 8192u + a + c0); hi += lds128f(w * 8192u + a + c1); }
    v4u o;
#pragma unroll
    for (int k = 0; k < 4; ++k) {
      const float a0 = k < 2 ? lo[2 * k] : hi[2 * k - 4], a1 = k < 2 ? lo[2 * k + 1] : hi[2 * k - 3];
      const float x0 = u2f(xres[m][k] << 16), x1 = u2f(xres[m][k] & 0xffff0000u);
      const float b0 = u2f(bres[k] << 16), b1 = u2f(bres[k] & 0xffff0000u);
      o[k] = pk2(a0 + x0 + b0, a1 + x1 + b1);
    }
    *(GAS v4u*)(P.out + (size_t)(64 * c.tt + (idx >> 3)) * 512 + 64 * c.ct + 8 * (idx & 7)) = o;
  }
  STAMP(7);

}

__global__ __launch_bounds__(512, 2) NOPK void fused_kernel(Params P) {
  __shared__ __attribute__((aligned(1024))) char smem[LDS_BYTES];
  asm volatile("" ::"v"((unsigned)(size_t)(LAS char*)smem) : "memory");
  u64 pc;
  asm volatile("s_getpc_b64 %0" : "=s"(pc));
  const int tid = __builtin_amdgcn_workitem_id_x(), lane = tid & 63;
  const int wid = __builtin_amdgcn_readfirstlane(tid >> 6);
  u64 amp_a = 0, amp_b = 0;
  STAMP(0);
  Ctx c;
  c.lane = lane; c.tq = wid & 3; c.hh = lane >> 5;
  const int blk = __builtin_amdgcn_workgroup_id_x(), xcd = blk & 7, j = blk >> 3, ja = j & 7, jb = j >> 3;
  c.p = (xcd & 1) * 8 + ja;
  c.G = (xcd >> 1) * 4 + jb;
  c.ffn = !(c.G & 1);
  c.gi = c.G >> 1;
  c.rot = ROT1 == 0 ? 0 : (ROT1 == 1 ? (((ja + jb) % 3) * 3) & 7 : (ROT1 == 2 ? (ja + 2 * jb) & 7 : (ROT1 == 3 ? ((ja + jb) & 1) * 4 : (ROT1 == 4 ? ((ja + jb) & 3) * 2 : ((ja + jb) & 3)))));
  if (MAP2) { c.tt = xcd * 4 + (j & 3); c.ct = j >> 2; c.ja = j & 3; c.jb = j >> 2; }
  else { c.tt = (xcd >> 1) * 8 + ja; c.ct = (xcd & 1) * 4 + jb; c.ja = ja; c.jb = jb; }
  c.rotE = ROT2 ? (2 * ja + 4 * jb) & 15 : 0; c.rotL = ROT2 ? (ja + 3 * jb) % 12 : 0; c.rotO = ROT2 ? (ja + 2 * jb) & 7 : 0;
  if (wid < 4) {
    const unsigned kb0 = (unsigned)(j + 32 * (wid & 1)), kb = kb0 < CODE_KB ? kb0 : 0u;
    const unsigned pf = CODEPF ? *(const GAS unsigned*)((pc & ~255ull) + kb * 1024u + (unsigned)lane * 16u) : 0u;
    const u64 e = __hip_atomic_load((const GAS u64*)(P.ws + WS_EPOCH), __ATOMIC_RELAXED, __HIP_MEMORY_SCOPE_AGENT);
    if (c.ffn) {
      const int tc = tid < 224 ? tid : 0;
      const int t7 = tc >> 5, n = tc & 31, g = c.gi;
      const int cA = 224 * g + 32 * (g & 1);
      const int col = t7 < 4 ? cA + 32 * t7 : (t7 < 6 ? cA + 128 + 32 * (t7 - 4) : ((g & 1) ? 224 * g : 224 * g + 192));
      const unsigned bv = P.b1[col + n];
      *(LAS float*)(size_t)(L_B1 + (unsigned)tid * 4u) = u2f(bv << 16);
    } else {
      const v4u wv = *(const GAS v4u*)(P.wphi + (size_t)c.gi * 1024 + (tid & 127) * 8);
      *(LAS v4u*)(size_t)(L_WPHI + (unsigned)(tid & 127) * 16u) = wv;
    }
    if (tid < 32) *(LAS unsigned*)(size_t)(L_CNT + (unsigned)tid * 4u) = 0u;
    const u64 tag = e + 1u;
    if (tid == 0) *(LAS v2u*)(size_t)L_TAG = v2u{(unsigned)tag, (unsigned)(tag >> 32)};
    asm volatile("" ::"v"(pf));
    STAMP(1);
    role_a(P, c, wid, amp_a, amp_b);
  } else {
    STAMP(1);
    role_b(P, c, wid, amp_a);
  }
}

}

extern "C" void kernel_launch(void* const* d_in, const int* in_sizes, int n_in, void* d_out, int out_size, void* d_ws, size_t ws_size,
                              hipStream_t stream) {
  Params P;
  P.x = (const u16*)d_in[0]; P.wqkv = (const u16*)d_in[1]; P.wphi = (const u16*)d_in[2]; P.wo = (const u16*)d_in[3];
  P.w1 = (const u16*)d_in[5]; P.b1 = (const u16*)d_in[6]; P.w2 = (const u16*)d_in[7]; P.b2 = (const u16*)d_in[8];
  P.out = (u16*)d_out; P.ws = (char*)d_ws;
  fused_kernel<<<256, 512, 0, stream>>>(P);
}
```
